```python
import torch

def _softmax(x, axis=-1):
    return torch.softmax(x, dim=axis)
def _gelu(x):
    return torch.nn.functional.gelu(x, approximate="tanh")
def _silu(x):
    return torch.nn.functional.silu(x)
def _sigmoid(x):
    return torch.sigmoid(x)
def _layernorm(x, axis=-1, eps=1e-5):
    m = x.mean(dim=axis, keepdim=True)
    v = x.var(dim=axis, keepdim=True, unbiased=False)
    return (x - m) / torch.sqrt(v + eps)
def _scatter_add(tgt, idx, src, axis=0):
    return tgt.clone().index_add_(axis, idx, src)
def _segment_sum(src, seg, n):
    out = torch.zeros((n,) + src.shape[1:], dtype=src.dtype)
    return out.index_add_(0, seg, src)
def _pad(x, pw):
    flat = tuple(v for pair in reversed(pw) for v in pair)
    return torch.nn.functional.pad(x, flat)

kernel_name = 'recurrent_scan__ssm_conv_s4__glu_gated_glu__prompt'

def setup_inputs():

    _w = (lambda a: (lambda t: t.to(torch.bfloat16) if t.is_floating_point() else t)(torch.as_tensor(a)))
    def _A_setup():
        B, L, D, N = 1, 2048, 512, 128
        A_re = -(lambda _x: torch.exp(torch.as_tensor(_x)))((lambda *s: torch.randn(*s, dtype=torch.bfloat16) * 6.4)(D, N))
        A_im = torch.pi * (lambda *s: torch.randn(*s, dtype=torch.bfloat16) * 0.05)(D, N)
        B_re = (lambda *s: torch.randn(*s, dtype=torch.bfloat16) * 0.05)(D, N)
        B_im = (lambda *s: torch.randn(*s, dtype=torch.bfloat16) * 0.05)(D, N)
        C_re = (lambda *s: torch.randn(*s, dtype=torch.bfloat16) * 0.05)(D, N)
        C_im = (lambda *s: torch.randn(*s, dtype=torch.bfloat16) * 0.05)(D, N)
        log_dt = (lambda *s: torch.randn(*s, dtype=torch.bfloat16) * 0.05)(D) * 0.5 - 4.0
        Dskip = (lambda *s: torch.randn(*s, dtype=torch.bfloat16) * 0.05)(D)
        return {
            "u": _w((lambda *s: torch.randn(*s, dtype=torch.bfloat16) * 0.05)(B, L, D)),
            "A_re": _w(A_re),
            "A_im": _w(A_im),
            "B_re": _w(B_re),
            "B_im": _w(B_im),
            "C_re": _w(C_re),
            "C_im": _w(C_im),
            "log_dt": _w(log_dt),
            "Dskip": _w(Dskip),
        }
    _A_in = _A_setup()

    def _B_setup():
        B, S, D, F = 1, 2048, 512, 1792
        return {
            "x": _w((lambda *s: torch.randn(*s, dtype=torch.bfloat16) * 0.05)(B, S, D)),
            "w_gate": _w((lambda *s: torch.randn(*s, dtype=torch.bfloat16) * 0.05)(D, F)),
            "w_up": _w((lambda *s: torch.randn(*s, dtype=torch.bfloat16) * 0.05)(D, F)),
            "w_down": _w((lambda *s: torch.randn(*s, dtype=torch.bfloat16) * 0.05)(F, D)),
        }
    _B_in = _B_setup()

    return {
        "A_u": _A_in["u"],
        "A_A_re": _A_in["A_re"],
        "A_A_im": _A_in["A_im"],
        "A_B_re": _A_in["B_re"],
        "A_B_im": _A_in["B_im"],
        "A_C_re": _A_in["C_re"],
        "A_C_im": _A_in["C_im"],
        "A_log_dt": _A_in["log_dt"],
        "A_Dskip": _A_in["Dskip"],
        "B_x": _B_in["x"],
        "B_w_gate": _B_in["w_gate"],
        "B_w_up": _B_in["w_up"],
        "B_w_down": _B_in["w_down"],
    }

def reference(A_u, A_A_re, A_A_im, A_B_re, A_B_im, A_C_re, A_C_im, A_log_dt, A_Dskip, B_x, B_w_gate, B_w_up, B_w_down):
    def _A_ref(u, A_re, A_im, B_re, B_im, C_re, C_im, log_dt, Dskip):
        Bsz, L, D = u.shape
        dt = torch.exp(log_dt)[:, None]
        A = A_re + 1j * A_im
        Bc = B_re + 1j * B_im
        Cc = C_re + 1j * C_im
        logdA = dt * A
        dB = dt * Bc
        l = (lambda x, dt: x.to(dt))(torch.arange(L), A_re.dtype)
        P = torch.exp(logdA[:, :, None] * l[None, None, :])
        K = torch.einsum("dn,dnl,dn->dl", Cc, P, dB).real
        L2 = 2 * L
        Kp = _pad(K, ((0, 0), (0, L)))
        up = (lambda x, *d: x.permute(*d))(u, 0, 2, 1)
        up = _pad(up, ((0, 0), (0, 0), (0, L)))
        Kf = (lambda x, axes=(-2,-1): torch.fft.rfft2(x.float(), dim=axes))(Kp[:, None, :], axes=(-2, -1))
        Uf = (lambda x, axes=(-2,-1): torch.fft.rfft2(x.float(), dim=axes))(up[:, :, None, :], axes=(-2, -1))
        Yf = Uf * Kf[None, :, :, :]
        y = (lambda x, s, axes=(-2,-1): torch.fft.irfft2(x, s=s, dim=axes).to(torch.bfloat16))(Yf, (1, L2), axes=(-2, -1))[:, :, 0, :L]
        y = (lambda x, *d: x.permute(*d))(y, 0, 2, 1) + u * Dskip[None, None, :]
        return y

    def _B_ref(x, w_gate, w_up, w_down):
        g = torch.einsum("bsd,df->bsf", x, w_gate)
        u = torch.einsum("bsd,df->bsf", x, w_up)
        h = _sigmoid(g) * u
        return torch.einsum("bsf,fd->bsd", h, w_down)

    x = A_u
    xn = _layernorm(x, axis=-1)
    s = _A_ref(xn, A_A_re, A_A_im, A_B_re, A_B_im, A_C_re, A_C_im, A_log_dt, A_Dskip)
    g = _silu(_B_ref(xn, B_w_gate, B_w_up, B_w_down))
    return x + s * g
```

```cpp
#include <hip/hip_runtime.h>
#include <hip/hip_bf16.h>
#include <stdint.h>

typedef __attribute__((ext_vector_type(4))) short s16x4;
typedef __attribute__((ext_vector_type(8))) short s16x8;
typedef __attribute__((ext_vector_type(8))) _Float16 f16x8;
typedef __attribute__((ext_vector_type(8))) __bf16 bf16x8;
typedef __attribute__((ext_vector_type(2))) float f32x2;
typedef __attribute__((ext_vector_type(4))) float f32x4;
typedef __attribute__((ext_vector_type(16))) float f32x16;
typedef __attribute__((ext_vector_type(4))) unsigned u32x4;
typedef __attribute__((ext_vector_type(2))) unsigned u32x2;
typedef __attribute__((ext_vector_type(2))) __bf16 bf16x2;
typedef __attribute__((ext_vector_type(2))) _Float16 f16x2;
#define LAS __attribute__((address_space(3)))
typedef __amdgpu_buffer_rsrc_t rsrc_t;

constexpr int SL = 2048;
constexpr int DM = 512;
constexpr int NM = 128;
constexpr int FF = 1792;

constexpr size_t WS_XNF = 0;
constexpr size_t WS_KF = WS_XNF + (size_t)SL * DM * 2;
constexpr size_t WS_ST = WS_KF + (size_t)SL * DM * 2;
constexpr size_t WS_HF = WS_ST + (size_t)SL * DM * 2;
constexpr size_t WS_KS = WS_HF + (size_t)SL * FF * 2;

__device__ __forceinline__ float bf2f(unsigned short v) { return __uint_as_float((unsigned)v << 16); }
__device__ __forceinline__ unsigned pk_bf16(float lo, float hi) {
  f32x2 v = {lo, hi};
  return __builtin_bit_cast(unsigned, __builtin_convertvector(v, bf16x2));
}
__device__ __forceinline__ unsigned pk_f16(float lo, float hi) {
  f32x2 v = {lo, hi};
  return __builtin_bit_cast(unsigned, __builtin_convertvector(v, f16x2));
}
__device__ __forceinline__ float lo_f(unsigned p) { return __uint_as_float(p << 16); }
__device__ __forceinline__ float hi_f(unsigned p) { return __uint_as_float(p & 0xffff0000u); }
__device__ __forceinline__ float rbf(float x) { return lo_f(pk_bf16(x, x)); }

template <int CTRL>
__device__ __forceinline__ float dpp_mov(float v) {
  return __int_as_float(__builtin_amdgcn_update_dpp(0, __float_as_int(v), CTRL, 0xf, 0xf, true));
}
__device__ __forceinline__ float rdlane(float v, int l) {
  return __int_as_float(__builtin_amdgcn_readlane(__float_as_int(v), l));
}
__device__ __forceinline__ float wave_sum(float v) {
  v += dpp_mov<0xB1>(v);
  v += dpp_mov<0x4E>(v);
  v += dpp_mov<0x141>(v);
  v += dpp_mov<0x140>(v);
  return (rdlane(v, 0) + rdlane(v, 16)) + (rdlane(v, 32) + rdlane(v, 48));
}
__device__ __forceinline__ float wave_max(float v) {
  v = fmaxf(v, dpp_mov<0xB1>(v));
  v = fmaxf(v, dpp_mov<0x4E>(v));
  v = fmaxf(v, dpp_mov<0x141>(v));
  v = fmaxf(v, dpp_mov<0x140>(v));
  return fmaxf(fmaxf(rdlane(v, 0), rdlane(v, 16)), fmaxf(rdlane(v, 32), rdlane(v, 48)));
}

__device__ __forceinline__ rsrc_t mk_rsrc(const void* p, unsigned bytes) {
  return __builtin_amdgcn_make_buffer_rsrc((void*)p, (short)0, (int)bytes, 0x00020000);
}
__device__ __forceinline__ void dma16(unsigned lds_dst, unsigned voff, rsrc_t r, unsigned soff) {
  asm volatile("s_mov_b32 m0, %0\n\ts_nop 0\n\tbuffer_load_dwordx4 %1, %2, %3 offen lds"
               :: "s"(lds_dst), "v"(voff), "s"(r), "s"(soff) : "memory");
}
__device__ __forceinline__ void ld128(u32x4& dst, unsigned voff, rsrc_t r, unsigned soff) {
  asm volatile("buffer_load_dwordx4 %0, %1, %2, %3 offen" : "=v"(dst) : "v"(voff), "s"(r), "s"(soff) : "memory");
}
__device__ __forceinline__ void ld32(unsigned& dst, unsigned voff, rsrc_t r, unsigned soff) {
  asm volatile("buffer_load_dword %0, %1, %2, %3 offen" : "=v"(dst) : "v"(voff), "s"(r), "s"(soff) : "memory");
}
__device__ __forceinline__ void ld16(unsigned& dst, unsigned voff, rsrc_t r, unsigned soff) {
  asm volatile("buffer_load_ushort %0, %1, %2, %3 offen" : "=v"(dst) : "v"(voff), "s"(r), "s"(soff) : "memory");
}
template <int N>
__device__ __forceinline__ void wait_vm() { asm volatile("s_waitcnt vmcnt(%0)" :: "n"(N) : "memory"); }
__device__ __forceinline__ void bar_lgkm() { asm volatile("s_waitcnt lgkmcnt(0)\n\ts_barrier" ::: "memory"); }
#define PIN(x) asm volatile("" : "+v"(x))

__device__ __forceinline__ s16x4 trrd(const LAS char* p) {
  return __builtin_amdgcn_ds_read_tr16_b64_v4i16((LAS s16x4*)p);
}
__device__ __forceinline__ s16x8 cat8(s16x4 a, s16x4 b) {
  return __builtin_shufflevector(a, b, 0, 1, 2, 3, 4, 5, 6, 7);
}

struct cplx { float re, im; };
__device__ __forceinline__ cplx cmul(cplx a, cplx b) {
  cplx r;
  r.re = fmaf(a.re, b.re, -(a.im * b.im));
  r.im = fmaf(a.re, b.im, a.im * b.re);
  return r;
}
__device__ __forceinline__ cplx cexp_l(float a, float b, float l) {
  const float mag = __builtin_amdgcn_exp2f(a * l * 1.4426950408889634f);
  const float x = b * l;
  const float C1 = 0.15915494309189535f;
  const float C2 = (float)(0.15915494309189533577 - (double)0.15915494309189535f);
  const float hi = x * C1;
  const float lo = fmaf(x, C1, -hi) + x * C2;
  const float r = (hi - __builtin_rintf(hi)) + lo;
  cplx z;
  z.re = mag * __builtin_amdgcn_cosf(r);
  z.im = mag * __builtin_amdgcn_sinf(r);
  return z;
}

constexpr int KG_ROW = 272;
constexpr int KG_TAB = 32 * KG_ROW;
constexpr int KG_WAVE = 2 * KG_TAB;
constexpr int K1_LDS = 4 * KG_WAVE + 4 * 648 * 4 + 2 * 656 * 2 + 64;

__global__ __launch_bounds__(256) void k1_ln_kgen(
    const unsigned short* __restrict__ x, const unsigned short* __restrict__ A_re,
    const unsigned short* __restrict__ A_im, const unsigned short* __restrict__ B_re,
    const unsigned short* __restrict__ B_im, const unsigned short* __restrict__ C_re,
    const unsigned short* __restrict__ C_im, const unsigned short* __restrict__ log_dt,
    char* __restrict__ ws) {
  extern __shared__ __attribute__((aligned(16))) char smem[];
  const int tid = threadIdx.x, lane = tid & 63, wv = tid >> 6;
  if (blockIdx.x < 256) {
    const int r0 = blockIdx.x * 8;
    u32x4 v[2];
#pragma unroll
    for (int i = 0; i < 2; ++i) v[i] = *(const u32x4*)(x + (size_t)(r0 + 2 * wv + i) * DM + lane * 8);
#pragma unroll
    for (int i = 0; i < 2; ++i) {
      float f[8];
#pragma unroll
      for (int j = 0; j < 4; ++j) { f[2 * j] = lo_f(v[i][j]); f[2 * j + 1] = hi_f(v[i][j]); }
      float s = ((f[0] + f[1]) + (f[2] + f[3])) + ((f[4] + f[5]) + (f[6] + f[7]));
      const float mean = wave_sum(s) * (1.0f / DM);
      float q = 0.f;
#pragma unroll
      for (int j = 0; j < 8; ++j) { const float d = f[j] - mean; q = fmaf(d, d, q); }
      const float var = wave_sum(q) * (1.0f / DM);
      const float mb = rbf(mean);
      const float sb = rbf(__builtin_sqrtf(rbf(rbf(var) + 1e-5f)));
      const float rs = 1.0f / sb;
      u32x4 o;
#pragma unroll
      for (int j = 0; j < 4; ++j) {
        const unsigned d = pk_bf16(f[2 * j] - mb, f[2 * j + 1] - mb);
        o[j] = pk_bf16(lo_f(d) * rs, hi_f(d) * rs);
      }
      *(u32x4*)(smem + (2 * wv + i) * 1040 + lane * 16) = o;
    }
    __syncthreads();
    char* xnf = ws + WS_XNF + (size_t)(r0 >> 5) * 32768 + (r0 & 31) * 16;
#pragma unroll
    for (int m = 0; m < 2; ++m) {
      const int item = tid + 256 * m, chunk = item >> 3, row = item & 7;
      const u32x4 o = *(const u32x4*)(smem + row * 1040 + chunk * 16);
      *(u32x4*)(xnf + chunk * 512 + row * 16) = o;
    }
    return;
  }
  const int chl = wv >> 1;
  const int d = (blockIdx.x - 256) * 2 + chl;
  const int n = (wv & 1) * 64 + lane;
  const int pi = d * NM + n;
  const float are = bf2f(A_re[pi]), aim = bf2f(A_im[pi]);
  const float bre = bf2f(B_re[pi]), bim = bf2f(B_im[pi]);
  const float cre = bf2f(C_re[pi]), cim = bf2f(C_im[pi]);
  const float dt = rbf((float)exp((double)bf2f(log_dt[d])));
  float a = dt * are;
  const float b = dt * aim;
  const float dbr = dt * bre, dbi = dt * bim;
  cplx w;
  w.re = fmaf(cre, dbr, -(cim * dbi));
  w.im = fmaf(cre, dbi, cim * dbr);
  const float amax = fmaxf(wave_max(a), 0.f);
  a -= amax;
  const float wm = wave_max(fmaxf(fabsf(w.re), fabsf(w.im)));
  int we = 0;
  (void)frexpf(wm, &we);
  const float wsc = ldexpf(1.0f, -we);
  w.re *= wsc; w.im *= wsc;

  const cplx z1 = cexp_l(a, b, 1.f), z8 = cexp_l(a, b, 8.f), z256 = cexp_l(a, b, 256.f);
  char* ptab = smem + wv * KG_WAVE + lane * 4;
  char* qtab = ptab + KG_TAB;
#define PUT_P(r, v) *(unsigned*)(ptab + (r) * KG_ROW) = pk_f16((v).re, -(v).im)
#define PUT_Q(c, v) *(unsigned*)(qtab + (c) * KG_ROW) = pk_f16((v).re, (v).im)
  {
    cplx q[4];
    q[0].re = 1.f; q[0].im = 0.f;
    q[1] = z8; q[2] = cmul(z8, z8); q[3] = cmul(q[2], z8);
    const cplx z32 = cmul(q[2], q[2]);
#pragma unroll
    for (int c = 0; c < 32; c += 4) {
#pragma unroll
      for (int i = 0; i < 4; ++i) {
        PUT_Q(c + i, q[i]);
        if (c + 4 < 32) q[i] = cmul(q[i], z32);
      }
    }
    const cplx z2 = cmul(z1, z1), z4 = cmul(z2, z2), z6 = cmul(z4, z2);
    const cplx p1 = cmul(w, z1);
    const cplx p2 = cmul(w, z2), p3 = cmul(p1, z2);
    PUT_P(0, w); PUT_P(1, p1); PUT_P(2, p2); PUT_P(3, p3);
    PUT_P(4, cmul(w, z4)); PUT_P(5, cmul(p1, z4)); PUT_P(6, cmul(p2, z4)); PUT_P(7, cmul(p3, z4));
    const cplx z512 = cmul(z256, z256);
    const cplx w1 = cmul(w, z256), w2 = cmul(w, z512);
    const cplx w3 = cmul(w1, z512), w4 = cmul(w2, z512), w5 = cmul(w3, z512), w6 = cmul(w4, z512);
    const cplx w7 = cmul(w5, z512), w8 = cmul(w6, z512);
    PUT_P(8, w1); PUT_P(9, cmul(w1, z2)); PUT_P(10, cmul(w1, z4)); PUT_P(11, cmul(w1, z6));
    PUT_P(12, w2); PUT_P(13, cmul(w2, z4)); PUT_P(14, w3); PUT_P(15, cmul(w3, z4));
    PUT_P(16, w4); PUT_P(17, w5); PUT_P(18, w6); PUT_P(19, w7); PUT_P(20, w8);
#pragma unroll
    for (int r = 21; r < 32; ++r) *(unsigned*)(ptab + r * KG_ROW) = 0u;
  }
  __builtin_amdgcn_fence(__ATOMIC_RELEASE, "wavefront");
  __builtin_amdgcn_wave_barrier();
  __builtin_amdgcn_fence(__ATOMIC_ACQUIRE, "wavefront");
  f32x16 acc = {};
  {
    const char* pa = smem + wv * KG_WAVE + (lane & 31) * KG_ROW + (lane >> 5) * 16;
#pragma unroll
    for (int s = 0; s < 8; ++s) {
      const f16x8 af = *(const f16x8*)(pa + s * 32);
      const f16x8 bf = *(const f16x8*)(pa + KG_TAB + s * 32);
      acc = __builtin_amdgcn_mfma_f32_32x32x16_f16(af, bf, acc, 0, 0, 0);
    }
  }
  float* kp = (float*)(smem + 4 * KG_WAVE) + wv * 648;
  float* kb = (float*)(smem + 4 * KG_WAVE + 4 * 648 * 4 + 2 * 656 * 2);
  unsigned short* kc = (unsigned short*)(smem + 4 * KG_WAVE + 4 * 648 * 4) + chl * 656;
  {
    const int c = lane & 31, h = lane >> 5;
    const float un = ldexpf(1.0f, we);
    const float g2 = amax * 1.4426950408889634f;
    int idx[9], ell[9];
#pragma unroll
    for (int i = 0; i < 4; ++i) {
      idx[i] = 4 * h + i + 8 * c; ell[i] = idx[i];
      idx[4 + i] = h ? 384 + 64 * (i >> 1) + (i & 1) + 2 * c : 256 + i + 4 * c;
      ell[4 + i] = h ? 512 + 256 * (i >> 1) + 4 * (i & 1) + 8 * c : 256 + 2 * i + 8 * c;
    }
#pragma unroll
    for (int i = 0; i < 8; ++i) {
      float v = acc[i] * un;
      if (amax > 0.f) v *= __builtin_amdgcn_exp2f(g2 * (float)ell[i]);
      kp[idx[i]] = v;
    }
#pragma unroll
    for (int i = 0; i < 4; ++i) {
      float v = acc[8 + i] * un;
      const int e = h ? 2048 : 1024 + 256 * i + 8 * c;
      if (amax > 0.f) v *= __builtin_amdgcn_exp2f(g2 * (float)e);
      if (h == 0) kp[512 + 32 * i + c] = v;
      else if (i == 0 && c == 0) kp[640] = v;
    }
    if (lane == 0) kb[wv] = (float)we + ceilf(g2 * 2048.f);
  }
  __syncthreads();
  {
    const float eb = fminf(fmaxf(fmaxf(kb[2 * chl], kb[2 * chl + 1]), -100.f), 120.f);
    const int E = (int)eb - 7;
    const float sc = ldexpf(1.0f, -E);
    const float* k0 = (const float*)(smem + 4 * KG_WAVE) + (2 * chl) * 648;
    const float* k1 = k0 + 648;
    const int t = tid & 127;
#pragma unroll
    for (int m = 0; m < 6; ++m) {
      const int i = t + 128 * m;
      if (i <= 640) kc[i] = (unsigned short)(pk_f16((k0[i] + k1[i]) * sc, 0.f) & 0xffffu);
    }
    if (t == 0) ((float*)(ws + WS_KS))[d] = ldexpf(1.0f, E);
  }
  __syncthreads();
  {
    const int t = tid & 127;
    unsigned short* kf = (unsigned short*)(ws + WS_KF) + (size_t)d * SL;
#pragma unroll
    for (int m = 0; m < 2; ++m) {
      const int l0 = (t + 128 * m) * 8;
      unsigned e[8];
#pragma unroll
      for (int j = 0; j < 8; ++j) {
        const int l = l0 + j;
        const unsigned bits = __float_as_uint((float)l);
        const int rb = (int)((bits + 0x7fffu + ((bits >> 16) & 1u)) >> 16) - 0x4300 + 128;
        e[j] = kc[l < 128 ? l : rb];
      }
      u32x4 o;
#pragma unroll
      for (int j = 0; j < 4; ++j) o[j] = e[2 * j] | (e[2 * j + 1] << 16);
      *(u32x4*)(kf + l0) = o;
    }
  }
}

constexpr int W_UPOFF = 14336 + 128;
constexpr int W_STAGE = W_UPOFF + 14336;
constexpr int W_NBUF = 5;
constexpr int CV_BASE = W_NBUF * W_STAGE;
constexpr int CV_KSTR = (48 + 2048) * 2;
constexpr int CV_USTR = (80 + 2048) * 2;
constexpr int CV_U = CV_BASE + 2 * CV_KSTR;
constexpr int K2_LDS = CV_U + 2 * CV_USTR;

template <int J0, int J1>
__device__ __forceinline__ void conv_part(const LAS char* kbase, const LAS char* ubase, int lane,
                                          float kscale, float dskip, unsigned short* sT) {
  constexpr int NT = J1 - J0 + 1;
  constexpr int NQ = J1 / 2 + 1;
  const int G = lane >> 4, q4 = (lane & 15) >> 2, p = lane & 3;
  const LAS char* pa = kbase + 2 * (76 + 16 * p - 32 * G - 4 * q4 - 16);
  const LAS char* pb = ubase + 2 * (-76 + 32 * G + 4 * q4 + 4 * p);
  f16x8 A[NQ];
#pragma unroll
  for (int q = 0; q < NQ; ++q)
    A[q] = __builtin_bit_cast(f16x8, cat8(trrd(pa + 256 * q + 32), trrd(pa + 256 * q)));
  f32x4 acc[NT];
#pragma unroll
  for (int j = 0; j < NT; ++j) acc[j] = (f32x4){0.f, 0.f, 0.f, 0.f};
#pragma unroll
  for (int w = 0; w <= J1; ++w) {
    const f16x8 B = __builtin_bit_cast(f16x8, cat8(trrd(pb + 128 * w), trrd(pb + 128 * w + 32)));
#pragma unroll
    for (int q = NQ - 1; q >= 0; --q) {
      const int j = w + 2 * q;
      if (j >= J0 && j <= J1) acc[j - J0] = __builtin_amdgcn_mfma_f32_16x16x32_f16(A[q], B, acc[j - J0], 0, 0, 0);
    }
  }
  float c3 = 0.f, c2 = 0.f, c1 = 0.f;
#pragma unroll
  for (int j = J0; j <= J1; ++j) {
    const f32x4 a = acc[j - J0];
    const float h3 = a[3];
    const float h2 = a[2] + __int_as_float(__builtin_amdgcn_update_dpp(__float_as_int(c3), __float_as_int(h3), 0x138, 0xf, 0xf, false));
    const float h1 = a[1] + __int_as_float(__builtin_amdgcn_update_dpp(__float_as_int(c2), __float_as_int(h2), 0x138, 0xf, 0xf, false));
    const float y = a[0] + __int_as_float(__builtin_amdgcn_update_dpp(__float_as_int(c1), __float_as_int(h1), 0x138, 0xf, 0xf, false));
    c3 = dpp_mov<0x13C>(h3); c2 = dpp_mov<0x13C>(h2); c1 = dpp_mov<0x13C>(h1);
    if (J0 == 0 || j > J0) {
      const int t = 64 * j + lane;
      const float u = (float)*(const LAS _Float16*)(ubase + 2 * t);
      const unsigned pr = pk_bf16(y * kscale, u * dskip);
      sT[t] = (unsigned short)(pk_bf16(lo_f(pr) + hi_f(pr), 0.f) & 0xffffu);
    }
  }
}

__global__ __launch_bounds__(256, 1) void k2_conv_gemm1(
    const unsigned short* __restrict__ wg, const unsigned short* __restrict__ wu,
    const unsigned short* __restrict__ dsk, char* __restrict__ ws) {
  extern __shared__ __attribute__((aligned(16))) char smem[];
  LAS char* lds = (LAS char*)smem;
  const unsigned ldsb = (unsigned)(uintptr_t)lds;
  const int tid = threadIdx.x, lane = tid & 63;
  const int wv = __builtin_amdgcn_readfirstlane(tid >> 6);
  const int xcd = blockIdx.x & 7, bi = blockIdx.x >> 3;
  const int mt = (xcd >> 2) * 8 + (bi >> 2);
  const int ft = (xcd & 3) * 4 + (bi & 3);
  const int cc = xcd * 8 + (bi >> 2);
  const int pair = bi & 3;
  const int d0 = cc * 8 + pair * 2;

  const rsrc_t r_xn = mk_rsrc(ws + WS_XNF, SL * DM * 2);
  const rsrc_t r_kf = mk_rsrc(ws + WS_KF, SL * DM * 2);
  const rsrc_t r_w = mk_rsrc(wv < 2 ? wg : wu, DM * FF * 2);

  unsigned uw[8];
  {
    const unsigned voff = (unsigned)(tid >> 5) * 32768u + (unsigned)(tid & 31) * 16u;
    const unsigned sb = (unsigned)(cc * 512 + pair * 4);
#pragma unroll
    for (int m = 0; m < 8; ++m) ld32(uw[m], voff, r_xn, sb + m * 8u * 32768u);
  }
  {
    const int chl = wv >> 1, hf = wv & 1;
#pragma unroll
    for (int i = 0; i < 2; ++i)
      dma16(ldsb + CV_BASE + chl * CV_KSTR + 96 + (hf * 2 + i) * 1024, lane * 16u, r_kf,
            (unsigned)((d0 + chl) * 4096 + (hf * 2 + i) * 1024));
  }
  unsigned woff[7];
#pragma unroll
  for (int i = 0; i < 7; ++i) {
    const int c = ((wv & 1) * 7 + i) * 64 + lane;
    const int row = c / 14, col = c - row * 14;
    woff[i] = (unsigned)(row * (FF * 2) + col * 16);
  }
  const unsigned wdst = ldsb + (wv >> 1) * W_UPOFF + (wv & 1) * 7 * 1024;
  const unsigned wsrc = (unsigned)(ft * 224);
#define W_STAGE_ISSUE(st, buf)                                                             \
  _Pragma("unroll") for (int i = 0; i < 7; ++i)                                            \
      dma16(wdst + (buf) * W_STAGE + i * 1024, woff[i], r_w, wsrc + (st) * 64u * (FF * 2))
  W_STAGE_ISSUE(0, 0);
  W_STAGE_ISSUE(1, 1);
  W_STAGE_ISSUE(2, 2);
  u32x4 xb[32];
  {
    const unsigned sb = (unsigned)(mt * 4 + wv) * 32768u;
#pragma unroll
    for (int s = 0; s < 32; ++s) ld128(xb[s], lane * 16u, r_xn, sb + s * 1024u);
  }
  if (tid < 32) {
    const u32x4 z = {0u, 0u, 0u, 0u};
    if (tid < 12) *(LAS u32x4*)(lds + CV_BASE + (tid / 6) * CV_KSTR + (tid % 6) * 16) = z;
    else *(LAS u32x4*)(lds + CV_U + ((tid - 12) / 10) * CV_USTR + ((tid - 12) % 10) * 16) = z;
  }
  const float kscale = ((const float*)(ws + WS_KS))[d0 + (wv >> 1)];
  const unsigned dsk2 = ((const unsigned*)dsk)[d0 >> 1];
  const float dskip = (wv >> 1) ? hi_f(dsk2) : lo_f(dsk2);
  wait_vm<53>();
#pragma unroll
  for (int m = 0; m < 8; ++m) {
    PIN(uw[m]);
    const unsigned hp = pk_f16(lo_f(uw[m]), hi_f(uw[m]));
    const int t = tid + 256 * m;
    *(LAS unsigned short*)(lds + CV_U + 160 + 2 * t) = (unsigned short)(hp & 0xffffu);
    *(LAS unsigned short*)(lds + CV_U + CV_USTR + 160 + 2 * t) = (unsigned short)(hp >> 16);
  }
  W_STAGE_ISSUE(3, 3);
  W_STAGE_ISSUE(4, 4);
  bar_lgkm();
  {
    const int chl = wv >> 1;
    const LAS char* kbase = lds + CV_BASE + chl * CV_KSTR + 96;
    const LAS char* ubase = lds + CV_U + chl * CV_USTR + 160;
    unsigned short* sT = (unsigned short*)(ws + WS_ST) + (size_t)(d0 + chl) * SL;
    if (wv & 1) conv_part<21, 31>(kbase, ubase, lane, kscale, dskip, sT);
    else conv_part<0, 21>(kbase, ubase, lane, kscale, dskip, sT);
  }
  wait_vm<0>();
#pragma unroll
  for (int s = 0; s < 32; ++s) PIN(xb[s]);
  f32x16 acc[7];
#pragma unroll
  for (int T = 0; T < 7; ++T) acc[T] = (f32x16){};
  const int G = lane >> 4;
  const LAS char* wa = lds + (G & 1) * W_UPOFF + (8 * (G >> 1) + ((lane & 15) >> 2)) * 224 + (lane & 3) * 8;
#pragma unroll
  for (int st = 0; st < 8; ++st) {
    if (st == 5) wait_vm<14>();
    if (st == 6) wait_vm<7>();
    if (st == 7) wait_vm<0>();
    if (st != 4) bar_lgkm();
    if (st >= 1 && st <= 3) { W_STAGE_ISSUE(st + 4, st - 1); }
    const LAS char* wb = wa + (st % W_NBUF) * W_STAGE;
#pragma unroll
    for (int ks = 0; ks < 4; ++ks) {
      const bf16x8 B = __builtin_bit_cast(bf16x8, xb[st * 4 + ks]);
#pragma unroll
      for (int T = 0; T < 7; ++T) {
        const bf16x8 A = __builtin_bit_cast(bf16x8, cat8(trrd(wb + (16 * ks) * 224 + 32 * T),
                                                         trrd(wb + (16 * ks + 4) * 224 + 32 * T)));
        acc[T] = __builtin_amdgcn_mfma_f32_32x32x16_bf16(A, B, acc[T], 0, 0, 0);
      }
    }
  }
  char* hf = ws + WS_HF + ((size_t)(mt * 4 + wv) * 112 + ft * 7) * 1024 + lane * 16;
#pragma unroll
  for (int T = 0; T < 7; ++T) {
    u32x4 o;
#pragma unroll
    for (int j = 0; j < 4; ++j) {
      const unsigned gp = pk_bf16(acc[T][2 * j], acc[T][2 * j + 1]);
      const unsigned up = pk_bf16(acc[T][8 + 2 * j], acc[T][8 + 2 * j + 1]);
      const float s0 = __builtin_amdgcn_rcpf(1.0f + __builtin_amdgcn_exp2f(lo_f(gp) * -1.4426950408889634f));
      const float s1 = __builtin_amdgcn_rcpf(1.0f + __builtin_amdgcn_exp2f(hi_f(gp) * -1.4426950408889634f));
      const unsigned sp = pk_bf16(s0, s1);
      o[j] = pk_bf16(lo_f(sp) * lo_f(up), hi_f(sp) * hi_f(up));
    }
    *(u32x4*)(hf + T * 1024) = o;
  }
}

constexpr int G2_DP = 14;
constexpr int G2_RED = 272;
constexpr int K3_LDS = 4 * G2_DP * 2048;

__global__ __launch_bounds__(256, 1) void k3_gemm2(
    const unsigned short* __restrict__ x, const unsigned short* __restrict__ wd,
    char* __restrict__ ws, unsigned short* __restrict__ out) {
  extern __shared__ __attribute__((aligned(16))) char smem[];
  LAS char* lds = (LAS char*)smem;
  const unsigned ldsb = (unsigned)(uintptr_t)lds;
  const int tid = threadIdx.x, lane = tid & 63;
  const int wv = __builtin_amdgcn_readfirstlane(tid >> 6);
  const int xcd = blockIdx.x & 7, bi = blockIdx.x >> 3;
  const int tt = xcd * 4 + (bi >> 3);
  const int dtile = bi & 7;
  const int t0 = tt * 64, dd0 = dtile * 64;

  const rsrc_t r_h = mk_rsrc(ws + WS_HF, SL * FF * 2);
  const rsrc_t r_wd = mk_rsrc(wd, FF * DM * 2);
  const rsrc_t r_x = mk_rsrc(x, SL * DM * 2);
  const rsrc_t r_s = mk_rsrc(ws + WS_ST, SL * DM * 2);

  const unsigned wvo = (unsigned)((lane >> 3) * 1024 + (((lane & 7) ^ (((lane >> 4) & 1) << 2)) * 16));
  const unsigned ring = ldsb + wv * (G2_DP * 2048);
  const unsigned ks0 = wv * 28;
  u32x4 hb[28][2];
#define G2_ISSUE(i)                                                                                     \
  do {                                                                                                  \
    ld128(hb[i][0], lane * 16u, r_h, (unsigned)(((tt * 2) * 112 + ks0 + (i)) * 1024));                  \
    ld128(hb[i][1], lane * 16u, r_h, (unsigned)(((tt * 2 + 1) * 112 + ks0 + (i)) * 1024));              \
    dma16(ring + ((i) % G2_DP) * 2048, wvo, r_wd, (unsigned)((ks0 + (i)) * 16384 + dd0 * 2));           \
    dma16(ring + ((i) % G2_DP) * 2048 + 1024, wvo, r_wd, (unsigned)((ks0 + (i)) * 16384 + 8192 + dd0 * 2)); \
  } while (0)
#pragma unroll
  for (int i = 0; i < G2_DP; ++i) G2_ISSUE(i);

  const int G = lane >> 4, q4 = (lane & 15) >> 2;
  const LAS char* wa0 = lds + wv * (G2_DP * 2048) + (4 * (G >> 1) + q4) * 128 + (G & 1) * 32 + (lane & 3) * 8;
  const LAS char* waT[2] = {wa0 + ((q4 >> 1) * 64), wa0 + (((q4 >> 1) ^ 1) * 64)};
  f32x16 acc[2][2];
#pragma unroll
  for (int a = 0; a < 2; ++a)
#pragma unroll
    for (int b = 0; b < 2; ++b) acc[a][b] = (f32x16){};
  unsigned sv[16];
  u32x4 xv[2];
#pragma unroll
  for (int i = 0; i < 28; ++i) {
    if (i + G2_DP <= 28) {
      if (i < 14) wait_vm<4 * (G2_DP - 1)>();
      else wait_vm<4 * (G2_DP - 1)>();
    } else {
      switch (27 - i) {
        case 0: wait_vm<18>(); break;
        case 1: wait_vm<22>(); break;
        case 2: wait_vm<26>(); break;
        case 3: wait_vm<30>(); break;
        case 4: wait_vm<34>(); break;
        case 5: wait_vm<38>(); break;
        case 6: wait_vm<42>(); break;
        case 7: wait_vm<46>(); break;
        case 8: wait_vm<50>(); break;
        case 9: wait_vm<54>(); break;
        case 10: wait_vm<58>(); break;
        case 11: wait_vm<62>(); break;
        default: wait_vm<63>(); break;
      }
    }
    PIN(hb[i][0]); PIN(hb[i][1]);
    bf16x8 A[2];
#pragma unroll
    for (int T = 0; T < 2; ++T)
      A[T] = __builtin_bit_cast(bf16x8, cat8(trrd(waT[T] + (i % G2_DP) * 2048), trrd(waT[T] + (i % G2_DP) * 2048 + 1024)));
#pragma unroll
    for (int T = 0; T < 2; ++T)
#pragma unroll
      for (int b = 0; b < 2; ++b)
        acc[T][b] = __builtin_amdgcn_mfma_f32_32x32x16_bf16(A[T], __builtin_bit_cast(bf16x8, hb[i][b]), acc[T][b], 0, 0, 0);
    if (i + G2_DP < 28) {
      asm volatile("s_waitcnt lgkmcnt(0)" ::: "memory");
      G2_ISSUE(i + G2_DP);
    }
    if (i + G2_DP == 28) {
      const unsigned xo = (unsigned)((tid >> 2) * (DM * 2) + (tid & 3) * 32);
      ld128(xv[0], xo, r_x, (unsigned)(t0 * DM * 2 + dd0 * 2));
      ld128(xv[1], xo, r_x, (unsigned)(t0 * DM * 2 + dd0 * 2 + 16));
      const unsigned so = (unsigned)((tid & 3) * 16 * 4096 + (tid >> 2) * 2);
#pragma unroll
      for (int j = 0; j < 16; ++j) ld16(sv[j], so, r_s, (unsigned)((dd0 + j) * 4096 + t0 * 2));
    }
  }
  bar_lgkm();
  {
    LAS char* rp = lds + wv * (64 * G2_RED) + (lane & 31) * G2_RED + (lane >> 5) * 16;
#pragma unroll
    for (int T = 0; T < 2; ++T)
#pragma unroll
      for (int b = 0; b < 2; ++b)
#pragma unroll
        for (int g = 0; g < 4; ++g) {
          const f32x4 v = {acc[T][b][4 * g], acc[T][b][4 * g + 1], acc[T][b][4 * g + 2], acc[T][b][4 * g + 3]};
          *(LAS f32x4*)(rp + b * 32 * G2_RED + T * 128 + g * 32) = v;
        }
  }
  bar_lgkm();
  wait_vm<0>();
  PIN(xv[0]); PIN(xv[1]);
#pragma unroll
  for (int j = 0; j < 16; ++j) PIN(sv[j]);
  {
    const LAS char* rp = lds + (tid >> 2) * G2_RED + (tid & 3) * 64;
    u32x4 o[2];
#pragma unroll
    for (int i = 0; i < 4; ++i) {
      f32x4 v = *(const LAS f32x4*)(rp + i * 16);
#pragma unroll
      for (int w = 1; w < 4; ++w) v += *(const LAS f32x4*)(rp + w * (64 * G2_RED) + i * 16);
#pragma unroll
      for (int jj = 0; jj < 2; ++jj) {
        const unsigned fp = pk_bf16(v[2 * jj], v[2 * jj + 1]);
        const float f0 = lo_f(fp), f1 = hi_f(fp);
        const float g0 = f0 * __builtin_amdgcn_rcpf(1.0f + __builtin_amdgcn_exp2f(f0 * -1.4426950408889634f));
        const float g1 = f1 * __builtin_amdgcn_rcpf(1.0f + __builtin_amdgcn_exp2f(f1 * -1.4426950408889634f));
        const unsigned gp = pk_bf16(g0, g1);
        const int j = 4 * i + 2 * jj;
        const unsigned sg = pk_bf16(lo_f(sv[j]) * lo_f(gp), lo_f(sv[j + 1]) * hi_f(gp));
        const unsigned xw = xv[i >> 1][(i & 1) * 2 + jj];
        o[i >> 1][(i & 1) * 2 + jj] = pk_bf16(lo_f(xw) + lo_f(sg), hi_f(xw) + hi_f(sg));
      }
    }
    unsigned short* op = out + (size_t)(t0 + (tid >> 2)) * DM + dd0 + (tid & 3) * 16;
    *(u32x4*)op = o[0];
    *(u32x4*)(op + 8) = o[1];
  }
}

extern "C" void kernel_launch(void* const* d_in, const int* in_sizes, int n_in, void* d_out,
                              int out_size, void* d_ws, size_t ws_size, hipStream_t stream) {
  const unsigned short* x = (const unsigned short*)d_in[0];
  char* ws = (char*)d_ws;
  (void)hipFuncSetAttribute((const void*)k1_ln_kgen, hipFuncAttributeMaxDynamicSharedMemorySize, K1_LDS);
  (void)hipFuncSetAttribute((const void*)k2_conv_gemm1, hipFuncAttributeMaxDynamicSharedMemorySize, K2_LDS);
  (void)hipFuncSetAttribute((const void*)k3_gemm2, hipFuncAttributeMaxDynamicSharedMemorySize, K3_LDS);
  k1_ln_kgen<<<512, 256, K1_LDS, stream>>>(
      x, (const unsigned short*)d_in[1], (const unsigned short*)d_in[2], (const unsigned short*)d_in[3],
      (const unsigned short*)d_in[4], (const unsigned short*)d_in[5], (const unsigned short*)d_in[6],
      (const unsigned short*)d_in[7], ws);
  k2_conv_gemm1<<<256, 256, K2_LDS, stream>>>((const unsigned short*)d_in[10], (const unsigned short*)d_in[11],
                                              (const unsigned short*)d_in[8], ws);
  k3_gemm2<<<256, 256, K3_LDS, stream>>>(x, (const unsigned short*)d_in[12], ws, (unsigned short*)d_out);
}
```

```cpp
#include <hip/hip_runtime.h>
#include <hip/hip_bf16.h>
#include <stdint.h>
#include <type_traits>

typedef __attribute__((ext_vector_type(4))) short s16x4;
typedef __attribute__((ext_vector_type(8))) short s16x8;
typedef __attribute__((ext_vector_type(8))) _Float16 f16x8;
typedef __attribute__((ext_vector_type(8))) __bf16 bf16x8;
typedef __attribute__((ext_vector_type(2))) float f32x2;
typedef __attribute__((ext_vector_type(4))) float f32x4;
typedef __attribute__((ext_vector_type(16))) float f32x16;
typedef __attribute__((ext_vector_type(4))) unsigned u32x4;
typedef __attribute__((ext_vector_type(3))) unsigned u32x3;
typedef __attribute__((ext_vector_type(2))) unsigned u32x2;
typedef __attribute__((ext_vector_type(2))) __bf16 bf16x2;
typedef __attribute__((ext_vector_type(2))) _Float16 f16x2;
#define LAS __attribute__((address_space(3)))
#define GAS __attribute__((address_space(1)))
typedef __amdgpu_buffer_rsrc_t rsrc_t;

constexpr int SL = 2048;
constexpr int DM = 512;
constexpr int NM = 128;
constexpr int FF = 1792;

constexpr size_t WS_XNF = 0;
constexpr size_t WS_ST = 2 * (size_t)SL * DM * 2;
constexpr size_t WS_HF = WS_ST + (size_t)SL * DM * 2;
constexpr size_t WS_FLAG = 16u << 20;
constexpr unsigned FL_XN = 0, FL_H = 1024, FL_S = 2048, FL_EP = 4096, FL_BYTES = 8192;

template <int I, int N, class F>
__device__ __forceinline__ void static_for(F&& f) {
  if constexpr (I < N) { f(std::integral_constant<int, I>{}); static_for<I + 1, N>(f); }
}

__device__ __forceinline__ unsigned pk_bf16(float lo, float hi) {
  f32x2 v = {lo, hi};
  return __builtin_bit_cast(unsigned, __builtin_convertvector(v, bf16x2));
}
__device__ __forceinline__ unsigned pk_f16(float lo, float hi) {
  f32x2 v = {lo, hi};
  return __builtin_bit_cast(unsigned, __builtin_convertvector(v, f16x2));
}
__device__ __forceinline__ float lo_f(unsigned p) { return __uint_as_float(p << 16); }
__device__ __forceinline__ float hi_f(unsigned p) { return __uint_as_float(p & 0xffff0000u); }
__device__ __forceinline__ float rbf(float x) { return lo_f(pk_bf16(x, x)); }

template <int CTRL>
__device__ __forceinline__ float dpp_mov(float v) {
  return __int_as_float(__builtin_amdgcn_update_dpp(0, __float_as_int(v), CTRL, 0xf, 0xf, true));
}
__device__ __forceinline__ float rdlane(float v, int l) {
  return __int_as_float(__builtin_amdgcn_readlane(__float_as_int(v), l));
}
__device__ __forceinline__ float wave_sum(float v) {
  v += dpp_mov<0xB1>(v);
  v += dpp_mov<0x4E>(v);
  v += dpp_mov<0x141>(v);
  v += dpp_mov<0x140>(v);
  return (rdlane(v, 0) + rdlane(v, 16)) + (rdlane(v, 32) + rdlane(v, 48));
}
__device__ __forceinline__ float wave_max(float v) {
  v = fmaxf(v, dpp_mov<0xB1>(v));
  v = fmaxf(v, dpp_mov<0x4E>(v));
  v = fmaxf(v, dpp_mov<0x141>(v));
  v = fmaxf(v, dpp_mov<0x140>(v));
  return fmaxf(fmaxf(rdlane(v, 0), rdlane(v, 16)), fmaxf(rdlane(v, 32), rdlane(v, 48)));
}

__device__ __forceinline__ rsrc_t mk_rsrc(const void* p, unsigned bytes) {
  return __builtin_amdgcn_make_buffer_rsrc((void*)p, (short)0, (int)bytes, 0x00020000);
}
__device__ __forceinline__ void dma16(unsigned lds_dst, unsigned voff, rsrc_t r, unsigned soff) {
  asm volatile("s_mov_b32 m0, %0\n\ts_nop 0\n\tbuffer_load_dwordx4 %1, %2, %3 offen lds"
               :: "s"(lds_dst), "v"(voff), "s"(r), "s"(soff) : "memory");
}
__device__ __forceinline__ void ld128(u32x4& dst, unsigned voff, rsrc_t r, unsigned soff) {
  asm volatile("buffer_load_dwordx4 %0, %1, %2, %3 offen" : "=v"(dst) : "v"(voff), "s"(r), "s"(soff) : "memory");
}
__device__ __forceinline__ void ld128c(u32x4& dst, unsigned voff, rsrc_t r, unsigned soff) {
  asm volatile("buffer_load_dwordx4 %0, %1, %2, %3 offen sc1" : "=v"(dst) : "v"(voff), "s"(r), "s"(soff) : "memory");
}
__device__ __forceinline__ void ld32c(unsigned& dst, unsigned voff, rsrc_t r, unsigned soff) {
  asm volatile("buffer_load_dword %0, %1, %2, %3 offen sc1" : "=v"(dst) : "v"(voff), "s"(r), "s"(soff) : "memory");
}
__device__ __forceinline__ void ld16(unsigned& dst, unsigned voff, rsrc_t r, unsigned soff) {
  asm volatile("buffer_load_ushort %0, %1, %2, %3 offen" : "=v"(dst) : "v"(voff), "s"(r), "s"(soff) : "memory");
}
__device__ __forceinline__ void st128c(u32x4 v, unsigned voff, rsrc_t r, unsigned soff) {
  __builtin_amdgcn_raw_buffer_store_b128(v, r, voff, soff, 16);
}
template <int N>
__device__ __forceinline__ void wait_vm() { asm volatile("s_waitcnt vmcnt(%0)" :: "n"(N > 63 ? 63 : N) : "memory"); }
__device__ __forceinline__ void bar_lgkm() { asm volatile("s_waitcnt lgkmcnt(0)\n\ts_barrier" ::: "memory"); }
#define PIN(x) asm volatile("" : "+v"(x))
#define SCHED_FENCE() __builtin_amdgcn_sched_barrier(0)

__device__ __forceinline__ s16x4 trrd(const LAS char* p) {
  return __builtin_amdgcn_ds_read_tr16_b64_v4i16((LAS s16x4*)p);
}
__device__ __forceinline__ s16x8 cat8(s16x4 a, s16x4 b) {
  return __builtin_shufflevector(a, b, 0, 1, 2, 3, 4, 5, 6, 7);
}

__device__ __forceinline__ void poll_flags(unsigned voff, rsrc_t r, unsigned soff, unsigned tag) {
  for (int it = 0; it < (1 << 18); ++it) {
    u32x4 v;
    asm volatile("buffer_load_dwordx4 %0, %1, %2, %3 offen sc1\n\ts_waitcnt vmcnt(0)"
                 : "=&v"(v) : "v"(voff), "s"(r), "s"(soff) : "memory");
    const bool ok = (v[0] == tag) & (v[1] == tag) & (v[2] == tag) & (v[3] == tag);
    if (__all(ok)) break;
    __builtin_amdgcn_s_sleep(1);
  }
}
__device__ __forceinline__ void set_flag(char* ws, unsigned off, unsigned tag) {
  __hip_atomic_store((GAS unsigned*)(ws + WS_FLAG + off), tag, __ATOMIC_RELAXED, __HIP_MEMORY_SCOPE_AGENT);
}

#define STAMP_DECL
#define STAMP(i)
#define STAMP_RT(i)
#define STAMP_OUT(kid)

__device__ __forceinline__ f32x2 cmul(f32x2 a, f32x2 b) {
  const f32x2 t = a.xx * b;
  const f32x2 bs = {-b.y, b.x};
  return __builtin_elementwise_fma(a.yy, bs, t);
}
__device__ __forceinline__ f32x2 cexp_l(float a, float b, float l) {
  const float mag = __builtin_amdgcn_exp2f(a * l * 1.4426950408889634f);
  const float x = b * l;
  const float C1 = 0.15915494309189535f;
  const float C2 = (float)(0.15915494309189533577 - (double)0.15915494309189535f);
  const float hi = x * C1;
  const float lo = fmaf(x, C1, -hi) + x * C2;
  const float r = (hi - __builtin_rintf(hi)) + lo;
  f32x2 z;
  z.x = mag * __builtin_amdgcn_cosf(r);
  z.y = mag * __builtin_amdgcn_sinf(r);
  return z;
}

constexpr int W_UPOFF = 14336 + 128;
constexpr int W_STAGE = W_UPOFF + 14336;
constexpr int AUX = 5 * W_STAGE;
constexpr int LN_STG = 4 * W_STAGE;
constexpr int KG_ROW = 272, KG_WAVE = 16 * KG_ROW;
constexpr int KG_EXP = AUX + 4 * KG_WAVE;
constexpr int CV_KSTR = (48 + 2048) * 2;
constexpr int CV_USTR = (80 + 2048) * 2;
constexpr int CV_U = AUX + 2 * CV_KSTR;
constexpr int CV_SC = CV_U + 2 * CV_USTR;
constexpr int LDS_BYTES = 161920;
constexpr int G2_HI = 3 * W_STAGE;
constexpr int CV_Y = 65536;
constexpr int CV_C = CV_Y + 16384;
constexpr int G2_NS = 15;
constexpr int G2_HB = 12;
constexpr int G2_RED = 272;

constexpr int g1_nw(int i) { return (i >= 3 && i <= 14) ? ((i & 3) == 2 ? 1 : 2) : 0; }
constexpr int g1_nx(int i) { return i + 8 < 32 ? 1 : 0; }
constexpr int g1_nu(int i) { return (i >= 16 && i < 20) ? 2 : 0; }
constexpr int g1_nd(int i) { return (i >= 20 && i < 27) ? 2 : 0; }
constexpr int g1_cnt(int i) { int c = 15; for (int j = 0; j < i; ++j) c += g1_nx(j) + g1_nw(j) + g1_nu(j) + g1_nd(j); return c; }
constexpr int g1_posx(int i) { return i < 8 ? i : g1_cnt(i - 8); }
constexpr int g1_posw(int s5) { return g1_cnt(4 * s5 + 6) + g1_nx(4 * s5 + 6); }
constexpr int g1_wait(int i) {
  int need = g1_posx(i);
  if ((i & 3) == 3 && (i + 1) / 4 >= 5 && (i + 1) / 4 <= 7) { const int p = g1_posw((i + 1) / 4 - 5); need = need > p ? need : p; }
  if (i == 15) need = need > 14 ? need : 14;
  return g1_cnt(i) - 1 - need;
}
constexpr int G2_PF = 16, G2_PC = 22;
constexpr int g2_nh(int i) { return i + G2_HB < 28 ? 2 : 0; }
constexpr int g2_nd(int i) { return i + G2_NS < 28 ? 2 : 0; }
constexpr int g2_ne(int i) { return i == G2_PF ? 3 : (i == G2_PC ? 8 : 0); }
constexpr int g2_cnt(int i) { int c = 2 * G2_HB + 2; for (int j = 0; j < i; ++j) c += g2_nh(j) + g2_nd(j) + g2_ne(j); return c; }
constexpr int g2_posh(int i) { return i < G2_HB ? 2 * i + 1 : g2_cnt(i - G2_HB) + 1; }
constexpr int g2_posd(int i) { return i < G2_NS ? -1 : g2_cnt(i - G2_NS) + g2_nh(i - G2_NS) + 1; }
constexpr int cmax(int a, int b) { return a > b ? a : b; }
constexpr int G2_SF = 8;
constexpr int g2_wait(int i) {
  int need = cmax(g2_posh(i), i + 1 < 28 ? g2_posd(i + 1) : -1);
  if (i == G2_SF) need = cmax(need, 2 * G2_HB + 1);
  if (i == G2_PC) need = cmax(need, g2_cnt(G2_PF) + g2_nh(G2_PF) + g2_nd(G2_PF));
  return g2_cnt(i) - 1 - need;
}

__global__ __launch_bounds__(256, 1) void fused_kernel(
    const unsigned short* __restrict__ x, const unsigned short* __restrict__ A_re,
    const unsigned short* __restrict__ A_im, const unsigned short* __restrict__ B_re,
    const unsigned short* __restrict__ B_im, const unsigned short* __restrict__ C_re,
    const unsigned short* __restrict__ C_im, const unsigned* __restrict__ log_dt2,
    const unsigned* __restrict__ dsk2p, const unsigned short* __restrict__ wg,
    const unsigned short* __restrict__ wu, const unsigned short* __restrict__ wd,
    char* ws, unsigned short* __restrict__ out) {
  extern __shared__ __attribute__((aligned(16))) char smem[];
  LAS char* lds = (LAS char*)smem;
  const unsigned ldsb = (unsigned)(uintptr_t)lds;
  const int tid = threadIdx.x, lane = tid & 63;
  const int wv = __builtin_amdgcn_readfirstlane(tid >> 6);
  STAMP_DECL; STAMP_RT(14); STAMP(0);
  const int bid = blockIdx.x;
  const int xcd = bid & 7, bi = bid >> 3;
  const int mt = (xcd >> 2) * 8 + (bi >> 2);
  const int ft = (xcd & 3) * 4 + (bi & 3);
  const int rot = ((bi >> 2) + 2 * (bi & 3)) & 7;
  const int cp = xcd * 32 + bi;
  const int d0 = cp * 2;
  const int chl = wv & 1, par = wv >> 1;
  const int tt = xcd * 4 + (bi >> 3);
  const int dtile = bi & 7;
  const int rot2 = ((bi >> 3) * 7 + dtile * 3) % 28;

  const rsrc_t r_x = mk_rsrc(x, SL * DM * 2);
  const rsrc_t r_xn = mk_rsrc(ws + WS_XNF, SL * DM * 2);
  const rsrc_t r_s = mk_rsrc(ws + WS_ST, SL * DM * 2);
  const rsrc_t r_h = mk_rsrc(ws + WS_HF, SL * FF * 2);
  const rsrc_t r_fl = mk_rsrc(ws + WS_FLAG, FL_BYTES);
  const rsrc_t r_w = mk_rsrc(wv < 2 ? wg : wu, DM * FF * 2);
  const rsrc_t r_wd = mk_rsrc(wd, FF * DM * 2);

  u32x4 xr[2];
  unsigned ep, prm[6];
  ld128(xr[0], lane * 16u, r_x, (unsigned)((bid * 8 + 2 * wv) * 1024));
  ld128(xr[1], lane * 16u, r_x, (unsigned)((bid * 8 + 2 * wv + 1) * 1024));
  ld32c(ep, 0u * lane, r_fl, FL_EP);
  {
    const unsigned po = (unsigned)(((d0 + chl) * NM + par * 64) * 2);
    ld16(prm[0], lane * 2u, mk_rsrc(A_re, DM * NM * 2), po);
    ld16(prm[1], lane * 2u, mk_rsrc(A_im, DM * NM * 2), po);
    ld16(prm[2], lane * 2u, mk_rsrc(B_re, DM * NM * 2), po);
    ld16(prm[3], lane * 2u, mk_rsrc(B_im, DM * NM * 2), po);
    ld16(prm[4], lane * 2u, mk_rsrc(C_re, DM * NM * 2), po);
    ld16(prm[5], lane * 2u, mk_rsrc(C_im, DM * NM * 2), po);
  }
  const unsigned ldt2 = log_dt2[cp], dsk2 = dsk2p[cp];
  unsigned woff[7];
#pragma unroll
  for (int i = 0; i < 7; ++i) {
    const int c = ((wv & 1) * 7 + i) * 64 + lane;
    const int row = c / 14, col = c - row * 14;
    woff[i] = (unsigned)(row * (FF * 2) + col * 16);
  }
  const unsigned wdst = ldsb + (wv >> 1) * W_UPOFF + (wv & 1) * 7 * 1024;
  const unsigned wsrc = (unsigned)(ft * 224);
#define W_PIECE(st, i) dma16(wdst + ((st) % 5) * W_STAGE + (i) * 1024, woff[i], r_w, wsrc + (unsigned)((((st) + rot) & 7) * (64 * FF * 2)))
#define W_ALL(st) do { _Pragma("unroll") for (int i_ = 0; i_ < 7; ++i_) W_PIECE(st, i_); } while (0)
  W_ALL(0);
  W_ALL(1);
  wait_vm<14>();
  STAMP(1);
  PIN(xr[0]); PIN(xr[1]); PIN(ep);
#pragma unroll
  for (int i = 0; i < 6; ++i) PIN(prm[i]);
  const unsigned tag = (unsigned)__builtin_amdgcn_readfirstlane(ep) + 1u;

#pragma unroll
  for (int i = 0; i < 2; ++i) {
    float f[8];
#pragma unroll
    for (int j = 0; j < 4; ++j) { f[2 * j] = lo_f(xr[i][j]); f[2 * j + 1] = hi_f(xr[i][j]); }
    const float s = ((f[0] + f[1]) + (f[2] + f[3])) + ((f[4] + f[5]) + (f[6] + f[7]));
    const float mean = wave_sum(s) * (1.0f / DM);
    float q = 0.f;
#pragma unroll
    for (int j = 0; j < 8; ++j) { const float d = f[j] - mean; q = fmaf(d, d, q); }
    const float var = wave_sum(q) * (1.0f / DM);
    const float mb = rbf(mean);
    const float sb = rbf(__builtin_sqrtf(rbf(rbf(var) + 1e-5f)));
    const float rs = 1.0f / sb;
    u32x4 o;
#pragma unroll
    for (int j = 0; j < 4; ++j) {
      const unsigned d = pk_bf16(f[2 * j] - mb, f[2 * j + 1] - mb);
      o[j] = pk_bf16(lo_f(d) * rs, hi_f(d) * rs);
    }
    *(LAS u32x4*)(lds + LN_STG + (2 * wv + i) * 1040 + lane * 16) = o;
  }
  bar_lgkm();
  {
    const unsigned sb = (unsigned)((bid >> 2) * 32768 + (bid & 3) * 128);
#pragma unroll
    for (int m = 0; m < 2; ++m) {
      const int item = tid + 256 * m, chunk = item >> 3, row = item & 7;
      const u32x4 o = *(const LAS u32x4*)(lds + LN_STG + row * 1040 + chunk * 16);
      st128c(o, (unsigned)(chunk * 512 + row * 16), r_xn, sb);
    }
  }
  STAMP(2);
  f32x4 kacc[3];
  int we;
  float g2;
  {
    const float ldt = chl ? hi_f(ldt2) : lo_f(ldt2);
    const float L2E = 1.4426950408889634f, L2E_LO = (float)(1.4426950408889634074 - (double)1.4426950408889634f);
    const float th = ldt * L2E;
    const float tl = fmaf(ldt, L2E, -th) + ldt * L2E_LO;
    const float e0 = __builtin_amdgcn_exp2f(th);
    const float dt = rbf(fmaf(e0 * 0.6931471805599453f, tl, e0));
    float a = dt * lo_f(prm[0]);
    const float b = dt * lo_f(prm[1]);
    const f32x2 db = {dt * lo_f(prm[2]), dt * lo_f(prm[3])};
    const f32x2 cc = {lo_f(prm[4]), lo_f(prm[5])};
    f32x2 w = cmul(cc, db);
    const float amax = fmaxf(wave_max(a), 0.f);
    a -= amax;
    g2 = amax * L2E;
    const float wm = wave_max(fmaxf(fabsf(w.x), fabsf(w.y)));
    we = __builtin_amdgcn_frexp_expf(wm);
    we = we < -100 ? -100 : we;
    w *= __builtin_amdgcn_ldexpf(1.0f, -we);
    const f32x2 z1 = cexp_l(a, b, 1.f), z16 = cexp_l(a, b, 16.f), z256 = cexp_l(a, b, 256.f);
    LAS char* tw = lds + AUX + wv * KG_WAVE + lane * 4;
    const LAS char* tr = lds + AUX + wv * KG_WAVE + (lane & 15) * KG_ROW + (lane >> 4) * 16;
#define PUT_P(r, v) do { const f32x2 v_ = (v); *(LAS unsigned*)(tw + (r) * KG_ROW) = pk_f16(v_.x, -v_.y); } while (0)
#define PUT_Q(c, v) do { const f32x2 v_ = (v); *(LAS unsigned*)(tw + (c) * KG_ROW) = pk_f16(v_.x, v_.y); } while (0)
    f16x8 qf[4], pf[4];
    {
      f32x2 q[4];
      q[0] = (f32x2){1.f, 0.f}; q[1] = z16; q[2] = cmul(z16, z16); q[3] = cmul(q[2], z16);
      const f32x2 z64 = cmul(q[2], q[2]);
#pragma unroll
      for (int c = 0; c < 16; c += 4) {
#pragma unroll
        for (int i = 0; i < 4; ++i) { PUT_Q(c + i, q[i]); if (c + 4 < 16) q[i] = cmul(q[i], z64); }
      }
#pragma unroll
      for (int s = 0; s < 4; ++s) qf[s] = *(const LAS f16x8*)(tr + s * 64);
    }
    W_PIECE(2, 0); W_PIECE(2, 1); W_PIECE(2, 2); W_PIECE(2, 3);
    const f32x2 z2 = cmul(z1, z1), z4 = cmul(z2, z2), z8 = cmul(z4, z4);
    {
      f32x2 p[4];
      p[0] = w; p[1] = cmul(w, z1); p[2] = cmul(w, z2); p[3] = cmul(p[1], z2);
#pragma unroll
      for (int r = 0; r < 16; r += 4) {
#pragma unroll
        for (int i = 0; i < 4; ++i) { PUT_P(r + i, p[i]); if (r + 4 < 16) p[i] = cmul(p[i], z4); }
      }
#pragma unroll
      for (int s = 0; s < 4; ++s) pf[s] = *(const LAS f16x8*)(tr + s * 64);
      kacc[0] = (f32x4){0.f, 0.f, 0.f, 0.f};
#pragma unroll
      for (int s = 0; s < 4; ++s) kacc[0] = __builtin_amdgcn_mfma_f32_16x16x32_f16(pf[s], qf[s], kacc[0], 0, 0, 0);
    }
    wait_vm<4>();
    bar_lgkm();
    if (tid == 0) set_flag(ws, FL_XN + bid * 4, tag);
    STAMP(3);
    W_PIECE(2, 4); W_PIECE(2, 5); W_PIECE(2, 6);
    const f32x2 z512 = cmul(z256, z256);
    const f32x2 w1 = cmul(w, z256), w2 = cmul(w, z512);
    const f32x2 w3 = cmul(w1, z512), w4 = cmul(w2, z512), w5 = cmul(w3, z512), w6 = cmul(w4, z512);
    const f32x2 w7 = cmul(w5, z512), w8 = cmul(w6, z512);
    {
      f32x2 p[4];
      p[0] = w1; p[1] = cmul(w1, z2); p[2] = w2; p[3] = w3;
#pragma unroll
      for (int i = 0; i < 4; ++i) {
        PUT_P(2 * i, p[0]); PUT_P(2 * i + 1, p[1]); PUT_P(8 + i, p[2]); PUT_P(12 + i, p[3]);
        if (i < 3) {
#pragma unroll
          for (int k = 0; k < 4; ++k) p[k] = cmul(p[k], z4);
        }
      }
#pragma unroll
      for (int s = 0; s < 4; ++s) pf[s] = *(const LAS f16x8*)(tr + s * 64);
      kacc[1] = (f32x4){0.f, 0.f, 0.f, 0.f};
#pragma unroll
      for (int s = 0; s < 4; ++s) kacc[1] = __builtin_amdgcn_mfma_f32_16x16x32_f16(pf[s], qf[s], kacc[1], 0, 0, 0);
    }
    W_PIECE(3, 0); W_PIECE(3, 1); W_PIECE(3, 2); W_PIECE(3, 3);
    {
      PUT_P(0, w4); PUT_P(1, cmul(w4, z8)); PUT_P(2, w5); PUT_P(3, cmul(w5, z8));
      PUT_P(4, w6); PUT_P(5, cmul(w6, z8)); PUT_P(6, w7); PUT_P(7, cmul(w7, z8));
      PUT_P(8, w8);
#pragma unroll
      for (int s = 0; s < 4; ++s) pf[s] = *(const LAS f16x8*)(tr + s * 64);
      kacc[2] = (f32x4){0.f, 0.f, 0.f, 0.f};
#pragma unroll
      for (int s = 0; s < 4; ++s) kacc[2] = __builtin_amdgcn_mfma_f32_16x16x32_f16(pf[s], qf[s], kacc[2], 0, 0, 0);
    }
    W_PIECE(3, 4); W_PIECE(3, 5); W_PIECE(3, 6);
  }
  {
    const int c = lane & 15, g = lane >> 4;
    const float un = __builtin_amdgcn_ldexpf(1.0f, we);
#pragma unroll
    for (int t = 0; t < 3; ++t) kacc[t] *= un;
    if (g2 > 0.f) {
#pragma unroll
      for (int i = 0; i < 4; ++i) {
        const int l0 = 16 * c + 4 * g + i;
        const int l1 = g < 2 ? 256 + 16 * c + 8 * g + 2 * i : 512 + 256 * (g - 2) + 16 * c + 4 * i;
        const int l2 = g < 2 ? 1024 + 256 * (2 * g + (i >> 1)) + 8 * (i & 1) + 16 * c : 2048;
        kacc[0][i] *= __builtin_amdgcn_exp2f(g2 * (float)l0);
        kacc[1][i] *= __builtin_amdgcn_exp2f(g2 * (float)l1);
        kacc[2][i] *= __builtin_amdgcn_exp2f(g2 * (float)l2);
      }
    }
    const float eb = (float)we + ceilf(g2 * 2048.f);
    if (par) {
#pragma unroll
      for (int t = 0; t < 3; ++t) *(LAS f32x4*)(lds + AUX + wv * KG_WAVE + t * 1024 + lane * 16) = kacc[t];
      if (lane == 0) *(LAS float*)(lds + KG_EXP + chl * 4) = eb;
    }
    bar_lgkm();
    if (!par) {
#pragma unroll
      for (int t = 0; t < 3; ++t) kacc[t] += *(const LAS f32x4*)(lds + AUX + (wv + 2) * KG_WAVE + t * 1024 + lane * 16);
      const float eo = *(const LAS float*)(lds + KG_EXP + chl * 4);
      const int E = (int)fminf(fmaxf(eb, eo), 120.f) - 7;
      const float sc = __builtin_amdgcn_ldexpf(1.0f, -E);
      if (lane == 0) *(LAS float*)(lds + CV_SC + chl * 4) = __builtin_amdgcn_ldexpf(1.0f, E);
      unsigned d[3][4];
#pragma unroll
      for (int t = 0; t < 3; ++t)
#pragma unroll
        for (int i = 0; i < 4; ++i) d[t][i] = pk_f16(kacc[t][i] * sc, kacc[t][i] * sc);
      LAS char* kf = lds + AUX + chl * CV_KSTR + 96;
#define MIX(a, b) (((a) & 0xffffu) | ((b) & 0xffff0000u))
      if (g < 2) {
#pragma unroll
        for (int h = 0; h < 2; ++h) {
          LAS char* p = kf + 2 * (1024 + 256 * (2 * g + h) + 16 * c);
          const unsigned v0 = d[2][2 * h], v1 = d[2][2 * h + 1];
          *(LAS u32x2*)(p - 8) = (u32x2){v0, v0};
          *(LAS u32x4*)(p) = (u32x4){v0, v0, MIX(v0, v1), v1};
          *(LAS u32x2*)(p + 16) = (u32x2){v1, v1};
        }
      } else {
        if (lane == 32) *(LAS u32x2*)(kf + 2 * 2044) = (u32x2){d[2][0], d[2][0]};
      }
      if (g >= 2) {
        LAS char* p = kf + 2 * (512 + 256 * (g - 2) + 16 * c);
        const unsigned v0 = d[1][0], v1 = d[1][1], v2 = d[1][2], v3 = d[1][3];
        *(LAS unsigned*)(p - 4) = v0;
        *(LAS u32x4*)(p) = (u32x4){v0, MIX(v0, v1), v1, v2};
        *(LAS u32x2*)(p + 16) = (u32x2){v2, MIX(v2, v3)};
        *(LAS unsigned*)(p + 24) = v3;
      }
      if (g < 2) {
        LAS char* p = kf + 2 * (256 + 16 * c + 8 * g);
        const unsigned v0 = d[1][0], v1 = d[1][1], v2 = d[1][2], v3 = d[1][3];
        *(LAS unsigned short*)(p - 2) = (unsigned short)v0;
        *(LAS u32x2*)(p) = (u32x2){v0, MIX(v1, v2)};
        *(LAS unsigned*)(p + 8) = v2;
        *(LAS unsigned short*)(p + 12) = (unsigned short)v3;
      }
      *(LAS u32x2*)(kf + 2 * (16 * c + 4 * g)) = (u32x2){MIX(d[0][0], d[0][1]), MIX(d[0][2], d[0][3])};
      if (lane < 6) *(LAS u32x4*)(kf - 96 + lane * 16) = (u32x4){0u, 0u, 0u, 0u};
    }
  }
  STAMP(4);
  poll_flags(lane * 16u, r_fl, FL_XN, tag);
  STAMP(5);
  u32x4 xb[32];
  const unsigned xsb = (unsigned)(mt * 4 + wv) * 32768u;
#define XB_LOAD(i) ld128c(xb[i], lane * 16u, r_xn, xsb + (unsigned)(((((i) >> 2) + rot) & 7) * 4096 + ((i) & 3) * 1024))
#pragma unroll
  for (int i = 0; i < 8; ++i) XB_LOAD(i);
  W_ALL(4);
  bar_lgkm();
  const unsigned wvo = (unsigned)((lane >> 3) * 1024 + (((lane & 7) ^ (((lane >> 4) & 1) << 2)) * 16));
  const unsigned wdcol = (unsigned)(dtile * 128);
#define G2_KS(i) ((unsigned)(wv * 28 + (((i) + rot2) >= 28 ? (i) + rot2 - 28 : (i) + rot2)))
#define G2_SLOT(j) ((j) < 7 ? G2_HI + (wv * 7 + (j)) * 2048 : (wv * 8 + (j) - 7) * 2048)
#define WD_PIECE(i, hlf) dma16(ldsb + G2_SLOT((i) % G2_NS) + (hlf) * 1024, wvo, r_wd, G2_KS(i) * 16384u + (hlf) * 8192u + wdcol)
  unsigned uw[8];
  const unsigned uoff = (unsigned)(tid >> 5) * 32768u + (unsigned)(tid & 31) * 16u;
  const unsigned usb = (unsigned)((cp >> 2) * 512 + (cp & 3) * 4);
  f32x16 acc[7];
  {
    const int G = lane >> 4;
    const LAS char* wa = lds + (G & 1) * W_UPOFF + (8 * (G >> 1) + ((lane & 15) >> 2)) * 224 + (lane & 3) * 8;
    bf16x8 Af[2][7];
#define A_READ(i, T) __builtin_bit_cast(bf16x8, cat8(trrd(wa + (((i) >> 2) % 5) * W_STAGE + (16 * ((i) & 3)) * 224 + 32 * (T)), \
                                                      trrd(wa + (((i) >> 2) % 5) * W_STAGE + (16 * ((i) & 3) + 4) * 224 + 32 * (T))))
#pragma unroll
    for (int T = 0; T < 7; ++T) { Af[0][T] = A_READ(0, T); acc[T] = (f32x16){}; }
    static_for<0, 32>([&](auto ic) {
      constexpr int i = decltype(ic)::value;
      wait_vm<g1_wait(i)>();
      PIN(xb[i]);
      if constexpr ((i & 3) == 3 && i < 31) bar_lgkm();
      const bf16x8 B = __builtin_bit_cast(bf16x8, xb[i]);
      static_for<0, 7>([&](auto tc) {
        constexpr int T = decltype(tc)::value;
        if constexpr (i + 1 < 32) Af[(i + 1) & 1][T] = A_READ(i + 1, T);
        acc[T] = __builtin_amdgcn_mfma_f32_32x32x16_bf16(Af[i & 1][T], B, acc[T], 0, 0, 0);
        if constexpr (T == 1 && g1_nx(i)) XB_LOAD(i + 8);
        if constexpr (g1_nw(i) > 0) {
          constexpr int s5 = (i - 3) >> 2, p0 = ((i - 3) & 3) * 2;
          if constexpr (T == 3) W_PIECE(5 + s5, p0);
          if constexpr (T == 5 && g1_nw(i) == 2) W_PIECE(5 + s5, p0 + 1);
        }
        if constexpr (g1_nu(i) > 0) {
          if constexpr (T == 3) ld32c(uw[(i - 16) * 2], uoff, r_xn, usb + (unsigned)((i - 16) * 2) * 8u * 32768u);
          if constexpr (T == 5) ld32c(uw[(i - 16) * 2 + 1], uoff, r_xn, usb + (unsigned)((i - 16) * 2 + 1) * 8u * 32768u);
        }
        if constexpr (g1_nd(i) > 0) {
          if constexpr (T == 3) WD_PIECE(i - 20, 0);
          if constexpr (T == 5) WD_PIECE(i - 20, 1);
        }
        SCHED_FENCE();
      });
    });
  }
  STAMP(6);
  {
    const unsigned hsb = (unsigned)(((mt * 4 + wv) * 112 + ft * 7) * 1024);
#pragma unroll
    for (int T = 0; T < 7; ++T) {
      u32x4 o;
#pragma unroll
      for (int j = 0; j < 4; ++j) {
        const float s0 = __builtin_amdgcn_rcpf(1.0f + __builtin_amdgcn_exp2f(acc[T][2 * j] * -1.4426950408889634f));
        const float s1 = __builtin_amdgcn_rcpf(1.0f + __builtin_amdgcn_exp2f(acc[T][2 * j + 1] * -1.4426950408889634f));
        o[j] = pk_bf16(s0 * acc[T][8 + 2 * j], s1 * acc[T][8 + 2 * j + 1]);
      }
      st128c(o, lane * 16u, r_h, hsb + T * 1024u);
    }
  }
  STAMP(7);
  wait_vm<7>();
#pragma unroll
  for (int m = 0; m < 8; ++m) {
    PIN(uw[m]);
    const unsigned hp = pk_f16(lo_f(uw[m]), hi_f(uw[m]));
    const int t = tid + 256 * m;
    *(LAS unsigned short*)(lds + CV_U + 160 + 2 * t) = (unsigned short)(hp & 0xffffu);
    *(LAS unsigned short*)(lds + CV_U + CV_USTR + 160 + 2 * t) = (unsigned short)(hp >> 16);
  }
  if (tid < 20) *(LAS u32x4*)(lds + CV_U + (tid / 10) * CV_USTR + (tid % 10) * 16) = (u32x4){0u, 0u, 0u, 0u};
  wait_vm<0>();
  bar_lgkm();
  if (tid == 0) set_flag(ws, FL_H + (mt * 16 + ft) * 4, tag);
  if (tid == 0 && bid == 0) set_flag(ws, FL_EP, tag);
  STAMP(8);
  {
    const int G = lane >> 4, q4 = (lane & 15) >> 2, p = lane & 3;
    const LAS char* pa = lds + AUX + chl * CV_KSTR + 96 + 2 * (76 + 16 * p - 32 * G - 4 * q4 - 16);
    const LAS char* pb = lds + CV_U + chl * CV_USTR + 160 + 2 * (-76 + 32 * G + 4 * q4 + 4 * p) + 128 * par;
    LAS char* yp = lds + CV_Y + chl * 8192 + par * 256 + lane * 4;
    LAS char* cpn = lds + CV_C + chl * 528 + (par + 1) * 16;
    if (tid < 2) *(LAS u32x4*)(lds + CV_C + tid * 528) = (u32x4){0u, 0u, 0u, 0u};
    f16x8 A[16], B[16];
#define CV_A(q) __builtin_bit_cast(f16x8, cat8(trrd(pa + 256 * (q) + 32), trrd(pa + 256 * (q))))
#define CV_B(w) __builtin_bit_cast(f16x8, cat8(trrd(pb + 256 * (w)), trrd(pb + 256 * (w) + 32)))
    A[0] = CV_A(0); B[0] = CV_B(0);
    A[1] = CV_A(1); B[1] = CV_B(1);
    A[2] = CV_A(2); B[2] = CV_B(2);
    f32x4 cacc[2];
    static_for<0, 17>([&](auto jc) {
      constexpr int jj = decltype(jc)::value;
      if constexpr (jj + 3 < 16) { A[jj + 3] = CV_A(jj + 3); B[jj + 3] = CV_B(jj + 3); }
      if constexpr (jj < 16) {
        f32x4 a = {0.f, 0.f, 0.f, 0.f};
#pragma unroll
        for (int ww = 0; ww <= jj; ++ww) a = __builtin_amdgcn_mfma_f32_16x16x32_f16(A[jj - ww], B[ww], a, 0, 0, 0);
        cacc[jj & 1] = a;
      }
      if constexpr (jj >= 1) {
        const f32x4 a = cacc[(jj - 1) & 1];
        const float h3 = a[3];
        const float h2 = a[2] + dpp_mov<0x138>(h3);
        const float h1 = a[1] + dpp_mov<0x138>(h2);
        const float y = a[0] + dpp_mov<0x138>(h1);
        *(LAS float*)(yp + (jj - 1) * 512) = y;
        if (lane == 63) *(LAS f32x4*)(cpn + (jj - 1) * 32) = (f32x4){h1, h2, h3, 0.f};
      }
      if constexpr (jj >= 4 && jj < 12) { WD_PIECE(7 + jj - 4, 0); WD_PIECE(7 + jj - 4, 1); }
    });
  }
  STAMP(9);
  poll_flags((lane & 3) * 16u, r_fl, FL_H + (unsigned)((tt >> 1) * 64), tag);
  STAMP(10);
  u32x4 hb[28][2];
#define HB_LOAD(i) do { ld128c(hb[i][0], lane * 16u, r_h, ((unsigned)(tt * 2) * 112u + G2_KS(i)) * 1024u); \
                        ld128c(hb[i][1], lane * 16u, r_h, ((unsigned)(tt * 2 + 1) * 112u + G2_KS(i)) * 1024u); } while (0)
#pragma unroll
  for (int i = 0; i < G2_HB; ++i) HB_LOAD(i);
  bar_lgkm();
  {
    const float kscale = *(const LAS float*)(lds + CV_SC + chl * 4);
    const float dskip = chl ? hi_f(dsk2) : lo_f(dsk2);
#pragma unroll
    for (int it = 0; it < 2; ++it) {
      const int t0 = 8 * (lane + 64 * (par * 2 + it));
      f32x4 y0 = *(const LAS f32x4*)(lds + CV_Y + chl * 8192 + t0 * 4);
      const f32x4 y1 = *(const LAS f32x4*)(lds + CV_Y + chl * 8192 + t0 * 4 + 16);
      const f16x8 u = *(const LAS f16x8*)(lds + CV_U + chl * CV_USTR + 160 + t0 * 2);
      const int slot = (lane & 7) == 0 ? (t0 >> 6) : 0;
      y0 += *(const LAS f32x4*)(lds + CV_C + chl * 528 + slot * 16);
      u32x4 o;
#pragma unroll
      for (int j = 0; j < 4; ++j) {
        const float ya = j < 2 ? y0[2 * j] : y1[2 * j - 4], yb = j < 2 ? y0[2 * j + 1] : y1[2 * j - 3];
        const unsigned yq = pk_bf16(ya * kscale, yb * kscale);
        const unsigned uq = pk_bf16((float)u[2 * j] * dskip, (float)u[2 * j + 1] * dskip);
        o[j] = pk_bf16(lo_f(yq) + lo_f(uq), hi_f(yq) + hi_f(uq));
      }
      st128c(o, (unsigned)(t0 * 2), r_s, (unsigned)((d0 + chl) * 4096));
    }
  }
  STAMP(11);
  f32x16 acc2[2][2];
  unsigned sv[8];
  u32x4 xv[2], fl;
  {
    const int G = lane >> 4, q4 = (lane & 15) >> 2;
    const LAS char* wa0 = lds + (4 * (G >> 1) + q4) * 128 + (G & 1) * 32 + (lane & 3) * 8;
    const LAS char* waT[2] = {wa0 + ((q4 >> 1) * 64), wa0 + (((q4 >> 1) ^ 1) * 64)};
#pragma unroll
    for (int a = 0; a < 2; ++a)
#pragma unroll
      for (int b = 0; b < 2; ++b) acc2[a][b] = (f32x16){};
    bf16x8 Af[2][2];
#define D_READ(i, T) __builtin_bit_cast(bf16x8, cat8(trrd(waT[T] + G2_SLOT((i) % G2_NS)), trrd(waT[T] + G2_SLOT((i) % G2_NS) + 1024)))
    Af[0][0] = D_READ(0, 0); Af[0][1] = D_READ(0, 1);
    const unsigned xo = (unsigned)((tid >> 3) * (2 * DM * 2) + (tid & 7) * 16);
    const unsigned so = (unsigned)((tid & 7) * 8 * 4096 + (tid >> 3) * 4);
    static_for<0, 28>([&](auto ic) {
      constexpr int i = decltype(ic)::value;
      wait_vm<g2_wait(i)>();
      PIN(hb[i][0]); PIN(hb[i][1]);
      if constexpr (i == G2_SF) {
        asm volatile("s_barrier" ::: "memory");
        if (tid == 0) set_flag(ws, FL_S + cp * 4, tag);
      }
      if constexpr (i == G2_PC) {
        PIN(fl);
        const bool ok = (fl[0] == tag) & (fl[1] == tag) & (fl[2] == tag) & (fl[3] == tag);
        if (!__all(ok)) poll_flags((lane & 7) * 16u, r_fl, FL_S + (unsigned)(dtile * 128), tag);
      }
      if constexpr (i + 1 < 28) { Af[(i + 1) & 1][0] = D_READ(i + 1, 0); Af[(i + 1) & 1][1] = D_READ(i + 1, 1); }
#pragma unroll
      for (int T = 0; T < 2; ++T)
#pragma unroll
        for (int b = 0; b < 2; ++b)
          acc2[T][b] = __builtin_amdgcn_mfma_f32_32x32x16_bf16(Af[i & 1][T], __builtin_bit_cast(bf16x8, hb[i][b]), acc2[T][b], 0, 0, 0);
      SCHED_FENCE();
      if constexpr (g2_nh(i) > 0) HB_LOAD(i + G2_HB);
      if constexpr (g2_nd(i) > 0) { WD_PIECE(i + G2_NS, 0); WD_PIECE(i + G2_NS, 1); }
      if constexpr (i == G2_PF) {
        ld128c(fl, (lane & 7) * 16u, r_fl, FL_S + (unsigned)(dtile * 128));
        ld128(xv[0], xo, r_x, (unsigned)(tt * 64 * DM * 2 + dtile * 128));
        ld128(xv[1], xo, r_x, (unsigned)(tt * 64 * DM * 2 + dtile * 128 + DM * 2));
      }
      if constexpr (i == G2_PC) {
#pragma unroll
        for (int j = 0; j < 8; ++j) ld32c(sv[j], so, r_s, (unsigned)((dtile * 64 + j) * 4096 + tt * 128));
      }
    });
  }
  STAMP(12);
  bar_lgkm();
  {
    LAS char* rp = lds + wv * (64 * G2_RED) + (lane & 31) * G2_RED + (lane >> 5) * 16;
#pragma unroll
    for (int T = 0; T < 2; ++T)
#pragma unroll
      for (int b = 0; b < 2; ++b)
#pragma unroll
        for (int g = 0; g < 4; ++g) {
          const f32x4 v = {acc2[T][b][4 * g], acc2[T][b][4 * g + 1], acc2[T][b][4 * g + 2], acc2[T][b][4 * g + 3]};
          *(LAS f32x4*)(rp + b * 32 * G2_RED + T * 128 + g * 32) = v;
        }
  }
  bar_lgkm();
  wait_vm<0>();
  PIN(xv[0]); PIN(xv[1]);
#pragma unroll
  for (int j = 0; j < 8; ++j) PIN(sv[j]);
#pragma unroll
  for (int r = 0; r < 2; ++r) {
    const LAS char* rp = lds + (2 * (tid >> 3) + r) * G2_RED + (tid & 7) * 32;
    u32x4 o;
#pragma unroll
    for (int i = 0; i < 2; ++i) {
      f32x4 v = *(const LAS f32x4*)(rp + i * 16);
#pragma unroll
      for (int w = 1; w < 4; ++w) v += *(const LAS f32x4*)(rp + w * (64 * G2_RED) + i * 16);
#pragma unroll
      for (int jj = 0; jj < 2; ++jj) {
        const unsigned fp = pk_bf16(v[2 * jj], v[2 * jj + 1]);
        const float f0 = lo_f(fp), f1 = hi_f(fp);
        const float g0 = f0 * __builtin_amdgcn_rcpf(1.0f + __builtin_amdgcn_exp2f(f0 * -1.4426950408889634f));
        const float g1 = f1 * __builtin_amdgcn_rcpf(1.0f + __builtin_amdgcn_exp2f(f1 * -1.4426950408889634f));
        const unsigned gp = pk_bf16(g0, g1);
        const int j = 4 * i + 2 * jj;
        const float s0 = r ? hi_f(sv[j]) : lo_f(sv[j]), s1 = r ? hi_f(sv[j + 1]) : lo_f(sv[j + 1]);
        const unsigned sg = pk_bf16(s0 * lo_f(gp), s1 * hi_f(gp));
        const unsigned xw = xv[r][2 * i + jj];
        o[2 * i + jj] = pk_bf16(lo_f(xw) + lo_f(sg), hi_f(xw) + hi_f(sg));
      }
    }
    *(u32x4*)(out + (size_t)(tt * 64 + 2 * (tid >> 3) + r) * DM + dtile * 64 + (tid & 7) * 8) = o;
  }
  STAMP(13); STAMP_RT(15);
  STAMP_OUT(0);
}

extern "C" void kernel_launch(void* const* d_in, const int* in_sizes, int n_in, void* d_out,
                              int out_size, void* d_ws, size_t ws_size, hipStream_t stream) {
  (void)hipFuncSetAttribute((const void*)fused_kernel, hipFuncAttributeMaxDynamicSharedMemorySize, LDS_BYTES);
  fused_kernel<<<256, 256, LDS_BYTES, stream>>>(
      (const unsigned short*)d_in[0], (const unsigned short*)d_in[1], (const unsigned short*)d_in[2],
      (const unsigned short*)d_in[3], (const unsigned short*)d_in[4], (const unsigned short*)d_in[5],
      (const unsigned short*)d_in[6], (const unsigned*)d_in[7], (const unsigned*)d_in[8],
      (const unsigned short*)d_in[10], (const unsigned short*)d_in[11], (const unsigned short*)d_in[12],
      (char*)d_ws, (unsigned short*)d_out);
}
```

```cpp
#include <hip/hip_runtime.h>
#include <hip/hip_bf16.h>
#include <stdint.h>
#include <type_traits>

typedef __attribute__((ext_vector_type(4))) short s16x4;
typedef __attribute__((ext_vector_type(8))) short s16x8;
typedef __attribute__((ext_vector_type(8))) _Float16 f16x8;
typedef __attribute__((ext_vector_type(8))) __bf16 bf16x8;
typedef __attribute__((ext_vector_type(2))) float f32x2;
typedef __attribute__((ext_vector_type(4))) float f32x4;
typedef __attribute__((ext_vector_type(16))) float f32x16;
typedef __attribute__((ext_vector_type(4))) unsigned u32x4;
typedef __attribute__((ext_vector_type(3))) unsigned u32x3;
typedef __attribute__((ext_vector_type(2))) unsigned u32x2;
typedef __attribute__((ext_vector_type(2))) __bf16 bf16x2;
typedef __attribute__((ext_vector_type(2))) _Float16 f16x2;
#define LAS __attribute__((address_space(3)))
#define GAS __attribute__((address_space(1)))
typedef __amdgpu_buffer_rsrc_t rsrc_t;

constexpr int SL = 2048;
constexpr int DM = 512;
constexpr int NM = 128;
constexpr int FF = 1792;

constexpr size_t WS_XNF = 0;
constexpr size_t WS_ST = 2 * (size_t)SL * DM * 2;
constexpr size_t WS_HF = WS_ST + (size_t)SL * DM * 2;
constexpr size_t WS_FLAG = 16u << 20;
constexpr unsigned FL_XN = 0, FL_H = 65536, FL_S = 131072, FL_ALL = 196608, FL_EP = 262144, FL_BYTES = 262400;

template <int I, int N, class F>
__device__ __forceinline__ void static_for(F&& f) {
  if constexpr (I < N) { f(std::integral_constant<int, I>{}); static_for<I + 1, N>(f); }
}

__device__ __forceinline__ unsigned pk_bf16(float lo, float hi) {
  f32x2 v = {lo, hi};
  return __builtin_bit_cast(unsigned, __builtin_convertvector(v, bf16x2));
}
__device__ __forceinline__ unsigned pk_f16(float lo, float hi) {
  f32x2 v = {lo, hi};
  return __builtin_bit_cast(unsigned, __builtin_convertvector(v, f16x2));
}
__device__ __forceinline__ float lo_f(unsigned p) { return __uint_as_float(p << 16); }
__device__ __forceinline__ float hi_f(unsigned p) { return __uint_as_float(p & 0xffff0000u); }
__device__ __forceinline__ float rbf(float x) { return lo_f(pk_bf16(x, x)); }

template <int CTRL>
__device__ __forceinline__ float dpp_mov(float v) {
  return __int_as_float(__builtin_amdgcn_update_dpp(0, __float_as_int(v), CTRL, 0xf, 0xf, true));
}
__device__ __forceinline__ float rdlane(float v, int l) {
  return __int_as_float(__builtin_amdgcn_readlane(__float_as_int(v), l));
}
__device__ __forceinline__ float wave_sum(float v) {
  v += dpp_mov<0xB1>(v);
  v += dpp_mov<0x4E>(v);
  v += dpp_mov<0x141>(v);
  v += dpp_mov<0x140>(v);
  return (rdlane(v, 0) + rdlane(v, 16)) + (rdlane(v, 32) + rdlane(v, 48));
}
__device__ __forceinline__ float half_sum(float v, int lane) {
  v += dpp_mov<0xB1>(v);
  v += dpp_mov<0x4E>(v);
  v += dpp_mov<0x141>(v);
  v += dpp_mov<0x140>(v);
  const float lo = rdlane(v, 0) + rdlane(v, 16), hi = rdlane(v, 32) + rdlane(v, 48);
  return lane < 32 ? lo : hi;
}
__device__ __forceinline__ float wave_max(float v) {
  v = fmaxf(v, dpp_mov<0xB1>(v));
  v = fmaxf(v, dpp_mov<0x4E>(v));
  v = fmaxf(v, dpp_mov<0x141>(v));
  v = fmaxf(v, dpp_mov<0x140>(v));
  return fmaxf(fmaxf(rdlane(v, 0), rdlane(v, 16)), fmaxf(rdlane(v, 32), rdlane(v, 48)));
}

__device__ __forceinline__ rsrc_t mk_rsrc(const void* p, unsigned bytes) {
  return __builtin_amdgcn_make_buffer_rsrc((void*)p, (short)0, (int)bytes, 0x00020000);
}
__device__ __forceinline__ void dma16(unsigned lds_dst, unsigned voff, rsrc_t r, unsigned soff) {
  asm volatile("s_mov_b32 m0, %0\n\ts_nop 0\n\tbuffer_load_dwordx4 %1, %2, %3 offen lds"
               :: "s"(lds_dst), "v"(voff), "s"(r), "s"(soff) : "memory");
}
__device__ __forceinline__ void ld128(u32x4& dst, unsigned voff, rsrc_t r, unsigned soff) {
  asm volatile("buffer_load_dwordx4 %0, %1, %2, %3 offen" : "=v"(dst) : "v"(voff), "s"(r), "s"(soff) : "memory");
}
__device__ __forceinline__ void ld128c(u32x4& dst, unsigned voff, rsrc_t r, unsigned soff) {
  asm volatile("buffer_load_dwordx4 %0, %1, %2, %3 offen sc1" : "=v"(dst) : "v"(voff), "s"(r), "s"(soff) : "memory");
}
__device__ __forceinline__ void ld32c(unsigned& dst, unsigned voff, rsrc_t r, unsigned soff) {
  asm volatile("buffer_load_dword %0, %1, %2, %3 offen sc1" : "=v"(dst) : "v"(voff), "s"(r), "s"(soff) : "memory");
}
__device__ __forceinline__ void ld16(unsigned& dst, unsigned voff, rsrc_t r, unsigned soff) {
  asm volatile("buffer_load_ushort %0, %1, %2, %3 offen" : "=v"(dst) : "v"(voff), "s"(r), "s"(soff) : "memory");
}
__device__ __forceinline__ void st128c(u32x4 v, unsigned voff, rsrc_t r, unsigned soff) {
  asm volatile("buffer_store_dwordx4 %0, %1, %2, %3 offen sc1\n\ts_nop 1" :: "v"(v), "v"(voff), "s"(r), "s"(soff) : "memory");
}
template <int N>
__device__ __forceinline__ void wait_vm() { asm volatile("s_waitcnt vmcnt(%0)" :: "n"(N > 63 ? 63 : N) : "memory"); }
__device__ __forceinline__ void bar_lgkm() { asm volatile("s_waitcnt lgkmcnt(0)\n\ts_barrier" ::: "memory"); }
#define PIN(x) asm volatile("" : "+v"(x))
#define SCHED_FENCE() __builtin_amdgcn_sched_barrier(0)

__device__ __forceinline__ s16x4 trrd(const LAS char* p) {
  return __builtin_amdgcn_ds_read_tr16_b64_v4i16((LAS s16x4*)p);
}
__device__ __forceinline__ s16x8 cat8(s16x4 a, s16x4 b) {
  return __builtin_shufflevector(a, b, 0, 1, 2, 3, 4, 5, 6, 7);
}

__device__ __forceinline__ void poll_flags(unsigned voff, rsrc_t r, unsigned soff, unsigned tag) {
#pragma unroll 1
  for (int it = 0; it < (1 << 18); ++it) {
    u32x4 v;
    asm volatile("buffer_load_dwordx4 %0, %1, %2, %3 offen sc1\n\ts_waitcnt vmcnt(0)"
                 : "=&v"(v) : "v"(voff), "s"(r), "s"(soff) : "memory");
    const bool ok = (v[0] == tag) & (v[1] == tag) & (v[2] == tag) & (v[3] == tag);
    if (__all(ok)) break;
    __builtin_amdgcn_s_sleep(2);
  }
}
__device__ __forceinline__ void set_flag(char* ws, unsigned off, unsigned tag) {
  __hip_atomic_store((GAS unsigned*)(ws + WS_FLAG + off), tag, __ATOMIC_RELAXED, __HIP_MEMORY_SCOPE_AGENT);
}

#define STAMP_DECL
#define STAMP(i)
#define STAMP_RT(i)
#define STAMP_OUT(kid)

__device__ __forceinline__ f32x2 cmul(f32x2 a, f32x2 b) {
  const f32x2 t = a.xx * b;
  const f32x2 bs = {-b.y, b.x};
  return __builtin_elementwise_fma(a.yy, bs, t);
}
__device__ __forceinline__ f32x2 cexp_l(float a, float b, float l) {
  const float mag = __builtin_amdgcn_exp2f(a * l * 1.4426950408889634f);
  const float x = b * l;
  const float C1 = 0.15915494309189535f;
  const float C2 = (float)(0.15915494309189533577 - (double)0.15915494309189535f);
  const float hi = x * C1;
  const float lo = fmaf(x, C1, -hi) + x * C2;
  const float r = (hi - __builtin_rintf(hi)) + lo;
  f32x2 z;
  z.x = mag * __builtin_amdgcn_cosf(r);
  z.y = mag * __builtin_amdgcn_sinf(r);
  return z;
}

constexpr int W_UPOFF = 14336 + 128;
constexpr int W_STAGE = W_UPOFF + 14336;
constexpr int AUX = 5 * W_STAGE;
constexpr int LN_STG = 4 * W_STAGE;
constexpr int KG_ROW = 272, KG_WAVE = 16 * KG_ROW;
constexpr int KG_EXP = AUX + 4 * KG_WAVE;
constexpr int CV_KSTR = (48 + 2048) * 2;
constexpr int CV_USTR = (80 + 2048) * 2;
constexpr int CV_U = AUX + 2 * CV_KSTR;
constexpr int CV_SC = CV_U + 2 * CV_USTR;
constexpr int SY = KG_EXP + 16;
constexpr int SY_LN1 = SY, SY_STG = SY + 16, SY_KG = SY + 32, SY_M0 = SY + 48, SY_U = SY + 64, SY_Y = SY + 80,
              SY_WD = SY + 96, SY_CNT = SY + 112, SY_FREE = SY + 128, SY_FULL = SY + 256, SY_UR = SY + 304, SY_PL = SY + 320, SY_F34 = SY + 336, SY_END = SY + 368;
constexpr int LDS_BYTES = 161920;
static_assert(SY_END <= LDS_BYTES, "lds");
constexpr int G2_HI = 3 * W_STAGE;
constexpr int CV_Y = 65536;
constexpr int CV_C = CV_Y + 16384;
constexpr int CV_DUMP = CV_C + 1056;
constexpr int G2_NS = 15;
constexpr int G2_HB = 12;
constexpr int G2_RED = 272;
constexpr int G1_XA = 8;

constexpr int cmax(int a, int b) { return a > b ? a : b; }
constexpr int cmin(int a, int b) { return a < b ? a : b; }
constexpr int G2_PF = 16, G2_PC = 24;
constexpr int g2_nh(int i) { return i + G2_HB < 28 ? 2 : 0; }
constexpr int g2_nd(int i) { return i + G2_NS < 28 ? 2 : 0; }
constexpr int g2_ne(int i) { return i == G2_PF ? 1 : (i == G2_PC ? 8 : 0); }
constexpr int g2_cnt(int i) { int c = 2 * G2_HB; for (int j = 0; j < i; ++j) c += g2_nh(j) + g2_nd(j) + g2_ne(j); return c; }
constexpr int g2_posh(int i) { return i < G2_HB ? 2 * i + 1 : g2_cnt(i - G2_HB) + 1; }
constexpr int g2_posd(int i) { return i < G2_NS ? -1 : g2_cnt(i - G2_NS) + g2_nh(i - G2_NS) + 1; }
constexpr int g2_wait(int i) {
  int need = cmax(g2_posh(i), i + 1 < 28 ? g2_posd(i + 1) : -1);
  return g2_cnt(i) - 1 - need;
}

__device__ __forceinline__ void lds_set(unsigned addr) {
  asm volatile("ds_write_b32 %0, %1" :: "v"(addr), "v"(1u) : "memory");
}
__device__ __forceinline__ void lds_wait4(unsigned addr) {
#pragma unroll 1
  for (int it = 0; it < (1 << 20); ++it) {
    u32x4 v;
    asm volatile("ds_read_b128 %0, %1\n\ts_waitcnt lgkmcnt(0)" : "=&v"(v) : "v"(addr) : "memory");
    if (__builtin_amdgcn_readfirstlane(v[0] & v[1] & v[2] & v[3])) break;
    __builtin_amdgcn_s_sleep(1);
  }
}
__device__ __forceinline__ void lds_wait1(unsigned addr) {
#pragma unroll 1
  for (int it = 0; it < (1 << 20); ++it) {
    unsigned v;
    asm volatile("ds_read_b32 %0, %1\n\ts_waitcnt lgkmcnt(0)" : "=&v"(v) : "v"(addr) : "memory");
    if (__builtin_amdgcn_readfirstlane(v)) break;
    __builtin_amdgcn_s_sleep(1);
  }
}
__device__ __forceinline__ void poll_group(int wv, unsigned ldsw, unsigned voff, rsrc_t r, unsigned soff, unsigned tag) {
  if (wv == 0) { poll_flags(voff, r, soff, tag); lds_set(ldsw); }
  else lds_wait1(ldsw);
}
__device__ __forceinline__ void publish4(LAS char* lds, int cnt, char* ws, unsigned off, int nl, unsigned tag, int lane) {
  wait_vm<0>();
  unsigned old = 0;
  if (lane == 0) old = __hip_atomic_fetch_add((LAS unsigned*)(lds + SY_CNT + cnt * 4), 1u, __ATOMIC_RELAXED, __HIP_MEMORY_SCOPE_WORKGROUP);
  if (__builtin_amdgcn_readfirstlane(old) == 3 && lane < nl) set_flag(ws, off, tag);
}

__global__ __launch_bounds__(512, 2) void fused_kernel(
    const unsigned short* __restrict__ x, const unsigned short* __restrict__ A_re,
    const unsigned short* __restrict__ A_im, const unsigned short* __restrict__ B_re,
    const unsigned short* __restrict__ B_im, const unsigned short* __restrict__ C_re,
    const unsigned short* __restrict__ C_im, const unsigned* __restrict__ log_dt2,
    const unsigned* __restrict__ dsk2p, const unsigned short* __restrict__ wg,
    const unsigned short* __restrict__ wu, const unsigned short* __restrict__ wd,
    char* ws, unsigned short* __restrict__ out) {
  extern __shared__ __attribute__((aligned(16))) char smem[];
  LAS char* lds = (LAS char*)smem;
  const unsigned ldsb = (unsigned)(uintptr_t)lds;
  const int tid = threadIdx.x, lane = tid & 63;
  const int w8 = __builtin_amdgcn_readfirstlane(tid >> 6);
  const int wv = w8 & 3;
  STAMP_DECL; STAMP_RT(14); STAMP(0);
  const int bid = blockIdx.x;
  const int xcd = bid & 7, bi = bid >> 3;
  const int mt = (xcd >> 2) * 8 + (bi >> 2);
  const int ft = (xcd & 3) * 4 + (bi & 3);
  const int rot = ((bi >> 2) + 2 * (bi & 3)) & 7;
  const int cp = xcd * 32 + bi;
  const int tt = xcd * 4 + (bi >> 3);
  const int dtile = bi & 7;
  const int rot2 = ((bi >> 3) * 7 + dtile * 3) % 28;

  const rsrc_t r_x = mk_rsrc(x, SL * DM * 2);
  const rsrc_t r_xn = mk_rsrc(ws + WS_XNF, SL * DM * 2);
  const rsrc_t r_s = mk_rsrc(ws + WS_ST, SL * DM * 2);
  const rsrc_t r_fl = mk_rsrc(ws + WS_FLAG, FL_BYTES);
  const rsrc_t r_w = mk_rsrc(wv < 2 ? wg : wu, DM * FF * 2);
  const rsrc_t r_wd = mk_rsrc(wd, FF * DM * 2);

  unsigned woff[7];
  const unsigned wdst = ldsb + (wv >> 1) * W_UPOFF + (wv & 1) * 7 * 1024;
  const unsigned wsrc = (unsigned)(ft * 224);
#define W_OFFS() _Pragma("unroll") for (int i = 0; i < 7; ++i) { const int c = ((wv & 1) * 7 + i) * 64 + lane; \
    const int row = c / 14, col = c - row * 14; woff[i] = (unsigned)(row * (FF * 2) + col * 16); }
#define W_PIECE(st, i) dma16(wdst + ((st) % 5) * W_STAGE + (i) * 1024, woff[i], r_w, wsrc + (unsigned)((((st) + rot) & 7) * (64 * FF * 2)))
#define W_ALL(st) do { _Pragma("unroll") for (int i_ = 0; i_ < 7; ++i_) W_PIECE(st, i_); } while (0)
  const unsigned wvo = (unsigned)((lane >> 3) * 1024 + (((lane & 7) ^ (((lane >> 4) & 1) << 2)) * 16));
  const unsigned wdcol = (unsigned)(dtile * 128);
#define G2_KS(i) ((unsigned)(wv * 28 + (((i) + rot2) >= 28 ? (i) + rot2 - 28 : (i) + rot2)))
#define G2_SLOT(j) ((j) < 7 ? G2_HI + (wv * 7 + (j)) * 2048 : (wv * 8 + (j) - 7) * 2048)
#define WD_PIECE(i, hlf) dma16(ldsb + G2_SLOT((i) % G2_NS) + (hlf) * 1024, wvo, r_wd, G2_KS(i) * 16384u + (hlf) * 8192u + wdcol)
  const unsigned xo = (unsigned)((tid >> 3) * (DM * 2) + (tid & 7) * 16);
  const unsigned so = (unsigned)((tid & 7) * 8 * 4096 + (tid >> 4) * 4);
  unsigned sv[8];
  u32x4 xv;
  unsigned ep;

  if (w8 < 4) {
    const rsrc_t r_h = mk_rsrc(ws + WS_HF, SL * FF * 2);
    if (tid < 23) *(LAS u32x4*)(lds + SY + tid * 16) = (u32x4){0u, 0u, 0u, 0u};
    W_OFFS();
    bar_lgkm();
    __builtin_amdgcn_s_sleep(8);
    static_for<0, 21>([&](auto pc) {
      constexpr int q = decltype(pc)::value;
      W_PIECE(q / 7, q % 7);
      wait_vm<6>();
    });
    STAMP(1);
    lds_wait4(ldsb + SY_STG);
    STAMP(2);
    const unsigned tag = (unsigned)__builtin_amdgcn_readfirstlane(*(const volatile LAS unsigned*)(lds + SY_CNT + 12));
    wait_vm<0>();
    lds_set(ldsb + SY_M0 + wv * 4);
    STAMP(3);
    poll_group(wv, ldsb + SY_PL, (lane & 3) * 16u, r_fl, FL_XN + (unsigned)((mt * 16 + ft) * 256), tag);
    STAMP(4);
    u32x4 xb[32];
    const unsigned xsb = (unsigned)(mt * 4 + wv) * 32768u;
#define XB_LOAD(i) ld128c(xb[i], lane * 16u, r_xn, xsb + (unsigned)(((((i) >> 2) + rot) & 7) * 4096 + ((i) & 3) * 1024))
#pragma unroll
    for (int i = 0; i < G1_XA; ++i) XB_LOAD(i);
    lds_wait4(ldsb + SY_M0);
    f32x16 acc[7];
    {
      const int G = lane >> 4;
      const LAS char* wa = lds + (G & 1) * W_UPOFF + (8 * (G >> 1) + ((lane & 15) >> 2)) * 224 + (lane & 3) * 8;
      bf16x8 Af[2][7];
#define A_READ(i, T) __builtin_bit_cast(bf16x8, cat8(trrd(wa + (((i) >> 2) % 5) * W_STAGE + (16 * ((i) & 3)) * 224 + 32 * (T)), \
                                                      trrd(wa + (((i) >> 2) % 5) * W_STAGE + (16 * ((i) & 3) + 4) * 224 + 32 * (T))))
#pragma unroll
      for (int T = 0; T < 7; ++T) { Af[0][T] = A_READ(0, T); acc[T] = (f32x16){}; }
      static_for<0, 32>([&](auto ic) {
        constexpr int i = decltype(ic)::value;
        wait_vm<cmin(G1_XA - 1, 31 - i)>();
        PIN(xb[i]);
        if constexpr (i == 1) STAMP(5);
        if constexpr (i == 16) STAMP(6);
        if constexpr ((i & 3) == 3) {
          lds_set(ldsb + SY_FREE + (i >> 2) * 16 + wv * 4);
          if constexpr ((i + 1) / 4 >= 5 && i < 31) lds_wait4(ldsb + SY_FULL + ((i + 1) / 4 - 5) * 16);
          if constexpr ((i + 1) / 4 == 3 || (i + 1) / 4 == 4) lds_wait4(ldsb + SY_F34 + ((i + 1) / 4 - 3) * 16);
        }
        const bf16x8 B = __builtin_bit_cast(bf16x8, xb[i]);
        static_for<0, 7>([&](auto tc) {
          constexpr int T = decltype(tc)::value;
          if constexpr (i + 1 < 32) Af[(i + 1) & 1][T] = A_READ(i + 1, T);
          acc[T] = __builtin_amdgcn_mfma_f32_32x32x16_bf16(Af[i & 1][T], B, acc[T], 0, 0, 0);
          if constexpr (T == 1 && i + G1_XA < 32) XB_LOAD(i + G1_XA);
          SCHED_FENCE();
        });
      });
    }
    STAMP(7);
    {
      const unsigned hsb = (unsigned)(((mt * 4 + wv) * 112 + ft * 7) * 1024);
#pragma unroll
      for (int T = 0; T < 7; ++T) {
        u32x4 o;
#pragma unroll
        for (int j = 0; j < 4; ++j) {
          const float s0 = __builtin_amdgcn_rcpf(1.0f + __builtin_amdgcn_exp2f(acc[T][2 * j] * -1.4426950408889634f));
          const float s1 = __builtin_amdgcn_rcpf(1.0f + __builtin_amdgcn_exp2f(acc[T][2 * j + 1] * -1.4426950408889634f));
          o[j] = pk_bf16(s0 * acc[T][8 + 2 * j], s1 * acc[T][8 + 2 * j + 1]);
        }
        st128c(o, lane * 16u, r_h, hsb + T * 1024u);
      }
    }
    STAMP(8);
    publish4(lds, 1, ws, FL_H + (unsigned)((mt * 16 + lane) * 256 + ft * 4), 16, tag, lane);
    STAMP(9);
    poll_group(wv, ldsb + SY_PL + 4, (lane & 3) * 16u, r_fl, FL_H + (unsigned)((tt * 8 + dtile) * 256), tag);
    STAMP(10);
    u32x4 hb[28][2];
#define HB_LOAD(i) do { ld128c(hb[i][0], lane * 16u, r_h, ((unsigned)(tt * 2) * 112u + G2_KS(i)) * 1024u); \
                        ld128c(hb[i][1], lane * 16u, r_h, ((unsigned)(tt * 2 + 1) * 112u + G2_KS(i)) * 1024u); } while (0)
#pragma unroll
    for (int i = 0; i < G2_HB; ++i) HB_LOAD(i);
    lds_wait1(ldsb + SY_WD + wv * 4);
    STAMP(11);
    f32x16 acc2[2][2];
    {
      const int G = lane >> 4, q4 = (lane & 15) >> 2;
      const LAS char* wa0 = lds + (4 * (G >> 1) + q4) * 128 + (G & 1) * 32 + (lane & 3) * 8;
      const LAS char* waT[2] = {wa0 + ((q4 >> 1) * 64), wa0 + (((q4 >> 1) ^ 1) * 64)};
#pragma unroll
      for (int a = 0; a < 2; ++a)
#pragma unroll
        for (int b = 0; b < 2; ++b) acc2[a][b] = (f32x16){};
      bf16x8 Af[2][2];
#define D_READ(i, T) __builtin_bit_cast(bf16x8, cat8(trrd(waT[T] + G2_SLOT((i) % G2_NS)), trrd(waT[T] + G2_SLOT((i) % G2_NS) + 1024)))
      Af[0][0] = D_READ(0, 0); Af[0][1] = D_READ(0, 1);
      static_for<0, 28>([&](auto ic) {
        constexpr int i = decltype(ic)::value;
        wait_vm<g2_wait(i)>();
        PIN(hb[i][0]); PIN(hb[i][1]);
        if constexpr (i == G2_PC) {
          lds_wait1(ldsb + SY_PL + 12);
        }
        if constexpr (i + 1 < 28) { Af[(i + 1) & 1][0] = D_READ(i + 1, 0); Af[(i + 1) & 1][1] = D_READ(i + 1, 1); }
#pragma unroll
        for (int T = 0; T < 2; ++T)
#pragma unroll
          for (int b = 0; b < 2; ++b)
            acc2[T][b] = __builtin_amdgcn_mfma_f32_32x32x16_bf16(Af[i & 1][T], __builtin_bit_cast(bf16x8, hb[i][b]), acc2[T][b], 0, 0, 0);
        SCHED_FENCE();
        if constexpr (g2_nh(i) > 0) HB_LOAD(i + G2_HB);
        if constexpr (g2_nd(i) > 0) { WD_PIECE(i + G2_NS, 0); WD_PIECE(i + G2_NS, 1); }
        if constexpr (i == G2_PF) {
          ld128(xv, xo, r_x, (unsigned)(tt * 64 * DM * 2 + dtile * 128));
        }
        if constexpr (i == G2_PC) {
#pragma unroll
          for (int j = 0; j < 8; ++j) ld32c(sv[j], so, r_s, (unsigned)((dtile * 64 + j) * 4096 + tt * 128));
        }
      });
    }
    STAMP(12);
    bar_lgkm();
    {
      LAS char* rp = lds + wv * (64 * G2_RED) + (lane & 31) * G2_RED + (lane >> 5) * 16;
#pragma unroll
      for (int T = 0; T < 2; ++T)
#pragma unroll
        for (int b = 0; b < 2; ++b)
#pragma unroll
          for (int g = 0; g < 4; ++g) {
            const f32x4 v = {acc2[T][b][4 * g], acc2[T][b][4 * g + 1], acc2[T][b][4 * g + 2], acc2[T][b][4 * g + 3]};
            *(LAS f32x4*)(rp + b * 32 * G2_RED + T * 128 + g * 32) = v;
          }
    }
  } else {
    const int ht = tid - 256;
    const int chl = wv & 1, par = wv >> 1;
    const int d0 = cp * 2;
    u32x4 xr[2];
    unsigned prm[6];
    {
      const unsigned vo = (unsigned)((lane >> 5) * 1024 + (lane & 31) * 16);
      ld128(xr[0], vo, r_x, (unsigned)((bid * 8 + 2 * wv) * 1024));
      ld128(xr[1], vo, r_x, (unsigned)((bid * 8 + 2 * wv) * 1024 + 512));
    }
    ld32c(ep, 0u * lane, r_fl, FL_EP);
    {
      const unsigned po = (unsigned)(((d0 + chl) * NM + par * 64) * 2);
      ld16(prm[0], lane * 2u, mk_rsrc(A_re, DM * NM * 2), po);
      ld16(prm[1], lane * 2u, mk_rsrc(A_im, DM * NM * 2), po);
      ld16(prm[2], lane * 2u, mk_rsrc(B_re, DM * NM * 2), po);
      ld16(prm[3], lane * 2u, mk_rsrc(B_im, DM * NM * 2), po);
      ld16(prm[4], lane * 2u, mk_rsrc(C_re, DM * NM * 2), po);
      ld16(prm[5], lane * 2u, mk_rsrc(C_im, DM * NM * 2), po);
    }
    const unsigned ldt2 = log_dt2[cp], dsk2 = dsk2p[cp];
    bar_lgkm();
    wait_vm<6>();
    STAMP(1);
    PIN(xr[0]); PIN(xr[1]); PIN(ep);
    const unsigned tag = (unsigned)__builtin_amdgcn_readfirstlane(ep) + 1u;
    *(volatile LAS unsigned*)(lds + SY_CNT + 12) = tag;
    {
      float f[16];
#pragma unroll
      for (int j = 0; j < 8; ++j) { f[2 * j] = lo_f(xr[j >> 2][j & 3]); f[2 * j + 1] = hi_f(xr[j >> 2][j & 3]); }
      float s = 0.f;
#pragma unroll
      for (int j = 0; j < 16; j += 4) s += (f[j] + f[j + 1]) + (f[j + 2] + f[j + 3]);
      const float mean = half_sum(s, lane) * (1.0f / DM);
      float q0 = 0.f, q1 = 0.f;
#pragma unroll
      for (int j = 0; j < 16; j += 2) {
        const float da = f[j] - mean, db = f[j + 1] - mean;
        q0 = fmaf(da, da, q0); q1 = fmaf(db, db, q1);
      }
      const float var = half_sum(q0 + q1, lane) * (1.0f / DM);
      const float mb = rbf(mean);
      const float sb = rbf(__builtin_amdgcn_sqrtf(rbf(rbf(var) + 1e-5f)));
      const float rs = __builtin_amdgcn_rcpf(sb);
      LAS char* sp = lds + LN_STG + (2 * wv + (lane >> 5)) * 1040 + (lane & 31) * 16;
#pragma unroll
      for (int i = 0; i < 2; ++i) {
        u32x4 o;
#pragma unroll
        for (int j = 0; j < 4; ++j) {
          const unsigned d = pk_bf16(f[8 * i + 2 * j] - mb, f[8 * i + 2 * j + 1] - mb);
          o[j] = pk_bf16(lo_f(d) * rs, hi_f(d) * rs);
        }
        *(LAS u32x4*)(sp + i * 512) = o;
      }
    }
    lds_set(ldsb + SY_LN1 + wv * 4);
    lds_wait4(ldsb + SY_LN1);
    {
      const unsigned sb = (unsigned)((bid >> 2) * 32768 + (bid & 3) * 128);
#pragma unroll
      for (int m = 0; m < 2; ++m) {
        const int item = ht + 256 * m, chunk = item >> 3, row = item & 7;
        const u32x4 o = *(const LAS u32x4*)(lds + LN_STG + row * 1040 + chunk * 16);
        st128c(o, (unsigned)(chunk * 512 + row * 16), r_xn, sb);
      }
    }
    lds_set(ldsb + SY_STG + wv * 4);
    STAMP(2);
    wait_vm<2>();
#pragma unroll
    for (int i = 0; i < 6; ++i) PIN(prm[i]);
    f32x4 kacc[3];
    int we;
    float g2;
    {
      const float ldt = chl ? hi_f(ldt2) : lo_f(ldt2);
      const float L2E = 1.4426950408889634f, L2E_LO = (float)(1.4426950408889634074 - (double)1.4426950408889634f);
      const float th = ldt * L2E;
      const float tl = fmaf(ldt, L2E, -th) + ldt * L2E_LO;
      const float e0 = __builtin_amdgcn_exp2f(th);
      const float dt = rbf(fmaf(e0 * 0.6931471805599453f, tl, e0));
      float a = dt * lo_f(prm[0]);
      const float b = dt * lo_f(prm[1]);
      const f32x2 db = {dt * lo_f(prm[2]), dt * lo_f(prm[3])};
      const f32x2 cc = {lo_f(prm[4]), lo_f(prm[5])};
      f32x2 w = cmul(cc, db);
      const float amax = fmaxf(wave_max(a), 0.f);
      a -= amax;
      g2 = amax * L2E;
      const float wm = wave_max(fmaxf(fabsf(w.x), fabsf(w.y)));
      we = __builtin_amdgcn_frexp_expf(wm);
      we = we < -100 ? -100 : we;
      w *= __builtin_amdgcn_ldexpf(1.0f, -we);
      const f32x2 z1 = cexp_l(a, b, 1.f), z16 = cexp_l(a, b, 16.f), z256 = cexp_l(a, b, 256.f);
      LAS char* tw = lds + AUX + wv * KG_WAVE + lane * 4;
      const LAS char* tr = lds + AUX + wv * KG_WAVE + (lane & 15) * KG_ROW + (lane >> 4) * 16;
#define PUT_P(r, v) do { const f32x2 v_ = (v); *(LAS unsigned*)(tw + (r) * KG_ROW) = pk_f16(v_.x, -v_.y); } while (0)
#define PUT_Q(c, v) do { const f32x2 v_ = (v); *(LAS unsigned*)(tw + (c) * KG_ROW) = pk_f16(v_.x, v_.y); } while (0)
      f16x8 qf[4], pf[4];
      {
        f32x2 q[4];
        q[0] = (f32x2){1.f, 0.f}; q[1] = z16; q[2] = cmul(z16, z16); q[3] = cmul(q[2], z16);
        const f32x2 z64 = cmul(q[2], q[2]);
#pragma unroll
        for (int c = 0; c < 16; c += 4) {
#pragma unroll
          for (int i = 0; i < 4; ++i) { PUT_Q(c + i, q[i]); if (c + 4 < 16) q[i] = cmul(q[i], z64); }
        }
#pragma unroll
        for (int s = 0; s < 4; ++s) qf[s] = *(const LAS f16x8*)(tr + s * 64);
      }
      STAMP(7);
      publish4(lds, 0, ws, lane < 16 ? FL_XN + (unsigned)(((bid >> 4) * 16 + lane) * 256 + (bid & 15) * 4) : FL_ALL + (unsigned)((lane - 16) * 4096 + bid * 4), 24, tag, lane);
      STAMP(3);
      W_OFFS();
      W_ALL(3);
      lds_wait4(ldsb + SY_STG);
      W_ALL(4);
      const f32x2 z2 = cmul(z1, z1), z4 = cmul(z2, z2), z8 = cmul(z4, z4);
      {
        f32x2 p[4];
        p[0] = w; p[1] = cmul(w, z1); p[2] = cmul(w, z2); p[3] = cmul(p[1], z2);
#pragma unroll
        for (int r = 0; r < 16; r += 4) {
#pragma unroll
          for (int i = 0; i < 4; ++i) { PUT_P(r + i, p[i]); if (r + 4 < 16) p[i] = cmul(p[i], z4); }
        }
#pragma unroll
        for (int s = 0; s < 4; ++s) pf[s] = *(const LAS f16x8*)(tr + s * 64);
        kacc[0] = (f32x4){0.f, 0.f, 0.f, 0.f};
#pragma unroll
        for (int s = 0; s < 4; ++s) kacc[0] = __builtin_amdgcn_mfma_f32_16x16x32_f16(pf[s], qf[s], kacc[0], 0, 0, 0);
      }
      wait_vm<7>();
      lds_set(ldsb + SY_F34 + wv * 4);
      const f32x2 z512 = cmul(z256, z256);
      const f32x2 w1 = cmul(w, z256), w2 = cmul(w, z512);
      const f32x2 w3 = cmul(w1, z512), w4 = cmul(w2, z512), w5 = cmul(w3, z512), w6 = cmul(w4, z512);
      const f32x2 w7 = cmul(w5, z512), w8_ = cmul(w6, z512);
      {
        f32x2 p[4];
        p[0] = w1; p[1] = cmul(w1, z2); p[2] = w2; p[3] = w3;
#pragma unroll
        for (int i = 0; i < 4; ++i) {
          PUT_P(2 * i, p[0]); PUT_P(2 * i + 1, p[1]); PUT_P(8 + i, p[2]); PUT_P(12 + i, p[3]);
          if (i < 3) {
#pragma unroll
            for (int k = 0; k < 4; ++k) p[k] = cmul(p[k], z4);
          }
        }
#pragma unroll
        for (int s = 0; s < 4; ++s) pf[s] = *(const LAS f16x8*)(tr + s * 64);
        kacc[1] = (f32x4){0.f, 0.f, 0.f, 0.f};
#pragma unroll
        for (int s = 0; s < 4; ++s) kacc[1] = __builtin_amdgcn_mfma_f32_16x16x32_f16(pf[s], qf[s], kacc[1], 0, 0, 0);
      }
      {
        PUT_P(0, w4); PUT_P(1, cmul(w4, z8)); PUT_P(2, w5); PUT_P(3, cmul(w5, z8));
        PUT_P(4, w6); PUT_P(5, cmul(w6, z8)); PUT_P(6, w7); PUT_P(7, cmul(w7, z8));
        PUT_P(8, w8_);
#pragma unroll
        for (int s = 0; s < 4; ++s) pf[s] = *(const LAS f16x8*)(tr + s * 64);
        kacc[2] = (f32x4){0.f, 0.f, 0.f, 0.f};
#pragma unroll
        for (int s = 0; s < 4; ++s) kacc[2] = __builtin_amdgcn_mfma_f32_16x16x32_f16(pf[s], qf[s], kacc[2], 0, 0, 0);
      }
    }
    {
      wait_vm<0>();
      lds_set(ldsb + SY_F34 + 16 + wv * 4);
      const int c = lane & 15, g = lane >> 4;
      const float un = __builtin_amdgcn_ldexpf(1.0f, we);
#pragma unroll
      for (int t = 0; t < 3; ++t) kacc[t] *= un;
      if (g2 > 0.f) {
#pragma unroll
        for (int i = 0; i < 4; ++i) {
          const int l0 = 16 * c + 4 * g + i;
          const int l1 = g < 2 ? 256 + 16 * c + 8 * g + 2 * i : 512 + 256 * (g - 2) + 16 * c + 4 * i;
          const int l2 = g < 2 ? 1024 + 256 * (2 * g + (i >> 1)) + 8 * (i & 1) + 16 * c : 2048;
          kacc[0][i] *= __builtin_amdgcn_exp2f(g2 * (float)l0);
          kacc[1][i] *= __builtin_amdgcn_exp2f(g2 * (float)l1);
          kacc[2][i] *= __builtin_amdgcn_exp2f(g2 * (float)l2);
        }
      }
      const float eb = (float)we + ceilf(g2 * 2048.f);
      if (par) {
#pragma unroll
        for (int t = 0; t < 3; ++t) *(LAS f32x4*)(lds + AUX + wv * KG_WAVE + t * 1024 + lane * 16) = kacc[t];
        if (lane == 0) *(LAS float*)(lds + KG_EXP + chl * 4) = eb;
      }
      lds_set(ldsb + SY_KG + wv * 4);
      if (!par) {
        lds_wait4(ldsb + SY_KG);
#pragma unroll
        for (int t = 0; t < 3; ++t) kacc[t] += *(const LAS f32x4*)(lds + AUX + (wv + 2) * KG_WAVE + t * 1024 + lane * 16);
        const float eo = *(const LAS float*)(lds + KG_EXP + chl * 4);
        const int E = (int)fminf(fmaxf(eb, eo), 120.f) - 7;
        const float sc = __builtin_amdgcn_ldexpf(1.0f, -E);
        if (lane == 0) *(LAS float*)(lds + CV_SC + chl * 4) = __builtin_amdgcn_ldexpf(1.0f, E);
        unsigned d[3][4];
#pragma unroll
        for (int t = 0; t < 3; ++t)
#pragma unroll
          for (int i = 0; i < 4; ++i) d[t][i] = pk_f16(kacc[t][i] * sc, kacc[t][i] * sc);
        LAS char* kf = lds + AUX + chl * CV_KSTR + 96;
#define MIX(a, b) (((a) & 0xffffu) | ((b) & 0xffff0000u))
        if (g < 2) {
#pragma unroll
          for (int h = 0; h < 2; ++h) {
            LAS char* p = kf + 2 * (1024 + 256 * (2 * g + h) + 16 * c);
            const unsigned v0 = d[2][2 * h], v1 = d[2][2 * h + 1];
            *(LAS u32x2*)(p - 8) = (u32x2){v0, v0};
            *(LAS u32x4*)(p) = (u32x4){v0, v0, MIX(v0, v1), v1};
            *(LAS u32x2*)(p + 16) = (u32x2){v1, v1};
          }
        } else {
          if (lane == 32) *(LAS u32x2*)(kf + 2 * 2044) = (u32x2){d[2][0], d[2][0]};
        }
        if (g >= 2) {
          LAS char* p = kf + 2 * (512 + 256 * (g - 2) + 16 * c);
          const unsigned v0 = d[1][0], v1 = d[1][1], v2 = d[1][2], v3 = d[1][3];
          *(LAS unsigned*)(p - 4) = v0;
          *(LAS u32x4*)(p) = (u32x4){v0, MIX(v0, v1), v1, v2};
          *(LAS u32x2*)(p + 16) = (u32x2){v2, MIX(v2, v3)};
          *(LAS unsigned*)(p + 24) = v3;
        }
        if (g < 2) {
          LAS char* p = kf + 2 * (256 + 16 * c + 8 * g);
          const unsigned v0 = d[1][0], v1 = d[1][1], v2 = d[1][2], v3 = d[1][3];
          *(LAS unsigned short*)(p - 2) = (unsigned short)v0;
          *(LAS u32x2*)(p) = (u32x2){v0, MIX(v1, v2)};
          *(LAS unsigned*)(p + 8) = v2;
          *(LAS unsigned short*)(p + 12) = (unsigned short)v3;
        }
        *(LAS u32x2*)(kf + 2 * (16 * c + 4 * g)) = (u32x2){MIX(d[0][0], d[0][1]), MIX(d[0][2], d[0][3])};
        if (lane < 6) *(LAS u32x4*)(kf - 96 + lane * 16) = (u32x4){0u, 0u, 0u, 0u};
        lds_set(ldsb + SY_U + chl * 4);
        lds_set(ldsb + SY_U + 8 + chl * 4);
      }
    }
    STAMP(4);
    lds_wait4(ldsb + SY_FREE);
    W_ALL(5);
    lds_wait4(ldsb + SY_FREE + 16);
    W_ALL(6);
    wait_vm<7>();
    lds_set(ldsb + SY_FULL + wv * 4);
    lds_wait4(ldsb + SY_FREE + 32);
    W_ALL(7);
    wait_vm<7>();
    lds_set(ldsb + SY_FULL + 16 + wv * 4);
    wait_vm<0>();
    lds_set(ldsb + SY_FULL + 32 + wv * 4);
    poll_group(wv, ldsb + SY_PL + 8, lane * 16u, r_fl, FL_ALL + (unsigned)(xcd * 4096), tag);
    if (ht == 0 && bid == 0) set_flag(ws, FL_EP, tag);
    STAMP(5);
    unsigned uw[8];
    {
      const unsigned uoff = (unsigned)(ht >> 5) * 32768u + (unsigned)(ht & 31) * 16u;
      const unsigned usb = (unsigned)((cp >> 2) * 512 + (cp & 3) * 4);
#pragma unroll
      for (int m = 0; m < 8; ++m) ld32c(uw[m], uoff, r_xn, usb + (unsigned)m * 8u * 32768u);
    }
    lds_wait4(ldsb + SY_U);
    wait_vm<0>();
#pragma unroll
    for (int m = 0; m < 8; ++m) {
      PIN(uw[m]);
      const unsigned hp = pk_f16(lo_f(uw[m]), hi_f(uw[m]));
      const int t = ht + 256 * m;
      *(LAS unsigned short*)(lds + CV_U + 160 + 2 * t) = (unsigned short)(hp & 0xffffu);
      *(LAS unsigned short*)(lds + CV_U + CV_USTR + 160 + 2 * t) = (unsigned short)(hp >> 16);
    }
    if (ht < 20) *(LAS u32x4*)(lds + CV_U + (ht / 10) * CV_USTR + (ht % 10) * 16) = (u32x4){0u, 0u, 0u, 0u};
    lds_set(ldsb + SY_UR + wv * 4);
    STAMP(6);
    lds_wait4(ldsb + SY_FREE + 64);
#pragma unroll
    for (int j = 0; j < 7; ++j) { WD_PIECE(j, 0); WD_PIECE(j, 1); }
    lds_wait4(ldsb + SY_UR);
    lds_wait4(ldsb + SY_FREE + 112);
    STAMP(8);
    {
      const int G = lane >> 4, q4 = (lane & 15) >> 2, p = lane & 3;
      const LAS char* pa = lds + AUX + chl * CV_KSTR + 96 + 2 * (76 + 16 * p - 32 * G - 4 * q4 - 16);
      const LAS char* pb = lds + CV_U + chl * CV_USTR + 160 + 2 * (-76 + 32 * G + 4 * q4 + 4 * p) + 128 * par;
      LAS char* yp = lds + CV_Y + chl * 8192 + par * 256 + lane * 4;
      LAS char* cpn = lane == 63 ? lds + CV_C + chl * 528 + (par + 1) * 16 : lds + CV_DUMP + (lane & 31) * 16;
      if (ht < 2) *(LAS u32x4*)(lds + CV_C + ht * 528) = (u32x4){0u, 0u, 0u, 0u};
      f16x8 A[16], B[16];
#define CV_A(q) __builtin_bit_cast(f16x8, cat8(trrd(pa + 256 * (q) + 32), trrd(pa + 256 * (q))))
#define CV_B(w) __builtin_bit_cast(f16x8, cat8(trrd(pb + 256 * (w)), trrd(pb + 256 * (w) + 32)))
      A[0] = CV_A(0); B[0] = CV_B(0);
      A[1] = CV_A(1); B[1] = CV_B(1);
      A[2] = CV_A(2); B[2] = CV_B(2);
      f32x4 cacc[2];
      static_for<0, 17>([&](auto jc) {
        constexpr int jj = decltype(jc)::value;
        if constexpr (jj + 3 < 16) { A[jj + 3] = CV_A(jj + 3); B[jj + 3] = CV_B(jj + 3); }
        if constexpr (jj < 16) {
          f32x4 a = {0.f, 0.f, 0.f, 0.f};
#pragma unroll
          for (int ww = 0; ww <= jj; ++ww) a = __builtin_amdgcn_mfma_f32_16x16x32_f16(A[jj - ww], B[ww], a, 0, 0, 0);
          cacc[jj & 1] = a;
        }
        if constexpr (jj >= 1) {
          const f32x4 a = cacc[(jj - 1) & 1];
          const float h3 = a[3];
          const float h2 = a[2] + dpp_mov<0x138>(h3);
          const float h1 = a[1] + dpp_mov<0x138>(h2);
          const float y = a[0] + dpp_mov<0x138>(h1);
          *(LAS float*)(yp + (jj - 1) * 512) = y;
          *(LAS f32x4*)(cpn + (jj - 1) * 32) = (f32x4){h1, h2, h3, 0.f};
        }
        if constexpr (jj < 8) { WD_PIECE(7 + jj, 0); WD_PIECE(7 + jj, 1); }
        if constexpr (jj == 13) { wait_vm<0>(); lds_set(ldsb + SY_WD + wv * 4); }
      });
    }
    lds_set(ldsb + SY_Y + wv * 4);
    STAMP(9);
    lds_wait4(ldsb + SY_Y);
    {
      const float kscale = *(const LAS float*)(lds + CV_SC + chl * 4);
      const float dskip = chl ? hi_f(dsk2) : lo_f(dsk2);
#pragma unroll
      for (int it = 0; it < 2; ++it) {
        const int t0 = 8 * (lane + 64 * (par * 2 + it));
        f32x4 y0 = *(const LAS f32x4*)(lds + CV_Y + chl * 8192 + t0 * 4);
        const f32x4 y1 = *(const LAS f32x4*)(lds + CV_Y + chl * 8192 + t0 * 4 + 16);
        const f16x8 u = *(const LAS f16x8*)(lds + CV_U + chl * CV_USTR + 160 + t0 * 2);
        const int slot = (lane & 7) == 0 ? (t0 >> 6) : 0;
        y0 += *(const LAS f32x4*)(lds + CV_C + chl * 528 + slot * 16);
        u32x4 o;
#pragma unroll
        for (int j = 0; j < 4; ++j) {
          const float ya = j < 2 ? y0[2 * j] : y1[2 * j - 4], yb = j < 2 ? y0[2 * j + 1] : y1[2 * j - 3];
          const unsigned yq = pk_bf16(ya * kscale, yb * kscale);
          const unsigned uq = pk_bf16((float)u[2 * j] * dskip, (float)u[2 * j + 1] * dskip);
          o[j] = pk_bf16(lo_f(yq) + lo_f(uq), hi_f(yq) + hi_f(uq));
        }
        st128c(o, (unsigned)(t0 * 2), r_s, (unsigned)((d0 + chl) * 4096));
      }
    }
    STAMP(10);
    publish4(lds, 2, ws, FL_S + (unsigned)((lane * 8 + (cp >> 5)) * 256 + (cp & 31) * 4), 32, tag, lane);
    STAMP(11);
    ld128(xv, xo, r_x, (unsigned)(tt * 64 * DM * 2 + dtile * 128));
    poll_group(wv, ldsb + SY_PL + 12, (lane & 7) * 16u, r_fl, FL_S + (unsigned)((tt * 8 + dtile) * 256), tag);
#pragma unroll
    for (int j = 0; j < 8; ++j) ld32c(sv[j], so, r_s, (unsigned)((dtile * 64 + j) * 4096 + tt * 128));
    STAMP(12);
    bar_lgkm();
  }
  bar_lgkm();
  wait_vm<0>();
  PIN(xv);
#pragma unroll
  for (int j = 0; j < 8; ++j) PIN(sv[j]);
  {
    const LAS char* rp = lds + (tid >> 3) * G2_RED + (tid & 7) * 32;
    const bool odd = (tid >> 3) & 1;
    u32x4 o;
#pragma unroll
    for (int i = 0; i < 2; ++i) {
      f32x4 v = *(const LAS f32x4*)(rp + i * 16);
#pragma unroll
      for (int w = 1; w < 4; ++w) v += *(const LAS f32x4*)(rp + w * (64 * G2_RED) + i * 16);
#pragma unroll
      for (int jj = 0; jj < 2; ++jj) {
        const unsigned fp = pk_bf16(v[2 * jj], v[2 * jj + 1]);
        const float f0 = lo_f(fp), f1 = hi_f(fp);
        const float g0 = f0 * __builtin_amdgcn_rcpf(1.0f + __builtin_amdgcn_exp2f(f0 * -1.4426950408889634f));
        const float g1 = f1 * __builtin_amdgcn_rcpf(1.0f + __builtin_amdgcn_exp2f(f1 * -1.4426950408889634f));
        const unsigned gp = pk_bf16(g0, g1);
        const int j = 4 * i + 2 * jj;
        const float s0 = odd ? hi_f(sv[j]) : lo_f(sv[j]), s1 = odd ? hi_f(sv[j + 1]) : lo_f(sv[j + 1]);
        const unsigned sg = pk_bf16(s0 * lo_f(gp), s1 * hi_f(gp));
        const unsigned xw = xv[2 * i + jj];
        o[2 * i + jj] = pk_bf16(lo_f(xw) + lo_f(sg), hi_f(xw) + hi_f(sg));
      }
    }
    *(u32x4*)(out + (size_t)(tt * 64 + (tid >> 3)) * DM + dtile * 64 + (tid & 7) * 8) = o;
  }
  STAMP(13); STAMP_RT(15);
  STAMP_OUT(0);
}

extern "C" void kernel_launch(void* const* d_in, const int* in_sizes, int n_in, void* d_out,
                              int out_size, void* d_ws, size_t ws_size, hipStream_t stream) {
  (void)hipFuncSetAttribute((const void*)fused_kernel, hipFuncAttributeMaxDynamicSharedMemorySize, LDS_BYTES);
  fused_kernel<<<256, 512, LDS_BYTES, stream>>>(
      (const unsigned short*)d_in[0], (const unsigned short*)d_in[1], (const unsigned short*)d_in[2],
      (const unsigned short*)d_in[3], (const unsigned short*)d_in[4], (const unsigned short*)d_in[5],
      (const unsigned short*)d_in[6], (const unsigned*)d_in[7], (const unsigned*)d_in[8],
      (const unsigned short*)d_in[10], (const unsigned short*)d_in[11], (const unsigned short*)d_in[12],
      (char*)d_ws, (unsigned short*)d_out);
}
```

```cpp
#include <hip/hip_runtime.h>
#include <hip/hip_bf16.h>
#include <stdint.h>
#include <type_traits>

typedef __attribute__((ext_vector_type(4))) short s16x4;
typedef __attribute__((ext_vector_type(8))) short s16x8;
typedef __attribute__((ext_vector_type(8))) _Float16 f16x8;
typedef __attribute__((ext_vector_type(8))) __bf16 bf16x8;
typedef __attribute__((ext_vector_type(2))) float f32x2;
typedef __attribute__((ext_vector_type(4))) float f32x4;
typedef __attribute__((ext_vector_type(16))) float f32x16;
typedef __attribute__((ext_vector_type(4))) unsigned u32x4;
typedef __attribute__((ext_vector_type(3))) unsigned u32x3;
typedef __attribute__((ext_vector_type(2))) unsigned u32x2;
typedef __attribute__((ext_vector_type(2))) __bf16 bf16x2;
typedef __attribute__((ext_vector_type(2))) _Float16 f16x2;
#define LAS __attribute__((address_space(3)))
#define GAS __attribute__((address_space(1)))
typedef __amdgpu_buffer_rsrc_t rsrc_t;

constexpr int SL = 2048;
constexpr int DM = 512;
constexpr int NM = 128;
constexpr int FF = 1792;

constexpr size_t WS_XNF = 0;
constexpr size_t WS_ST = 2 * (size_t)SL * DM * 2;
constexpr size_t WS_HF = WS_ST + (size_t)SL * DM * 2;
constexpr size_t WS_FLAG = 16u << 20;
constexpr unsigned FL_ALL = 0, FL_STAT = 65536, FL_H = 131072, FL_S = 196608, FL_EP = 262144, FL_BYTES = 262400;

template <int I, int N, class F>
__device__ __forceinline__ void static_for(F&& f) {
  if constexpr (I < N) { f(std::integral_constant<int, I>{}); static_for<I + 1, N>(f); }
}

__device__ __forceinline__ unsigned pk_bf16(float lo, float hi) {
  f32x2 v = {lo, hi};
  return __builtin_bit_cast(unsigned, __builtin_convertvector(v, bf16x2));
}
__device__ __forceinline__ unsigned pk_f16(float lo, float hi) {
  f32x2 v = {lo, hi};
  return __builtin_bit_cast(unsigned, __builtin_convertvector(v, f16x2));
}
__device__ __forceinline__ float lo_f(unsigned p) { return __uint_as_float(p << 16); }
__device__ __forceinline__ float hi_f(unsigned p) { return __uint_as_float(p & 0xffff0000u); }
__device__ __forceinline__ float rbf(float x) { return lo_f(pk_bf16(x, x)); }

template <int CTRL>
__device__ __forceinline__ float dpp_mov(float v) {
  return __int_as_float(__builtin_amdgcn_update_dpp(0, __float_as_int(v), CTRL, 0xf, 0xf, true));
}
__device__ __forceinline__ float shr_carry(float c, float v) {
  return __builtin_bit_cast(float, __builtin_amdgcn_update_dpp(__builtin_bit_cast(int, c), __builtin_bit_cast(int, v), 0x138, 0xf, 0xf, false));
}
__device__ __forceinline__ float rdlane(float v, int l) {
  return __int_as_float(__builtin_amdgcn_readlane(__float_as_int(v), l));
}
__device__ __forceinline__ float wave_sum(float v) {
  v += dpp_mov<0xB1>(v);
  v += dpp_mov<0x4E>(v);
  v += dpp_mov<0x141>(v);
  v += dpp_mov<0x140>(v);
  return (rdlane(v, 0) + rdlane(v, 16)) + (rdlane(v, 32) + rdlane(v, 48));
}
__device__ __forceinline__ float row_sum(float v) {
  v += dpp_mov<0xB1>(v);
  v += dpp_mov<0x4E>(v);
  v += dpp_mov<0x141>(v);
  v += dpp_mov<0x140>(v);
  return v;
}
__device__ __forceinline__ float half_sum(float v, int lane) {
  v += dpp_mov<0xB1>(v);
  v += dpp_mov<0x4E>(v);
  v += dpp_mov<0x141>(v);
  v += dpp_mov<0x140>(v);
  const float lo = rdlane(v, 0) + rdlane(v, 16), hi = rdlane(v, 32) + rdlane(v, 48);
  return lane < 32 ? lo : hi;
}
__device__ __forceinline__ float wave_max(float v) {
  v = fmaxf(v, dpp_mov<0xB1>(v));
  v = fmaxf(v, dpp_mov<0x4E>(v));
  v = fmaxf(v, dpp_mov<0x141>(v));
  v = fmaxf(v, dpp_mov<0x140>(v));
  return fmaxf(fmaxf(rdlane(v, 0), rdlane(v, 16)), fmaxf(rdlane(v, 32), rdlane(v, 48)));
}

__device__ __forceinline__ rsrc_t mk_rsrc(const void* p, unsigned bytes) {
  return __builtin_amdgcn_make_buffer_rsrc((void*)p, (short)0, (int)bytes, 0x00020000);
}
__device__ __forceinline__ void dma16(unsigned lds_dst, unsigned voff, rsrc_t r, unsigned soff) {
  asm volatile("s_mov_b32 m0, %0\n\ts_nop 0\n\tbuffer_load_dwordx4 %1, %2, %3 offen lds"
               :: "s"(lds_dst), "v"(voff), "s"(r), "s"(soff) : "memory");
}
__device__ __forceinline__ void dma16x4(unsigned lds_dst, unsigned v0, unsigned v1, unsigned v2, unsigned v3, rsrc_t r, unsigned soff) {
  asm volatile("s_mov_b32 m0, %0\n\ts_nop 0\n\t"
               "buffer_load_dwordx4 %1, %5, %6 offen lds\n\t"
               "buffer_load_dwordx4 %2, %5, %6 offen offset:1024 lds\n\t"
               "buffer_load_dwordx4 %3, %5, %6 offen offset:2048 lds\n\t"
               "buffer_load_dwordx4 %4, %5, %6 offen offset:3072 lds"
               :: "s"(lds_dst), "v"(v0), "v"(v1), "v"(v2), "v"(v3), "s"(r), "s"(soff) : "memory");
}
__device__ __forceinline__ void dma16x3(unsigned lds_dst, unsigned v0, unsigned v1, unsigned v2, rsrc_t r, unsigned soff) {
  asm volatile("s_mov_b32 m0, %0\n\ts_nop 0\n\t"
               "buffer_load_dwordx4 %1, %4, %5 offen lds\n\t"
               "buffer_load_dwordx4 %2, %4, %5 offen offset:1024 lds\n\t"
               "buffer_load_dwordx4 %3, %4, %5 offen offset:2048 lds"
               :: "s"(lds_dst), "v"(v0), "v"(v1), "v"(v2), "s"(r), "s"(soff) : "memory");
}
__device__ __forceinline__ void ld128(u32x4& dst, unsigned voff, rsrc_t r, unsigned soff) {
  asm volatile("buffer_load_dwordx4 %0, %1, %2, %3 offen" : "=v"(dst) : "v"(voff), "s"(r), "s"(soff) : "memory");
}
__device__ __forceinline__ void ld128c(u32x4& dst, unsigned voff, rsrc_t r, unsigned soff) {
  asm volatile("buffer_load_dwordx4 %0, %1, %2, %3 offen sc1" : "=v"(dst) : "v"(voff), "s"(r), "s"(soff) : "memory");
}
__device__ __forceinline__ void ld32(unsigned& dst, unsigned voff, rsrc_t r, unsigned soff) {
  asm volatile("buffer_load_dword %0, %1, %2, %3 offen" : "=v"(dst) : "v"(voff), "s"(r), "s"(soff) : "memory");
}
__device__ __forceinline__ void ld32c(unsigned& dst, unsigned voff, rsrc_t r, unsigned soff) {
  asm volatile("buffer_load_dword %0, %1, %2, %3 offen sc1" : "=v"(dst) : "v"(voff), "s"(r), "s"(soff) : "memory");
}
__device__ __forceinline__ void st32c(unsigned v, unsigned voff, rsrc_t r, unsigned soff) {
  asm volatile("buffer_store_dword %0, %1, %2, %3 offen sc1" :: "v"(v), "v"(voff), "s"(r), "s"(soff) : "memory");
}
__device__ __forceinline__ void st64c(u32x2 v, unsigned voff, rsrc_t r, unsigned soff) {
  asm volatile("buffer_store_dwordx2 %0, %1, %2, %3 offen sc1\n\ts_nop 1" :: "v"(v), "v"(voff), "s"(r), "s"(soff) : "memory");
}
__device__ __forceinline__ void ld16(unsigned& dst, unsigned voff, rsrc_t r, unsigned soff) {
  asm volatile("buffer_load_ushort %0, %1, %2, %3 offen" : "=v"(dst) : "v"(voff), "s"(r), "s"(soff) : "memory");
}
__device__ __forceinline__ void st128c(u32x4 v, unsigned voff, rsrc_t r, unsigned soff) {
  asm volatile("buffer_store_dwordx4 %0, %1, %2, %3 offen sc1\n\ts_nop 1" :: "v"(v), "v"(voff), "s"(r), "s"(soff) : "memory");
}
template <int N>
__device__ __forceinline__ void wait_vm() { asm volatile("s_waitcnt vmcnt(%0)" :: "n"(N > 63 ? 63 : N) : "memory"); }
__device__ __forceinline__ void bar_lgkm() { asm volatile("s_waitcnt lgkmcnt(0)\n\ts_barrier" ::: "memory"); }
__device__ __forceinline__ void bar_only() { asm volatile("s_barrier" ::: "memory"); }
#define BW_DECL
#define BARX(f) f()
#define BW_OUT()
#define PIN(x) asm volatile("" : "+v"(x))
#define SCHED_FENCE() __builtin_amdgcn_sched_barrier(0)

__device__ __forceinline__ s16x4 trrd(const LAS char* p) {
  return __builtin_amdgcn_ds_read_tr16_b64_v4i16((LAS s16x4*)p);
}
__device__ __forceinline__ s16x8 cat8(s16x4 a, s16x4 b) {
  return __builtin_shufflevector(a, b, 0, 1, 2, 3, 4, 5, 6, 7);
}

__device__ __forceinline__ bool wave_all(bool p) { return __builtin_amdgcn_ballot_w64(!p) == 0ull; }
template <int SLP = 2>
__device__ __forceinline__ void poll_flags(unsigned voff, rsrc_t r, unsigned soff, unsigned tag) {
#pragma unroll 1
  for (int it = 0; it < (1 << 18); ++it) {
    u32x4 v;
    asm volatile("buffer_load_dwordx4 %0, %1, %2, %3 offen sc1\n\ts_waitcnt vmcnt(0)"
                 : "=&v"(v) : "v"(voff), "s"(r), "s"(soff) : "memory");
    const bool ok = (v[0] == tag) & (v[1] == tag) & (v[2] == tag) & (v[3] == tag);
    if (wave_all(ok)) break;
    __builtin_amdgcn_s_sleep(SLP);
  }
}
__device__ __forceinline__ void set_flag(char* ws, unsigned off, unsigned tag) {
  __hip_atomic_store((GAS unsigned*)(ws + WS_FLAG + off), tag, __ATOMIC_RELAXED, __HIP_MEMORY_SCOPE_AGENT);
}

#define STAMP_DECL
#define STAMP(i)
#define STAMP_RT(i)
#define STAMP_OUT(kid)

__device__ __forceinline__ f32x2 cmul(f32x2 a, f32x2 b) {
  f32x2 r;
  r.x = fmaf(a.x, b.x, -(a.y * b.y));
  r.y = fmaf(a.x, b.y, a.y * b.x);
  return r;
}
__device__ __forceinline__ f32x2 cexp_l(float a, float b, float l) {
  const float mag = __builtin_amdgcn_exp2f(a * l * 1.4426950408889634f);
  const float x = b * l;
  const float C1 = 0.15915494309189535f;
  const float C2 = (float)(0.15915494309189533577 - (double)0.15915494309189535f);
  const float hi = x * C1;
  const float lo = fmaf(x, C1, -hi) + x * C2;
  const float r = (hi - __builtin_rintf(hi)) + lo;
  f32x2 z;
  z.x = mag * __builtin_amdgcn_cosf(r);
  z.y = mag * __builtin_amdgcn_sinf(r);
  return z;
}

constexpr int W_UPOFF = 14336 + 128;
constexpr int XOFF = W_UPOFF + 14336;
constexpr int STG = XOFF + 16384;
constexpr int KGS = 3 * STG;
constexpr int KF = KGS + 8192;
constexpr int CV_KSTR = (48 + 2048) * 2;
constexpr int CV_USTR = (80 + 2048) * 2;
constexpr int CV_U = KF + 2 * CV_KSTR;
constexpr int CSUM = CV_U + 2 * CV_USTR;
constexpr int STAT = CSUM + 896;
constexpr int SY = STAT + 512;
constexpr int SY_LD = SY, SY_MC = SY + 16, SY_ST = SY + 4, SY_HR = SY + 8, SY_SR = SY + 12, SY_UR = SY + 32  ,
              SY_CNT = SY + 48, SY_VAL = SY + 64, SY_KG = SY + 80, SY_END = SY + 96;
constexpr int LDS_BYTES = SY_END;
static_assert(LDS_BYTES <= 163840, "lds");
constexpr int G2_RED = 272;
constexpr int EPX = KGS;
constexpr int EPS = KF;
constexpr int CV_NP = 13;
constexpr int G2_NR = 13;
constexpr int cmax(int a, int b) { return a > b ? a : b; }
constexpr int cmin(int a, int b) { return a < b ? a : b; }
constexpr int g2_base(int i) { return (((i % 15) / 5 + 2) % 3) * STG + (i % 5) * 8192; }
constexpr int G2_H0 = 6;
constexpr int g2_issued(int i) { return cmin(28, G2_H0 + (3 * i) / 2); }

__device__ __forceinline__ void lds_setv(unsigned addr, unsigned v) {
  asm volatile("ds_write_b32 %0, %1" :: "v"(addr), "v"(v) : "memory");
}
__device__ __forceinline__ unsigned lds_min4(unsigned addr) {
  u32x4 v;
  asm volatile("ds_read_b128 %0, %1\n\ts_waitcnt lgkmcnt(0)" : "=&v"(v) : "v"(addr) : "memory");
  return (unsigned)__builtin_amdgcn_readfirstlane(min(min(v[0], v[1]), min(v[2], v[3])));
}
__device__ __forceinline__ unsigned lds_get1(unsigned addr) {
  unsigned v;
  asm volatile("ds_read_b32 %0, %1\n\ts_waitcnt lgkmcnt(0)" : "=&v"(v) : "v"(addr) : "memory");
  return (unsigned)__builtin_amdgcn_readfirstlane(v);
}
template <int W>
__device__ __forceinline__ unsigned lds_val(unsigned addr) { return W == 4 ? lds_min4(addr) : lds_get1(addr); }
template <int W>
__device__ __forceinline__ void lds_spin(unsigned addr, unsigned need) {
#pragma unroll 1
  for (int it = 0; it < (1 << 20); ++it) {
    if (lds_val<W>(addr) >= need) break;
    __builtin_amdgcn_s_sleep(1);
  }
}
template <int W>
__device__ __forceinline__ void lds_wait(unsigned addr, unsigned need) {
  if (__builtin_expect(lds_val<W>(addr) < need, 0)) lds_spin<W>(addr, need);
}
template <int NW, bool DRAIN = true>
__device__ __forceinline__ void publish(LAS char* lds, int cnt, char* ws, unsigned off, int nl, unsigned tag, int lane) {
  if (DRAIN) wait_vm<0>();
  unsigned old = 0;
  if (lane == 0) old = __hip_atomic_fetch_add((LAS unsigned*)(lds + SY_CNT + cnt * 4), 1u, __ATOMIC_RELAXED, __HIP_MEMORY_SCOPE_WORKGROUP);
  if (__builtin_amdgcn_readfirstlane(old) == NW - 1 && lane < nl) set_flag(ws, off, tag);
}

#if defined(__HIP_DEVICE_COMPILE__)
__attribute__((target("no-packed-fp32-ops")))
#endif
__global__ __launch_bounds__(512, 2) void fused_kernel(
    const unsigned short* __restrict__ x, const unsigned short* __restrict__ A_re,
    const unsigned short* __restrict__ A_im, const unsigned short* __restrict__ B_re,
    const unsigned short* __restrict__ B_im, const unsigned short* __restrict__ C_re,
    const unsigned short* __restrict__ C_im, const unsigned* __restrict__ log_dt2,
    const unsigned* __restrict__ dsk2p, const unsigned short* __restrict__ wg,
    const unsigned short* __restrict__ wu, const unsigned short* __restrict__ wd,
    char* ws, unsigned short* __restrict__ out) {
  extern __shared__ __attribute__((aligned(16))) char smem[];
  LAS char* lds = (LAS char*)smem;
  const unsigned ldsb = (unsigned)(uintptr_t)lds;
  const int tid = threadIdx.x, lane = tid & 63;
  const int w8 = __builtin_amdgcn_readfirstlane(tid >> 6);
  const int wv = w8 & 3;
  STAMP_DECL; BW_DECL; STAMP_RT(14); STAMP(0);
  const int bid = blockIdx.x;
  const int xcd = bid & 7, bi = bid >> 3;
  const int mt = (xcd >> 2) * 8 + (bi >> 2);
  const int ft = (xcd & 3) * 4 + (bi & 3);
  const int rot = ((bi >> 2) + 2 * (bi & 3)) & 7;
  const int cp = xcd * 32 + bi;
  const int tt = xcd * 4 + (bi >> 3);
  const int dtile = bi & 7;
  const int rot2 = ((bi >> 3) * 7 + dtile * 3) % 28;

  const rsrc_t r_x = mk_rsrc(x, SL * DM * 2);
  const rsrc_t r_s = mk_rsrc(ws + WS_ST, SL * DM * 2);
  const rsrc_t r_fl = mk_rsrc(ws + WS_FLAG, FL_BYTES);
#define G2_KS(w, i) ((unsigned)((w) * 28 + (((i) + rot2) >= 28 ? (i) + rot2 - 28 : (i) + rot2)))
#define XO(t_) ((unsigned)((tt * 64 + ((t_) >> 3)) * (DM * 2) + dtile * 128 + ((t_) & 7) * 16))
#define EPI_X() do { int l_ = lane; PIN(l_); u32x4 xw_[2]; \
    _Pragma("unroll") for (int j = 0; j < 2; ++j) ld128(xw_[j], XO(wv * 128 + j * 64 + l_), r_x, 0u); \
    lds_wait<4>(ldsb + SY_UR, 1u); wait_vm<0>(); \
    _Pragma("unroll") for (int j = 0; j < 2; ++j) { PIN(xw_[j]); *(LAS u32x4*)(lds + EPX + (wv * 128 + j * 64 + l_) * 16) = xw_[j]; } } while (0)
#define EPI_S() do { int l_ = lane; PIN(l_); u32x4 sw_[4]; \
    const unsigned so = (unsigned)((dtile * 64 + wv * 16 + (l_ >> 4)) * 4096 + (tt >> 1) * 256 + (l_ & 15) * 16); \
    _Pragma("unroll") for (int j = 0; j < 4; ++j) ld128c(sw_[j], so, r_s, (unsigned)(j * 16384)); \
    wait_vm<0>(); \
    _Pragma("unroll") for (int j = 0; j < 4; ++j) { PIN(sw_[j]); \
      *(LAS u32x4*)(lds + EPS + (wv * 16 + j * 4 + (l_ >> 4)) * 256 + ((l_ + wv * 2 + (j >> 1)) & 15) * 16) = sw_[j]; } } while (0)

  if (w8 < 4) {
    const rsrc_t r_h = mk_rsrc(ws + WS_HF, SL * FF * 2);
    {
    const int hc = wv & 1, m = wv >> 1;
    const int dch = cp * 2 + hc;
    unsigned prm[6];
    {
      const unsigned po = (unsigned)(dch * NM * 2);
      ld32(prm[0], lane * 4u, mk_rsrc(A_re, DM * NM * 2), po);
      ld32(prm[1], lane * 4u, mk_rsrc(A_im, DM * NM * 2), po);
      ld32(prm[2], lane * 4u, mk_rsrc(B_re, DM * NM * 2), po);
      ld32(prm[3], lane * 4u, mk_rsrc(B_im, DM * NM * 2), po);
      ld32(prm[4], lane * 4u, mk_rsrc(C_re, DM * NM * 2), po);
      ld32(prm[5], lane * 4u, mk_rsrc(C_im, DM * NM * 2), po);
    }
    if (tid < 5) *(LAS u32x4*)(lds + SY + tid * 16) = (u32x4){0u, 0u, 0u, tid == 2 ? 1u : 0u};
    bar_lgkm();
    __builtin_amdgcn_s_setprio(2);
    const unsigned ldt2 = log_dt2[cp];
    {
      const float ldt = hc ? hi_f(ldt2) : lo_f(ldt2);
      const float L2E = 1.4426950408889634f, L2E_LO = (float)(1.4426950408889634074 - (double)1.4426950408889634f);
      const float th = ldt * L2E;
      const float tl = fmaf(ldt, L2E, -th) + ldt * L2E_LO;
      const float e0 = __builtin_amdgcn_exp2f(th);
      const float dt = rbf(fmaf(e0 * 0.6931471805599453f, tl, e0));
      wait_vm<0>();
#pragma unroll
      for (int i = 0; i < 6; ++i) PIN(prm[i]);
      float av[2], bv[2];
      f32x2 wq[2];
#pragma unroll
      for (int m = 0; m < 2; ++m) {
        av[m] = dt * (m ? hi_f(prm[0]) : lo_f(prm[0]));
        bv[m] = dt * (m ? hi_f(prm[1]) : lo_f(prm[1]));
        const f32x2 db = {dt * (m ? hi_f(prm[2]) : lo_f(prm[2])), dt * (m ? hi_f(prm[3]) : lo_f(prm[3]))};
        const f32x2 cc = {m ? hi_f(prm[4]) : lo_f(prm[4]), m ? hi_f(prm[5]) : lo_f(prm[5])};
        wq[m] = cmul(cc, db);
      }
      const float amax = fmaxf(wave_max(fmaxf(av[0], av[1])), 0.f);
      const float g2 = amax * L2E;
      const float wm = wave_max(fmaxf(fmaxf(fabsf(wq[0].x), fabsf(wq[0].y)), fmaxf(fabsf(wq[1].x), fabsf(wq[1].y))));
      int we = __builtin_amdgcn_frexp_expf(wm);
      we = we < -100 ? -100 : we;
      const float wsc = __builtin_amdgcn_ldexpf(1.0f, -we);
      f32x4 kacc[3];
#pragma unroll
      for (int t = 0; t < 3; ++t) kacc[t] = (f32x4){0.f, 0.f, 0.f, 0.f};
      const int kgs = (m ? CV_U : KGS) + hc * 4096;
      LAS char* tw[4];
#pragma unroll
      for (int c = 0; c < 4; ++c) tw[c] = lds + kgs + lane * 64 + ((c ^ ((lane >> 1) & 3)) << 4);
      const LAS char* tr[2];
      {
        const int G = lane >> 4, q4 = (lane & 15) >> 2, p = lane & 3;
#pragma unroll
        for (int t = 0; t < 2; ++t)
          tr[t] = lds + kgs + (4 * G + 2 * t + (q4 >> 1)) * 64 + ((((q4 & 1) * 2 + (p >> 1)) ^ ((2 * G + t) & 3)) << 4) + (p & 1) * 8;
      }
      auto put16 = [&](const f32x2 (&e)[16]) __attribute__((always_inline)) {
#pragma unroll
        for (int h = 0; h < 2; ++h) {
          u32x4 re, im;
#pragma unroll
          for (int j = 0; j < 4; ++j) {
            re[j] = pk_f16(e[8 * h + 2 * j].x, e[8 * h + 2 * j + 1].x);
            im[j] = pk_f16(e[8 * h + 2 * j].y, e[8 * h + 2 * j + 1].y);
          }
          *(LAS u32x4*)tw[h] = re;
          *(LAS u32x4*)tw[2 + h] = im;
        }
      };
#define KG_FRAG(s) __builtin_bit_cast(f16x8, cat8(trrd(tr[0] + (s) * 1024), trrd(tr[1] + (s) * 1024)))
#define KG_MMA(t) do { _Pragma("unroll") for (int s = 0; s < 4; ++s) \
        kacc[t] = __builtin_amdgcn_mfma_f32_16x16x32_f16(KG_FRAG(s), qf[s], kacc[t], 0, 0, 0); } while (0)
      {
        const float a = (m ? av[1] : av[0]) - amax, b = m ? bv[1] : bv[0];
        f32x2 w = m ? wq[1] : wq[0];
        w.x *= wsc; w.y *= -wsc;
        f32x2 e[16];
        {
          const f32x2 z16 = cexp_l(a, b, 16.f);
          e[0] = (f32x2){1.f, 0.f}; e[1] = z16; e[2] = cmul(z16, z16); e[3] = cmul(e[2], z16);
          const f32x2 z64 = cmul(e[2], e[2]);
#pragma unroll
          for (int c = 4; c < 16; ++c) e[c] = cmul(e[c - 4], z64);
          put16(e);
        }
        f16x8 qf[4];
#pragma unroll
        for (int s = 0; s < 4; ++s) qf[s] = KG_FRAG(s);
        const f32x2 z1 = cexp_l(a, -b, 1.f);
        const f32x2 z2 = cmul(z1, z1), z4 = cmul(z2, z2);
        e[0] = w; e[1] = cmul(w, z1); e[2] = cmul(w, z2); e[3] = cmul(e[1], z2);
#pragma unroll
        for (int c = 4; c < 16; ++c) e[c] = cmul(e[c - 4], z4);
        put16(e);
        KG_MMA(0);
        const f32x2 z256 = cexp_l(a, -b, 256.f);
        const f32x2 z512 = cmul(z256, z256);
        const f32x2 w2 = cmul(w, z512);
        e[0] = cmul(w, z256); e[1] = cmul(e[0], z2); e[8] = w2; e[12] = cmul(e[0], z512);
        const f32x2 w3 = e[12];
#pragma unroll
        for (int i = 1; i < 4; ++i) {
          e[2 * i] = cmul(e[2 * i - 2], z4); e[2 * i + 1] = cmul(e[2 * i - 1], z4);
          e[8 + i] = cmul(e[7 + i], z4); e[12 + i] = cmul(e[11 + i], z4);
        }
        put16(e);
        KG_MMA(1);
        const f32x2 z8 = cmul(z4, z4);
        e[0] = cmul(w2, z512); e[2] = cmul(w3, z512); e[4] = cmul(e[0], z512); e[6] = cmul(e[2], z512);
        e[8] = cmul(e[4], z512);
#pragma unroll
        for (int i = 0; i < 4; ++i) e[2 * i + 1] = cmul(e[2 * i], z8);
#pragma unroll
        for (int i = 9; i < 16; ++i) e[i] = (f32x2){0.f, 0.f};
        put16(e);
        KG_MMA(2);
      }
#pragma unroll
      for (int t = 0; t < 3; ++t) *(LAS f32x4*)(lds + (m ? KF + hc * CV_KSTR : KGS + hc * 4096) + t * 1024 + lane * 16) = kacc[t];
      if (m) *(LAS f32x2*)(lds + SY_KG + hc * 8) = (f32x2){g2, (float)we};
    }
    }
    f32x16 acc[7];
    f32x4 csa[7];
    {
      const int G = lane >> 4;
      const LAS char* wa0 = lds + (G & 1) * W_UPOFF + (8 * (G >> 1) + ((lane & 15) >> 2)) * 224 + (lane & 3) * 8;
      const LAS char* wa[7];
#pragma unroll
      for (int T = 0; T < 7; ++T) wa[T] = wa0 + 32 * ((T + 2 * wv) % 7);
      const LAS char* xa[4];
      {
        const int row = 32 * wv + (lane & 31), sw = (row >> 1) & 7;
#pragma unroll
        for (int ks = 0; ks < 4; ++ks) xa[ks] = lds + XOFF + row * 128 + (((2 * ks + (lane >> 5)) ^ sw) << 4);
      }
      constexpr int PD = 3;
      bf16x8 Af[224], Bf[32];
      const unsigned o1 = ((lane & 15) == 4 * ((lane >> 4) & 1)) ? 0x3f803f80u : 0u;
      u32x4 onesq = {o1, o1, o1, o1};
      PIN(onesq);
      const bf16x8 ones = __builtin_bit_cast(bf16x8, onesq);
#define A_READ(i, T) __builtin_bit_cast(bf16x8, cat8(trrd(wa[T] + (((i) >> 2) % 3) * STG + (16 * ((i) & 3)) * 224), \
                                                      trrd(wa[T] + (((i) >> 2) % 3) * STG + (16 * ((i) & 3) + 4) * 224)))
#define B_READ(i) (*(const LAS bf16x8*)(xa[(i) & 3] + (((i) >> 2) % 3) * STG))
#pragma unroll
      for (int T = 0; T < 7; ++T) { acc[T] = (f32x16){}; csa[T] = (f32x4){0.f, 0.f, 0.f, 0.f}; }
      BARX(bar_lgkm);
      STAMP(1);
      Bf[0] = B_READ(0);
#pragma unroll
      for (int T = 0; T < PD; ++T) Af[T] = A_READ(0, T);
      static_for<0, 224>([&](auto nc) {
        constexpr int n = decltype(nc)::value, i = n / 7, T = n % 7;
        constexpr int m = n + PD, im = m / 7, Tm = m % 7;
        if constexpr (n == 27 * 7 + 2) {
          if (lane < 32) {
#pragma unroll
            for (int j = 0; j < 2; ++j) *(LAS float*)(lds + CSUM + ((j + 2 * wv) % 7) * 128 + lane * 4) = csa[j][0];
          }
          csa[0] = (f32x4){0.f, 0.f, 0.f, 0.f}; csa[1] = csa[0];
        }
        if constexpr (Tm == 0 && (im & 3) == 0) BARX(bar_lgkm);
        if constexpr (m < 224) {
          if constexpr (Tm == 0) Bf[im] = B_READ(im);
          Af[m] = A_READ(im, Tm);
        }
        acc[T] = __builtin_amdgcn_mfma_f32_32x32x16_bf16(Af[n], Bf[i], acc[T], 0, 0, 0);
        if constexpr (T < 2 || i >= 28)
          csa[T] = __builtin_amdgcn_mfma_f32_16x16x32_bf16(ones, Af[n], csa[T], 0, 0, 0);
        SCHED_FENCE();
      });
    }
    STAMP(5);
    lds_wait<1>(ldsb + SY_ST, 1u);
    const unsigned stw = *(const LAS unsigned*)(lds + STAT + (32 * wv + (lane & 31)) * 4);
    const unsigned tag = lds_get1(ldsb + SY_VAL);
    {
      const unsigned nm = (stw & 0xffffu) ^ 0x8000u;
      u32x4 bq = {nm | (nm << 16), nm, 0u, 0u};
      if (lane >= 32) bq = (u32x4){0u, 0u, 0u, 0u};
#pragma unroll
      for (int T = 0; T < 7; ++T) {
        const float c = csa[T][0] + *(const LAS float*)(lds + CSUM + ((T + 2 * wv) % 7) * 128 + (lane & 31) * 4);
        const float ch = rbf(c), cm = rbf(c - ch), cl = (c - ch) - cm;
        const u32x4 aq = {pk_bf16(ch, cm), pk_bf16(cl, 0.f), 0u, 0u};
        acc[T] = __builtin_amdgcn_mfma_f32_32x32x16_bf16(__builtin_bit_cast(bf16x8, aq), __builtin_bit_cast(bf16x8, bq), acc[T], 0, 0, 0);
      }
    }
    STAMP(6);
    {
      const float sd = hi_f(stw);
      const float ga = -1.4426950408889634f * __builtin_amdgcn_rcpf(sd);
      const unsigned hsb = (unsigned)(((mt * 4 + wv) * 112 + ft * 7) * 1024);
#pragma unroll
      for (int T = 0; T < 7; ++T) {
        u32x4 o;
#pragma unroll
        for (int j = 0; j < 4; ++j) {
          const float s0 = __builtin_amdgcn_rcpf(fmaf(__builtin_amdgcn_exp2f(acc[T][2 * j] * ga), sd, sd));
          const float s1 = __builtin_amdgcn_rcpf(fmaf(__builtin_amdgcn_exp2f(acc[T][2 * j + 1] * ga), sd, sd));
          o[j] = pk_bf16(s0 * acc[T][8 + 2 * j], s1 * acc[T][8 + 2 * j + 1]);
        }
        st128c(o, lane * 16u, r_h, hsb + (unsigned)((T + 2 * wv) % 7) * 1024u);
      }
    }
    STAMP(7);
    const unsigned ldw = ldsb + SY_LD;
    const LAS char* waT[2];
    {
      const int G = lane >> 4, q4 = (lane & 15) >> 2;
      const LAS char* wa0 = lds + wv * 2048 + (4 * (G >> 1) + q4) * 128 + (G & 1) * 32 + (lane & 3) * 8;
      waT[0] = wa0 + ((q4 >> 1) * 64); waT[1] = wa0 + (((q4 >> 1) ^ 1) * 64);
    }
#define D_READ(i, T) __builtin_bit_cast(bf16x8, cat8(trrd(waT[T] + g2_base(i)), trrd(waT[T] + g2_base(i) + 1024)))
    bf16x8 Wr[28][2];
    static_for<0, 5>([&](auto ic) { constexpr int i = decltype(ic)::value; Wr[i][0] = D_READ(i, 0); Wr[i][1] = D_READ(i, 1); });
    publish<4>(lds, 1, ws, FL_H + (unsigned)((mt * 16 + lane) * 256 + ft * 4), 16, tag, lane);
    lds_setv(ldsb + SY_MC + wv * 4, 1u);
    STAMP(8);
    lds_wait<1>(ldw, 2u);
    static_for<5, 10>([&](auto ic) { constexpr int i = decltype(ic)::value; Wr[i][0] = D_READ(i, 0); Wr[i][1] = D_READ(i, 1); });
    lds_setv(ldsb + SY_MC + wv * 4, 2u);
    if (wv < 2) {
      lds_wait<4>(ldsb + SY_UR, 1u);
      const float kscale = *(const LAS float*)(lds + SY_VAL + 4 + wv * 4);
      const unsigned dsk2 = dsk2p[cp];
      const float dskip = wv ? hi_f(dsk2) : lo_f(dsk2);
      const int G = lane >> 4, q4 = (lane & 15) >> 2, p = lane & 3;
      const LAS char* pa = lds + KF + wv * CV_KSTR + 96 + 2 * (76 + 16 * p - 32 * G - 4 * q4 - 16);
      const LAS char* pb = lds + CV_U + wv * CV_USTR + 160 + 2 * (-76 + 32 * G + 4 * q4 + 4 * p);
      const LAS char* pu = lds + CV_U + wv * CV_USTR + 160 + lane * 2;
      constexpr int J0 = 2 * CV_NP - 1;
      f16x8 A[16], B[32];
      f32x4 ca[32 - J0];
#define CV_A(q) __builtin_bit_cast(f16x8, cat8(trrd(pa + 256 * (q) + 32), trrd(pa + 256 * (q))))
#define CV_B(w) __builtin_bit_cast(f16x8, cat8(trrd(pb + 128 * (w)), trrd(pb + 128 * (w) + 32)))
      STAMP(2);
      B[0] = CV_B(0); B[1] = CV_B(1);
      constexpr int QA = (J0 - 1) / 2;
#pragma unroll
      for (int q = 15; q >= QA; --q) A[q] = CV_A(q);
#pragma unroll
      for (int j = 0; j < 32 - J0; ++j) ca[j] = (f32x4){0.f, 0.f, 0.f, 0.f};
      static_for<0, 32>([&](auto wc) {
        constexpr int w = decltype(wc)::value;
        if constexpr (w + 2 < 32) B[w + 2] = CV_B(w + 2);
        if constexpr ((w & 1) == 0 && QA - 1 - w / 2 >= 0) A[QA - 1 - w / 2] = CV_A(QA - 1 - w / 2);
#pragma unroll
        for (int q = 15; q >= 0; --q) {
          const int j = w + 2 * q;
          if (j >= J0 && j < 32) ca[j - J0] = __builtin_amdgcn_mfma_f32_16x16x32_f16(A[q], B[w], ca[j - J0], 0, 0, 0);
        }
        SCHED_FENCE();
      });
      STAMP(3);
      float c3 = 0.f, c2 = 0.f, c1 = 0.f, yv[32 - J0];
#pragma unroll
      for (int j = 0; j < 32 - J0; ++j) {
        const f32x4 a = ca[j];
        const float h3 = a[3];
        const float h2 = a[2] + shr_carry(c3, h3);
        const float h1 = a[1] + shr_carry(c2, h2);
        yv[j] = a[0] + shr_carry(c1, h1);
        c3 = dpp_mov<0x13C>(h3); c2 = dpp_mov<0x13C>(h2); c1 = dpp_mov<0x13C>(h1);
      }
#pragma unroll
      for (int jp = CV_NP; jp < 16; ++jp) {
        const float u0 = (float)*(const LAS _Float16*)(pu + jp * 256), u1 = (float)*(const LAS _Float16*)(pu + jp * 256 + 128);
        const unsigned yq = pk_bf16(yv[2 * jp - J0] * kscale, yv[2 * jp + 1 - J0] * kscale);
        const unsigned uq = pk_bf16(u0 * dskip, u1 * dskip);
        st32c(pk_bf16(lo_f(yq) + lo_f(uq), hi_f(yq) + hi_f(uq)), lane * 4u, r_s, (unsigned)((cp * 2 + wv) * 4096 + jp * 256));
      }
#undef CV_A
#undef CV_B
      STAMP(4);
    }
    lds_wait<1>(ldw, 3u);
    static_for<10, G2_NR>([&](auto ic) { constexpr int i = decltype(ic)::value; Wr[i][0] = D_READ(i, 0); Wr[i][1] = D_READ(i, 1); });
    lds_setv(ldsb + SY_MC + wv * 4, 3u);
    STAMP(9);
    __builtin_amdgcn_s_setprio(0);
    lds_wait<1>(ldsb + SY_HR, 1u);
    __builtin_amdgcn_s_setprio(2);
    STAMP(10);
    u32x4 hb[28][2];
#define HB_LOAD(i) do { ld128c(hb[i][0], lane * 16u, r_h, ((unsigned)(tt * 2) * 112u + G2_KS(wv, i)) * 1024u); \
                        ld128c(hb[i][1], lane * 16u, r_h, ((unsigned)(tt * 2 + 1) * 112u + G2_KS(wv, i)) * 1024u); } while (0)
#pragma unroll
    for (int i = 0; i < G2_H0; ++i) HB_LOAD(i);
    f32x16 acc2[2][2];
    {
#pragma unroll
      for (int a = 0; a < 2; ++a)
#pragma unroll
        for (int b = 0; b < 2; ++b) acc2[a][b] = (f32x16){};
      static_for<0, 28>([&](auto ic) {
        constexpr int i = decltype(ic)::value;
        if constexpr (i == 14) STAMP(11);
        if constexpr (i + 2 == 15) lds_wait<1>(ldw, 6u);
        if constexpr (i + 2 >= G2_NR && i + 2 < 28) { Wr[i + 2][0] = D_READ(i + 2, 0); Wr[i + 2][1] = D_READ(i + 2, 1); }
        wait_vm<2 * (g2_issued(i) - i - 1)>();
        PIN(hb[i][0]); PIN(hb[i][1]);
        if constexpr (i == 0) {
          if (wv < 2) publish<4, false>(lds, 2, ws, FL_S + (unsigned)((lane * 8 + (cp >> 5)) * 256 + (cp & 31) * 4), 32, tag, lane);
        }
#pragma unroll
        for (int T = 0; T < 2; ++T)
#pragma unroll
          for (int b = 0; b < 2; ++b)
            acc2[T][b] = __builtin_amdgcn_mfma_f32_32x32x16_bf16(Wr[i][T], __builtin_bit_cast(bf16x8, hb[i][b]), acc2[T][b], 0, 0, 0);
        SCHED_FENCE();
        static_for<g2_issued(i), g2_issued(i + 1)>([&](auto jc) { constexpr int j = decltype(jc)::value; HB_LOAD(j); });
      });
    }
    STAMP(12);
    bar_lgkm();
    {
      LAS char* rp = lds + wv * (64 * G2_RED) + (lane & 31) * G2_RED + (lane >> 5) * 16;
#pragma unroll
      for (int T = 0; T < 2; ++T)
#pragma unroll
        for (int b = 0; b < 2; ++b)
#pragma unroll
          for (int g = 0; g < 4; ++g) {
            const f32x4 v = {acc2[T][b][4 * g], acc2[T][b][4 * g + 1], acc2[T][b][4 * g + 2], acc2[T][b][4 * g + 3]};
            *(LAS f32x4*)(rp + b * 32 * G2_RED + T * 128 + g * 32) = v;
          }
    }
  } else if (w8 == 4) {
    __builtin_amdgcn_s_setprio(3);
    const rsrc_t r_wg = mk_rsrc(wg, DM * FF * 2), r_wu = mk_rsrc(wu, DM * FF * 2);
    const rsrc_t r_wd = mk_rsrc(wd, FF * DM * 2);
    unsigned woff[7], xoff[4], wv4[4];
#pragma unroll
    for (int i = 0; i < 4; ++i) {
      xoff[i] = (unsigned)((lane >> 3) * 1024 + (((lane & 7) ^ ((lane >> 4) + 4 * (i & 1))) * 16) + i * 7168);
      wv4[i] = (unsigned)((lane >> 3) * 1024 + (((lane & 7) ^ (((lane >> 4) & 1) << 2)) * 16) + (i >> 1) * 458752 + (i & 1) * 8192 - i * 1024);
    }
#pragma unroll
    for (int i = 0; i < 7; ++i) {
      const int c = i * 64 + lane, row = c / 14, col = c - row * 14;
      woff[i] = (unsigned)(row * (FF * 2) + col * 16 - (i & 3) * 1024);
    }
    const unsigned ldw = ldsb + SY_LD;
    auto g1_stage = [&](int s) __attribute__((always_inline)) {
      const unsigned st = (unsigned)((s + rot) & 7), lb = ldsb + (unsigned)((s % 3) * STG);
#pragma unroll
      for (int i = 0; i < 4; ++i)
        dma16x4(lb + XOFF + i * 4096, xoff[0], xoff[1], xoff[2], xoff[3], r_x, (unsigned)((mt * 128 + i * 32) * 1024) + st * 128u);
#pragma unroll
      for (int h = 0; h < 2; ++h) {
        const unsigned wso = (unsigned)(ft * 224) + (st * 64u + h * 32u) * (FF * 2);
        dma16x4(lb + h * 7168, woff[0], woff[1], woff[2], woff[3], r_wg, wso);
        dma16x3(lb + h * 7168 + 4096, woff[4], woff[5], woff[6], r_wg, wso);
        dma16x4(lb + W_UPOFF + h * 7168, woff[0], woff[1], woff[2], woff[3], r_wu, wso);
        dma16x3(lb + W_UPOFF + h * 7168 + 4096, woff[4], woff[5], woff[6], r_wu, wso);
      }
    };
#define G2_GROUP(g) static_for<5 * (g), cmin(5 * (g) + 5, 28)>([&](auto ic) { constexpr int i = decltype(ic)::value; \
        const unsigned so_ = G2_KS(0, i) * 16384u + (unsigned)(dtile * 128); \
        dma16x4(ldsb + g2_base(i), wv4[0], wv4[1], wv4[2], wv4[3], r_wd, so_); \
        dma16x4(ldsb + g2_base(i) + 4096, wv4[0], wv4[1], wv4[2], wv4[3], r_wd, so_ + 2u * 458752u); })
    bar_only();
    g1_stage(0);
    g1_stage(1);
    wait_vm<44>();
    STAMP(1);
    BARX(bar_only);
#pragma unroll
    for (int k = 0; k < 6; ++k) {
      g1_stage(k + 2);
      wait_vm<44>();
      BARX(bar_only);
    }
    STAMP(2);
    G2_GROUP(0);
    wait_vm<40>();
    BARX(bar_only);
    G2_GROUP(1);
    wait_vm<40>();
    BARX(bar_only);
    STAMP(3);
    G2_GROUP(2);
    wait_vm<40>();
    lds_setv(ldw, 2u);
    wait_vm<0>();
    lds_setv(ldw, 3u);
    STAMP(4);
    lds_wait<4>(ldsb + SY_MC, 1u);
    G2_GROUP(3);
    lds_wait<4>(ldsb + SY_MC, 2u);
    G2_GROUP(4);
    lds_wait<4>(ldsb + SY_MC, 3u);
    G2_GROUP(5);
    wait_vm<0>();
    lds_setv(ldw, 6u);
    STAMP(5);
    __builtin_amdgcn_s_setprio(0);
    EPI_X();
    lds_wait<1>(ldsb + SY_SR, 1u);
    EPI_S();
    STAMP(12);
    bar_lgkm();
  } else if (w8 == 5) {
    const rsrc_t r_xn = mk_rsrc(ws + WS_XNF, SL * DM * 2);
    bar_only();
    __builtin_amdgcn_s_setprio(3);
    BARX(bar_only);
    const unsigned tag = lds_get1(ldsb + SY_VAL);
    BARX(bar_only);
    BARX(bar_only);
    BARX(bar_only);
    bool st_ok = false, all_ok = false;
    u32x4 vs, vf;
    unsigned uw[32];
    const unsigned usb = (unsigned)((cp >> 2) * 32768 + (cp & 3) * 4);
    ld128c(vs, lane * 16u, r_fl, FL_STAT + (unsigned)(mt * 1024));
    BARX(bar_only);
    BARX(bar_only);
    wait_vm<0>();
    PIN(vs);
    if (wave_all((vs[1] == tag) & (vs[3] == tag))) {
      *(LAS u32x2*)(lds + STAT + lane * 8) = (u32x2){vs[0], vs[2]};
      lds_setv(ldsb + SY_ST, 1u);
      st_ok = true;
    }
    ld128c(vf, lane * 16u, r_fl, FL_ALL + (unsigned)(xcd * 4096));
    BARX(bar_only);
    wait_vm<0>();
    PIN(vf);
    all_ok = wave_all((vf[0] == tag) & (vf[1] == tag) & (vf[2] == tag) & (vf[3] == tag));
    if (all_ok) {
#pragma unroll
      for (int m = 0; m < 16; ++m) ld32c(uw[m], lane * 16u, r_xn, usb + (unsigned)m * 1024u);
    }
    BARX(bar_only);
    if (all_ok) {
#pragma unroll
      for (int m = 16; m < 32; ++m) ld32c(uw[m], lane * 16u, r_xn, usb + (unsigned)m * 1024u);
    }
    BARX(bar_only);
    STAMP(2);
    if (!st_ok) {
#pragma unroll 1
      for (int it = 0; it < (1 << 18); ++it) {
        ld128c(vs, lane * 16u, r_fl, FL_STAT + (unsigned)(mt * 1024));
        wait_vm<0>();
        PIN(vs);
        if (wave_all((vs[1] == tag) & (vs[3] == tag))) break;
        __builtin_amdgcn_s_sleep(2);
      }
      *(LAS u32x2*)(lds + STAT + lane * 8) = (u32x2){vs[0], vs[2]};
      lds_setv(ldsb + SY_ST, 1u);
    }
    if (!all_ok) {
      poll_flags<4>(lane * 16u, r_fl, FL_ALL + (unsigned)(xcd * 4096), tag);
#pragma unroll
      for (int m = 0; m < 32; ++m) ld32c(uw[m], lane * 16u, r_xn, usb + (unsigned)m * 1024u);
    }
    if (lane < 20) *(LAS u32x4*)(lds + CV_U + (lane / 10) * CV_USTR + (lane % 10) * 16) = (u32x4){0u, 0u, 0u, 0u};
    wait_vm<0>();
    if (lane == 0 && bid == 0) set_flag(ws, FL_EP, tag);
    STAMP(3);
    {
      const bool od = lane & 1;
      LAS char* up = lds + CV_U + 160 + (od ? 128 : 0) + (lane >> 1) * 4;
#pragma unroll
      for (int m = 0; m < 32; m += 2) {
        PIN(uw[m]); PIN(uw[m + 1]);
        const unsigned a = pk_f16(lo_f(uw[m]), hi_f(uw[m])), b = pk_f16(lo_f(uw[m + 1]), hi_f(uw[m + 1]));
        const unsigned y = (unsigned)__builtin_amdgcn_update_dpp(0, (int)(od ? a : b), 0xB1, 0xf, 0xf, true);
        const unsigned f0 = od ? y : a, f1 = od ? b : y;
        *(LAS unsigned*)(up + m * 128) = __builtin_amdgcn_perm(f1, f0, 0x05040100u);
        *(LAS unsigned*)(up + CV_USTR + m * 128) = __builtin_amdgcn_perm(f1, f0, 0x07060302u);
      }
      lds_setv(ldsb + SY_UR, 1u);
    }
    __builtin_amdgcn_s_setprio(1);
    STAMP(4);
    lds_wait<1>(ldsb + SY_CNT + 4, 4u);
    poll_flags<2>((lane & 3) * 16u, r_fl, FL_H + (unsigned)((tt * 8 + dtile) * 256), tag);
    lds_setv(ldsb + SY_HR, 1u);
    STAMP(5);
    EPI_X();
    lds_wait<1>(ldsb + SY_CNT + 8, 4u);
    poll_flags<4>((lane & 7) * 16u, r_fl, FL_S + (unsigned)((tt * 8 + dtile) * 256), tag);
    lds_setv(ldsb + SY_SR, 1u);
    STAMP(6);
    EPI_S();
    STAMP(12);
    bar_lgkm();
  } else {
    const int hc = wv - 2;
    const int dch = cp * 2 + hc;
    const rsrc_t r_xn = mk_rsrc(ws + WS_XNF, SL * DM * 2);
    u32x4 xr[4];
    unsigned ep;
    const int row0 = bid * 8 + hc * 4;
    {
      const unsigned vo = (unsigned)((lane >> 4) * 1024 + (lane & 15) * 16);
#pragma unroll
      for (int i = 0; i < 4; ++i) ld128(xr[i], vo, r_x, (unsigned)(row0 * 1024 + i * 256));
      ld32c(ep, 0u * lane, r_fl, FL_EP);
    }
    bar_only();
    const unsigned dsk2 = dsk2p[cp];
    wait_vm<0>();
    STAMP(1);
#pragma unroll
    for (int i = 0; i < 4; ++i) PIN(xr[i]);
    PIN(ep);
    const unsigned tag = (unsigned)__builtin_amdgcn_readfirstlane(ep) + 1u;
    lds_setv(ldsb + SY_VAL, tag);
    {
      float f[32];
#pragma unroll
      for (int j = 0; j < 16; ++j) { f[2 * j] = lo_f(xr[j >> 2][j & 3]); f[2 * j + 1] = hi_f(xr[j >> 2][j & 3]); }
      float s0 = 0.f, s1 = 0.f;
#pragma unroll
      for (int j = 0; j < 32; j += 4) { s0 += f[j] + f[j + 1]; s1 += f[j + 2] + f[j + 3]; }
      const float mean = row_sum(s0 + s1) * (1.0f / DM);
      float q0 = 0.f, q1 = 0.f;
#pragma unroll
      for (int j = 0; j < 32; j += 2) {
        const float da = f[j] - mean, db = f[j + 1] - mean;
        q0 = fmaf(da, da, q0); q1 = fmaf(db, db, q1);
      }
      const float var = row_sum(q0 + q1) * (1.0f / DM);
      const float mb = rbf(mean);
      const float sb = rbf(__builtin_amdgcn_sqrtf(rbf(rbf(var) + 1e-5f)));
      if ((lane & 15) == 0)
        st64c((u32x2){pk_bf16(mb, sb), tag}, (unsigned)((lane >> 4) * 8), r_fl, FL_STAT + (unsigned)(row0 * 8));
      const float rs = __builtin_amdgcn_rcpf(sb);
      const unsigned vo = (unsigned)((lane & 15) * 32768 + (lane >> 4) * 16);
      STAMP(2);
#pragma unroll
      for (int i = 0; i < 4; ++i) {
        if ((i & 1) == 0) { SCHED_FENCE(); BARX(bar_lgkm); SCHED_FENCE(); }
        u32x4 o;
#pragma unroll
        for (int j = 0; j < 4; ++j) {
          const unsigned d = pk_bf16(f[8 * i + 2 * j] - mb, f[8 * i + 2 * j + 1] - mb);
          o[j] = pk_bf16(lo_f(d) * rs, hi_f(d) * rs);
        }
        st128c(o, vo, r_xn, (unsigned)(i * 16 * 32768 + row0 * 16));
      }
    }
    BARX(bar_only);
    wait_vm<0>();
    BARX(bar_only);
    if (hc == 0 && lane < 8) set_flag(ws, FL_ALL + (unsigned)(lane * 4096 + bid * 4), tag);
    BARX(bar_only);
    BARX(bar_only);
    BARX(bar_only);
    BARX(bar_only);
    BARX(bar_only);
    __builtin_amdgcn_s_setprio(3);
    float kscale;
    {
      f32x4 kacc[3];
#pragma unroll
      for (int t = 0; t < 3; ++t)
        kacc[t] = *(const LAS f32x4*)(lds + KF + hc * CV_KSTR + t * 1024 + lane * 16) + *(const LAS f32x4*)(lds + KGS + hc * 4096 + t * 1024 + lane * 16);
      const f32x2 gw = *(const LAS f32x2*)(lds + SY_KG + hc * 8);
      const float g2 = gw.x;
      const int we = (int)gw.y;
      const int c = lane & 15, g = lane >> 4;
      if (g2 > 0.f) {
#pragma unroll
        for (int i = 0; i < 4; ++i) {
          const int l0 = 16 * c + 4 * g + i;
          const int l1 = g < 2 ? 256 + 16 * c + 8 * g + 2 * i : 512 + 256 * (g - 2) + 16 * c + 4 * i;
          const int l2 = g < 2 ? 1024 + 256 * (2 * g + (i >> 1)) + 8 * (i & 1) + 16 * c : 2048;
          kacc[0][i] *= __builtin_amdgcn_exp2f(g2 * (float)(l0 - 2048));
          kacc[1][i] *= __builtin_amdgcn_exp2f(g2 * (float)(l1 - 2048));
          kacc[2][i] *= __builtin_amdgcn_exp2f(g2 * (float)(l2 - 2048));
        }
      }
      kscale = __builtin_amdgcn_ldexpf(1.0f, we - 7) * __builtin_amdgcn_exp2f(g2 * 2048.f);
      *(LAS float*)(lds + SY_VAL + 4 + hc * 4) = kscale;
      unsigned d[3][4];
#pragma unroll
      for (int t = 0; t < 3; ++t)
#pragma unroll
        for (int i = 0; i < 4; ++i) d[t][i] = pk_f16(kacc[t][i] * 128.f, kacc[t][i] * 128.f);
      LAS char* kf = lds + KF + hc * CV_KSTR + 96;
#define MIX(a, b) (((a) & 0xffffu) | ((b) & 0xffff0000u))
      if (g < 2) {
#pragma unroll
        for (int h = 0; h < 2; ++h) {
          LAS char* p = kf + 2 * (1024 + 256 * (2 * g + h) + 16 * c);
          const unsigned v0 = d[2][2 * h], v1 = d[2][2 * h + 1];
          *(LAS u32x2*)(p - 8) = (u32x2){v0, v0};
          *(LAS u32x4*)(p) = (u32x4){v0, v0, MIX(v0, v1), v1};
          *(LAS u32x2*)(p + 16) = (u32x2){v1, v1};
        }
      } else {
        if (lane == 32) *(LAS u32x2*)(kf + 2 * 2044) = (u32x2){d[2][0], d[2][0]};
      }
      if (g >= 2) {
        LAS char* p = kf + 2 * (512 + 256 * (g - 2) + 16 * c);
        const unsigned v0 = d[1][0], v1 = d[1][1], v2 = d[1][2], v3 = d[1][3];
        *(LAS unsigned*)(p - 4) = v0;
        *(LAS u32x4*)(p) = (u32x4){v0, MIX(v0, v1), v1, v2};
        *(LAS u32x2*)(p + 16) = (u32x2){v2, MIX(v2, v3)};
        *(LAS unsigned*)(p + 24) = v3;
      }
      if (g < 2) {
        LAS char* p = kf + 2 * (256 + 16 * c + 8 * g);
        const unsigned v0 = d[1][0], v1 = d[1][1], v2 = d[1][2], v3 = d[1][3];
        *(LAS unsigned short*)(p - 2) = (unsigned short)v0;
        *(LAS u32x2*)(p) = (u32x2){v0, MIX(v1, v2)};
        *(LAS unsigned*)(p + 8) = v2;
        *(LAS unsigned short*)(p + 12) = (unsigned short)v3;
      }
      *(LAS u32x2*)(kf + 2 * (16 * c + 4 * g)) = (u32x2){MIX(d[0][0], d[0][1]), MIX(d[0][2], d[0][3])};
      if (lane < 6) *(LAS u32x4*)(kf - 96 + lane * 16) = (u32x4){0u, 0u, 0u, 0u};
    }
    lds_setv(ldsb + SY_UR + 4 + hc * 4, 1u);
    __builtin_amdgcn_s_setprio(1);
    STAMP(3);
    lds_wait<1>(ldsb + SY_UR, 1u);
    STAMP(6);
    {
      const float dskip = hc ? hi_f(dsk2) : lo_f(dsk2);
      const int G = lane >> 4, q4 = (lane & 15) >> 2, p = lane & 3;
      const LAS char* pa = lds + KF + hc * CV_KSTR + 96 + 2 * (76 + 16 * p - 32 * G - 4 * q4 - 16);
      const LAS char* pb = lds + CV_U + hc * CV_USTR + 160 + 2 * (-76 + 32 * G + 4 * q4 + 4 * p);
      const LAS char* pu = lds + CV_U + hc * CV_USTR + 160 + lane * 2;
      f16x8 A[16], B[32];
#define CV_A(q) __builtin_bit_cast(f16x8, cat8(trrd(pa + 256 * (q) + 32), trrd(pa + 256 * (q))))
#define CV_B(w) __builtin_bit_cast(f16x8, cat8(trrd(pb + 128 * (w)), trrd(pb + 128 * (w) + 32)))
      A[0] = CV_A(0); B[0] = CV_B(0); B[1] = CV_B(1);
      A[1] = CV_A(1); B[2] = CV_B(2); B[3] = CV_B(3);
      f32x4 ca[2][2];
      _Float16 us[2][2];
      unsigned so16[16];
      float c3 = 0.f, c2 = 0.f, c1 = 0.f;
      auto tile_y = [&](const f32x4 a) __attribute__((always_inline)) {
        const float h3 = a[3];
        const float h2 = a[2] + shr_carry(c3, h3);
        const float h1 = a[1] + shr_carry(c2, h2);
        const float y = a[0] + shr_carry(c1, h1);
        c3 = dpp_mov<0x13C>(h3); c2 = dpp_mov<0x13C>(h2); c1 = dpp_mov<0x13C>(h1);
        return y * kscale;
      };
      static_for<0, CV_NP + 1>([&](auto jc) {
        constexpr int jj = decltype(jc)::value;
        if constexpr (jj + 2 < CV_NP) { A[jj + 2] = CV_A(jj + 2); B[2 * jj + 4] = CV_B(2 * jj + 4); B[2 * jj + 5] = CV_B(2 * jj + 5); }
        if constexpr (jj < CV_NP) {
          us[jj & 1][0] = *(const LAS _Float16*)(pu + jj * 256); us[jj & 1][1] = *(const LAS _Float16*)(pu + jj * 256 + 128);
          f32x4 a0 = {0.f, 0.f, 0.f, 0.f}, a1 = {0.f, 0.f, 0.f, 0.f};
#pragma unroll
          for (int q = jj; q >= 0; --q) {
            a0 = __builtin_amdgcn_mfma_f32_16x16x32_f16(A[q], B[2 * (jj - q)], a0, 0, 0, 0);
            a1 = __builtin_amdgcn_mfma_f32_16x16x32_f16(A[q], B[2 * (jj - q) + 1], a1, 0, 0, 0);
          }
          ca[jj & 1][0] = a0; ca[jj & 1][1] = a1;
        }
        if constexpr (jj >= 1) {
          const float y0 = tile_y(ca[(jj - 1) & 1][0]), y1 = tile_y(ca[(jj - 1) & 1][1]);
          const unsigned yq = pk_bf16(y0, y1);
          const unsigned uq = pk_bf16((float)us[(jj - 1) & 1][0] * dskip, (float)us[(jj - 1) & 1][1] * dskip);
          so16[jj - 1] = pk_bf16(lo_f(yq) + lo_f(uq), hi_f(yq) + hi_f(uq));
        }
        if constexpr (jj >= 1 && jj < CV_NP) {
#pragma unroll
          for (int k = 0; k < 2 * (jj + 1); ++k) {
            __builtin_amdgcn_sched_group_barrier(0x8, 1, 0);
            __builtin_amdgcn_sched_group_barrier(0x100, 1, 0);
            __builtin_amdgcn_sched_group_barrier(0x2, jj < 7 ? 4 : 2, 0);
          }
        }
        SCHED_FENCE();
      });
      STAMP(8);
#pragma unroll
      for (int j = 0; j < CV_NP; ++j) st32c(so16[j], lane * 4u, r_s, (unsigned)(dch * 4096 + j * 256));
    }
    STAMP(7);
    publish<4>(lds, 2, ws, FL_S + (unsigned)((lane * 8 + (cp >> 5)) * 256 + (cp & 31) * 4), 32, tag, lane);
    STAMP(9);
    EPI_X();
    lds_wait<1>(ldsb + SY_SR, 1u);
    EPI_S();
    STAMP(12);
    bar_lgkm();
  }
  {
    int t2 = tid; PIN(t2);
    const u32x4 xv = *(const LAS u32x4*)(lds + EPX + t2 * 16);
    unsigned sv[8];
#pragma unroll
    for (int j = 0; j < 8; ++j) sv[j] = *(const LAS unsigned*)(lds + EPS + ((t2 & 7) * 8 + j) * 256 + (((t2 >> 5) + (t2 & 7)) & 15) * 16 + ((t2 >> 3) & 3) * 4);
    bar_lgkm();
    const LAS char* rp = lds + (t2 >> 3) * G2_RED + (t2 & 7) * 32;
    const bool odd = tt & 1;
    u32x4 o;
#pragma unroll
    for (int i = 0; i < 2; ++i) {
      f32x4 v = *(const LAS f32x4*)(rp + i * 16);
#pragma unroll
      for (int w = 1; w < 4; ++w) v += *(const LAS f32x4*)(rp + w * (64 * G2_RED) + i * 16);
#pragma unroll
      for (int jj = 0; jj < 2; ++jj) {
        const unsigned fp = pk_bf16(v[2 * jj], v[2 * jj + 1]);
        const float f0 = lo_f(fp), f1 = hi_f(fp);
        const float g0 = f0 * __builtin_amdgcn_rcpf(1.0f + __builtin_amdgcn_exp2f(f0 * -1.4426950408889634f));
        const float g1 = f1 * __builtin_amdgcn_rcpf(1.0f + __builtin_amdgcn_exp2f(f1 * -1.4426950408889634f));
        const unsigned gp = pk_bf16(g0, g1);
        const int j = 4 * i + 2 * jj;
        const float s0 = odd ? hi_f(sv[j]) : lo_f(sv[j]), s1 = odd ? hi_f(sv[j + 1]) : lo_f(sv[j + 1]);
        const unsigned sg = pk_bf16(s0 * lo_f(gp), s1 * hi_f(gp));
        const unsigned xw = xv[2 * i + jj];
        o[2 * i + jj] = pk_bf16(lo_f(xw) + lo_f(sg), hi_f(xw) + hi_f(sg));
      }
    }
    *(u32x4*)((char*)out + XO(t2)) = o;
  }
  STAMP(13); STAMP_RT(15);
  STAMP_OUT(0);
  BW_OUT();
}

extern "C" void kernel_launch(void* const* d_in, const int* in_sizes, int n_in, void* d_out,
                              int out_size, void* d_ws, size_t ws_size, hipStream_t stream) {
  (void)hipFuncSetAttribute((const void*)fused_kernel, hipFuncAttributeMaxDynamicSharedMemorySize, LDS_BYTES);
  fused_kernel<<<256, 512, LDS_BYTES, stream>>>(
      (const unsigned short*)d_in[0], (const unsigned short*)d_in[1], (const unsigned short*)d_in[2],
      (const unsigned short*)d_in[3], (const unsigned short*)d_in[4], (const unsigned short*)d_in[5],
      (const unsigned short*)d_in[6], (const unsigned*)d_in[7], (const unsigned*)d_in[8],
      (const unsigned short*)d_in[10], (const unsigned short*)d_in[11], (const unsigned short*)d_in[12],
      (char*)d_ws, (unsigned short*)d_out);
}
```

```cpp
#include <hip/hip_runtime.h>
#include <hip/hip_bf16.h>
#include <stdint.h>
#include <type_traits>

typedef __attribute__((ext_vector_type(4))) short s16x4;
typedef __attribute__((ext_vector_type(8))) short s16x8;
typedef __attribute__((ext_vector_type(8))) _Float16 f16x8;
typedef __attribute__((ext_vector_type(8))) __bf16 bf16x8;
typedef __attribute__((ext_vector_type(2))) float f32x2;
typedef __attribute__((ext_vector_type(4))) float f32x4;
typedef __attribute__((ext_vector_type(16))) float f32x16;
typedef __attribute__((ext_vector_type(4))) unsigned u32x4;
typedef __attribute__((ext_vector_type(3))) unsigned u32x3;
typedef __attribute__((ext_vector_type(2))) unsigned u32x2;
typedef __attribute__((ext_vector_type(2))) __bf16 bf16x2;
typedef __attribute__((ext_vector_type(2))) _Float16 f16x2;
#define LAS __attribute__((address_space(3)))
#define GAS __attribute__((address_space(1)))
typedef __amdgpu_buffer_rsrc_t rsrc_t;

constexpr int SL = 2048;
constexpr int DM = 512;
constexpr int NM = 128;
constexpr int FF = 1792;

constexpr size_t WS_XNF = 0;
constexpr size_t WS_ST = 2 * (size_t)SL * DM * 2;
constexpr size_t WS_HF = WS_ST + (size_t)SL * DM * 2;
constexpr size_t WS_FLAG = 16u << 20;
constexpr unsigned FL_ALL = 0, FL_STAT = 65536, FL_H = 131072, FL_S = 196608, FL_EP = 262144, FL_BYTES = 262400;

template <int I, int N, class F>
__device__ __forceinline__ void static_for(F&& f) {
  if constexpr (I < N) { f(std::integral_constant<int, I>{}); static_for<I + 1, N>(f); }
}

__device__ __forceinline__ unsigned pk_bf16(float lo, float hi) {
  f32x2 v = {lo, hi};
  return __builtin_bit_cast(unsigned, __builtin_convertvector(v, bf16x2));
}
__device__ __forceinline__ unsigned pk_f16(float lo, float hi) {
  f32x2 v = {lo, hi};
  return __builtin_bit_cast(unsigned, __builtin_convertvector(v, f16x2));
}
__device__ __forceinline__ float lo_f(unsigned p) { return __uint_as_float(p << 16); }
__device__ __forceinline__ float hi_f(unsigned p) { return __uint_as_float(p & 0xffff0000u); }
__device__ __forceinline__ float rbf(float x) { return lo_f(pk_bf16(x, x)); }

template <int CTRL>
__device__ __forceinline__ float dpp_mov(float v) {
  return __int_as_float(__builtin_amdgcn_update_dpp(0, __float_as_int(v), CTRL, 0xf, 0xf, true));
}
__device__ __forceinline__ float shr_carry(float c, float v) {
  return __builtin_bit_cast(float, __builtin_amdgcn_update_dpp(__builtin_bit_cast(int, c), __builtin_bit_cast(int, v), 0x138, 0xf, 0xf, false));
}
__device__ __forceinline__ float rdlane(float v, int l) {
  return __int_as_float(__builtin_amdgcn_readlane(__float_as_int(v), l));
}
__device__ __forceinline__ float wave_sum(float v) {
  v += dpp_mov<0xB1>(v);
  v += dpp_mov<0x4E>(v);
  v += dpp_mov<0x141>(v);
  v += dpp_mov<0x140>(v);
  return (rdlane(v, 0) + rdlane(v, 16)) + (rdlane(v, 32) + rdlane(v, 48));
}
__device__ __forceinline__ float row_sum(float v) {
  v += dpp_mov<0xB1>(v);
  v += dpp_mov<0x4E>(v);
  v += dpp_mov<0x141>(v);
  v += dpp_mov<0x140>(v);
  return v;
}
__device__ __forceinline__ float half_sum(float v, int lane) {
  v += dpp_mov<0xB1>(v);
  v += dpp_mov<0x4E>(v);
  v += dpp_mov<0x141>(v);
  v += dpp_mov<0x140>(v);
  const float lo = rdlane(v, 0) + rdlane(v, 16), hi = rdlane(v, 32) + rdlane(v, 48);
  return lane < 32 ? lo : hi;
}
__device__ __forceinline__ float wave_max(float v) {
  v = fmaxf(v, dpp_mov<0xB1>(v));
  v = fmaxf(v, dpp_mov<0x4E>(v));
  v = fmaxf(v, dpp_mov<0x141>(v));
  v = fmaxf(v, dpp_mov<0x140>(v));
  return fmaxf(fmaxf(rdlane(v, 0), rdlane(v, 16)), fmaxf(rdlane(v, 32), rdlane(v, 48)));
}

__device__ __forceinline__ rsrc_t mk_rsrc(const void* p, unsigned bytes) {
  return __builtin_amdgcn_make_buffer_rsrc((void*)p, (short)0, (int)bytes, 0x00020000);
}
__device__ __forceinline__ void dma16(unsigned lds_dst, unsigned voff, rsrc_t r, unsigned soff) {
  asm volatile("s_mov_b32 m0, %0\n\ts_nop 0\n\tbuffer_load_dwordx4 %1, %2, %3 offen lds"
               :: "s"(lds_dst), "v"(voff), "s"(r), "s"(soff) : "memory");
}
__device__ __forceinline__ void dma16x4(unsigned lds_dst, unsigned v0, unsigned v1, unsigned v2, unsigned v3, rsrc_t r, unsigned soff) {
  asm volatile("s_mov_b32 m0, %0\n\ts_nop 0\n\t"
               "buffer_load_dwordx4 %1, %5, %6 offen lds\n\t"
               "buffer_load_dwordx4 %2, %5, %6 offen offset:1024 lds\n\t"
               "buffer_load_dwordx4 %3, %5, %6 offen offset:2048 lds\n\t"
               "buffer_load_dwordx4 %4, %5, %6 offen offset:3072 lds"
               :: "s"(lds_dst), "v"(v0), "v"(v1), "v"(v2), "v"(v3), "s"(r), "s"(soff) : "memory");
}
__device__ __forceinline__ void dma16x3(unsigned lds_dst, unsigned v0, unsigned v1, unsigned v2, rsrc_t r, unsigned soff) {
  asm volatile("s_mov_b32 m0, %0\n\ts_nop 0\n\t"
               "buffer_load_dwordx4 %1, %4, %5 offen lds\n\t"
               "buffer_load_dwordx4 %2, %4, %5 offen offset:1024 lds\n\t"
               "buffer_load_dwordx4 %3, %4, %5 offen offset:2048 lds"
               :: "s"(lds_dst), "v"(v0), "v"(v1), "v"(v2), "s"(r), "s"(soff) : "memory");
}
__device__ __forceinline__ void ld128(u32x4& dst, unsigned voff, rsrc_t r, unsigned soff) {
  asm volatile("buffer_load_dwordx4 %0, %1, %2, %3 offen" : "=v"(dst) : "v"(voff), "s"(r), "s"(soff) : "memory");
}
__device__ __forceinline__ void ld128c(u32x4& dst, unsigned voff, rsrc_t r, unsigned soff) {
  asm volatile("buffer_load_dwordx4 %0, %1, %2, %3 offen sc1" : "=v"(dst) : "v"(voff), "s"(r), "s"(soff) : "memory");
}
__device__ __forceinline__ void ld32(unsigned& dst, unsigned voff, rsrc_t r, unsigned soff) {
  asm volatile("buffer_load_dword %0, %1, %2, %3 offen" : "=v"(dst) : "v"(voff), "s"(r), "s"(soff) : "memory");
}
__device__ __forceinline__ void ld64c(u32x2& dst, unsigned voff, rsrc_t r, unsigned soff) {
  asm volatile("buffer_load_dwordx2 %0, %1, %2, %3 offen sc1" : "=v"(dst) : "v"(voff), "s"(r), "s"(soff) : "memory");
}
__device__ __forceinline__ void ld32c(unsigned& dst, unsigned voff, rsrc_t r, unsigned soff) {
  asm volatile("buffer_load_dword %0, %1, %2, %3 offen sc1" : "=v"(dst) : "v"(voff), "s"(r), "s"(soff) : "memory");
}
__device__ __forceinline__ void st32c(unsigned v, unsigned voff, rsrc_t r, unsigned soff) {
  asm volatile("buffer_store_dword %0, %1, %2, %3 offen sc1" :: "v"(v), "v"(voff), "s"(r), "s"(soff) : "memory");
}
__device__ __forceinline__ void st64c(u32x2 v, unsigned voff, rsrc_t r, unsigned soff) {
  asm volatile("buffer_store_dwordx2 %0, %1, %2, %3 offen sc1\n\ts_nop 1" :: "v"(v), "v"(voff), "s"(r), "s"(soff) : "memory");
}
__device__ __forceinline__ void ld16(unsigned& dst, unsigned voff, rsrc_t r, unsigned soff) {
  asm volatile("buffer_load_ushort %0, %1, %2, %3 offen" : "=v"(dst) : "v"(voff), "s"(r), "s"(soff) : "memory");
}
__device__ __forceinline__ void st128c(u32x4 v, unsigned voff, rsrc_t r, unsigned soff) {
  asm volatile("buffer_store_dwordx4 %0, %1, %2, %3 offen sc1\n\ts_nop 1" :: "v"(v), "v"(voff), "s"(r), "s"(soff) : "memory");
}
template <int N>
__device__ __forceinline__ void wait_vm() { asm volatile("s_waitcnt vmcnt(%0)" :: "n"(N > 63 ? 63 : N) : "memory"); }
__device__ __forceinline__ void bar_lgkm() { asm volatile("s_waitcnt lgkmcnt(0)\n\ts_barrier" ::: "memory"); }
__device__ __forceinline__ void bar_only() { asm volatile("s_barrier" ::: "memory"); }
#define BW_DECL
#define BARX(f) f()
#define BW_OUT()
#define PIN(x) asm volatile("" : "+v"(x))
#define SCHED_FENCE() __builtin_amdgcn_sched_barrier(0)

__device__ __forceinline__ s16x4 trrd(const LAS char* p) {
  return __builtin_amdgcn_ds_read_tr16_b64_v4i16((LAS s16x4*)p);
}
__device__ __forceinline__ s16x8 cat8(s16x4 a, s16x4 b) {
  return __builtin_shufflevector(a, b, 0, 1, 2, 3, 4, 5, 6, 7);
}

__device__ __forceinline__ bool wave_all(bool p) { return __builtin_amdgcn_ballot_w64(!p) == 0ull; }
template <int SLP = 2>
__device__ __forceinline__ void poll_flags(unsigned voff, rsrc_t r, unsigned soff, unsigned tag) {
#pragma unroll 1
  for (int it = 0; it < (1 << 18); ++it) {
    u32x4 v;
    asm volatile("buffer_load_dwordx4 %0, %1, %2, %3 offen sc1\n\ts_waitcnt vmcnt(0)"
                 : "=&v"(v) : "v"(voff), "s"(r), "s"(soff) : "memory");
    const bool ok = (v[0] == tag) & (v[1] == tag) & (v[2] == tag) & (v[3] == tag);
    if (wave_all(ok)) break;
    __builtin_amdgcn_s_sleep(SLP);
  }
}
__device__ __forceinline__ void set_flag(char* ws, unsigned off, unsigned tag) {
  __hip_atomic_store((GAS unsigned*)(ws + WS_FLAG + off), tag, __ATOMIC_RELAXED, __HIP_MEMORY_SCOPE_AGENT);
}

#define STAMP_DECL
#define STAMP(i)
#define STAMP_RT(i)
#define STAMP_OUT(kid)

__device__ __forceinline__ f32x2 cmul(f32x2 a, f32x2 b) {
  f32x2 r;
  r.x = fmaf(a.x, b.x, -(a.y * b.y));
  r.y = fmaf(a.x, b.y, a.y * b.x);
  return r;
}
__device__ __forceinline__ f32x2 cexp_l(float a, float b, float l) {
  const float mag = __builtin_amdgcn_exp2f(a * l * 1.4426950408889634f);
  const float x = b * l;
  const float C1 = 0.15915494309189535f;
  const float C2 = (float)(0.15915494309189533577 - (double)0.15915494309189535f);
  const float hi = x * C1;
  const float lo = fmaf(x, C1, -hi) + x * C2;
  const float r = (hi - __builtin_rintf(hi)) + lo;
  f32x2 z;
  z.x = mag * __builtin_amdgcn_cosf(r);
  z.y = mag * __builtin_amdgcn_sinf(r);
  return z;
}

constexpr int W_UPOFF = 14336 + 128;
constexpr int XOFF = W_UPOFF + 14336;
constexpr int STG = XOFF + 16384;
constexpr int KGS = 3 * STG;
constexpr int KF = KGS + 8192;
constexpr int CV_KSTR = (48 + 2048) * 2;
constexpr int CV_USTR = (80 + 2048) * 2;
constexpr int CV_U = KF + 2 * CV_KSTR;
constexpr int CSUM = CV_U + 2 * CV_USTR;
constexpr int STAT = CSUM + 896;
constexpr int SY = STAT + 512;
constexpr int SY_LD = SY, SY_MC = SY + 16, SY_ST = SY + 4, SY_HR = SY + 8, SY_SR = SY + 12, SY_UR = SY + 32  ,
              SY_CNT = SY + 48, SY_VAL = SY + 64, SY_KG = SY + 80, SY_END = SY + 96;
constexpr int LDS_BYTES = SY_END;
static_assert(LDS_BYTES <= 163840, "lds");
constexpr int G2_RED = 272;
constexpr int EPX = KGS;
constexpr int EPS = KF;
#define WSL 4
constexpr int CV_NP = 13;
constexpr int G2_NR = 13;
constexpr int cmax(int a, int b) { return a > b ? a : b; }
constexpr int cmin(int a, int b) { return a < b ? a : b; }
constexpr int g2_base(int i) { return (((i % 15) / 5 + 2) % 3) * STG + (i % 5) * 8192; }
constexpr int G2_H0 = 6;
constexpr int g2_issued(int i) { return cmin(28, G2_H0 + (3 * i) / 2); }

__device__ __forceinline__ void lds_setv(unsigned addr, unsigned v) {
  asm volatile("ds_write_b32 %0, %1" :: "v"(addr), "v"(v) : "memory");
}
__device__ __forceinline__ unsigned lds_min4(unsigned addr) {
  u32x4 v;
  asm volatile("ds_read_b128 %0, %1\n\ts_waitcnt lgkmcnt(0)" : "=&v"(v) : "v"(addr) : "memory");
  return (unsigned)__builtin_amdgcn_readfirstlane(min(min(v[0], v[1]), min(v[2], v[3])));
}
__device__ __forceinline__ unsigned lds_get1(unsigned addr) {
  unsigned v;
  asm volatile("ds_read_b32 %0, %1\n\ts_waitcnt lgkmcnt(0)" : "=&v"(v) : "v"(addr) : "memory");
  return (unsigned)__builtin_amdgcn_readfirstlane(v);
}
template <int W>
__device__ __forceinline__ unsigned lds_val(unsigned addr) { return W == 4 ? lds_min4(addr) : lds_get1(addr); }
template <int W>
__device__ __forceinline__ void lds_spin(unsigned addr, unsigned need) {
#pragma unroll 1
  for (int it = 0; it < (1 << 20); ++it) {
    if (lds_val<W>(addr) >= need) break;
    __builtin_amdgcn_s_sleep(1);
  }
}
template <int W>
__device__ __forceinline__ void lds_wait(unsigned addr, unsigned need) {
  if (__builtin_expect(lds_val<W>(addr) < need, 0)) lds_spin<W>(addr, need);
}
template <int NW, bool DRAIN = true>
__device__ __forceinline__ void publish(LAS char* lds, int cnt, char* ws, unsigned off, int nl, unsigned tag, int lane) {
  if (DRAIN) wait_vm<0>();
  unsigned old = 0;
  if (lane == 0) old = __hip_atomic_fetch_add((LAS unsigned*)(lds + SY_CNT + cnt * 4), 1u, __ATOMIC_RELAXED, __HIP_MEMORY_SCOPE_WORKGROUP);
  if (__builtin_amdgcn_readfirstlane(old) == NW - 1 && lane < nl) set_flag(ws, off, tag);
}

#if defined(__HIP_DEVICE_COMPILE__)
__attribute__((target("no-packed-fp32-ops")))
#endif
__global__ __launch_bounds__(512, 2) void fused_kernel(
    const unsigned short* __restrict__ x, const unsigned short* __restrict__ A_re,
    const unsigned short* __restrict__ A_im, const unsigned short* __restrict__ B_re,
    const unsigned short* __restrict__ B_im, const unsigned short* __restrict__ C_re,
    const unsigned short* __restrict__ C_im, const unsigned* __restrict__ log_dt2,
    const unsigned* __restrict__ dsk2p, const unsigned short* __restrict__ wg,
    const unsigned short* __restrict__ wu, const unsigned short* __restrict__ wd,
    char* ws, unsigned short* __restrict__ out) {
  extern __shared__ __attribute__((aligned(16))) char smem[];
  LAS char* lds = (LAS char*)smem;
  const unsigned ldsb = (unsigned)(uintptr_t)lds;
  const int tid = threadIdx.x, lane = tid & 63;
  const int w8 = __builtin_amdgcn_readfirstlane(tid >> 6);
  const int wv = w8 & 3;
  STAMP_DECL; BW_DECL; STAMP_RT(14); STAMP(0);
  const int bid = blockIdx.x;
  const int xcd = bid & 7, bi = bid >> 3;
  const int mt = (xcd >> 2) * 8 + (bi >> 2);
  const int ft = (xcd & 3) * 4 + (bi & 3);
  const int rot = ((bi >> 2) + 2 * (bi & 3)) & 7;
  const int cp = xcd * 32 + bi;
  const int tt = xcd * 4 + (bi >> 3);
  const int dtile = bi & 7;
  const int rot2 = ((bi >> 3) * 7 + dtile * 3) % 28;

  const rsrc_t r_x = mk_rsrc(x, SL * DM * 2);
  const rsrc_t r_s = mk_rsrc(ws + WS_ST, SL * DM * 2);
  const rsrc_t r_fl = mk_rsrc(ws + WS_FLAG, FL_BYTES);
#define G2_KS(w, i) ((unsigned)((w) * 28 + (((i) + rot2) >= 28 ? (i) + rot2 - 28 : (i) + rot2)))
#define XO(t_) ((unsigned)((tt * 64 + ((t_) >> 3)) * (DM * 2) + dtile * 128 + ((t_) & 7) * 16))
#define EPI_X() do { int l_ = lane; PIN(l_); u32x4 xw_[2]; \
    _Pragma("unroll") for (int j = 0; j < 2; ++j) ld128(xw_[j], XO(wv * 128 + j * 64 + l_), r_x, 0u); \
    lds_wait<4>(ldsb + SY_UR, 1u); wait_vm<0>(); \
    _Pragma("unroll") for (int j = 0; j < 2; ++j) { PIN(xw_[j]); *(LAS u32x4*)(lds + EPX + (wv * 128 + j * 64 + l_) * 16) = xw_[j]; } } while (0)
#define EPI_S() do { int l_ = lane; PIN(l_); u32x4 sw_[4]; \
    const unsigned so = (unsigned)((dtile * 64 + wv * 16 + (l_ >> 4)) * 4096 + (tt >> 1) * 256 + (l_ & 15) * 16); \
    _Pragma("unroll") for (int j = 0; j < 4; ++j) ld128c(sw_[j], so, r_s, (unsigned)(j * 16384)); \
    wait_vm<0>(); \
    _Pragma("unroll") for (int j = 0; j < 4; ++j) { PIN(sw_[j]); \
      *(LAS u32x4*)(lds + EPS + (wv * 16 + j * 4 + (l_ >> 4)) * 256 + ((l_ + wv * 2 + (j >> 1)) & 15) * 16) = sw_[j]; } } while (0)

  if (w8 < 4) {
    const rsrc_t r_h = mk_rsrc(ws + WS_HF, SL * FF * 2);
    {
    const int hc = wv & 1, m = wv >> 1;
    const int dch = cp * 2 + hc;
    unsigned prm[6];
    {
      const unsigned po = (unsigned)(dch * NM * 2);
      ld32(prm[0], lane * 4u, mk_rsrc(A_re, DM * NM * 2), po);
      ld32(prm[1], lane * 4u, mk_rsrc(A_im, DM * NM * 2), po);
      ld32(prm[2], lane * 4u, mk_rsrc(B_re, DM * NM * 2), po);
      ld32(prm[3], lane * 4u, mk_rsrc(B_im, DM * NM * 2), po);
      ld32(prm[4], lane * 4u, mk_rsrc(C_re, DM * NM * 2), po);
      ld32(prm[5], lane * 4u, mk_rsrc(C_im, DM * NM * 2), po);
    }
    if (tid < 5) *(LAS u32x4*)(lds + SY + tid * 16) = (u32x4){0u, 0u, 0u, tid == 2 ? 1u : 0u};
    bar_lgkm();
    __builtin_amdgcn_s_setprio(2);
    const unsigned ldt2 = log_dt2[cp];
    {
      const float ldt = hc ? hi_f(ldt2) : lo_f(ldt2);
      const float L2E = 1.4426950408889634f, L2E_LO = (float)(1.4426950408889634074 - (double)1.4426950408889634f);
      const float th = ldt * L2E;
      const float tl = fmaf(ldt, L2E, -th) + ldt * L2E_LO;
      const float e0 = __builtin_amdgcn_exp2f(th);
      const float dt = rbf(fmaf(e0 * 0.6931471805599453f, tl, e0));
      wait_vm<0>();
#pragma unroll
      for (int i = 0; i < 6; ++i) PIN(prm[i]);
      float av[2], bv[2];
      f32x2 wq[2];
#pragma unroll
      for (int m = 0; m < 2; ++m) {
        av[m] = dt * (m ? hi_f(prm[0]) : lo_f(prm[0]));
        bv[m] = dt * (m ? hi_f(prm[1]) : lo_f(prm[1]));
        const f32x2 db = {dt * (m ? hi_f(prm[2]) : lo_f(prm[2])), dt * (m ? hi_f(prm[3]) : lo_f(prm[3]))};
        const f32x2 cc = {m ? hi_f(prm[4]) : lo_f(prm[4]), m ? hi_f(prm[5]) : lo_f(prm[5])};
        wq[m] = cmul(cc, db);
      }
      const float amax = fmaxf(wave_max(fmaxf(av[0], av[1])), 0.f);
      const float g2 = amax * L2E;
      const float wm = wave_max(fmaxf(fmaxf(fabsf(wq[0].x), fabsf(wq[0].y)), fmaxf(fabsf(wq[1].x), fabsf(wq[1].y))));
      int we = __builtin_amdgcn_frexp_expf(wm);
      we = we < -100 ? -100 : we;
      const float wsc = __builtin_amdgcn_ldexpf(1.0f, -we);
      f32x4 kacc[3];
#pragma unroll
      for (int t = 0; t < 3; ++t) kacc[t] = (f32x4){0.f, 0.f, 0.f, 0.f};
      const int kgs = (m ? CV_U : KGS) + hc * 4096;
      LAS char* tw[4];
#pragma unroll
      for (int c = 0; c < 4; ++c) tw[c] = lds + kgs + lane * 64 + ((c ^ ((lane >> 1) & 3)) << 4);
      const LAS char* tr[2];
      {
        const int G = lane >> 4, q4 = (lane & 15) >> 2, p = lane & 3;
#pragma unroll
        for (int t = 0; t < 2; ++t)
          tr[t] = lds + kgs + (4 * G + 2 * t + (q4 >> 1)) * 64 + ((((q4 & 1) * 2 + (p >> 1)) ^ ((2 * G + t) & 3)) << 4) + (p & 1) * 8;
      }
      auto put16 = [&](const f32x2 (&e)[16]) __attribute__((always_inline)) {
#pragma unroll
        for (int h = 0; h < 2; ++h) {
          u32x4 re, im;
#pragma unroll
          for (int j = 0; j < 4; ++j) {
            re[j] = pk_f16(e[8 * h + 2 * j].x, e[8 * h + 2 * j + 1].x);
            im[j] = pk_f16(e[8 * h + 2 * j].y, e[8 * h + 2 * j + 1].y);
          }
          *(LAS u32x4*)tw[h] = re;
          *(LAS u32x4*)tw[2 + h] = im;
        }
      };
#define KG_FRAG(s) __builtin_bit_cast(f16x8, cat8(trrd(tr[0] + (s) * 1024), trrd(tr[1] + (s) * 1024)))
#define KG_MMA(t) do { _Pragma("unroll") for (int s = 0; s < 4; ++s) \
        kacc[t] = __builtin_amdgcn_mfma_f32_16x16x32_f16(KG_FRAG(s), qf[s], kacc[t], 0, 0, 0); } while (0)
      {
        const float a = (m ? av[1] : av[0]) - amax, b = m ? bv[1] : bv[0];
        f32x2 w = m ? wq[1] : wq[0];
        w.x *= wsc; w.y *= -wsc;
        f32x2 e[16];
        {
          const f32x2 z16 = cexp_l(a, b, 16.f);
          e[0] = (f32x2){1.f, 0.f}; e[1] = z16; e[2] = cmul(z16, z16); e[3] = cmul(e[2], z16);
          const f32x2 z64 = cmul(e[2], e[2]);
#pragma unroll
          for (int c = 4; c < 16; ++c) e[c] = cmul(e[c - 4], z64);
          put16(e);
        }
        f16x8 qf[4];
#pragma unroll
        for (int s = 0; s < 4; ++s) qf[s] = KG_FRAG(s);
        const f32x2 z1 = cexp_l(a, -b, 1.f);
        const f32x2 z2 = cmul(z1, z1), z4 = cmul(z2, z2);
        e[0] = w; e[1] = cmul(w, z1); e[2] = cmul(w, z2); e[3] = cmul(e[1], z2);
#pragma unroll
        for (int c = 4; c < 16; ++c) e[c] = cmul(e[c - 4], z4);
        put16(e);
        KG_MMA(0);
        const f32x2 z256 = cexp_l(a, -b, 256.f);
        const f32x2 z512 = cmul(z256, z256);
        const f32x2 w2 = cmul(w, z512);
        e[0] = cmul(w, z256); e[1] = cmul(e[0], z2); e[8] = w2; e[12] = cmul(e[0], z512);
        const f32x2 w3 = e[12];
#pragma unroll
        for (int i = 1; i < 4; ++i) {
          e[2 * i] = cmul(e[2 * i - 2], z4); e[2 * i + 1] = cmul(e[2 * i - 1], z4);
          e[8 + i] = cmul(e[7 + i], z4); e[12 + i] = cmul(e[11 + i], z4);
        }
        put16(e);
        KG_MMA(1);
        const f32x2 z8 = cmul(z4, z4);
        e[0] = cmul(w2, z512); e[2] = cmul(w3, z512); e[4] = cmul(e[0], z512); e[6] = cmul(e[2], z512);
        e[8] = cmul(e[4], z512);
#pragma unroll
        for (int i = 0; i < 4; ++i) e[2 * i + 1] = cmul(e[2 * i], z8);
#pragma unroll
        for (int i = 9; i < 16; ++i) e[i] = (f32x2){0.f, 0.f};
        put16(e);
        KG_MMA(2);
      }
#pragma unroll
      for (int t = 0; t < 3; ++t) *(LAS f32x4*)(lds + (m ? KF + hc * CV_KSTR : KGS + hc * 4096) + t * 1024 + lane * 16) = kacc[t];
      if (m) *(LAS f32x2*)(lds + SY_KG + hc * 8) = (f32x2){g2, (float)we};
    }
    }
    f32x16 acc[7];
    f32x4 csa[2];
    unsigned tag;
    {
      const int G = lane >> 4;
      const LAS char* wa0 = lds + (G & 1) * W_UPOFF + (8 * (G >> 1) + ((lane & 15) >> 2)) * 224 + (lane & 3) * 8;
      const LAS char* wa[7];
#pragma unroll
      for (int T = 0; T < 7; ++T) wa[T] = wa0 + 32 * ((T + 2 * wv) % 7);
      const LAS char* xa[4];
      {
        const int row = 32 * wv + (lane & 31), sw = (row >> 1) & 7;
#pragma unroll
        for (int ks = 0; ks < 4; ++ks) xa[ks] = lds + XOFF + row * 128 + (((2 * ks + (lane >> 5)) ^ sw) << 4);
      }
      constexpr int PD = 3;
      bf16x8 Af[196], Bf[32];
      const unsigned o1 = ((lane & 15) == 4 * ((lane >> 4) & 1)) ? 0x3f803f80u : 0u;
      u32x4 onesq = {o1, o1, o1, o1};
      PIN(onesq);
      const bf16x8 ones = __builtin_bit_cast(bf16x8, onesq);
#define A_READ(i, T) __builtin_bit_cast(bf16x8, cat8(trrd(wa[T] + (((i) >> 2) % 3) * STG + (16 * ((i) & 3)) * 224), \
                                                      trrd(wa[T] + (((i) >> 2) % 3) * STG + (16 * ((i) & 3) + 4) * 224)))
#define B_READ(i) (*(const LAS bf16x8*)(xa[(i) & 3] + (((i) >> 2) % 3) * STG))
#pragma unroll
      for (int T = 0; T < 7; ++T) acc[T] = (f32x16){};
      csa[0] = (f32x4){0.f, 0.f, 0.f, 0.f}; csa[1] = csa[0];
      BARX(bar_lgkm);
      STAMP(1);
      tag = lds_get1(ldsb + SY_VAL);
      Bf[0] = B_READ(0);
#pragma unroll
      for (int T = 0; T < PD; ++T) Af[T] = A_READ(0, T);
      bf16x8 Al[7][4];
      u32x2 stg;
      float cs[7];
      static_for<0, 196>([&](auto nc) {
        constexpr int n = decltype(nc)::value, i = n / 7, T = n % 7;
        constexpr int m = n + PD, im = m / 7, Tm = m % 7;
        if constexpr (n == 70) ld64c(stg, (unsigned)((lane & 31) * 8), r_fl, FL_STAT + (unsigned)((mt * 128 + wv * 32) * 8));
        if constexpr (n == 27 * 7 + 2) {
          if (lane < 32) {
#pragma unroll
            for (int j = 0; j < 2; ++j) *(LAS float*)(lds + CSUM + ((j + 2 * wv) % 7) * 128 + lane * 4) = csa[j][0];
          }
        }
        if constexpr (Tm == 0 && (im & 3) == 0) BARX(bar_lgkm);
        if constexpr (m < 196) {
          if constexpr (Tm == 0) Bf[im] = B_READ(im);
          Af[m] = A_READ(im, Tm);
        } else if constexpr (m == 196) {
#pragma unroll
          for (int k = 28; k < 32; ++k) Bf[k] = B_READ(k);
        } else if constexpr (m == 197) {
#pragma unroll
          for (int t = 0; t < 7; ++t) cs[t] = *(const LAS float*)(lds + CSUM + ((t + 2 * wv) % 7) * 128 + (lane & 31) * 4);
        } else {
#pragma unroll
          for (int k = 0; k < 4; ++k) Al[0][k] = A_READ(28 + k, 0);
        }
        acc[T] = __builtin_amdgcn_mfma_f32_32x32x16_bf16(Af[n], Bf[i], acc[T], 0, 0, 0);
        if constexpr (T < 2)
          csa[T] = __builtin_amdgcn_mfma_f32_16x16x32_bf16(ones, Af[n], csa[T], 0, 0, 0);
        SCHED_FENCE();
      });
      STAMP(5);
      wait_vm<0>();
      PIN(stg);
      if (__builtin_expect(!wave_all(stg[1] == tag), 0)) {
#pragma unroll 1
        for (int it = 0; it < (1 << 18); ++it) {
          ld64c(stg, (unsigned)((lane & 31) * 8), r_fl, FL_STAT + (unsigned)((mt * 128 + wv * 32) * 8));
          wait_vm<0>();
          PIN(stg);
          if (wave_all(stg[1] == tag)) break;
          __builtin_amdgcn_s_sleep(2);
        }
      }
      const unsigned stw = stg[0];
      const float mb = lo_f(stw), sd = hi_f(stw);
#pragma unroll
      for (int k = 28; k < 32; ++k) {
        u32x4 v = __builtin_bit_cast(u32x4, Bf[k]);
#pragma unroll
        for (int j = 0; j < 4; ++j) v[j] = pk_bf16(lo_f(v[j]) - mb, hi_f(v[j]) - mb);
        Bf[k] = __builtin_bit_cast(bf16x8, v);
      }
      const unsigned nm = (stw & 0xffffu) ^ 0x8000u;
      u32x4 bq = {nm | (nm << 16), nm, 0u, 0u};
      if (lane >= 32) bq = (u32x4){0u, 0u, 0u, 0u};
      const float ga = -1.4426950408889634f * __builtin_amdgcn_rcpf(sd);
      const unsigned hsb = (unsigned)(((mt * 4 + wv) * 112 + ft * 7) * 1024);
      STAMP(6);
      static_for<0, 8>([&](auto tc) {
        constexpr int T = decltype(tc)::value;
        u32x4 o;
        if constexpr (T < 7) {
          const float c = cs[T];
          const float ch = rbf(c), cm = rbf(c - ch), cl = (c - ch) - cm;
          const u32x4 aq = {pk_bf16(ch, cm), pk_bf16(cl, 0.f), 0u, 0u};
          acc[T] = __builtin_amdgcn_mfma_f32_32x32x16_bf16(__builtin_bit_cast(bf16x8, aq), __builtin_bit_cast(bf16x8, bq), acc[T], 0, 0, 0);
          SCHED_FENCE();
        }
#pragma unroll
        for (int j = 0; j < 4; ++j) {
          if constexpr (T + 1 < 7) Al[T + 1][j] = A_READ(28 + j, T + 1);
          if constexpr (T < 7) acc[T] = __builtin_amdgcn_mfma_f32_32x32x16_bf16(Al[T][j], Bf[28 + j], acc[T], 0, 0, 0);
          SCHED_FENCE();
          if constexpr (T >= 1) {
            const float s0 = __builtin_amdgcn_rcpf(fmaf(__builtin_amdgcn_exp2f(acc[T - 1][2 * j] * ga), sd, sd));
            const float s1 = __builtin_amdgcn_rcpf(fmaf(__builtin_amdgcn_exp2f(acc[T - 1][2 * j + 1] * ga), sd, sd));
            o[j] = pk_bf16(s0 * acc[T - 1][8 + 2 * j], s1 * acc[T - 1][8 + 2 * j + 1]);
            PIN(o[j]);
          }
          SCHED_FENCE();
        }
        if constexpr (T >= 1) st128c(o, lane * 16u, r_h, hsb + (unsigned)((T - 1 + 2 * wv) % 7) * 1024u);
      });
    }
    STAMP(7);
    const unsigned ldw = ldsb + SY_LD;
    const LAS char* waT[2];
    {
      const int G = lane >> 4, q4 = (lane & 15) >> 2;
      const LAS char* wa0 = lds + wv * 2048 + (4 * (G >> 1) + q4) * 128 + (G & 1) * 32 + (lane & 3) * 8;
      waT[0] = wa0 + ((q4 >> 1) * 64); waT[1] = wa0 + (((q4 >> 1) ^ 1) * 64);
    }
#define D_READ(i, T) __builtin_bit_cast(bf16x8, cat8(trrd(waT[T] + g2_base(i)), trrd(waT[T] + g2_base(i) + 1024)))
    bf16x8 Wr[28][2];
    lds_wait<1>(ldw, 2u);
    static_for<0, 5>([&](auto ic) { constexpr int i = decltype(ic)::value; Wr[i][0] = D_READ(i, 0); Wr[i][1] = D_READ(i, 1); });
    static_for<5, 10>([&](auto ic) { constexpr int i = decltype(ic)::value; Wr[i][0] = D_READ(i, 0); Wr[i][1] = D_READ(i, 1); });
    wait_vm<0>();
    if (lane < 8) set_flag(ws, FL_H + (unsigned)(((mt * 2 + (wv >> 1)) * 8 + lane) * 256 + (ft * 2 + (wv & 1)) * 4), tag);
    STAMP(8);
    lds_setv(ldsb + SY_MC + wv * 4, 2u);
    lds_wait<1>(ldw, 3u);
    static_for<10, G2_NR>([&](auto ic) { constexpr int i = decltype(ic)::value; Wr[i][0] = D_READ(i, 0); Wr[i][1] = D_READ(i, 1); });
    lds_setv(ldsb + SY_MC + wv * 4, 3u);
    STAMP(9);
    __builtin_amdgcn_s_setprio(0);
    poll_flags<2>((lane & 1) * 16u, r_fl, FL_H + (unsigned)((tt * 8 + dtile) * 256 + wv * 32), tag);
    __builtin_amdgcn_s_setprio(2);
    STAMP(10);
    u32x4 hb[28][2];
#define HB_LOAD(i) do { ld128c(hb[i][0], lane * 16u, r_h, ((unsigned)(tt * 2) * 112u + G2_KS(wv, i)) * 1024u); \
                        ld128c(hb[i][1], lane * 16u, r_h, ((unsigned)(tt * 2 + 1) * 112u + G2_KS(wv, i)) * 1024u); } while (0)
#pragma unroll
    for (int i = 0; i < G2_H0; ++i) HB_LOAD(i);
    f32x16 acc2[2][2];
    {
#pragma unroll
      for (int a = 0; a < 2; ++a)
#pragma unroll
        for (int b = 0; b < 2; ++b) acc2[a][b] = (f32x16){};
      static_for<0, 28>([&](auto ic) {
        constexpr int i = decltype(ic)::value;
        if constexpr (i == 14) STAMP(11);
        if constexpr (i + 2 == 15) lds_wait<1>(ldw, 6u);
        if constexpr (i + 2 >= G2_NR && i + 2 < 28) { Wr[i + 2][0] = D_READ(i + 2, 0); Wr[i + 2][1] = D_READ(i + 2, 1); }
        wait_vm<2 * (g2_issued(i) - i - 1)>();
        PIN(hb[i][0]); PIN(hb[i][1]);
#pragma unroll
        for (int T = 0; T < 2; ++T)
#pragma unroll
          for (int b = 0; b < 2; ++b)
            acc2[T][b] = __builtin_amdgcn_mfma_f32_32x32x16_bf16(Wr[i][T], __builtin_bit_cast(bf16x8, hb[i][b]), acc2[T][b], 0, 0, 0);
        SCHED_FENCE();
        static_for<g2_issued(i), g2_issued(i + 1)>([&](auto jc) { constexpr int j = decltype(jc)::value; HB_LOAD(j); });
      });
    }
    STAMP(12);
    bar_lgkm();
    {
      LAS char* rp = lds + wv * (64 * G2_RED) + (lane & 31) * G2_RED + (lane >> 5) * 16;
#pragma unroll
      for (int T = 0; T < 2; ++T)
#pragma unroll
        for (int b = 0; b < 2; ++b)
#pragma unroll
          for (int g = 0; g < 4; ++g) {
            const f32x4 v = {acc2[T][b][4 * g], acc2[T][b][4 * g + 1], acc2[T][b][4 * g + 2], acc2[T][b][4 * g + 3]};
            *(LAS f32x4*)(rp + b * 32 * G2_RED + T * 128 + g * 32) = v;
          }
    }
  } else if (w8 == 4) {
    __builtin_amdgcn_s_setprio(3);
    const rsrc_t r_wg = mk_rsrc(wg, DM * FF * 2), r_wu = mk_rsrc(wu, DM * FF * 2);
    const rsrc_t r_wd = mk_rsrc(wd, FF * DM * 2);
    unsigned woff[7], xoff[4], wv4[4];
#pragma unroll
    for (int i = 0; i < 4; ++i) {
      xoff[i] = (unsigned)((lane >> 3) * 1024 + (((lane & 7) ^ ((lane >> 4) + 4 * (i & 1))) * 16) + i * 7168);
      wv4[i] = (unsigned)((lane >> 3) * 1024 + (((lane & 7) ^ (((lane >> 4) & 1) << 2)) * 16) + (i >> 1) * 458752 + (i & 1) * 8192 - i * 1024);
    }
#pragma unroll
    for (int i = 0; i < 7; ++i) {
      const int c = i * 64 + lane, row = c / 14, col = c - row * 14;
      woff[i] = (unsigned)(row * (FF * 2) + col * 16 - (i & 3) * 1024);
    }
    const unsigned ldw = ldsb + SY_LD;
    auto g1_stage = [&](int s) __attribute__((always_inline)) {
      const unsigned st = (unsigned)((s + rot) & 7), lb = ldsb + (unsigned)((s % 3) * STG);
#pragma unroll
      for (int i = 0; i < 4; ++i)
        dma16x4(lb + XOFF + i * 4096, xoff[0], xoff[1], xoff[2], xoff[3], r_x, (unsigned)((mt * 128 + i * 32) * 1024) + st * 128u);
#pragma unroll
      for (int h = 0; h < 2; ++h) {
        const unsigned wso = (unsigned)(ft * 224) + (st * 64u + h * 32u) * (FF * 2);
        dma16x4(lb + h * 7168, woff[0], woff[1], woff[2], woff[3], r_wg, wso);
        dma16x3(lb + h * 7168 + 4096, woff[4], woff[5], woff[6], r_wg, wso);
        dma16x4(lb + W_UPOFF + h * 7168, woff[0], woff[1], woff[2], woff[3], r_wu, wso);
        dma16x3(lb + W_UPOFF + h * 7168 + 4096, woff[4], woff[5], woff[6], r_wu, wso);
      }
    };
#define G2_GROUP(g) static_for<5 * (g), cmin(5 * (g) + 5, 28)>([&](auto ic) { constexpr int i = decltype(ic)::value; \
        const unsigned so_ = G2_KS(0, i) * 16384u + (unsigned)(dtile * 128); \
        dma16x4(ldsb + g2_base(i), wv4[0], wv4[1], wv4[2], wv4[3], r_wd, so_); \
        dma16x4(ldsb + g2_base(i) + 4096, wv4[0], wv4[1], wv4[2], wv4[3], r_wd, so_ + 2u * 458752u); })
    bar_only();
    g1_stage(0);
    g1_stage(1);
    wait_vm<44>();
    STAMP(1);
    BARX(bar_only);
#pragma unroll
    for (int k = 0; k < 6; ++k) {
      g1_stage(k + 2);
      wait_vm<44>();
      BARX(bar_only);
    }
    STAMP(2);
    G2_GROUP(0);
    wait_vm<40>();
    BARX(bar_only);
    G2_GROUP(1);
    wait_vm<0>();
    lds_setv(ldw, 2u);
    STAMP(3);
    lds_wait<4>(ldsb + SY_MC, 2u);
    G2_GROUP(2);
    G2_GROUP(3);
    wait_vm<40>();
    lds_setv(ldw, 3u);
    STAMP(4);
    G2_GROUP(4);
    lds_wait<4>(ldsb + SY_MC, 3u);
    G2_GROUP(5);
    wait_vm<0>();
    lds_setv(ldw, 6u);
    STAMP(5);
    __builtin_amdgcn_s_setprio(0);
    EPI_X();
    lds_wait<1>(ldsb + SY_SR, 1u);
    EPI_S();
    STAMP(12);
    bar_lgkm();
  } else if (w8 == 5) {
    bar_only();
    BARX(bar_only);
    const unsigned tag = lds_get1(ldsb + SY_VAL);
    BARX(bar_only);
    {
      const int c = lane & 15, g = lane >> 4;
      unsigned d[2][3][4];
      float kscale[2];
#pragma unroll
      for (int hc = 0; hc < 2; ++hc) {
        f32x4 kacc[3];
#pragma unroll
        for (int t = 0; t < 3; ++t)
          kacc[t] = *(const LAS f32x4*)(lds + KF + hc * CV_KSTR + t * 1024 + lane * 16) + *(const LAS f32x4*)(lds + KGS + hc * 4096 + t * 1024 + lane * 16);
        const f32x2 gw = *(const LAS f32x2*)(lds + SY_KG + hc * 8);
        const float g2 = gw.x;
        const int we = (int)gw.y;
        if (g2 > 0.f) {
#pragma unroll
          for (int i = 0; i < 4; ++i) {
            const int l0 = 16 * c + 4 * g + i;
            const int l1 = g < 2 ? 256 + 16 * c + 8 * g + 2 * i : 512 + 256 * (g - 2) + 16 * c + 4 * i;
            const int l2 = g < 2 ? 1024 + 256 * (2 * g + (i >> 1)) + 8 * (i & 1) + 16 * c : 2048;
            kacc[0][i] *= __builtin_amdgcn_exp2f(g2 * (float)(l0 - 2048));
            kacc[1][i] *= __builtin_amdgcn_exp2f(g2 * (float)(l1 - 2048));
            kacc[2][i] *= __builtin_amdgcn_exp2f(g2 * (float)(l2 - 2048));
          }
        }
        kscale[hc] = __builtin_amdgcn_ldexpf(1.0f, we - 7) * __builtin_amdgcn_exp2f(g2 * 2048.f);
#pragma unroll
        for (int t = 0; t < 3; ++t)
#pragma unroll
          for (int i = 0; i < 4; ++i) { d[hc][t][i] = pk_f16(kacc[t][i] * 128.f, kacc[t][i] * 128.f); PIN(d[hc][t][i]); }
        BARX(bar_only);
      }
      BARX(bar_only);
      BARX(bar_only);
      BARX(bar_only);
      BARX(bar_only);
      __builtin_amdgcn_s_sleep(WSL);
      __builtin_amdgcn_s_setprio(3);
      STAMP(2);
      *(LAS f32x2*)(lds + SY_VAL + 8) = (f32x2){kscale[0], kscale[1]};
#define MIX(a, b) (((a) & 0xffffu) | ((b) & 0xffff0000u))
#pragma unroll
      for (int hc = 0; hc < 2; ++hc) {
        LAS char* kf = lds + KF + hc * CV_KSTR + 96;
        if (g < 2) {
#pragma unroll
          for (int h = 0; h < 2; ++h) {
            LAS char* p = kf + 2 * (1024 + 256 * (2 * g + h) + 16 * c);
            const unsigned v0 = d[hc][2][2 * h], v1 = d[hc][2][2 * h + 1];
            *(LAS u32x2*)(p - 8) = (u32x2){v0, v0};
            *(LAS u32x4*)(p) = (u32x4){v0, v0, MIX(v0, v1), v1};
            *(LAS u32x2*)(p + 16) = (u32x2){v1, v1};
          }
        } else {
          if (lane == 32) *(LAS u32x2*)(kf + 2 * 2044) = (u32x2){d[hc][2][0], d[hc][2][0]};
        }
        if (g >= 2) {
          LAS char* p = kf + 2 * (512 + 256 * (g - 2) + 16 * c);
          const unsigned v0 = d[hc][1][0], v1 = d[hc][1][1], v2 = d[hc][1][2], v3 = d[hc][1][3];
          *(LAS unsigned*)(p - 4) = v0;
          *(LAS u32x4*)(p) = (u32x4){v0, MIX(v0, v1), v1, v2};
          *(LAS u32x2*)(p + 16) = (u32x2){v2, MIX(v2, v3)};
          *(LAS unsigned*)(p + 24) = v3;
        }
        if (g < 2) {
          LAS char* p = kf + 2 * (256 + 16 * c + 8 * g);
          const unsigned v0 = d[hc][1][0], v1 = d[hc][1][1], v2 = d[hc][1][2], v3 = d[hc][1][3];
          *(LAS unsigned short*)(p - 2) = (unsigned short)v0;
          *(LAS u32x2*)(p) = (u32x2){v0, MIX(v1, v2)};
          *(LAS unsigned*)(p + 8) = v2;
          *(LAS unsigned short*)(p + 12) = (unsigned short)v3;
        }
        *(LAS u32x2*)(kf + 2 * (16 * c + 4 * g)) = (u32x2){MIX(d[hc][0][0], d[hc][0][1]), MIX(d[hc][0][2], d[hc][0][3])};
        if (lane < 6) *(LAS u32x4*)(kf - 96 + lane * 16) = (u32x4){0u, 0u, 0u, 0u};
      }
      lds_setv(ldsb + SY_UR + 8, 1u);
      STAMP(3);
    }
    __builtin_amdgcn_s_setprio(1);
    lds_wait<4>(ldsb + SY_UR, 1u);
#pragma unroll 1
    for (int hc = 0; hc < 2; ++hc) {
      const float kscale = *(const LAS float*)(lds + SY_VAL + 8 + hc * 4);
      const unsigned dsk2 = dsk2p[cp];
      const float dskip = hc ? hi_f(dsk2) : lo_f(dsk2);
      const int G = lane >> 4, q4 = (lane & 15) >> 2, p = lane & 3;
      const LAS char* pa = lds + KF + hc * CV_KSTR + 96 + 2 * (76 + 16 * p - 32 * G - 4 * q4 - 16);
      const LAS char* pb = lds + CV_U + hc * CV_USTR + 160 + 2 * (-76 + 32 * G + 4 * q4 + 4 * p);
      const LAS char* pu = lds + CV_U + hc * CV_USTR + 160 + lane * 2;
      constexpr int J0 = 2 * CV_NP - 1;
      f16x8 A[16], B[32];
      f32x4 ca[32 - J0];
#define CV_A(q) __builtin_bit_cast(f16x8, cat8(trrd(pa + 256 * (q) + 32), trrd(pa + 256 * (q))))
#define CV_B(w) __builtin_bit_cast(f16x8, cat8(trrd(pb + 128 * (w)), trrd(pb + 128 * (w) + 32)))
      B[0] = CV_B(0); B[1] = CV_B(1);
      constexpr int QA = (J0 - 1) / 2;
#pragma unroll
      for (int q = 15; q >= QA; --q) A[q] = CV_A(q);
#pragma unroll
      for (int j = 0; j < 32 - J0; ++j) ca[j] = (f32x4){0.f, 0.f, 0.f, 0.f};
      static_for<0, 32>([&](auto wc) {
        constexpr int w = decltype(wc)::value;
        if constexpr (w + 2 < 32) B[w + 2] = CV_B(w + 2);
        if constexpr ((w & 1) == 0 && QA - 1 - w / 2 >= 0) A[QA - 1 - w / 2] = CV_A(QA - 1 - w / 2);
#pragma unroll
        for (int q = 15; q >= 0; --q) {
          const int j = w + 2 * q;
          if (j >= J0 && j < 32) ca[j - J0] = __builtin_amdgcn_mfma_f32_16x16x32_f16(A[q], B[w], ca[j - J0], 0, 0, 0);
        }
        SCHED_FENCE();
      });
      float c3 = 0.f, c2 = 0.f, c1 = 0.f, yv[32 - J0];
#pragma unroll
      for (int j = 0; j < 32 - J0; ++j) {
        const f32x4 a = ca[j];
        const float h3 = a[3];
        const float h2 = a[2] + shr_carry(c3, h3);
        const float h1 = a[1] + shr_carry(c2, h2);
        yv[j] = a[0] + shr_carry(c1, h1);
        c3 = dpp_mov<0x13C>(h3); c2 = dpp_mov<0x13C>(h2); c1 = dpp_mov<0x13C>(h1);
      }
#pragma unroll
      for (int jp = CV_NP; jp < 16; ++jp) {
        const float u0 = (float)*(const LAS _Float16*)(pu + jp * 256), u1 = (float)*(const LAS _Float16*)(pu + jp * 256 + 128);
        const unsigned yq = pk_bf16(yv[2 * jp - J0] * kscale, yv[2 * jp + 1 - J0] * kscale);
        const unsigned uq = pk_bf16(u0 * dskip, u1 * dskip);
        st32c(pk_bf16(lo_f(yq) + lo_f(uq), hi_f(yq) + hi_f(uq)), lane * 4u, r_s, (unsigned)((cp * 2 + hc) * 4096 + jp * 256));
      }
#undef CV_A
#undef CV_B
    }
    STAMP(4);
    publish<3>(lds, 2, ws, FL_S + (unsigned)((lane * 8 + (cp >> 5)) * 256 + (cp & 31) * 4), 32, tag, lane);
    STAMP(5);
    EPI_X();
    lds_wait<1>(ldsb + SY_CNT + 8, 3u);
    poll_flags<4>((lane & 7) * 16u, r_fl, FL_S + (unsigned)((tt * 8 + dtile) * 256), tag);
    lds_setv(ldsb + SY_SR, 1u);
    STAMP(6);
    EPI_S();
    STAMP(12);
    bar_lgkm();
  } else {
    const int hc = wv - 2;
    const int dch = cp * 2 + hc;
    const rsrc_t r_xn = mk_rsrc(ws + WS_XNF, SL * DM * 2);
    u32x4 xr[4];
    unsigned ep;
    const int row0 = bid * 8 + hc * 4;
    {
      const unsigned vo = (unsigned)((lane >> 4) * 1024 + (lane & 15) * 16);
#pragma unroll
      for (int i = 0; i < 4; ++i) ld128(xr[i], vo, r_x, (unsigned)(row0 * 1024 + i * 256));
      ld32c(ep, 0u * lane, r_fl, FL_EP);
    }
    bar_only();
    const unsigned dsk2 = dsk2p[cp];
    wait_vm<0>();
    STAMP(1);
#pragma unroll
    for (int i = 0; i < 4; ++i) PIN(xr[i]);
    PIN(ep);
    const unsigned tag = (unsigned)__builtin_amdgcn_readfirstlane(ep) + 1u;
    lds_setv(ldsb + SY_VAL, tag);
    {
      float f[32];
#pragma unroll
      for (int j = 0; j < 16; ++j) { f[2 * j] = lo_f(xr[j >> 2][j & 3]); f[2 * j + 1] = hi_f(xr[j >> 2][j & 3]); }
      float s0 = 0.f, s1 = 0.f;
#pragma unroll
      for (int j = 0; j < 32; j += 4) { s0 += f[j] + f[j + 1]; s1 += f[j + 2] + f[j + 3]; }
      const float mean = row_sum(s0 + s1) * (1.0f / DM);
      float q0 = 0.f, q1 = 0.f;
#pragma unroll
      for (int j = 0; j < 32; j += 2) {
        const float da = f[j] - mean, db = f[j + 1] - mean;
        q0 = fmaf(da, da, q0); q1 = fmaf(db, db, q1);
      }
      const float var = row_sum(q0 + q1) * (1.0f / DM);
      const float mb = rbf(mean);
      const float sb = rbf(__builtin_amdgcn_sqrtf(rbf(rbf(var) + 1e-5f)));
      if ((lane & 15) == 0)
        st64c((u32x2){pk_bf16(mb, sb), tag}, (unsigned)((lane >> 4) * 8), r_fl, FL_STAT + (unsigned)(row0 * 8));
      const float rs = __builtin_amdgcn_rcpf(sb);
      const unsigned vo = (unsigned)((lane & 15) * 32768 + (lane >> 4) * 16);
      STAMP(2);
#pragma unroll
      for (int i = 0; i < 4; ++i) {
        u32x4 o;
#pragma unroll
        for (int j = 0; j < 4; ++j) {
          const unsigned d = pk_bf16(f[8 * i + 2 * j] - mb, f[8 * i + 2 * j + 1] - mb);
          o[j] = pk_bf16(lo_f(d) * rs, hi_f(d) * rs);
        }
        st128c(o, vo, r_xn, (unsigned)(i * 16 * 32768 + row0 * 16));
      }
    }
    BARX(bar_lgkm);
    wait_vm<0>();
    BARX(bar_only);
    if (hc == 0 && lane < 8) set_flag(ws, FL_ALL + (unsigned)(lane * 4096 + bid * 4), tag);
    BARX(bar_only);
    BARX(bar_only);
    u32x4 vf;
    ld128c(vf, lane * 16u, r_fl, FL_ALL + (unsigned)(xcd * 4096));
    BARX(bar_only);
    wait_vm<0>();
    PIN(vf);
    const bool all_ok = wave_all((vf[0] == tag) & (vf[1] == tag) & (vf[2] == tag) & (vf[3] == tag));
    unsigned uw[16];
    const unsigned usb = (unsigned)((cp >> 2) * 32768 + (cp & 3) * 4 + hc * 16384);
    if (!all_ok) {
      for (int r = 5; r <= 7; ++r) BARX(bar_only);
      poll_flags<4>(lane * 16u, r_fl, FL_ALL + (unsigned)(xcd * 4096), tag);
    }
#pragma unroll
    for (int m = 0; m < 16; ++m) ld32c(uw[m], lane * 16u, r_xn, usb + (unsigned)m * 1024u);
    if (lane == 0 && bid == 0 && hc == 0) set_flag(ws, FL_EP, tag);
    if (all_ok) { BARX(bar_only); BARX(bar_only); }
    wait_vm<0>();
    {
      const bool od = lane & 1;
      LAS char* up = lds + CV_U + 160 + hc * 2048 + (od ? 128 : 0) + (lane >> 1) * 4;
#pragma unroll
      for (int m = 0; m < 16; m += 2) {
        PIN(uw[m]); PIN(uw[m + 1]);
        const unsigned a = pk_f16(lo_f(uw[m]), hi_f(uw[m])), b = pk_f16(lo_f(uw[m + 1]), hi_f(uw[m + 1]));
        const unsigned y = (unsigned)__builtin_amdgcn_update_dpp(0, (int)(od ? a : b), 0xB1, 0xf, 0xf, true);
        const unsigned f0 = od ? y : a, f1 = od ? b : y;
        uw[m] = __builtin_amdgcn_perm(f1, f0, 0x05040100u);
        uw[m + 1] = __builtin_amdgcn_perm(f1, f0, 0x07060302u);
        PIN(uw[m]); PIN(uw[m + 1]);
      }
      if (all_ok) BARX(bar_only);
      __builtin_amdgcn_s_sleep(WSL);
      __builtin_amdgcn_s_setprio(3);
      if (lane < 10) *(LAS u32x4*)(lds + CV_U + hc * CV_USTR + lane * 16) = (u32x4){0u, 0u, 0u, 0u};
#pragma unroll
      for (int m = 0; m < 16; m += 2) {
        *(LAS unsigned*)(up + m * 128) = uw[m];
        *(LAS unsigned*)(up + CV_USTR + m * 128) = uw[m + 1];
      }
      lds_setv(ldsb + SY_UR + hc * 4, 1u);
    }
    STAMP(3);
    __builtin_amdgcn_s_setprio(1);
    lds_wait<4>(ldsb + SY_UR, 1u);
    const float kscale = *(const LAS float*)(lds + SY_VAL + 8 + hc * 4);
    STAMP(6);
    {
      const float dskip = hc ? hi_f(dsk2) : lo_f(dsk2);
      const int G = lane >> 4, q4 = (lane & 15) >> 2, p = lane & 3;
      const LAS char* pa = lds + KF + hc * CV_KSTR + 96 + 2 * (76 + 16 * p - 32 * G - 4 * q4 - 16);
      const LAS char* pb = lds + CV_U + hc * CV_USTR + 160 + 2 * (-76 + 32 * G + 4 * q4 + 4 * p);
      const LAS char* pu = lds + CV_U + hc * CV_USTR + 160 + lane * 2;
      f16x8 A[16], B[32];
#define CV_A(q) __builtin_bit_cast(f16x8, cat8(trrd(pa + 256 * (q) + 32), trrd(pa + 256 * (q))))
#define CV_B(w) __builtin_bit_cast(f16x8, cat8(trrd(pb + 128 * (w)), trrd(pb + 128 * (w) + 32)))
      A[0] = CV_A(0); B[0] = CV_B(0); B[1] = CV_B(1);
      A[1] = CV_A(1); B[2] = CV_B(2); B[3] = CV_B(3);
      f32x4 ca[2][2];
      _Float16 us[2][2];
      unsigned so16[16];
      float c3 = 0.f, c2 = 0.f, c1 = 0.f;
      auto tile_y = [&](const f32x4 a) __attribute__((always_inline)) {
        const float h3 = a[3];
        const float h2 = a[2] + shr_carry(c3, h3);
        const float h1 = a[1] + shr_carry(c2, h2);
        const float y = a[0] + shr_carry(c1, h1);
        c3 = dpp_mov<0x13C>(h3); c2 = dpp_mov<0x13C>(h2); c1 = dpp_mov<0x13C>(h1);
        return y * kscale;
      };
      static_for<0, CV_NP + 1>([&](auto jc) {
        constexpr int jj = decltype(jc)::value;
        if constexpr (jj + 2 < CV_NP) { A[jj + 2] = CV_A(jj + 2); B[2 * jj + 4] = CV_B(2 * jj + 4); B[2 * jj + 5] = CV_B(2 * jj + 5); }
        if constexpr (jj < CV_NP) {
          us[jj & 1][0] = *(const LAS _Float16*)(pu + jj * 256); us[jj & 1][1] = *(const LAS _Float16*)(pu + jj * 256 + 128);
          f32x4 a0 = {0.f, 0.f, 0.f, 0.f}, a1 = {0.f, 0.f, 0.f, 0.f};
#pragma unroll
          for (int q = jj; q >= 0; --q) {
            a0 = __builtin_amdgcn_mfma_f32_16x16x32_f16(A[q], B[2 * (jj - q)], a0, 0, 0, 0);
            a1 = __builtin_amdgcn_mfma_f32_16x16x32_f16(A[q], B[2 * (jj - q) + 1], a1, 0, 0, 0);
          }
          ca[jj & 1][0] = a0; ca[jj & 1][1] = a1;
        }
        if constexpr (jj >= 1) {
          const float y0 = tile_y(ca[(jj - 1) & 1][0]), y1 = tile_y(ca[(jj - 1) & 1][1]);
          const unsigned yq = pk_bf16(y0, y1);
          const unsigned uq = pk_bf16((float)us[(jj - 1) & 1][0] * dskip, (float)us[(jj - 1) & 1][1] * dskip);
          so16[jj - 1] = pk_bf16(lo_f(yq) + lo_f(uq), hi_f(yq) + hi_f(uq));
        }
        if constexpr (jj >= 1 && jj < CV_NP) {
#pragma unroll
          for (int k = 0; k < 2 * (jj + 1); ++k) {
            __builtin_amdgcn_sched_group_barrier(0x8, 1, 0);
            __builtin_amdgcn_sched_group_barrier(0x100, 1, 0);
            __builtin_amdgcn_sched_group_barrier(0x2, jj < 7 ? 4 : 2, 0);
          }
        }
        SCHED_FENCE();
      });
      STAMP(8);
#pragma unroll
      for (int j = 0; j < CV_NP; ++j) st32c(so16[j], lane * 4u, r_s, (unsigned)(dch * 4096 + j * 256));
    }
    STAMP(7);
    publish<3>(lds, 2, ws, FL_S + (unsigned)((lane * 8 + (cp >> 5)) * 256 + (cp & 31) * 4), 32, tag, lane);
    STAMP(9);
    EPI_X();
    lds_wait<1>(ldsb + SY_SR, 1u);
    EPI_S();
    STAMP(12);
    bar_lgkm();
  }
  {
    int t2 = tid; PIN(t2);
    const u32x4 xv = *(const LAS u32x4*)(lds + EPX + t2 * 16);
    unsigned sv[8];
#pragma unroll
    for (int j = 0; j < 8; ++j) sv[j] = *(const LAS unsigned*)(lds + EPS + ((t2 & 7) * 8 + j) * 256 + (((t2 >> 5) + (t2 & 7)) & 15) * 16 + ((t2 >> 3) & 3) * 4);
    bar_lgkm();
    const LAS char* rp = lds + (t2 >> 3) * G2_RED + (t2 & 7) * 32;
    const bool odd = tt & 1;
    u32x4 o;
#pragma unroll
    for (int i = 0; i < 2; ++i) {
      f32x4 v = *(const LAS f32x4*)(rp + i * 16);
#pragma unroll
      for (int w = 1; w < 4; ++w) v += *(const LAS f32x4*)(rp + w * (64 * G2_RED) + i * 16);
#pragma unroll
      for (int jj = 0; jj < 2; ++jj) {
        const unsigned fp = pk_bf16(v[2 * jj], v[2 * jj + 1]);
        const float f0 = lo_f(fp), f1 = hi_f(fp);
        const float g0 = f0 * __builtin_amdgcn_rcpf(1.0f + __builtin_amdgcn_exp2f(f0 * -1.4426950408889634f));
        const float g1 = f1 * __builtin_amdgcn_rcpf(1.0f + __builtin_amdgcn_exp2f(f1 * -1.4426950408889634f));
        const unsigned gp = pk_bf16(g0, g1);
        const int j = 4 * i + 2 * jj;
        const float s0 = odd ? hi_f(sv[j]) : lo_f(sv[j]), s1 = odd ? hi_f(sv[j + 1]) : lo_f(sv[j + 1]);
        const unsigned sg = pk_bf16(s0 * lo_f(gp), s1 * hi_f(gp));
        const unsigned xw = xv[2 * i + jj];
        o[2 * i + jj] = pk_bf16(lo_f(xw) + lo_f(sg), hi_f(xw) + hi_f(sg));
      }
    }
    *(u32x4*)((char*)out + XO(t2)) = o;
  }
  STAMP(13); STAMP_RT(15);
  STAMP_OUT(0);
  BW_OUT();
}

extern "C" void kernel_launch(void* const* d_in, const int* in_sizes, int n_in, void* d_out,
                              int out_size, void* d_ws, size_t ws_size, hipStream_t stream) {
  (void)hipFuncSetAttribute((const void*)fused_kernel, hipFuncAttributeMaxDynamicSharedMemorySize, LDS_BYTES);
  fused_kernel<<<256, 512, LDS_BYTES, stream>>>(
      (const unsigned short*)d_in[0], (const unsigned short*)d_in[1], (const unsigned short*)d_in[2],
      (const unsigned short*)d_in[3], (const unsigned short*)d_in[4], (const unsigned short*)d_in[5],
      (const unsigned short*)d_in[6], (const unsigned*)d_in[7], (const unsigned*)d_in[8],
      (const unsigned short*)d_in[10], (const unsigned short*)d_in[11], (const unsigned short*)d_in[12],
      (char*)d_ws, (unsigned short*)d_out);
}
```

```cpp
#include <hip/hip_runtime.h>
#include <hip/hip_bf16.h>
#include <stdint.h>
#include <type_traits>

typedef __attribute__((ext_vector_type(4))) short s16x4;
typedef __attribute__((ext_vector_type(8))) short s16x8;
typedef __attribute__((ext_vector_type(8))) _Float16 f16x8;
typedef __attribute__((ext_vector_type(8))) __bf16 bf16x8;
typedef __attribute__((ext_vector_type(2))) float f32x2;
typedef __attribute__((ext_vector_type(4))) float f32x4;
typedef __attribute__((ext_vector_type(16))) float f32x16;
typedef __attribute__((ext_vector_type(4))) unsigned u32x4;
typedef __attribute__((ext_vector_type(3))) unsigned u32x3;
typedef __attribute__((ext_vector_type(2))) unsigned u32x2;
typedef __attribute__((ext_vector_type(2))) __bf16 bf16x2;
typedef __attribute__((ext_vector_type(2))) _Float16 f16x2;
#define LAS __attribute__((address_space(3)))
#define GAS __attribute__((address_space(1)))
typedef __amdgpu_buffer_rsrc_t rsrc_t;

constexpr int SL = 2048;
constexpr int DM = 512;
constexpr int NM = 128;
constexpr int FF = 1792;

constexpr size_t WS_XNF = 0;
constexpr size_t WS_ST = 2 * (size_t)SL * DM * 2;
constexpr size_t WS_HF = WS_ST + (size_t)SL * DM * 2;
constexpr size_t WS_FLAG = 16u << 20;
constexpr unsigned FL_ALL = 0, FL_STAT = 65536, FL_H = 131072, FL_S = 196608, FL_EP = 262144, FL_BYTES = 262400;

template <int I, int N, class F>
__device__ __forceinline__ void static_for(F&& f) {
  if constexpr (I < N) { f(std::integral_constant<int, I>{}); static_for<I + 1, N>(f); }
}

__device__ __forceinline__ unsigned pk_bf16(float lo, float hi) {
  f32x2 v = {lo, hi};
  return __builtin_bit_cast(unsigned, __builtin_convertvector(v, bf16x2));
}
__device__ __forceinline__ unsigned pk_f16(float lo, float hi) {
  f32x2 v = {lo, hi};
  return __builtin_bit_cast(unsigned, __builtin_convertvector(v, f16x2));
}
__device__ __forceinline__ float lo_f(unsigned p) { return __uint_as_float(p << 16); }
__device__ __forceinline__ float hi_f(unsigned p) { return __uint_as_float(p & 0xffff0000u); }
__device__ __forceinline__ float rbf(float x) { return lo_f(pk_bf16(x, x)); }

template <int CTRL>
__device__ __forceinline__ float dpp_mov(float v) {
  return __int_as_float(__builtin_amdgcn_update_dpp(0, __float_as_int(v), CTRL, 0xf, 0xf, true));
}
__device__ __forceinline__ float shr_carry(float c, float v) {
  return __builtin_bit_cast(float, __builtin_amdgcn_update_dpp(__builtin_bit_cast(int, c), __builtin_bit_cast(int, v), 0x138, 0xf, 0xf, false));
}
__device__ __forceinline__ float rdlane(float v, int l) {
  return __int_as_float(__builtin_amdgcn_readlane(__float_as_int(v), l));
}
__device__ __forceinline__ float wave_sum(float v) {
  v += dpp_mov<0xB1>(v);
  v += dpp_mov<0x4E>(v);
  v += dpp_mov<0x141>(v);
  v += dpp_mov<0x140>(v);
  return (rdlane(v, 0) + rdlane(v, 16)) + (rdlane(v, 32) + rdlane(v, 48));
}
__device__ __forceinline__ float row_sum(float v) {
  v += dpp_mov<0xB1>(v);
  v += dpp_mov<0x4E>(v);
  v += dpp_mov<0x141>(v);
  v += dpp_mov<0x140>(v);
  return v;
}
__device__ __forceinline__ float half_sum(float v, int lane) {
  v += dpp_mov<0xB1>(v);
  v += dpp_mov<0x4E>(v);
  v += dpp_mov<0x141>(v);
  v += dpp_mov<0x140>(v);
  const float lo = rdlane(v, 0) + rdlane(v, 16), hi = rdlane(v, 32) + rdlane(v, 48);
  return lane < 32 ? lo : hi;
}
__device__ __forceinline__ float wave_max(float v) {
  v = fmaxf(v, dpp_mov<0xB1>(v));
  v = fmaxf(v, dpp_mov<0x4E>(v));
  v = fmaxf(v, dpp_mov<0x141>(v));
  v = fmaxf(v, dpp_mov<0x140>(v));
  return fmaxf(fmaxf(rdlane(v, 0), rdlane(v, 16)), fmaxf(rdlane(v, 32), rdlane(v, 48)));
}

__device__ __forceinline__ rsrc_t mk_rsrc(const void* p, unsigned bytes) {
  return __builtin_amdgcn_make_buffer_rsrc((void*)p, (short)0, (int)bytes, 0x00020000);
}
__device__ __forceinline__ void dma16(unsigned lds_dst, unsigned voff, rsrc_t r, unsigned soff) {
  asm volatile("s_mov_b32 m0, %0\n\ts_nop 0\n\tbuffer_load_dwordx4 %1, %2, %3 offen lds"
               :: "s"(lds_dst), "v"(voff), "s"(r), "s"(soff) : "memory");
}
__device__ __forceinline__ void dma16x4(unsigned lds_dst, unsigned v0, unsigned v1, unsigned v2, unsigned v3, rsrc_t r, unsigned soff) {
  asm volatile("s_mov_b32 m0, %0\n\ts_nop 0\n\t"
               "buffer_load_dwordx4 %1, %5, %6 offen lds\n\t"
               "buffer_load_dwordx4 %2, %5, %6 offen offset:1024 lds\n\t"
               "buffer_load_dwordx4 %3, %5, %6 offen offset:2048 lds\n\t"
               "buffer_load_dwordx4 %4, %5, %6 offen offset:3072 lds"
               :: "s"(lds_dst), "v"(v0), "v"(v1), "v"(v2), "v"(v3), "s"(r), "s"(soff) : "memory");
}
__device__ __forceinline__ void dma16x3(unsigned lds_dst, unsigned v0, unsigned v1, unsigned v2, rsrc_t r, unsigned soff) {
  asm volatile("s_mov_b32 m0, %0\n\ts_nop 0\n\t"
               "buffer_load_dwordx4 %1, %4, %5 offen lds\n\t"
               "buffer_load_dwordx4 %2, %4, %5 offen offset:1024 lds\n\t"
               "buffer_load_dwordx4 %3, %4, %5 offen offset:2048 lds"
               :: "s"(lds_dst), "v"(v0), "v"(v1), "v"(v2), "s"(r), "s"(soff) : "memory");
}
__device__ __forceinline__ void ld128(u32x4& dst, unsigned voff, rsrc_t r, unsigned soff) {
  asm volatile("buffer_load_dwordx4 %0, %1, %2, %3 offen" : "=v"(dst) : "v"(voff), "s"(r), "s"(soff) : "memory");
}
__device__ __forceinline__ void ld128c(u32x4& dst, unsigned voff, rsrc_t r, unsigned soff) {
  asm volatile("buffer_load_dwordx4 %0, %1, %2, %3 offen sc1" : "=v"(dst) : "v"(voff), "s"(r), "s"(soff) : "memory");
}
__device__ __forceinline__ void ld32(unsigned& dst, unsigned voff, rsrc_t r, unsigned soff) {
  asm volatile("buffer_load_dword %0, %1, %2, %3 offen" : "=v"(dst) : "v"(voff), "s"(r), "s"(soff) : "memory");
}
__device__ __forceinline__ void ld64c(u32x2& dst, unsigned voff, rsrc_t r, unsigned soff) {
  asm volatile("buffer_load_dwordx2 %0, %1, %2, %3 offen sc1" : "=v"(dst) : "v"(voff), "s"(r), "s"(soff) : "memory");
}
__device__ __forceinline__ void ld32c(unsigned& dst, unsigned voff, rsrc_t r, unsigned soff) {
  asm volatile("buffer_load_dword %0, %1, %2, %3 offen sc1" : "=v"(dst) : "v"(voff), "s"(r), "s"(soff) : "memory");
}
__device__ __forceinline__ void st32c(unsigned v, unsigned voff, rsrc_t r, unsigned soff) {
  asm volatile("buffer_store_dword %0, %1, %2, %3 offen sc1" :: "v"(v), "v"(voff), "s"(r), "s"(soff) : "memory");
}
__device__ __forceinline__ void st64c(u32x2 v, unsigned voff, rsrc_t r, unsigned soff) {
  asm volatile("buffer_store_dwordx2 %0, %1, %2, %3 offen sc1\n\ts_nop 1" :: "v"(v), "v"(voff), "s"(r), "s"(soff) : "memory");
}
__device__ __forceinline__ void ld16(unsigned& dst, unsigned voff, rsrc_t r, unsigned soff) {
  asm volatile("buffer_load_ushort %0, %1, %2, %3 offen" : "=v"(dst) : "v"(voff), "s"(r), "s"(soff) : "memory");
}
__device__ __forceinline__ void st128c(u32x4 v, unsigned voff, rsrc_t r, unsigned soff) {
  asm volatile("buffer_store_dwordx4 %0, %1, %2, %3 offen sc1\n\ts_nop 1" :: "v"(v), "v"(voff), "s"(r), "s"(soff) : "memory");
}
template <int N>
__device__ __forceinline__ void wait_vm() { asm volatile("s_waitcnt vmcnt(%0)" :: "n"(N > 63 ? 63 : N) : "memory"); }
__device__ __forceinline__ void bar_lgkm() { asm volatile("s_waitcnt lgkmcnt(0)\n\ts_barrier" ::: "memory"); }
__device__ __forceinline__ void bar_only() { asm volatile("s_barrier" ::: "memory"); }
#define BW_DECL
#define BARX(f) f()
#define BW_OUT()
#define PIN(x) asm volatile("" : "+v"(x))
#define SCHED_FENCE() __builtin_amdgcn_sched_barrier(0)

__device__ __forceinline__ s16x4 trrd(const LAS char* p) {
  return __builtin_amdgcn_ds_read_tr16_b64_v4i16((LAS s16x4*)p);
}
__device__ __forceinline__ s16x8 cat8(s16x4 a, s16x4 b) {
  return __builtin_shufflevector(a, b, 0, 1, 2, 3, 4, 5, 6, 7);
}

__device__ __forceinline__ bool wave_all(bool p) { return __builtin_amdgcn_ballot_w64(!p) == 0ull; }
template <int SLP = 2>
__device__ __forceinline__ void poll_flags(unsigned voff, rsrc_t r, unsigned soff, unsigned tag) {
#pragma unroll 1
  for (int it = 0; it < (1 << 18); ++it) {
    u32x4 v;
    asm volatile("buffer_load_dwordx4 %0, %1, %2, %3 offen sc1\n\ts_waitcnt vmcnt(0)"
                 : "=&v"(v) : "v"(voff), "s"(r), "s"(soff) : "memory");
    const bool ok = (v[0] == tag) & (v[1] == tag) & (v[2] == tag) & (v[3] == tag);
    if (wave_all(ok)) break;
    __builtin_amdgcn_s_sleep(SLP);
  }
}
__device__ __forceinline__ void set_flag(char* ws, unsigned off, unsigned tag) {
  __hip_atomic_store((GAS unsigned*)(ws + WS_FLAG + off), tag, __ATOMIC_RELAXED, __HIP_MEMORY_SCOPE_AGENT);
}

#define STAMP_DECL
#define STAMP(i)
#define STAMP_RT(i)
#define STAMP_OUT(kid)

__device__ __forceinline__ f32x2 cmul(f32x2 a, f32x2 b) {
  f32x2 r;
  r.x = fmaf(a.x, b.x, -(a.y * b.y));
  r.y = fmaf(a.x, b.y, a.y * b.x);
  return r;
}
__device__ __forceinline__ f32x2 cexp_l(float a, float b, float l) {
  const float mag = __builtin_amdgcn_exp2f(a * l * 1.4426950408889634f);
  const float x = b * l;
  const float C1 = 0.15915494309189535f;
  const float C2 = (float)(0.15915494309189533577 - (double)0.15915494309189535f);
  const float hi = x * C1;
  const float lo = fmaf(x, C1, -hi) + x * C2;
  const float r = (hi - __builtin_rintf(hi)) + lo;
  f32x2 z;
  z.x = mag * __builtin_amdgcn_cosf(r);
  z.y = mag * __builtin_amdgcn_sinf(r);
  return z;
}

constexpr int W_UPOFF = 14336 + 128;
constexpr int XOFF = W_UPOFF + 14336;
constexpr int STG = XOFF + 16384;
constexpr int KGS = 3 * STG;
constexpr int KF = KGS + 8192;
constexpr int CV_KSTR = (48 + 2048) * 2;
constexpr int CV_USTR = (80 + 2048) * 2;
constexpr int CV_U = KF + 2 * CV_KSTR;
constexpr int CSUM = CV_U + 2 * CV_USTR;
constexpr int STAT = CSUM + 896;
constexpr int SY = STAT + 512;
constexpr int SY_LD = SY, SY_MC = SY + 16, SY_ST = SY + 4, SY_HR = SY + 8, SY_SR = SY + 12, SY_UR = SY + 32  ,
              SY_CNT = SY + 48, SY_VAL = SY + 64, SY_KG = SY + 80, SY_END = SY + 96;
constexpr int LDS_BYTES = SY_END;
static_assert(LDS_BYTES <= 163840, "lds");
constexpr int G2_RED = 272;
constexpr int EPX = KGS;
constexpr int EPS = KF;
#define WSL 4
constexpr unsigned CODE_BYTES = 49152;
constexpr int CV_NP = 12;
constexpr int G2_NR = 13;
constexpr int cmax(int a, int b) { return a > b ? a : b; }
constexpr int cmin(int a, int b) { return a < b ? a : b; }
constexpr int g2_base(int i) { return (((i % 15) / 5 + 2) % 3) * STG + (i % 5) * 8192; }
constexpr int RED_A = g2_base(15), RED_B = g2_base(20);
constexpr int G2_H0 = 6;
constexpr int g2_issued(int i) { return cmin(28, G2_H0 + (3 * i) / 2); }

__device__ __forceinline__ void lds_setv(unsigned addr, unsigned v) {
  asm volatile("ds_write_b32 %0, %1" :: "v"(addr), "v"(v) : "memory");
}
__device__ __forceinline__ unsigned lds_min4(unsigned addr) {
  u32x4 v;
  asm volatile("ds_read_b128 %0, %1\n\ts_waitcnt lgkmcnt(0)" : "=&v"(v) : "v"(addr) : "memory");
  return (unsigned)__builtin_amdgcn_readfirstlane(min(min(v[0], v[1]), min(v[2], v[3])));
}
__device__ __forceinline__ unsigned lds_get1(unsigned addr) {
  unsigned v;
  asm volatile("ds_read_b32 %0, %1\n\ts_waitcnt lgkmcnt(0)" : "=&v"(v) : "v"(addr) : "memory");
  return (unsigned)__builtin_amdgcn_readfirstlane(v);
}
template <int W>
__device__ __forceinline__ unsigned lds_val(unsigned addr) { return W == 4 ? lds_min4(addr) : lds_get1(addr); }
template <int W>
__device__ __forceinline__ void lds_spin(unsigned addr, unsigned need) {
#pragma unroll 1
  for (int it = 0; it < (1 << 20); ++it) {
    if (lds_val<W>(addr) >= need) break;
    __builtin_amdgcn_s_sleep(1);
  }
}
template <int W>
__device__ __forceinline__ void lds_wait(unsigned addr, unsigned need) {
  if (__builtin_expect(lds_val<W>(addr) < need, 0)) lds_spin<W>(addr, need);
}
template <int NW, bool DRAIN = true>
__device__ __forceinline__ void publish(LAS char* lds, int cnt, char* ws, unsigned off, int nl, unsigned tag, int lane) {
  if (DRAIN) wait_vm<0>();
  unsigned old = 0;
  if (lane == 0) old = __hip_atomic_fetch_add((LAS unsigned*)(lds + SY_CNT + cnt * 4), 1u, __ATOMIC_RELAXED, __HIP_MEMORY_SCOPE_WORKGROUP);
  if (__builtin_amdgcn_readfirstlane(old) == NW - 1 && lane < nl) set_flag(ws, off, tag);
}

template <int PA, int PB>
__device__ __forceinline__ void conv_upper(LAS char* lds, int lane, int hc, int cp, unsigned dsk2, rsrc_t r_s) {
  const float kscale = *(const LAS float*)(lds + SY_VAL + 8 + hc * 4);
  const float dskip = hc ? hi_f(dsk2) : lo_f(dsk2);
  const int G = lane >> 4, q4 = (lane & 15) >> 2, p = lane & 3;
  const LAS char* pa = lds + KF + hc * CV_KSTR + 96 + 2 * (76 + 16 * p - 32 * G - 4 * q4 - 16);
  const LAS char* pb = lds + CV_U + hc * CV_USTR + 160 + 2 * (-76 + 32 * G + 4 * q4 + 4 * p);
  const LAS char* pu = lds + CV_U + hc * CV_USTR + 160 + lane * 2;
  constexpr int J0 = 2 * PA - 1, J1 = 2 * PB, NT = J1 - J0;
  f16x8 A[16], B[32];
  f32x4 ca[NT];
#define CV_A(q) __builtin_bit_cast(f16x8, cat8(trrd(pa + 256 * (q) + 32), trrd(pa + 256 * (q))))
#define CV_B(w) __builtin_bit_cast(f16x8, cat8(trrd(pb + 128 * (w)), trrd(pb + 128 * (w) + 32)))
  B[0] = CV_B(0); B[1] = CV_B(1); B[2] = CV_B(2);
  constexpr int QA = (J0 - 1) / 2;
#pragma unroll
  for (int q = PB - 1; q >= QA; --q) A[q] = CV_A(q);
#pragma unroll
  for (int j = 0; j < NT; ++j) ca[j] = (f32x4){0.f, 0.f, 0.f, 0.f};
  static_for<0, J1>([&](auto wc) {
    constexpr int w = decltype(wc)::value;
    if constexpr (w + 3 < J1) B[w + 3] = CV_B(w + 3);
    if constexpr ((w & 1) == 0 && QA - 1 - w / 2 >= 0) A[QA - 1 - w / 2] = CV_A(QA - 1 - w / 2);
#pragma unroll
    for (int q = PB - 1; q >= 0; --q) {
      const int j = w + 2 * q;
      if (j >= J0 && j < J1) ca[j - J0] = __builtin_amdgcn_mfma_f32_16x16x32_f16(A[q], B[w], ca[j - J0], 0, 0, 0);
    }
    SCHED_FENCE();
  });
  float c3 = 0.f, c2 = 0.f, c1 = 0.f, yv[NT];
#pragma unroll
  for (int j = 0; j < NT; ++j) {
    const f32x4 a = ca[j];
    const float h3 = a[3];
    const float h2 = a[2] + shr_carry(c3, h3);
    const float h1 = a[1] + shr_carry(c2, h2);
    yv[j] = a[0] + shr_carry(c1, h1);
    c3 = dpp_mov<0x13C>(h3); c2 = dpp_mov<0x13C>(h2); c1 = dpp_mov<0x13C>(h1);
  }
#pragma unroll
  for (int jp = PA; jp < PB; ++jp) {
    const float u0 = (float)*(const LAS _Float16*)(pu + jp * 256), u1 = (float)*(const LAS _Float16*)(pu + jp * 256 + 128);
    const unsigned yq = pk_bf16(yv[2 * jp - J0] * kscale, yv[2 * jp + 1 - J0] * kscale);
    const unsigned uq = pk_bf16(u0 * dskip, u1 * dskip);
    st32c(pk_bf16(lo_f(yq) + lo_f(uq), hi_f(yq) + hi_f(uq)), lane * 4u, r_s, (unsigned)((cp * 2 + hc) * 4096 + jp * 256));
  }
#undef CV_A
#undef CV_B
}

#if defined(__HIP_DEVICE_COMPILE__)
__attribute__((target("no-packed-fp32-ops")))
#endif
__global__ __launch_bounds__(512, 2) void fused_kernel(
    const unsigned short* __restrict__ x, const unsigned short* __restrict__ A_re,
    const unsigned short* __restrict__ A_im, const unsigned short* __restrict__ B_re,
    const unsigned short* __restrict__ B_im, const unsigned short* __restrict__ C_re,
    const unsigned short* __restrict__ C_im, const unsigned* __restrict__ log_dt2,
    const unsigned* __restrict__ dsk2p, const unsigned short* __restrict__ wg,
    const unsigned short* __restrict__ wu, const unsigned short* __restrict__ wd,
    char* ws, unsigned short* __restrict__ out) {
  extern __shared__ __attribute__((aligned(16))) char smem[];
  unsigned long long pc0;
  asm volatile("s_getpc_b64 %0" : "=s"(pc0));
  LAS char* lds = (LAS char*)smem;
  const unsigned ldsb = (unsigned)(uintptr_t)lds;
  const int tid = threadIdx.x, lane = tid & 63;
  const int w8 = __builtin_amdgcn_readfirstlane(tid >> 6);
  const int wv = w8 & 3;
  STAMP_DECL; BW_DECL; STAMP_RT(14); STAMP(0);
  const int bid = blockIdx.x;
  const int xcd = bid & 7, bi = bid >> 3;
  const int mt = (xcd >> 2) * 8 + (bi >> 2);
  const int ft = (xcd & 3) * 4 + (bi & 3);
  const int rot = ((bi >> 2) + 2 * (bi & 3)) & 7;
  const int cp = xcd * 32 + bi;
  const int tt = xcd * 4 + (bi >> 3);
  const int dtile = bi & 7;
  const int rot2 = ((bi >> 3) * 7 + dtile * 3) % 28;

  const rsrc_t r_x = mk_rsrc(x, SL * DM * 2);
  const rsrc_t r_s = mk_rsrc(ws + WS_ST, SL * DM * 2);
  const rsrc_t r_fl = mk_rsrc(ws + WS_FLAG, FL_BYTES);
#define G2_KS(w, i) ((unsigned)((w) * 28 + (((i) + rot2) >= 28 ? (i) + rot2 - 28 : (i) + rot2)))
#define XO(t_) ((unsigned)((tt * 64 + ((t_) >> 3)) * (DM * 2) + dtile * 128 + ((t_) & 7) * 16))
#define EPI_X() do { int l_ = lane; PIN(l_); u32x4 xw_[2]; \
    _Pragma("unroll") for (int j = 0; j < 2; ++j) ld128(xw_[j], XO(wv * 128 + j * 64 + l_), r_x, 0u); \
    lds_wait<4>(ldsb + SY_UR, 1u); wait_vm<0>(); \
    _Pragma("unroll") for (int j = 0; j < 2; ++j) { PIN(xw_[j]); *(LAS u32x4*)(lds + EPX + (wv * 128 + j * 64 + l_) * 16) = xw_[j]; } } while (0)
#define EPI_S() do { int l_ = lane; PIN(l_); u32x4 sw_[4]; \
    const unsigned so = (unsigned)((dtile * 64 + wv * 16 + (l_ >> 4)) * 4096 + (tt >> 1) * 256 + (l_ & 15) * 16); \
    _Pragma("unroll") for (int j = 0; j < 4; ++j) ld128c(sw_[j], so, r_s, (unsigned)(j * 16384)); \
    wait_vm<0>(); \
    _Pragma("unroll") for (int j = 0; j < 4; ++j) { PIN(sw_[j]); \
      *(LAS u32x4*)(lds + EPS + (wv * 16 + j * 4 + (l_ >> 4)) * 256 + ((l_ + wv * 2 + (j >> 1)) & 15) * 16) = sw_[j]; } } while (0)

  if (w8 < 4) {
    const rsrc_t r_h = mk_rsrc(ws + WS_HF, SL * FF * 2);
    {
    const int hc = wv & 1, m = wv >> 1;
    const int dch = cp * 2 + hc;
    unsigned prm[6];
    {
      const unsigned po = (unsigned)(dch * NM * 2);
      ld32(prm[0], lane * 4u, mk_rsrc(A_re, DM * NM * 2), po);
      ld32(prm[1], lane * 4u, mk_rsrc(A_im, DM * NM * 2), po);
      ld32(prm[2], lane * 4u, mk_rsrc(B_re, DM * NM * 2), po);
      ld32(prm[3], lane * 4u, mk_rsrc(B_im, DM * NM * 2), po);
      ld32(prm[4], lane * 4u, mk_rsrc(C_re, DM * NM * 2), po);
      ld32(prm[5], lane * 4u, mk_rsrc(C_im, DM * NM * 2), po);
    }
    if (tid < 5) *(LAS u32x4*)(lds + SY + tid * 16) = (u32x4){0u, 0u, 0u, tid == 2 ? 1u : 0u};
    bar_lgkm();
    __builtin_amdgcn_s_setprio(2);
    const unsigned ldt2 = log_dt2[cp];
    {
      const float ldt = hc ? hi_f(ldt2) : lo_f(ldt2);
      const float L2E = 1.4426950408889634f, L2E_LO = (float)(1.4426950408889634074 - (double)1.4426950408889634f);
      const float th = ldt * L2E;
      const float tl = fmaf(ldt, L2E, -th) + ldt * L2E_LO;
      const float e0 = __builtin_amdgcn_exp2f(th);
      const float dt = rbf(fmaf(e0 * 0.6931471805599453f, tl, e0));
      wait_vm<0>();
#pragma unroll
      for (int i = 0; i < 6; ++i) PIN(prm[i]);
      float av[2], bv[2];
      f32x2 wq[2];
#pragma unroll
      for (int m = 0; m < 2; ++m) {
        av[m] = dt * (m ? hi_f(prm[0]) : lo_f(prm[0]));
        bv[m] = dt * (m ? hi_f(prm[1]) : lo_f(prm[1]));
        const f32x2 db = {dt * (m ? hi_f(prm[2]) : lo_f(prm[2])), dt * (m ? hi_f(prm[3]) : lo_f(prm[3]))};
        const f32x2 cc = {m ? hi_f(prm[4]) : lo_f(prm[4]), m ? hi_f(prm[5]) : lo_f(prm[5])};
        wq[m] = cmul(cc, db);
      }
      const float amax = fmaxf(wave_max(fmaxf(av[0], av[1])), 0.f);
      const float g2 = amax * L2E;
      const float wm = wave_max(fmaxf(fmaxf(fabsf(wq[0].x), fabsf(wq[0].y)), fmaxf(fabsf(wq[1].x), fabsf(wq[1].y))));
      int we = __builtin_amdgcn_frexp_expf(wm);
      we = we < -100 ? -100 : we;
      const float wsc = __builtin_amdgcn_ldexpf(1.0f, -we);
      f32x4 kacc[3];
#pragma unroll
      for (int t = 0; t < 3; ++t) kacc[t] = (f32x4){0.f, 0.f, 0.f, 0.f};
      const int kgs = (m ? CV_U : KGS) + hc * 4096;
      LAS char* tw[4];
#pragma unroll
      for (int c = 0; c < 4; ++c) tw[c] = lds + kgs + lane * 64 + ((c ^ ((lane >> 1) & 3)) << 4);
      const LAS char* tr[2];
      {
        const int G = lane >> 4, q4 = (lane & 15) >> 2, p = lane & 3;
#pragma unroll
        for (int t = 0; t < 2; ++t)
          tr[t] = lds + kgs + (4 * G + 2 * t + (q4 >> 1)) * 64 + ((((q4 & 1) * 2 + (p >> 1)) ^ ((2 * G + t) & 3)) << 4) + (p & 1) * 8;
      }
      auto put16 = [&](const f32x2 (&e)[16]) __attribute__((always_inline)) {
#pragma unroll
        for (int h = 0; h < 2; ++h) {
          u32x4 re, im;
#pragma unroll
          for (int j = 0; j < 4; ++j) {
            re[j] = pk_f16(e[8 * h + 2 * j].x, e[8 * h + 2 * j + 1].x);
            im[j] = pk_f16(e[8 * h + 2 * j].y, e[8 * h + 2 * j + 1].y);
          }
          *(LAS u32x4*)tw[h] = re;
          *(LAS u32x4*)tw[2 + h] = im;
        }
      };
#define KG_FRAG(s) __builtin_bit_cast(f16x8, cat8(trrd(tr[0] + (s) * 1024), trrd(tr[1] + (s) * 1024)))
#define KG_RD() do { _Pragma("unroll") for (int s = 0; s < 4; ++s) pf[s] = KG_FRAG(s); SCHED_FENCE(); } while (0)
#define KG_MMA(t) do { SCHED_FENCE(); _Pragma("unroll") for (int s = 0; s < 4; ++s) \
        kacc[t] = __builtin_amdgcn_mfma_f32_16x16x32_f16(pf[s], qf[s], kacc[t], 0, 0, 0); } while (0)
      {
        const float a = (m ? av[1] : av[0]) - amax, b = m ? bv[1] : bv[0];
        f32x2 w = m ? wq[1] : wq[0];
        w.x *= wsc; w.y *= -wsc;
        f32x2 e[16];
        {
          const f32x2 z16 = cexp_l(a, b, 16.f);
          e[0] = (f32x2){1.f, 0.f}; e[1] = z16; e[2] = cmul(z16, z16); e[3] = cmul(e[2], z16);
          const f32x2 z64 = cmul(e[2], e[2]);
#pragma unroll
          for (int c = 4; c < 16; ++c) e[c] = cmul(e[c - 4], z64);
          put16(e);
        }
        f16x8 qf[4], pf[4];
#pragma unroll
        for (int s = 0; s < 4; ++s) qf[s] = KG_FRAG(s);
        SCHED_FENCE();
        const f32x2 z1 = cexp_l(a, -b, 1.f);
        const f32x2 z2 = cmul(z1, z1), z4 = cmul(z2, z2);
        e[0] = w; e[1] = cmul(w, z1); e[2] = cmul(w, z2); e[3] = cmul(e[1], z2);
#pragma unroll
        for (int c = 4; c < 16; ++c) e[c] = cmul(e[c - 4], z4);
        put16(e);
        KG_RD();
        const f32x2 z256 = cexp_l(a, -b, 256.f);
        const f32x2 z512 = cmul(z256, z256);
        const f32x2 w2 = cmul(w, z512);
        e[0] = cmul(w, z256); e[1] = cmul(e[0], z2); e[8] = w2; e[12] = cmul(e[0], z512);
        const f32x2 w3 = e[12];
#pragma unroll
        for (int i = 1; i < 4; ++i) {
          e[2 * i] = cmul(e[2 * i - 2], z4); e[2 * i + 1] = cmul(e[2 * i - 1], z4);
          e[8 + i] = cmul(e[7 + i], z4); e[12 + i] = cmul(e[11 + i], z4);
        }
        KG_MMA(0);
        put16(e);
        KG_RD();
        const f32x2 z8 = cmul(z4, z4);
        e[0] = cmul(w2, z512); e[2] = cmul(w3, z512); e[4] = cmul(e[0], z512); e[6] = cmul(e[2], z512);
        e[8] = cmul(e[4], z512);
#pragma unroll
        for (int i = 0; i < 4; ++i) e[2 * i + 1] = cmul(e[2 * i], z8);
#pragma unroll
        for (int i = 9; i < 16; ++i) e[i] = (f32x2){0.f, 0.f};
        KG_MMA(1);
        put16(e);
        KG_RD();
        KG_MMA(2);
      }
#pragma unroll
      for (int t = 0; t < 3; ++t) *(LAS f32x4*)(lds + (m ? KF + hc * CV_KSTR : KGS + hc * 4096) + t * 1024 + lane * 16) = kacc[t];
      if (m) *(LAS f32x2*)(lds + SY_KG + hc * 8) = (f32x2){g2, (float)we};
    }
    }
    f32x16 acc[7];
    f32x4 csa[2];
    unsigned tag;
    {
      const int G = lane >> 4;
      const LAS char* wa0 = lds + (G & 1) * W_UPOFF + (8 * (G >> 1) + ((lane & 15) >> 2)) * 224 + (lane & 3) * 8;
      const LAS char* wa[7];
#pragma unroll
      for (int T = 0; T < 7; ++T) wa[T] = wa0 + 32 * ((T + 2 * wv) % 7);
      const LAS char* xa[4];
      {
        const int row = 32 * wv + (lane & 31), sw = (row >> 1) & 7;
#pragma unroll
        for (int ks = 0; ks < 4; ++ks) xa[ks] = lds + XOFF + row * 128 + (((2 * ks + (lane >> 5)) ^ sw) << 4);
      }
      constexpr int PD = 3;
      bf16x8 Af[196], Bf[32];
      const unsigned o1 = ((lane & 15) == 4 * ((lane >> 4) & 1)) ? 0x3f803f80u : 0u;
      u32x4 onesq = {o1, o1, o1, o1};
      PIN(onesq);
      const bf16x8 ones = __builtin_bit_cast(bf16x8, onesq);
#define A_READ(i, T) __builtin_bit_cast(bf16x8, cat8(trrd(wa[T] + (((i) >> 2) % 3) * STG + (16 * ((i) & 3)) * 224), \
                                                      trrd(wa[T] + (((i) >> 2) % 3) * STG + (16 * ((i) & 3) + 4) * 224)))
#define B_READ(i) (*(const LAS bf16x8*)(xa[(i) & 3] + (((i) >> 2) % 3) * STG))
#pragma unroll
      for (int T = 0; T < 7; ++T) acc[T] = (f32x16){};
      csa[0] = (f32x4){0.f, 0.f, 0.f, 0.f}; csa[1] = csa[0];
      BARX(bar_lgkm);
      STAMP(1);
      tag = lds_get1(ldsb + SY_VAL);
      Bf[0] = B_READ(0);
#pragma unroll
      for (int T = 0; T < PD; ++T) Af[T] = A_READ(0, T);
      bf16x8 Al[7][4];
      u32x2 stg;
      float cs[7];
      static_for<0, 196>([&](auto nc) {
        constexpr int n = decltype(nc)::value, i = n / 7, T = n % 7;
        constexpr int m = n + PD, im = m / 7, Tm = m % 7;
        if constexpr (n == 70) ld64c(stg, (unsigned)((lane & 31) * 8), r_fl, FL_STAT + (unsigned)((mt * 128 + wv * 32) * 8));
        if constexpr (n == 27 * 7 + 2) {
          if (lane < 32) {
#pragma unroll
            for (int j = 0; j < 2; ++j) *(LAS float*)(lds + CSUM + ((j + 2 * wv) % 7) * 128 + lane * 4) = csa[j][0];
          }
        }
        if constexpr (Tm == 0 && (im & 3) == 0) BARX(bar_lgkm);
        if constexpr (m < 196) {
          if constexpr (Tm == 0) Bf[im] = B_READ(im);
          Af[m] = A_READ(im, Tm);
        } else if constexpr (m == 196) {
#pragma unroll
          for (int k = 28; k < 32; ++k) Bf[k] = B_READ(k);
        } else if constexpr (m == 197) {
#pragma unroll
          for (int t = 0; t < 7; ++t) cs[t] = *(const LAS float*)(lds + CSUM + ((t + 2 * wv) % 7) * 128 + (lane & 31) * 4);
        } else {
#pragma unroll
          for (int k = 0; k < 4; ++k) Al[0][k] = A_READ(28 + k, 0);
        }
        acc[T] = __builtin_amdgcn_mfma_f32_32x32x16_bf16(Af[n], Bf[i], acc[T], 0, 0, 0);
        if constexpr (T < 2)
          csa[T] = __builtin_amdgcn_mfma_f32_16x16x32_bf16(ones, Af[n], csa[T], 0, 0, 0);
        SCHED_FENCE();
      });
      STAMP(5);
      wait_vm<0>();
      PIN(stg);
      if (__builtin_expect(!wave_all(stg[1] == tag), 0)) {
#pragma unroll 1
        for (int it = 0; it < (1 << 18); ++it) {
          ld64c(stg, (unsigned)((lane & 31) * 8), r_fl, FL_STAT + (unsigned)((mt * 128 + wv * 32) * 8));
          wait_vm<0>();
          PIN(stg);
          if (wave_all(stg[1] == tag)) break;
          __builtin_amdgcn_s_sleep(2);
        }
      }
      const unsigned stw = stg[0];
      const float mb = lo_f(stw), sd = hi_f(stw);
#pragma unroll
      for (int k = 28; k < 32; ++k) {
        u32x4 v = __builtin_bit_cast(u32x4, Bf[k]);
#pragma unroll
        for (int j = 0; j < 4; ++j) v[j] = pk_bf16(lo_f(v[j]) - mb, hi_f(v[j]) - mb);
        Bf[k] = __builtin_bit_cast(bf16x8, v);
      }
      const unsigned nm = (stw & 0xffffu) ^ 0x8000u;
      u32x4 bq = {nm | (nm << 16), nm, 0u, 0u};
      if (lane >= 32) bq = (u32x4){0u, 0u, 0u, 0u};
      const float ga = -1.4426950408889634f * __builtin_amdgcn_rcpf(sd);
      const unsigned hsb = (unsigned)(((mt * 4 + wv) * 112 + ft * 7) * 1024);
      STAMP(6);
      static_for<0, 8>([&](auto tc) {
        constexpr int T = decltype(tc)::value;
        u32x4 o;
        if constexpr (T < 7) {
          const float c = cs[T];
          const float ch = rbf(c), cm = rbf(c - ch), cl = (c - ch) - cm;
          const u32x4 aq = {pk_bf16(ch, cm), pk_bf16(cl, 0.f), 0u, 0u};
          acc[T] = __builtin_amdgcn_mfma_f32_32x32x16_bf16(__builtin_bit_cast(bf16x8, aq), __builtin_bit_cast(bf16x8, bq), acc[T], 0, 0, 0);
          SCHED_FENCE();
        }
#pragma unroll
        for (int j = 0; j < 4; ++j) {
          if constexpr (T + 1 < 7) Al[T + 1][j] = A_READ(28 + j, T + 1);
          if constexpr (T < 7) acc[T] = __builtin_amdgcn_mfma_f32_32x32x16_bf16(Al[T][j], Bf[28 + j], acc[T], 0, 0, 0);
          SCHED_FENCE();
          if constexpr (T >= 1) {
            const float s0 = __builtin_amdgcn_rcpf(fmaf(__builtin_amdgcn_exp2f(acc[T - 1][2 * j] * ga), sd, sd));
            const float s1 = __builtin_amdgcn_rcpf(fmaf(__builtin_amdgcn_exp2f(acc[T - 1][2 * j + 1] * ga), sd, sd));
            o[j] = pk_bf16(s0 * acc[T - 1][8 + 2 * j], s1 * acc[T - 1][8 + 2 * j + 1]);
            PIN(o[j]);
          }
          SCHED_FENCE();
        }
        if constexpr (T >= 1) st128c(o, lane * 16u, r_h, hsb + (unsigned)((T - 1 + 2 * wv) % 7) * 1024u);
      });
    }
    STAMP(7);
    const unsigned ldw = ldsb + SY_LD;
    const LAS char* waT[2];
    {
      const int G = lane >> 4, q4 = (lane & 15) >> 2;
      const LAS char* wa0 = lds + wv * 2048 + (4 * (G >> 1) + q4) * 128 + (G & 1) * 32 + (lane & 3) * 8;
      waT[0] = wa0 + ((q4 >> 1) * 64); waT[1] = wa0 + (((q4 >> 1) ^ 1) * 64);
    }
#define D_READ(i, T) __builtin_bit_cast(bf16x8, cat8(trrd(waT[T] + g2_base(i)), trrd(waT[T] + g2_base(i) + 1024)))
    bf16x8 Wr[28][2];
    lds_wait<1>(ldw, 2u);
    static_for<0, 5>([&](auto ic) { constexpr int i = decltype(ic)::value; Wr[i][0] = D_READ(i, 0); Wr[i][1] = D_READ(i, 1); });
    static_for<5, 10>([&](auto ic) { constexpr int i = decltype(ic)::value; Wr[i][0] = D_READ(i, 0); Wr[i][1] = D_READ(i, 1); });
    wait_vm<0>();
    if (lane < 8) set_flag(ws, FL_H + (unsigned)(((mt * 2 + (wv >> 1)) * 8 + lane) * 256 + (ft * 2 + (wv & 1)) * 4), tag);
    STAMP(8);
    lds_setv(ldsb + SY_MC + wv * 4, 2u);
    if (wv == 0) {
      lds_wait<4>(ldsb + SY_UR, 1u);
      conv_upper<14, 16>(lds, lane, 0, cp, dsk2p[cp], r_s);
    }
    lds_wait<1>(ldw, 3u);
    static_for<10, G2_NR>([&](auto ic) { constexpr int i = decltype(ic)::value; Wr[i][0] = D_READ(i, 0); Wr[i][1] = D_READ(i, 1); });
    lds_setv(ldsb + SY_MC + wv * 4, 3u);
    STAMP(9);
    __builtin_amdgcn_s_setprio(0);
    poll_flags<2>((lane & 1) * 16u, r_fl, FL_H + (unsigned)((tt * 8 + dtile) * 256 + wv * 32), tag);
    if (wv == 0) publish<4>(lds, 2, ws, FL_S + (unsigned)((lane * 8 + (cp >> 5)) * 256 + (cp & 31) * 4), 32, tag, lane);
    __builtin_amdgcn_s_setprio(2);
    STAMP(10);
    u32x4 hb[28][2];
#define HB_LOAD(i) do { ld128c(hb[i][0], lane * 16u, r_h, ((unsigned)(tt * 2) * 112u + G2_KS(wv, i)) * 1024u); \
                        ld128c(hb[i][1], lane * 16u, r_h, ((unsigned)(tt * 2 + 1) * 112u + G2_KS(wv, i)) * 1024u); } while (0)
#pragma unroll
    for (int i = 0; i < G2_H0; ++i) HB_LOAD(i);
    f32x16 acc2[2][2];
    {
#pragma unroll
      for (int a = 0; a < 2; ++a)
#pragma unroll
        for (int b = 0; b < 2; ++b) acc2[a][b] = (f32x16){};
      static_for<0, 28>([&](auto ic) {
        constexpr int i = decltype(ic)::value;
        if constexpr (i == 14) STAMP(11);
        if constexpr (i + 2 == 13) lds_wait<1>(ldw, 6u);
        if constexpr (i + 2 >= G2_NR && i + 2 < 28) { Wr[i + 2][0] = D_READ(i + 2, 0); Wr[i + 2][1] = D_READ(i + 2, 1); }
        wait_vm<2 * (g2_issued(i) - i - 1)>();
        PIN(hb[i][0]); PIN(hb[i][1]);
#pragma unroll
        for (int T = 0; T < 2; ++T)
#pragma unroll
          for (int b = 0; b < 2; ++b)
            acc2[T][b] = __builtin_amdgcn_mfma_f32_32x32x16_bf16(Wr[i][T], __builtin_bit_cast(bf16x8, hb[i][b]), acc2[T][b], 0, 0, 0);
        SCHED_FENCE();
        static_for<g2_issued(i), g2_issued(i + 1)>([&](auto jc) { constexpr int j = decltype(jc)::value; HB_LOAD(j); });
      });
    }
    STAMP(12);
    {
      int l2 = lane; PIN(l2);
      const int tl = l2 & 31, x7 = (tl & 7) ^ (l2 >> 5);
#pragma unroll
      for (int b = 0; b < 2; ++b) {
        const int c8 = 4 * b + (tl >> 3);
        const int o4 = c8 < 5 ? RED_A + c8 * 8192 : RED_B + (c8 - 5) * 8192;
        LAS char* rp = lds + o4 + wv * 2048 + (tl & 7) * 256;
#pragma unroll
        for (int T = 0; T < 2; ++T)
#pragma unroll
          for (int g = 0; g < 4; ++g) {
            const f32x4 v = {acc2[T][b][4 * g], acc2[T][b][4 * g + 1], acc2[T][b][4 * g + 2], acc2[T][b][4 * g + 3]};
            *(LAS f32x4*)(rp + T * 128 + (((2 * g) ^ x7) << 4)) = v;
          }
      }
    }
  } else if (w8 == 4) {
    __builtin_amdgcn_s_setprio(3);
    const rsrc_t r_wg = mk_rsrc(wg, DM * FF * 2), r_wu = mk_rsrc(wu, DM * FF * 2);
    const rsrc_t r_wd = mk_rsrc(wd, FF * DM * 2);
    unsigned woff[7], xoff[4], wv4[4];
#pragma unroll
    for (int i = 0; i < 4; ++i) {
      xoff[i] = (unsigned)((lane >> 3) * 1024 + (((lane & 7) ^ ((lane >> 4) + 4 * (i & 1))) * 16) + i * 7168);
      wv4[i] = (unsigned)((lane >> 3) * 1024 + (((lane & 7) ^ (((lane >> 4) & 1) << 2)) * 16) + (i >> 1) * 458752 + (i & 1) * 8192 - i * 1024);
    }
#pragma unroll
    for (int i = 0; i < 7; ++i) {
      const int c = i * 64 + lane, row = c / 14, col = c - row * 14;
      woff[i] = (unsigned)(row * (FF * 2) + col * 16 - (i & 3) * 1024);
    }
    const unsigned ldw = ldsb + SY_LD;
    auto g1_stage = [&](int s) __attribute__((always_inline)) {
      const unsigned st = (unsigned)((s + rot) & 7), lb = ldsb + (unsigned)((s % 3) * STG);
#pragma unroll
      for (int i = 0; i < 4; ++i)
        dma16x4(lb + XOFF + i * 4096, xoff[0], xoff[1], xoff[2], xoff[3], r_x, (unsigned)((mt * 128 + i * 32) * 1024) + st * 128u);
#pragma unroll
      for (int h = 0; h < 2; ++h) {
        const unsigned wso = (unsigned)(ft * 224) + (st * 64u + h * 32u) * (FF * 2);
        dma16x4(lb + h * 7168, woff[0], woff[1], woff[2], woff[3], r_wg, wso);
        dma16x3(lb + h * 7168 + 4096, woff[4], woff[5], woff[6], r_wg, wso);
        dma16x4(lb + W_UPOFF + h * 7168, woff[0], woff[1], woff[2], woff[3], r_wu, wso);
        dma16x3(lb + W_UPOFF + h * 7168 + 4096, woff[4], woff[5], woff[6], r_wu, wso);
      }
    };
#define G2_GROUP(g) G2_STEPS(5 * (g), cmin(5 * (g) + 5, 28))
#define G2_STEPS(a_, b_) static_for<a_, b_>([&](auto ic) { constexpr int i = decltype(ic)::value; \
        const unsigned so_ = G2_KS(0, i) * 16384u + (unsigned)(dtile * 128); \
        dma16x4(ldsb + g2_base(i), wv4[0], wv4[1], wv4[2], wv4[3], r_wd, so_); \
        dma16x4(ldsb + g2_base(i) + 4096, wv4[0], wv4[1], wv4[2], wv4[3], r_wd, so_ + 2u * 458752u); })
    unsigned tch = 0;
    bar_only();
    g1_stage(0);
    g1_stage(1);
    wait_vm<44>();
    STAMP(1);
    BARX(bar_only);
#pragma unroll
    for (int k = 0; k < 6; ++k) {
      g1_stage(k + 2);
      wait_vm<44>();
      BARX(bar_only);
    }
    STAMP(2);
    G2_GROUP(0);
    wait_vm<40>();
    BARX(bar_only);
    G2_GROUP(1);
    wait_vm<0>();
    asm volatile("" :: "v"(tch));
    lds_setv(ldw, 2u);
    STAMP(3);
    lds_wait<4>(ldsb + SY_MC, 2u);
    G2_STEPS(10, 15);
    wait_vm<16>();
    lds_setv(ldw, 3u);
    STAMP(4);
    G2_STEPS(15, 25);
    lds_wait<4>(ldsb + SY_MC, 3u);
    G2_GROUP(5);
    wait_vm<0>();
    lds_setv(ldw, 6u);
    STAMP(5);
    __builtin_amdgcn_s_setprio(0);
    EPI_X();
    lds_wait<1>(ldsb + SY_SR, 1u);
    EPI_S();
    STAMP(12);
  } else if (w8 == 5) {
    unsigned cpf;
    ld32(cpf, (unsigned)((bi * 16 + (lane & 15)) * 128), mk_rsrc((const void*)pc0, CODE_BYTES), 0u);
    bar_only();
    wait_vm<0>();
    asm volatile("" :: "v"(cpf));
    BARX(bar_only);
    const unsigned tag = lds_get1(ldsb + SY_VAL);
    BARX(bar_only);
    {
      const int c = lane & 15, g = lane >> 4;
      unsigned d[2][3][4];
      float kscale[2];
#pragma unroll
      for (int hc = 0; hc < 2; ++hc) {
        f32x4 kacc[3];
#pragma unroll
        for (int t = 0; t < 3; ++t)
          kacc[t] = *(const LAS f32x4*)(lds + KF + hc * CV_KSTR + t * 1024 + lane * 16) + *(const LAS f32x4*)(lds + KGS + hc * 4096 + t * 1024 + lane * 16);
        const f32x2 gw = *(const LAS f32x2*)(lds + SY_KG + hc * 8);
        const float g2 = gw.x;
        const int we = (int)gw.y;
        if (g2 > 0.f) {
#pragma unroll
          for (int i = 0; i < 4; ++i) {
            const int l0 = 16 * c + 4 * g + i;
            const int l1 = g < 2 ? 256 + 16 * c + 8 * g + 2 * i : 512 + 256 * (g - 2) + 16 * c + 4 * i;
            const int l2 = g < 2 ? 1024 + 256 * (2 * g + (i >> 1)) + 8 * (i & 1) + 16 * c : 2048;
            kacc[0][i] *= __builtin_amdgcn_exp2f(g2 * (float)(l0 - 2048));
            kacc[1][i] *= __builtin_amdgcn_exp2f(g2 * (float)(l1 - 2048));
            kacc[2][i] *= __builtin_amdgcn_exp2f(g2 * (float)(l2 - 2048));
          }
        }
        kscale[hc] = __builtin_amdgcn_ldexpf(1.0f, we - 7) * __builtin_amdgcn_exp2f(g2 * 2048.f);
#pragma unroll
        for (int t = 0; t < 3; ++t)
#pragma unroll
          for (int i = 0; i < 4; ++i) { d[hc][t][i] = pk_f16(kacc[t][i] * 128.f, kacc[t][i] * 128.f); PIN(d[hc][t][i]); }
        BARX(bar_only);
      }
      BARX(bar_only);
      BARX(bar_only);
      BARX(bar_only);
      BARX(bar_only);
      __builtin_amdgcn_s_sleep(WSL);
      __builtin_amdgcn_s_setprio(3);
      STAMP(2);
      *(LAS f32x2*)(lds + SY_VAL + 8) = (f32x2){kscale[0], kscale[1]};
#define MIX(a, b) (((a) & 0xffffu) | ((b) & 0xffff0000u))
#pragma unroll
      for (int hc = 0; hc < 2; ++hc) {
        LAS char* kf = lds + KF + hc * CV_KSTR + 96;
        if (g < 2) {
#pragma unroll
          for (int h = 0; h < 2; ++h) {
            LAS char* p = kf + 2 * (1024 + 256 * (2 * g + h) + 16 * c);
            const unsigned v0 = d[hc][2][2 * h], v1 = d[hc][2][2 * h + 1];
            *(LAS u32x2*)(p - 8) = (u32x2){v0, v0};
            *(LAS u32x4*)(p) = (u32x4){v0, v0, MIX(v0, v1), v1};
            *(LAS u32x2*)(p + 16) = (u32x2){v1, v1};
          }
        } else {
          if (lane == 32) *(LAS u32x2*)(kf + 2 * 2044) = (u32x2){d[hc][2][0], d[hc][2][0]};
        }
        if (g >= 2) {
          LAS char* p = kf + 2 * (512 + 256 * (g - 2) + 16 * c);
          const unsigned v0 = d[hc][1][0], v1 = d[hc][1][1], v2 = d[hc][1][2], v3 = d[hc][1][3];
          *(LAS unsigned*)(p - 4) = v0;
          *(LAS u32x4*)(p) = (u32x4){v0, MIX(v0, v1), v1, v2};
          *(LAS u32x2*)(p + 16) = (u32x2){v2, MIX(v2, v3)};
          *(LAS unsigned*)(p + 24) = v3;
        }
        if (g < 2) {
          LAS char* p = kf + 2 * (256 + 16 * c + 8 * g);
          const unsigned v0 = d[hc][1][0], v1 = d[hc][1][1], v2 = d[hc][1][2], v3 = d[hc][1][3];
          *(LAS unsigned short*)(p - 2) = (unsigned short)v0;
          *(LAS u32x2*)(p) = (u32x2){v0, MIX(v1, v2)};
          *(LAS unsigned*)(p + 8) = v2;
          *(LAS unsigned short*)(p + 12) = (unsigned short)v3;
        }
        *(LAS u32x2*)(kf + 2 * (16 * c + 4 * g)) = (u32x2){MIX(d[hc][0][0], d[hc][0][1]), MIX(d[hc][0][2], d[hc][0][3])};
        if (lane < 6) *(LAS u32x4*)(kf - 96 + lane * 16) = (u32x4){0u, 0u, 0u, 0u};
      }
      lds_setv(ldsb + SY_UR + 8, 1u);
      STAMP(3);
    }
    __builtin_amdgcn_s_setprio(1);
    lds_wait<4>(ldsb + SY_UR, 1u);
    conv_upper<CV_NP, 16>(lds, lane, 1, cp, dsk2p[cp], r_s);
    conv_upper<CV_NP, 14>(lds, lane, 0, cp, dsk2p[cp], r_s);
    STAMP(4);
    publish<4>(lds, 2, ws, FL_S + (unsigned)((lane * 8 + (cp >> 5)) * 256 + (cp & 31) * 4), 32, tag, lane);
    STAMP(5);
    EPI_X();
    lds_wait<1>(ldsb + SY_CNT + 8, 4u);
    poll_flags<4>((lane & 7) * 16u, r_fl, FL_S + (unsigned)((tt * 8 + dtile) * 256), tag);
    lds_setv(ldsb + SY_SR, 1u);
    STAMP(6);
    EPI_S();
    STAMP(12);
  } else {
    const int hc = wv - 2;
    const int dch = cp * 2 + hc;
    const rsrc_t r_xn = mk_rsrc(ws + WS_XNF, SL * DM * 2);
    u32x4 xr[4];
    unsigned ep;
    const int row0 = bid * 8 + hc * 4;
    {
      const unsigned vo = (unsigned)((lane >> 4) * 1024 + (lane & 15) * 16);
#pragma unroll
      for (int i = 0; i < 4; ++i) ld128(xr[i], vo, r_x, (unsigned)(row0 * 1024 + i * 256));
      ld32c(ep, 0u * lane, r_fl, FL_EP);
    }
    bar_only();
    const unsigned dsk2 = dsk2p[cp];
    wait_vm<0>();
    STAMP(1);
#pragma unroll
    for (int i = 0; i < 4; ++i) PIN(xr[i]);
    PIN(ep);
    const unsigned tag = (unsigned)__builtin_amdgcn_readfirstlane(ep) + 1u;
    lds_setv(ldsb + SY_VAL, tag);
    {
      float f[32];
#pragma unroll
      for (int j = 0; j < 16; ++j) { f[2 * j] = lo_f(xr[j >> 2][j & 3]); f[2 * j + 1] = hi_f(xr[j >> 2][j & 3]); }
      float s0 = 0.f, s1 = 0.f;
#pragma unroll
      for (int j = 0; j < 32; j += 4) { s0 += f[j] + f[j + 1]; s1 += f[j + 2] + f[j + 3]; }
      const float mean = row_sum(s0 + s1) * (1.0f / DM);
      float q0 = 0.f, q1 = 0.f;
#pragma unroll
      for (int j = 0; j < 32; j += 2) {
        const float da = f[j] - mean, db = f[j + 1] - mean;
        q0 = fmaf(da, da, q0); q1 = fmaf(db, db, q1);
      }
      const float var = row_sum(q0 + q1) * (1.0f / DM);
      const float mb = rbf(mean);
      const float sb = rbf(__builtin_amdgcn_sqrtf(rbf(rbf(var) + 1e-5f)));
      if ((lane & 15) == 0)
        st64c((u32x2){pk_bf16(mb, sb), tag}, (unsigned)((lane >> 4) * 8), r_fl, FL_STAT + (unsigned)(row0 * 8));
      const float rs = __builtin_amdgcn_rcpf(sb);
      const unsigned vo = (unsigned)((lane & 15) * 32768 + (lane >> 4) * 16);
      STAMP(2);
#pragma unroll
      for (int i = 0; i < 4; ++i) {
        u32x4 o;
#pragma unroll
        for (int j = 0; j < 4; ++j) {
          const unsigned d = pk_bf16(f[8 * i + 2 * j] - mb, f[8 * i + 2 * j + 1] - mb);
          o[j] = pk_bf16(lo_f(d) * rs, hi_f(d) * rs);
        }
        st128c(o, vo, r_xn, (unsigned)(i * 16 * 32768 + row0 * 16));
      }
    }
    BARX(bar_lgkm);
    wait_vm<0>();
    BARX(bar_only);
    if (hc == 0 && lane < 8) set_flag(ws, FL_ALL + (unsigned)(lane * 4096 + bid * 4), tag);
    BARX(bar_only);
    BARX(bar_only);
    u32x4 vf;
    ld128c(vf, lane * 16u, r_fl, FL_ALL + (unsigned)(xcd * 4096));
    BARX(bar_only);
    wait_vm<0>();
    PIN(vf);
    const bool all_ok = wave_all((vf[0] == tag) & (vf[1] == tag) & (vf[2] == tag) & (vf[3] == tag));
    unsigned uw[16];
    const unsigned usb = (unsigned)((cp >> 2) * 32768 + (cp & 3) * 4 + hc * 16384);
    if (!all_ok) {
      for (int r = 5; r <= 7; ++r) BARX(bar_only);
      poll_flags<4>(lane * 16u, r_fl, FL_ALL + (unsigned)(xcd * 4096), tag);
    }
#pragma unroll
    for (int m = 0; m < 16; ++m) ld32c(uw[m], lane * 16u, r_xn, usb + (unsigned)m * 1024u);
    if (lane == 0 && bid == 0 && hc == 0) set_flag(ws, FL_EP, tag);
    if (all_ok) { BARX(bar_only); BARX(bar_only); }
    wait_vm<0>();
    {
      const bool od = lane & 1;
      LAS char* up = lds + CV_U + 160 + hc * 2048 + (od ? 128 : 0) + (lane >> 1) * 4;
#pragma unroll
      for (int m = 0; m < 16; m += 2) {
        PIN(uw[m]); PIN(uw[m + 1]);
        const unsigned a = pk_f16(lo_f(uw[m]), hi_f(uw[m])), b = pk_f16(lo_f(uw[m + 1]), hi_f(uw[m + 1]));
        const unsigned y = (unsigned)__builtin_amdgcn_update_dpp(0, (int)(od ? a : b), 0xB1, 0xf, 0xf, true);
        const unsigned f0 = od ? y : a, f1 = od ? b : y;
        uw[m] = __builtin_amdgcn_perm(f1, f0, 0x05040100u);
        uw[m + 1] = __builtin_amdgcn_perm(f1, f0, 0x07060302u);
        PIN(uw[m]); PIN(uw[m + 1]);
      }
      if (all_ok) BARX(bar_only);
      __builtin_amdgcn_s_sleep(WSL);
      __builtin_amdgcn_s_setprio(3);
      if (lane < 10) *(LAS u32x4*)(lds + CV_U + hc * CV_USTR + lane * 16) = (u32x4){0u, 0u, 0u, 0u};
#pragma unroll
      for (int m = 0; m < 16; m += 2) {
        *(LAS unsigned*)(up + m * 128) = uw[m];
        *(LAS unsigned*)(up + CV_USTR + m * 128) = uw[m + 1];
      }
      lds_setv(ldsb + SY_UR + hc * 4, 1u);
    }
    STAMP(3);
    __builtin_amdgcn_s_setprio(1);
    lds_wait<4>(ldsb + SY_UR, 1u);
    const float kscale = *(const LAS float*)(lds + SY_VAL + 8 + hc * 4);
    STAMP(6);
    {
      const float dskip = hc ? hi_f(dsk2) : lo_f(dsk2);
      const int G = lane >> 4, q4 = (lane & 15) >> 2, p = lane & 3;
      const LAS char* pa = lds + KF + hc * CV_KSTR + 96 + 2 * (76 + 16 * p - 32 * G - 4 * q4 - 16);
      const LAS char* pb = lds + CV_U + hc * CV_USTR + 160 + 2 * (-76 + 32 * G + 4 * q4 + 4 * p);
      const LAS char* pu = lds + CV_U + hc * CV_USTR + 160 + lane * 2;
      f16x8 A[16], B[32];
#define CV_A(q) __builtin_bit_cast(f16x8, cat8(trrd(pa + 256 * (q) + 32), trrd(pa + 256 * (q))))
#define CV_B(w) __builtin_bit_cast(f16x8, cat8(trrd(pb + 128 * (w)), trrd(pb + 128 * (w) + 32)))
      A[0] = CV_A(0); B[0] = CV_B(0); B[1] = CV_B(1);
      A[1] = CV_A(1); B[2] = CV_B(2); B[3] = CV_B(3);
      f32x4 ca[2][2];
      _Float16 us[2][2];
      unsigned so16[16];
      float c3 = 0.f, c2 = 0.f, c1 = 0.f;
      auto tile_y = [&](const f32x4 a) __attribute__((always_inline)) {
        const float h3 = a[3];
        const float h2 = a[2] + shr_carry(c3, h3);
        const float h1 = a[1] + shr_carry(c2, h2);
        const float y = a[0] + shr_carry(c1, h1);
        c3 = dpp_mov<0x13C>(h3); c2 = dpp_mov<0x13C>(h2); c1 = dpp_mov<0x13C>(h1);
        return y * kscale;
      };
      static_for<0, CV_NP + 1>([&](auto jc) {
        constexpr int jj = decltype(jc)::value;
        if constexpr (jj + 2 < CV_NP) { A[jj + 2] = CV_A(jj + 2); B[2 * jj + 4] = CV_B(2 * jj + 4); B[2 * jj + 5] = CV_B(2 * jj + 5); }
        if constexpr (jj < CV_NP) {
          us[jj & 1][0] = *(const LAS _Float16*)(pu + jj * 256); us[jj & 1][1] = *(const LAS _Float16*)(pu + jj * 256 + 128);
          f32x4 a0 = {0.f, 0.f, 0.f, 0.f}, a1 = {0.f, 0.f, 0.f, 0.f};
#pragma unroll
          for (int q = jj; q >= 0; --q) {
            a0 = __builtin_amdgcn_mfma_f32_16x16x32_f16(A[q], B[2 * (jj - q)], a0, 0, 0, 0);
            a1 = __builtin_amdgcn_mfma_f32_16x16x32_f16(A[q], B[2 * (jj - q) + 1], a1, 0, 0, 0);
          }
          ca[jj & 1][0] = a0; ca[jj & 1][1] = a1;
        }
        if constexpr (jj >= 1) {
          const float y0 = tile_y(ca[(jj - 1) & 1][0]), y1 = tile_y(ca[(jj - 1) & 1][1]);
          const unsigned yq = pk_bf16(y0, y1);
          const unsigned uq = pk_bf16((float)us[(jj - 1) & 1][0] * dskip, (float)us[(jj - 1) & 1][1] * dskip);
          so16[jj - 1] = pk_bf16(lo_f(yq) + lo_f(uq), hi_f(yq) + hi_f(uq));
        }
        if constexpr (jj >= 1 && jj < CV_NP) {
#pragma unroll
          for (int k = 0; k < 2 * (jj + 1); ++k) {
            __builtin_amdgcn_sched_group_barrier(0x8, 1, 0);
            __builtin_amdgcn_sched_group_barrier(0x100, 1, 0);
            __builtin_amdgcn_sched_group_barrier(0x2, jj < 7 ? 4 : 2, 0);
          }
        }
        SCHED_FENCE();
        if constexpr (jj >= 1) st32c(so16[jj - 1], lane * 4u, r_s, (unsigned)(dch * 4096 + (jj - 1) * 256));
      });
    }
    STAMP(7);
    publish<4>(lds, 2, ws, FL_S + (unsigned)((lane * 8 + (cp >> 5)) * 256 + (cp & 31) * 4), 32, tag, lane);
    STAMP(9);
    EPI_X();
    lds_wait<1>(ldsb + SY_SR, 1u);
    EPI_S();
    STAMP(12);
  }
  bar_lgkm();
  {
    int t2 = tid; PIN(t2);
    const u32x4 xv = *(const LAS u32x4*)(lds + EPX + t2 * 16);
    unsigned sv[8];
#pragma unroll
    for (int j = 0; j < 8; ++j) sv[j] = *(const LAS unsigned*)(lds + EPS + ((t2 & 7) * 8 + j) * 256 + (((t2 >> 5) + (t2 & 7)) & 15) * 16 + ((t2 >> 3) & 3) * 4);
    const int rb = w8 < 5 ? RED_A + w8 * 8192 : RED_B + (w8 - 5) * 8192;
    const int tr7 = (t2 >> 3) & 7;
    const LAS char* rp = lds + rb + tr7 * 256 + ((t2 >> 2) & 1) * 128;
    const bool odd = tt & 1;
    u32x4 o;
#pragma unroll
    for (int i = 0; i < 2; ++i) {
      const int ch = (((t2 & 3) * 2 + i) ^ tr7) << 4;
      f32x4 v = *(const LAS f32x4*)(rp + ch);
#pragma unroll
      for (int w = 1; w < 4; ++w) v += *(const LAS f32x4*)(rp + w * 2048 + ch);
#pragma unroll
      for (int jj = 0; jj < 2; ++jj) {
        const unsigned fp = pk_bf16(v[2 * jj], v[2 * jj + 1]);
        const float f0 = lo_f(fp), f1 = hi_f(fp);
        const float g0 = f0 * __builtin_amdgcn_rcpf(1.0f + __builtin_amdgcn_exp2f(f0 * -1.4426950408889634f));
        const float g1 = f1 * __builtin_amdgcn_rcpf(1.0f + __builtin_amdgcn_exp2f(f1 * -1.4426950408889634f));
        const unsigned gp = pk_bf16(g0, g1);
        const int j = 4 * i + 2 * jj;
        const float s0 = odd ? hi_f(sv[j]) : lo_f(sv[j]), s1 = odd ? hi_f(sv[j + 1]) : lo_f(sv[j + 1]);
        const unsigned sg = pk_bf16(s0 * lo_f(gp), s1 * hi_f(gp));
        const unsigned xw = xv[2 * i + jj];
        o[2 * i + jj] = pk_bf16(lo_f(xw) + lo_f(sg), hi_f(xw) + hi_f(sg));
      }
    }
    *(u32x4*)((char*)out + XO(t2)) = o;
  }
  STAMP(13); STAMP_RT(15);
  STAMP_OUT(0);
  BW_OUT();
}

extern "C" void kernel_launch(void* const* d_in, const int* in_sizes, int n_in, void* d_out,
                              int out_size, void* d_ws, size_t ws_size, hipStream_t stream) {
  (void)hipFuncSetAttribute((const void*)fused_kernel, hipFuncAttributeMaxDynamicSharedMemorySize, LDS_BYTES);
  fused_kernel<<<256, 512, LDS_BYTES, stream>>>(
      (const unsigned short*)d_in[0], (const unsigned short*)d_in[1], (const unsigned short*)d_in[2],
      (const unsigned short*)d_in[3], (const unsigned short*)d_in[4], (const unsigned short*)d_in[5],
      (const unsigned short*)d_in[6], (const unsigned*)d_in[7], (const unsigned*)d_in[8],
      (const unsigned short*)d_in[10], (const unsigned short*)d_in[11], (const unsigned short*)d_in[12],
      (char*)d_ws, (unsigned short*)d_out);
}
```

```cpp
#include <hip/hip_runtime.h>
#include <hip/hip_bf16.h>
#include <stdint.h>
#include <type_traits>

typedef __attribute__((ext_vector_type(4))) short s16x4;
typedef __attribute__((ext_vector_type(8))) short s16x8;
typedef __attribute__((ext_vector_type(8))) _Float16 f16x8;
typedef __attribute__((ext_vector_type(8))) __bf16 bf16x8;
typedef __attribute__((ext_vector_type(2))) float f32x2;
typedef __attribute__((ext_vector_type(4))) float f32x4;
typedef __attribute__((ext_vector_type(16))) float f32x16;
typedef __attribute__((ext_vector_type(4))) unsigned u32x4;
typedef __attribute__((ext_vector_type(3))) unsigned u32x3;
typedef __attribute__((ext_vector_type(2))) unsigned u32x2;
typedef __attribute__((ext_vector_type(2))) __bf16 bf16x2;
typedef __attribute__((ext_vector_type(2))) _Float16 f16x2;
#define LAS __attribute__((address_space(3)))
#define GAS __attribute__((address_space(1)))
typedef __amdgpu_buffer_rsrc_t rsrc_t;

constexpr int SL = 2048;
constexpr int DM = 512;
constexpr int NM = 128;
constexpr int FF = 1792;

constexpr size_t WS_XNF = 0;
constexpr size_t WS_ST = 2 * (size_t)SL * DM * 2;
constexpr size_t WS_HF = WS_ST + (size_t)SL * DM * 2;
constexpr size_t WS_FLAG = 2 * (size_t)SL * DM;
constexpr unsigned FL_ALL = 0, FL_STAT = 65536, FL_H = 131072, FL_S = 196608, FL_EP = 262144, FL_BYTES = 262400;

template <int I, int N, class F>
__device__ __forceinline__ void static_for(F&& f) {
  if constexpr (I < N) { f(std::integral_constant<int, I>{}); static_for<I + 1, N>(f); }
}

__device__ __forceinline__ unsigned pk_bf16(float lo, float hi) {
  f32x2 v = {lo, hi};
  return __builtin_bit_cast(unsigned, __builtin_convertvector(v, bf16x2));
}
__device__ __forceinline__ unsigned pk_f16(float lo, float hi) {
  f32x2 v = {lo, hi};
  return __builtin_bit_cast(unsigned, __builtin_convertvector(v, f16x2));
}
__device__ __forceinline__ float lo_f(unsigned p) { return __uint_as_float(p << 16); }
__device__ __forceinline__ float hi_f(unsigned p) { return __uint_as_float(p & 0xffff0000u); }
__device__ __forceinline__ float rbf(float x) { return lo_f(pk_bf16(x, x)); }

template <int CTRL>
__device__ __forceinline__ float dpp_mov(float v) {
  return __int_as_float(__builtin_amdgcn_update_dpp(0, __float_as_int(v), CTRL, 0xf, 0xf, true));
}
__device__ __forceinline__ float shr_carry(float c, float v) {
  return __builtin_bit_cast(float, __builtin_amdgcn_update_dpp(__builtin_bit_cast(int, c), __builtin_bit_cast(int, v), 0x138, 0xf, 0xf, false));
}
__device__ __forceinline__ float rdlane(float v, int l) {
  return __int_as_float(__builtin_amdgcn_readlane(__float_as_int(v), l));
}
__device__ __forceinline__ float wave_sum(float v) {
  v += dpp_mov<0xB1>(v);
  v += dpp_mov<0x4E>(v);
  v += dpp_mov<0x141>(v);
  v += dpp_mov<0x140>(v);
  return (rdlane(v, 0) + rdlane(v, 16)) + (rdlane(v, 32) + rdlane(v, 48));
}
__device__ __forceinline__ float row_sum(float v) {
  v += dpp_mov<0xB1>(v);
  v += dpp_mov<0x4E>(v);
  v += dpp_mov<0x141>(v);
  v += dpp_mov<0x140>(v);
  return v;
}
__device__ __forceinline__ float half_sum(float v, int lane) {
  v += dpp_mov<0xB1>(v);
  v += dpp_mov<0x4E>(v);
  v += dpp_mov<0x141>(v);
  v += dpp_mov<0x140>(v);
  const float lo = rdlane(v, 0) + rdlane(v, 16), hi = rdlane(v, 32) + rdlane(v, 48);
  return lane < 32 ? lo : hi;
}
__device__ __forceinline__ float wave_max(float v) {
  v = fmaxf(v, dpp_mov<0xB1>(v));
  v = fmaxf(v, dpp_mov<0x4E>(v));
  v = fmaxf(v, dpp_mov<0x141>(v));
  v = fmaxf(v, dpp_mov<0x140>(v));
  return fmaxf(fmaxf(rdlane(v, 0), rdlane(v, 16)), fmaxf(rdlane(v, 32), rdlane(v, 48)));
}

__device__ __forceinline__ rsrc_t mk_rsrc(const void* p, unsigned bytes) {
  return __builtin_amdgcn_make_buffer_rsrc((void*)p, (short)0, (int)bytes, 0x00020000);
}
__device__ __forceinline__ void dma16(unsigned lds_dst, unsigned voff, rsrc_t r, unsigned soff) {
  asm volatile("s_mov_b32 m0, %0\n\ts_nop 0\n\tbuffer_load_dwordx4 %1, %2, %3 offen lds"
               :: "s"(lds_dst), "v"(voff), "s"(r), "s"(soff) : "memory");
}
__device__ __forceinline__ void dma16x4(unsigned lds_dst, unsigned v0, unsigned v1, unsigned v2, unsigned v3, rsrc_t r, unsigned soff) {
  asm volatile("s_mov_b32 m0, %0\n\ts_nop 0\n\t"
               "buffer_load_dwordx4 %1, %5, %6 offen lds\n\t"
               "buffer_load_dwordx4 %2, %5, %6 offen offset:1024 lds\n\t"
               "buffer_load_dwordx4 %3, %5, %6 offen offset:2048 lds\n\t"
               "buffer_load_dwordx4 %4, %5, %6 offen offset:3072 lds"
               :: "s"(lds_dst), "v"(v0), "v"(v1), "v"(v2), "v"(v3), "s"(r), "s"(soff) : "memory");
}
__device__ __forceinline__ void dma16x3(unsigned lds_dst, unsigned v0, unsigned v1, unsigned v2, rsrc_t r, unsigned soff) {
  asm volatile("s_mov_b32 m0, %0\n\ts_nop 0\n\t"
               "buffer_load_dwordx4 %1, %4, %5 offen lds\n\t"
               "buffer_load_dwordx4 %2, %4, %5 offen offset:1024 lds\n\t"
               "buffer_load_dwordx4 %3, %4, %5 offen offset:2048 lds"
               :: "s"(lds_dst), "v"(v0), "v"(v1), "v"(v2), "s"(r), "s"(soff) : "memory");
}
__device__ __forceinline__ void ld128(u32x4& dst, unsigned voff, rsrc_t r, unsigned soff) {
  asm volatile("buffer_load_dwordx4 %0, %1, %2, %3 offen" : "=v"(dst) : "v"(voff), "s"(r), "s"(soff) : "memory");
}
__device__ __forceinline__ void ld128c(u32x4& dst, unsigned voff, rsrc_t r, unsigned soff) {
  asm volatile("buffer_load_dwordx4 %0, %1, %2, %3 offen sc1" : "=v"(dst) : "v"(voff), "s"(r), "s"(soff) : "memory");
}
__device__ __forceinline__ void ld32(unsigned& dst, unsigned voff, rsrc_t r, unsigned soff) {
  asm volatile("buffer_load_dword %0, %1, %2, %3 offen" : "=v"(dst) : "v"(voff), "s"(r), "s"(soff) : "memory");
}
__device__ __forceinline__ void ld64c(u32x2& dst, unsigned voff, rsrc_t r, unsigned soff) {
  asm volatile("buffer_load_dwordx2 %0, %1, %2, %3 offen sc1" : "=v"(dst) : "v"(voff), "s"(r), "s"(soff) : "memory");
}
__device__ __forceinline__ void ld32c(unsigned& dst, unsigned voff, rsrc_t r, unsigned soff) {
  asm volatile("buffer_load_dword %0, %1, %2, %3 offen sc1" : "=v"(dst) : "v"(voff), "s"(r), "s"(soff) : "memory");
}
__device__ __forceinline__ void st32c(unsigned v, unsigned voff, rsrc_t r, unsigned soff) {
  asm volatile("buffer_store_dword %0, %1, %2, %3 offen sc1" :: "v"(v), "v"(voff), "s"(r), "s"(soff) : "memory");
}
__device__ __forceinline__ void st64c(u32x2 v, unsigned voff, rsrc_t r, unsigned soff) {
  asm volatile("buffer_store_dwordx2 %0, %1, %2, %3 offen sc1\n\ts_nop 1" :: "v"(v), "v"(voff), "s"(r), "s"(soff) : "memory");
}
__device__ __forceinline__ void ld16(unsigned& dst, unsigned voff, rsrc_t r, unsigned soff) {
  asm volatile("buffer_load_ushort %0, %1, %2, %3 offen" : "=v"(dst) : "v"(voff), "s"(r), "s"(soff) : "memory");
}
__device__ __forceinline__ void st128c(u32x4 v, unsigned voff, rsrc_t r, unsigned soff) {
  asm volatile("buffer_store_dwordx4 %0, %1, %2, %3 offen sc1\n\ts_nop 1" :: "v"(v), "v"(voff), "s"(r), "s"(soff) : "memory");
}
template <int N>
__device__ __forceinline__ void wait_vm() { asm volatile("s_waitcnt vmcnt(%0)" :: "n"(N > 63 ? 63 : N) : "memory"); }
__device__ __forceinline__ void bar_lgkm() { asm volatile("s_waitcnt lgkmcnt(0)\n\ts_barrier" ::: "memory"); }
__device__ __forceinline__ void bar_only() { asm volatile("s_barrier" ::: "memory"); }
#define BW_DECL
#define BARX(f) f()
#define BW_OUT()
#define PIN(x) asm volatile("" : "+v"(x))
#define SCHED_FENCE() __builtin_amdgcn_sched_barrier(0)

__device__ __forceinline__ s16x4 trrd(const LAS char* p) {
  return __builtin_amdgcn_ds_read_tr16_b64_v4i16((LAS s16x4*)p);
}
__device__ __forceinline__ s16x8 cat8(s16x4 a, s16x4 b) {
  return __builtin_shufflevector(a, b, 0, 1, 2, 3, 4, 5, 6, 7);
}

__device__ __forceinline__ bool wave_all(bool p) { return __builtin_amdgcn_ballot_w64(!p) == 0ull; }
template <int SLP = 2>
__device__ __forceinline__ void poll_flags(unsigned voff, rsrc_t r, unsigned soff, unsigned tag) {
#pragma unroll 1
  for (int it = 0; it < (1 << 18); ++it) {
    u32x4 v;
    asm volatile("buffer_load_dwordx4 %0, %1, %2, %3 offen sc1\n\ts_waitcnt vmcnt(0)"
                 : "=&v"(v) : "v"(voff), "s"(r), "s"(soff) : "memory");
    const bool ok = (v[0] == tag) & (v[1] == tag) & (v[2] == tag) & (v[3] == tag);
    if (wave_all(ok)) break;
    __builtin_amdgcn_s_sleep(SLP);
  }
}
__device__ __forceinline__ void set_flag(char* ws, unsigned off, unsigned tag) {
  __hip_atomic_store((GAS unsigned*)(ws + WS_FLAG + off), tag, __ATOMIC_RELAXED, __HIP_MEMORY_SCOPE_AGENT);
}

#define STAMP_DECL
#define STAMP(i)
#define STAMP_RT(i)
#define STAMP_OUT(kid)

__device__ __forceinline__ f32x2 cmul(f32x2 a, f32x2 b) {
  f32x2 r;
  r.x = fmaf(a.x, b.x, -(a.y * b.y));
  r.y = fmaf(a.x, b.y, a.y * b.x);
  return r;
}
__device__ __forceinline__ f32x2 cexp_l(float a, float b, float l) {
  const float mag = __builtin_amdgcn_exp2f(a * l * 1.4426950408889634f);
  const float x = b * l;
  const float C1 = 0.15915494309189535f;
  const float C2 = (float)(0.15915494309189533577 - (double)0.15915494309189535f);
  const float hi = x * C1;
  const float lo = fmaf(x, C1, -hi) + x * C2;
  const float r = (hi - __builtin_rintf(hi)) + lo;
  f32x2 z;
  z.x = mag * __builtin_amdgcn_cosf(r);
  z.y = mag * __builtin_amdgcn_sinf(r);
  return z;
}

constexpr int W_UPOFF = 14336 + 128;
constexpr int XOFF = W_UPOFF + 14336;
constexpr int STG = XOFF + 16384;
constexpr int KGS = 3 * STG;
constexpr int KF = KGS + 8192;
constexpr int CV_KSTR = (48 + 2048) * 2;
constexpr int CV_USTR = (80 + 2048) * 2;
constexpr int CV_U = KF + 2 * CV_KSTR;
constexpr int CSUM = CV_U + 2 * CV_USTR;
constexpr int STAT = CSUM + 896;
constexpr int SY = STAT + 512;
constexpr int SY_LD = SY, SY_MC = SY + 16, SY_ST = SY + 4, SY_HR = SY + 8, SY_SR = SY + 12, SY_UR = SY + 32  ,
              SY_CNT = SY + 48, SY_VAL = SY + 64, SY_KG = SY + 80, SY_END = SY + 96;
constexpr int LDS_BYTES = SY_END;
static_assert(LDS_BYTES <= 163840, "lds");
constexpr int G2_RED = 272;
constexpr int EPX = KGS;
constexpr int EPS = KF;
#define WSL 4
constexpr unsigned WS_USED = (unsigned)(WS_HF + (size_t)SL * FF * 2);
constexpr unsigned CODE_BYTES = 49152;
constexpr int CV_NP = 12;
constexpr int G2_NR = 13;
constexpr int cmax(int a, int b) { return a > b ? a : b; }
constexpr int cmin(int a, int b) { return a < b ? a : b; }
constexpr int g2_base(int i) { return (((i % 15) / 5 + 2) % 3) * STG + (i % 5) * 8192; }
constexpr int RED_A = g2_base(15), RED_B = g2_base(20);
constexpr int G2_H0 = 6;
constexpr int g2_issued(int i) { return cmin(28, G2_H0 + (3 * i) / 2); }

__device__ __forceinline__ void lds_setv(unsigned addr, unsigned v) {
  asm volatile("ds_write_b32 %0, %1" :: "v"(addr), "v"(v) : "memory");
}
__device__ __forceinline__ unsigned lds_min4(unsigned addr) {
  u32x4 v;
  asm volatile("ds_read_b128 %0, %1\n\ts_waitcnt lgkmcnt(0)" : "=&v"(v) : "v"(addr) : "memory");
  return (unsigned)__builtin_amdgcn_readfirstlane(min(min(v[0], v[1]), min(v[2], v[3])));
}
__device__ __forceinline__ unsigned lds_get1(unsigned addr) {
  unsigned v;
  asm volatile("ds_read_b32 %0, %1\n\ts_waitcnt lgkmcnt(0)" : "=&v"(v) : "v"(addr) : "memory");
  return (unsigned)__builtin_amdgcn_readfirstlane(v);
}
template <int W>
__device__ __forceinline__ unsigned lds_val(unsigned addr) { return W == 4 ? lds_min4(addr) : lds_get1(addr); }
template <int W>
__device__ __forceinline__ void lds_spin(unsigned addr, unsigned need) {
#pragma unroll 1
  for (int it = 0; it < (1 << 20); ++it) {
    if (lds_val<W>(addr) >= need) break;
    __builtin_amdgcn_s_sleep(1);
  }
}
template <int W>
__device__ __forceinline__ void lds_wait(unsigned addr, unsigned need) {
  if (__builtin_expect(lds_val<W>(addr) < need, 0)) lds_spin<W>(addr, need);
}
template <int NW, bool DRAIN = true>
__device__ __forceinline__ void publish(LAS char* lds, int cnt, char* ws, unsigned off, int nl, unsigned tag, int lane) {
  if (DRAIN) wait_vm<0>();
  unsigned old = 0;
  if (lane == 0) old = __hip_atomic_fetch_add((LAS unsigned*)(lds + SY_CNT + cnt * 4), 1u, __ATOMIC_RELAXED, __HIP_MEMORY_SCOPE_WORKGROUP);
  if (__builtin_amdgcn_readfirstlane(old) == NW - 1 && lane < nl) set_flag(ws, off, tag);
}

template <int PA, int PB>
__device__ __forceinline__ void conv_upper(LAS char* lds, int lane, int hc, int cp, unsigned dsk2, rsrc_t r_s) {
  const float kscale = *(const LAS float*)(lds + SY_VAL + 8 + hc * 4);
  const float dskip = hc ? hi_f(dsk2) : lo_f(dsk2);
  const int G = lane >> 4, q4 = (lane & 15) >> 2, p = lane & 3;
  const LAS char* pa = lds + KF + hc * CV_KSTR + 96 + 2 * (76 + 16 * p - 32 * G - 4 * q4 - 16);
  const LAS char* pb = lds + CV_U + hc * CV_USTR + 160 + 2 * (-76 + 32 * G + 4 * q4 + 4 * p);
  const LAS char* pu = lds + CV_U + hc * CV_USTR + 160 + lane * 2;
  constexpr int J0 = 2 * PA - 1, J1 = 2 * PB, NT = J1 - J0;
  f16x8 A[16], B[32];
  f32x4 ca[NT];
#define CV_A(q) __builtin_bit_cast(f16x8, cat8(trrd(pa + 256 * (q) + 32), trrd(pa + 256 * (q))))
#define CV_B(w) __builtin_bit_cast(f16x8, cat8(trrd(pb + 128 * (w)), trrd(pb + 128 * (w) + 32)))
  B[0] = CV_B(0); B[1] = CV_B(1); B[2] = CV_B(2);
  constexpr int QA = (J0 - 1) / 2;
#pragma unroll
  for (int q = PB - 1; q >= QA; --q) A[q] = CV_A(q);
#pragma unroll
  for (int j = 0; j < NT; ++j) ca[j] = (f32x4){0.f, 0.f, 0.f, 0.f};
  static_for<0, J1>([&](auto wc) {
    constexpr int w = decltype(wc)::value;
    if constexpr (w + 3 < J1) B[w + 3] = CV_B(w + 3);
    if constexpr ((w & 1) == 0 && QA - 1 - w / 2 >= 0) A[QA - 1 - w / 2] = CV_A(QA - 1 - w / 2);
#pragma unroll
    for (int q = PB - 1; q >= 0; --q) {
      const int j = w + 2 * q;
      if (j >= J0 && j < J1) ca[j - J0] = __builtin_amdgcn_mfma_f32_16x16x32_f16(A[q], B[w], ca[j - J0], 0, 0, 0);
    }
    SCHED_FENCE();
  });
  float c3 = 0.f, c2 = 0.f, c1 = 0.f, yv[NT];
#pragma unroll
  for (int j = 0; j < NT; ++j) {
    const f32x4 a = ca[j];
    const float h3 = a[3];
    const float h2 = a[2] + shr_carry(c3, h3);
    const float h1 = a[1] + shr_carry(c2, h2);
    yv[j] = a[0] + shr_carry(c1, h1);
    c3 = dpp_mov<0x13C>(h3); c2 = dpp_mov<0x13C>(h2); c1 = dpp_mov<0x13C>(h1);
  }
#pragma unroll
  for (int jp = PA; jp < PB; ++jp) {
    const float u0 = (float)*(const LAS _Float16*)(pu + jp * 256), u1 = (float)*(const LAS _Float16*)(pu + jp * 256 + 128);
    const unsigned yq = pk_bf16(yv[2 * jp - J0] * kscale, yv[2 * jp + 1 - J0] * kscale);
    const unsigned uq = pk_bf16(u0 * dskip, u1 * dskip);
    st32c(pk_bf16(lo_f(yq) + lo_f(uq), hi_f(yq) + hi_f(uq)), lane * 4u, r_s, (unsigned)((cp * 2 + hc) * 4096 + jp * 256));
  }
#undef CV_A
#undef CV_B
}

#if defined(__HIP_DEVICE_COMPILE__)
__attribute__((target("no-packed-fp32-ops")))
#endif
__global__ __launch_bounds__(512, 2) void fused_kernel(
    const unsigned short* __restrict__ x, const unsigned short* __restrict__ A_re,
    const unsigned short* __restrict__ A_im, const unsigned short* __restrict__ B_re,
    const unsigned short* __restrict__ B_im, const unsigned short* __restrict__ C_re,
    const unsigned short* __restrict__ C_im, const unsigned* __restrict__ log_dt2,
    const unsigned* __restrict__ dsk2p, const unsigned short* __restrict__ wg,
    const unsigned short* __restrict__ wu, const unsigned short* __restrict__ wd,
    char* ws, unsigned short* __restrict__ out) {
  extern __shared__ __attribute__((aligned(16))) char smem[];
  unsigned long long pc0;
  asm volatile("s_getpc_b64 %0" : "=s"(pc0));
  LAS char* lds = (LAS char*)smem;
  const unsigned ldsb = (unsigned)(uintptr_t)lds;
  const int tid = threadIdx.x, lane = tid & 63;
  const int w8 = __builtin_amdgcn_readfirstlane(tid >> 6);
  const int wv = w8 & 3;
  STAMP_DECL; BW_DECL; STAMP_RT(14); STAMP(0);
  const int bid = blockIdx.x;
  const int xcd = bid & 7, bi = bid >> 3;
  unsigned pf0 = 0, pf1 = 0, pf2 = 0, pf3 = 0;
  if (w8 == 5) ld32(pf0, (unsigned)((bi * 16 + (lane & 15)) * 128), mk_rsrc((const void*)pc0, CODE_BYTES), 0u);
  asm volatile("" :: "s"(x), "s"(A_re), "s"(A_im), "s"(B_re), "s"(B_im), "s"(C_re), "s"(C_im), "s"(log_dt2),
               "s"(dsk2p), "s"(wg), "s"(wu), "s"(wd), "s"(ws), "s"(out));
  if (w8 == 5) {
  }
  const int mt = (xcd >> 2) * 8 + (bi >> 2);
  const int ft = (xcd & 3) * 4 + (bi & 3);
  const int rot = ((bi >> 2) + 2 * (bi & 3)) & 7;
  const int cp = xcd * 32 + bi;
  const int tt = xcd * 4 + (bi >> 3);
  const int dtile = bi & 7;
  const int rot2 = ((bi >> 3) * 7 + dtile * 3) % 28;

  const rsrc_t r_x = mk_rsrc(x, SL * DM * 2);
  const rsrc_t r_s = mk_rsrc(ws + WS_ST, SL * DM * 2);
  const rsrc_t r_fl = mk_rsrc(ws + WS_FLAG, FL_BYTES);
#define G2_KS(w, i) ((unsigned)((w) * 28 + (((i) + rot2) >= 28 ? (i) + rot2 - 28 : (i) + rot2)))
#define XO(t_) ((unsigned)((tt * 64 + ((t_) >> 3)) * (DM * 2) + dtile * 128 + ((t_) & 7) * 16))
#define EPI_X() do { int l_ = lane; PIN(l_); u32x4 xw_[2]; \
    _Pragma("unroll") for (int j = 0; j < 2; ++j) ld128(xw_[j], XO(wv * 128 + j * 64 + l_), r_x, 0u); \
    lds_wait<4>(ldsb + SY_UR, 1u); wait_vm<0>(); \
    _Pragma("unroll") for (int j = 0; j < 2; ++j) { PIN(xw_[j]); *(LAS u32x4*)(lds + EPX + (wv * 128 + j * 64 + l_) * 16) = xw_[j]; } } while (0)
#define EPI_S() do { int l_ = lane; PIN(l_); u32x4 sw_[4]; \
    const unsigned so = (unsigned)((dtile * 64 + wv * 16 + (l_ >> 4)) * 4096 + (tt >> 1) * 256 + (l_ & 15) * 16); \
    _Pragma("unroll") for (int j = 0; j < 4; ++j) ld128c(sw_[j], so, r_s, (unsigned)(j * 16384)); \
    wait_vm<0>(); \
    _Pragma("unroll") for (int j = 0; j < 4; ++j) { PIN(sw_[j]); \
      *(LAS u32x4*)(lds + EPS + (wv * 16 + j * 4 + (l_ >> 4)) * 256 + ((l_ + wv * 2 + (j >> 1)) & 15) * 16) = sw_[j]; } } while (0)

  if (w8 < 4) {
    const rsrc_t r_h = mk_rsrc(ws + WS_HF, SL * FF * 2);
    {
    const int hc = wv & 1, m = wv >> 1;
    const int dch = cp * 2 + hc;
    unsigned prm[6];
    {
      const unsigned po = (unsigned)(dch * NM * 2);
      ld32(prm[0], lane * 4u, mk_rsrc(A_re, DM * NM * 2), po);
      ld32(prm[1], lane * 4u, mk_rsrc(A_im, DM * NM * 2), po);
      ld32(prm[2], lane * 4u, mk_rsrc(B_re, DM * NM * 2), po);
      ld32(prm[3], lane * 4u, mk_rsrc(B_im, DM * NM * 2), po);
      ld32(prm[4], lane * 4u, mk_rsrc(C_re, DM * NM * 2), po);
      ld32(prm[5], lane * 4u, mk_rsrc(C_im, DM * NM * 2), po);
    }
    if (tid < 5) *(LAS u32x4*)(lds + SY + tid * 16) = (u32x4){0u, 0u, 0u, tid == 2 ? 1u : 0u};
    bar_lgkm();
    __builtin_amdgcn_s_setprio(2);
    const unsigned ldt2 = log_dt2[cp];
    {
      const float ldt = hc ? hi_f(ldt2) : lo_f(ldt2);
      const float L2E = 1.4426950408889634f, L2E_LO = (float)(1.4426950408889634074 - (double)1.4426950408889634f);
      const float th = ldt * L2E;
      const float tl = fmaf(ldt, L2E, -th) + ldt * L2E_LO;
      const float e0 = __builtin_amdgcn_exp2f(th);
      const float dt = rbf(fmaf(e0 * 0.6931471805599453f, tl, e0));
      wait_vm<0>();
#pragma unroll
      for (int i = 0; i < 6; ++i) PIN(prm[i]);
      float av[2], bv[2];
      f32x2 wq[2];
#pragma unroll
      for (int m = 0; m < 2; ++m) {
        av[m] = dt * (m ? hi_f(prm[0]) : lo_f(prm[0]));
        bv[m] = dt * (m ? hi_f(prm[1]) : lo_f(prm[1]));
        const f32x2 db = {dt * (m ? hi_f(prm[2]) : lo_f(prm[2])), dt * (m ? hi_f(prm[3]) : lo_f(prm[3]))};
        const f32x2 cc = {m ? hi_f(prm[4]) : lo_f(prm[4]), m ? hi_f(prm[5]) : lo_f(prm[5])};
        wq[m] = cmul(cc, db);
      }
      const float amax = fmaxf(wave_max(fmaxf(av[0], av[1])), 0.f);
      const float g2 = amax * L2E;
      const float wm = wave_max(fmaxf(fmaxf(fabsf(wq[0].x), fabsf(wq[0].y)), fmaxf(fabsf(wq[1].x), fabsf(wq[1].y))));
      int we = __builtin_amdgcn_frexp_expf(wm);
      we = we < -100 ? -100 : we;
      const float wsc = __builtin_amdgcn_ldexpf(1.0f, -we);
      f32x4 kacc[3];
#pragma unroll
      for (int t = 0; t < 3; ++t) kacc[t] = (f32x4){0.f, 0.f, 0.f, 0.f};
      const int kgs = (m ? CV_U : KGS) + hc * 4096;
      LAS char* tw[4];
#pragma unroll
      for (int c = 0; c < 4; ++c) tw[c] = lds + kgs + lane * 64 + ((c ^ ((lane >> 1) & 3)) << 4);
      const LAS char* tr[2];
      {
        const int G = lane >> 4, q4 = (lane & 15) >> 2, p = lane & 3;
#pragma unroll
        for (int t = 0; t < 2; ++t)
          tr[t] = lds + kgs + (4 * G + 2 * t + (q4 >> 1)) * 64 + ((((q4 & 1) * 2 + (p >> 1)) ^ ((2 * G + t) & 3)) << 4) + (p & 1) * 8;
      }
      auto put16 = [&](const f32x2 (&e)[16]) __attribute__((always_inline)) {
#pragma unroll
        for (int h = 0; h < 2; ++h) {
          u32x4 re, im;
#pragma unroll
          for (int j = 0; j < 4; ++j) {
            re[j] = pk_f16(e[8 * h + 2 * j].x, e[8 * h + 2 * j + 1].x);
            im[j] = pk_f16(e[8 * h + 2 * j].y, e[8 * h + 2 * j + 1].y);
          }
          *(LAS u32x4*)tw[h] = re;
          *(LAS u32x4*)tw[2 + h] = im;
        }
      };
#define KG_FRAG(s) __builtin_bit_cast(f16x8, cat8(trrd(tr[0] + (s) * 1024), trrd(tr[1] + (s) * 1024)))
#define KG_RD() do { _Pragma("unroll") for (int s = 0; s < 4; ++s) pf[s] = KG_FRAG(s); SCHED_FENCE(); } while (0)
#define KG_MMA(t) do { SCHED_FENCE(); _Pragma("unroll") for (int s = 0; s < 4; ++s) \
        kacc[t] = __builtin_amdgcn_mfma_f32_16x16x32_f16(pf[s], qf[s], kacc[t], 0, 0, 0); } while (0)
      {
        const float a = (m ? av[1] : av[0]) - amax, b = m ? bv[1] : bv[0];
        f32x2 w = m ? wq[1] : wq[0];
        w.x *= wsc; w.y *= -wsc;
        f32x2 e[16];
        {
          const f32x2 z16 = cexp_l(a, b, 16.f);
          e[0] = (f32x2){1.f, 0.f}; e[1] = z16; e[2] = cmul(z16, z16); e[3] = cmul(e[2], z16);
          const f32x2 z64 = cmul(e[2], e[2]);
#pragma unroll
          for (int c = 4; c < 16; ++c) e[c] = cmul(e[c - 4], z64);
          put16(e);
        }
        f16x8 qf[4], pf[4];
#pragma unroll
        for (int s = 0; s < 4; ++s) qf[s] = KG_FRAG(s);
        SCHED_FENCE();
        const f32x2 z1 = cexp_l(a, -b, 1.f);
        const f32x2 z2 = cmul(z1, z1), z4 = cmul(z2, z2);
        e[0] = w; e[1] = cmul(w, z1); e[2] = cmul(w, z2); e[3] = cmul(e[1], z2);
#pragma unroll
        for (int c = 4; c < 16; ++c) e[c] = cmul(e[c - 4], z4);
        put16(e);
        KG_RD();
        const f32x2 z256 = cexp_l(a, -b, 256.f);
        const f32x2 z512 = cmul(z256, z256);
        const f32x2 w2 = cmul(w, z512);
        e[0] = cmul(w, z256); e[1] = cmul(e[0], z2); e[8] = w2; e[12] = cmul(e[0], z512);
        const f32x2 w3 = e[12];
#pragma unroll
        for (int i = 1; i < 4; ++i) {
          e[2 * i] = cmul(e[2 * i - 2], z4); e[2 * i + 1] = cmul(e[2 * i - 1], z4);
          e[8 + i] = cmul(e[7 + i], z4); e[12 + i] = cmul(e[11 + i], z4);
        }
        KG_MMA(0);
        put16(e);
        KG_RD();
        const f32x2 z8 = cmul(z4, z4);
        e[0] = cmul(w2, z512); e[2] = cmul(w3, z512); e[4] = cmul(e[0], z512); e[6] = cmul(e[2], z512);
        e[8] = cmul(e[4], z512);
#pragma unroll
        for (int i = 0; i < 4; ++i) e[2 * i + 1] = cmul(e[2 * i], z8);
#pragma unroll
        for (int i = 9; i < 16; ++i) e[i] = (f32x2){0.f, 0.f};
        KG_MMA(1);
        put16(e);
        KG_RD();
        KG_MMA(2);
      }
#pragma unroll
      for (int t = 0; t < 3; ++t) *(LAS f32x4*)(lds + (m ? KF + hc * CV_KSTR : KGS + hc * 4096) + t * 1024 + lane * 16) = kacc[t];
      if (m) *(LAS f32x2*)(lds + SY_KG + hc * 8) = (f32x2){g2, (float)we};
    }
    }
    f32x16 acc[7];
    f32x4 csa[2];
    unsigned tag;
    {
      const int G = lane >> 4;
      const LAS char* wa0 = lds + (G & 1) * W_UPOFF + (8 * (G >> 1) + ((lane & 15) >> 2)) * 224 + (lane & 3) * 8;
      const LAS char* wa[7];
#pragma unroll
      for (int T = 0; T < 7; ++T) wa[T] = wa0 + 32 * ((T + 2 * wv) % 7);
      const LAS char* xa[4];
      {
        const int row = 32 * wv + (lane & 31), sw = (row >> 1) & 7;
#pragma unroll
        for (int ks = 0; ks < 4; ++ks) xa[ks] = lds + XOFF + row * 128 + (((2 * ks + (lane >> 5)) ^ sw) << 4);
      }
      constexpr int PD = 3;
      bf16x8 Af[196], Bf[32];
      const unsigned o1 = ((lane & 15) == 4 * ((lane >> 4) & 1)) ? 0x3f803f80u : 0u;
      u32x4 onesq = {o1, o1, o1, o1};
      PIN(onesq);
      const bf16x8 ones = __builtin_bit_cast(bf16x8, onesq);
#define A_READ(i, T) __builtin_bit_cast(bf16x8, cat8(trrd(wa[T] + (((i) >> 2) % 3) * STG + (16 * ((i) & 3)) * 224), \
                                                      trrd(wa[T] + (((i) >> 2) % 3) * STG + (16 * ((i) & 3) + 4) * 224)))
#define B_READ(i) (*(const LAS bf16x8*)(xa[(i) & 3] + (((i) >> 2) % 3) * STG))
#pragma unroll
      for (int T = 0; T < 7; ++T) acc[T] = (f32x16){};
      csa[0] = (f32x4){0.f, 0.f, 0.f, 0.f}; csa[1] = csa[0];
      BARX(bar_lgkm);
      STAMP(1);
      tag = lds_get1(ldsb + SY_VAL);
      Bf[0] = B_READ(0);
#pragma unroll
      for (int T = 0; T < PD; ++T) Af[T] = A_READ(0, T);
      bf16x8 Al[7][4];
      u32x2 stg;
      float cs[7];
      static_for<0, 196>([&](auto nc) {
        constexpr int n = decltype(nc)::value, i = n / 7, T = n % 7;
        constexpr int m = n + PD, im = m / 7, Tm = m % 7;
        if constexpr (n == 70) ld64c(stg, (unsigned)((lane & 31) * 8), r_fl, FL_STAT + (unsigned)((mt * 128 + wv * 32) * 8));
        if constexpr (n == 27 * 7 + 2) {
          if (lane < 32) {
#pragma unroll
            for (int j = 0; j < 2; ++j) *(LAS float*)(lds + CSUM + ((j + 2 * wv) % 7) * 128 + lane * 4) = csa[j][0];
          }
        }
        if constexpr (Tm == 0 && (im & 3) == 0) BARX(bar_lgkm);
        if constexpr (m < 196) {
          if constexpr (Tm == 0) Bf[im] = B_READ(im);
          Af[m] = A_READ(im, Tm);
        } else if constexpr (m == 196) {
#pragma unroll
          for (int k = 28; k < 32; ++k) Bf[k] = B_READ(k);
        } else if constexpr (m == 197) {
#pragma unroll
          for (int t = 0; t < 7; ++t) cs[t] = *(const LAS float*)(lds + CSUM + ((t + 2 * wv) % 7) * 128 + (lane & 31) * 4);
        } else {
#pragma unroll
          for (int k = 0; k < 4; ++k) Al[0][k] = A_READ(28 + k, 0);
        }
        acc[T] = __builtin_amdgcn_mfma_f32_32x32x16_bf16(Af[n], Bf[i], acc[T], 0, 0, 0);
        if constexpr (T < 2)
          csa[T] = __builtin_amdgcn_mfma_f32_16x16x32_bf16(ones, Af[n], csa[T], 0, 0, 0);
        SCHED_FENCE();
      });
      STAMP(5);
      wait_vm<0>();
      PIN(stg);
      if (__builtin_expect(!wave_all(stg[1] == tag), 0)) {
#pragma unroll 1
        for (int it = 0; it < (1 << 18); ++it) {
          ld64c(stg, (unsigned)((lane & 31) * 8), r_fl, FL_STAT + (unsigned)((mt * 128 + wv * 32) * 8));
          wait_vm<0>();
          PIN(stg);
          if (wave_all(stg[1] == tag)) break;
          __builtin_amdgcn_s_sleep(2);
        }
      }
      const unsigned stw = stg[0];
      const float mb = lo_f(stw), sd = hi_f(stw);
#pragma unroll
      for (int k = 28; k < 32; ++k) {
        u32x4 v = __builtin_bit_cast(u32x4, Bf[k]);
#pragma unroll
        for (int j = 0; j < 4; ++j) v[j] = pk_bf16(lo_f(v[j]) - mb, hi_f(v[j]) - mb);
        Bf[k] = __builtin_bit_cast(bf16x8, v);
      }
      const unsigned nm = (stw & 0xffffu) ^ 0x8000u;
      u32x4 bq = {nm | (nm << 16), nm, 0u, 0u};
      if (lane >= 32) bq = (u32x4){0u, 0u, 0u, 0u};
      const float ga = -1.4426950408889634f * __builtin_amdgcn_rcpf(sd);
      const unsigned hsb = (unsigned)(((mt * 4 + wv) * 112 + ft * 7) * 1024);
      STAMP(6);
      static_for<0, 8>([&](auto tc) {
        constexpr int T = decltype(tc)::value;
        u32x4 o;
        if constexpr (T < 7) {
          const float c = cs[T];
          const float ch = rbf(c), cm = rbf(c - ch), cl = (c - ch) - cm;
          const u32x4 aq = {pk_bf16(ch, cm), pk_bf16(cl, 0.f), 0u, 0u};
          acc[T] = __builtin_amdgcn_mfma_f32_32x32x16_bf16(__builtin_bit_cast(bf16x8, aq), __builtin_bit_cast(bf16x8, bq), acc[T], 0, 0, 0);
          SCHED_FENCE();
        }
#pragma unroll
        for (int j = 0; j < 4; ++j) {
          if constexpr (T + 1 < 7) Al[T + 1][j] = A_READ(28 + j, T + 1);
          if constexpr (T < 7) acc[T] = __builtin_amdgcn_mfma_f32_32x32x16_bf16(Al[T][j], Bf[28 + j], acc[T], 0, 0, 0);
          SCHED_FENCE();
          if constexpr (T >= 1) {
            const float s0 = __builtin_amdgcn_rcpf(fmaf(__builtin_amdgcn_exp2f(acc[T - 1][2 * j] * ga), sd, sd));
            const float s1 = __builtin_amdgcn_rcpf(fmaf(__builtin_amdgcn_exp2f(acc[T - 1][2 * j + 1] * ga), sd, sd));
            o[j] = pk_bf16(s0 * acc[T - 1][8 + 2 * j], s1 * acc[T - 1][8 + 2 * j + 1]);
            PIN(o[j]);
          }
          SCHED_FENCE();
        }
        if constexpr (T >= 1) st128c(o, lane * 16u, r_h, hsb + (unsigned)((T - 1 + 2 * wv) % 7) * 1024u);
      });
    }
    STAMP(7);
    const unsigned ldw = ldsb + SY_LD;
    const LAS char* waT[2];
    {
      const int G = lane >> 4, q4 = (lane & 15) >> 2;
      const LAS char* wa0 = lds + wv * 2048 + (4 * (G >> 1) + q4) * 128 + (G & 1) * 32 + (lane & 3) * 8;
      waT[0] = wa0 + ((q4 >> 1) * 64); waT[1] = wa0 + (((q4 >> 1) ^ 1) * 64);
    }
#define D_READ(i, T) __builtin_bit_cast(bf16x8, cat8(trrd(waT[T] + g2_base(i)), trrd(waT[T] + g2_base(i) + 1024)))
    bf16x8 Wr[28][2];
    lds_wait<1>(ldw, 2u);
    static_for<0, 5>([&](auto ic) { constexpr int i = decltype(ic)::value; Wr[i][0] = D_READ(i, 0); Wr[i][1] = D_READ(i, 1); });
    static_for<5, 10>([&](auto ic) { constexpr int i = decltype(ic)::value; Wr[i][0] = D_READ(i, 0); Wr[i][1] = D_READ(i, 1); });
    wait_vm<0>();
    if (lane < 8) set_flag(ws, FL_H + (unsigned)(((mt * 2 + (wv >> 1)) * 8 + lane) * 256 + (ft * 2 + (wv & 1)) * 4), tag);
    STAMP(8);
    lds_setv(ldsb + SY_MC + wv * 4, 2u);
    if (wv == 0) {
      lds_wait<4>(ldsb + SY_UR, 1u);
      conv_upper<14, 16>(lds, lane, 0, cp, dsk2p[cp], r_s);
    }
    lds_wait<1>(ldw, 3u);
    static_for<10, G2_NR>([&](auto ic) { constexpr int i = decltype(ic)::value; Wr[i][0] = D_READ(i, 0); Wr[i][1] = D_READ(i, 1); });
    lds_setv(ldsb + SY_MC + wv * 4, 3u);
    STAMP(9);
    __builtin_amdgcn_s_setprio(0);
    poll_flags<2>((lane & 1) * 16u, r_fl, FL_H + (unsigned)((tt * 8 + dtile) * 256 + wv * 32), tag);
    if (wv == 0) publish<4>(lds, 2, ws, FL_S + (unsigned)((lane * 8 + (cp >> 5)) * 256 + (cp & 31) * 4), 32, tag, lane);
    __builtin_amdgcn_s_setprio(2);
    STAMP(10);
    u32x4 hb[28][2];
#define HB_LOAD(i) do { ld128c(hb[i][0], lane * 16u, r_h, ((unsigned)(tt * 2) * 112u + G2_KS(wv, i)) * 1024u); \
                        ld128c(hb[i][1], lane * 16u, r_h, ((unsigned)(tt * 2 + 1) * 112u + G2_KS(wv, i)) * 1024u); } while (0)
#pragma unroll
    for (int i = 0; i < G2_H0; ++i) HB_LOAD(i);
    f32x16 acc2[2][2];
    {
#pragma unroll
      for (int a = 0; a < 2; ++a)
#pragma unroll
        for (int b = 0; b < 2; ++b) acc2[a][b] = (f32x16){};
      static_for<0, 28>([&](auto ic) {
        constexpr int i = decltype(ic)::value;
        if constexpr (i == 14) STAMP(11);
        if constexpr (i + 2 == 13) lds_wait<1>(ldw, 6u);
        if constexpr (i + 2 >= G2_NR && i + 2 < 28) { Wr[i + 2][0] = D_READ(i + 2, 0); Wr[i + 2][1] = D_READ(i + 2, 1); }
        wait_vm<2 * (g2_issued(i) - i - 1)>();
        PIN(hb[i][0]); PIN(hb[i][1]);
#pragma unroll
        for (int T = 0; T < 2; ++T)
#pragma unroll
          for (int b = 0; b < 2; ++b)
            acc2[T][b] = __builtin_amdgcn_mfma_f32_32x32x16_bf16(Wr[i][T], __builtin_bit_cast(bf16x8, hb[i][b]), acc2[T][b], 0, 0, 0);
        SCHED_FENCE();
        static_for<g2_issued(i), g2_issued(i + 1)>([&](auto jc) { constexpr int j = decltype(jc)::value; HB_LOAD(j); });
      });
    }
    STAMP(12);
    {
      int l2 = lane; PIN(l2);
      const int tl = l2 & 31, x7 = (tl & 7) ^ (l2 >> 5);
#pragma unroll
      for (int b = 0; b < 2; ++b) {
        const int c8 = 4 * b + (tl >> 3);
        const int o4 = c8 < 5 ? RED_A + c8 * 8192 : RED_B + (c8 - 5) * 8192;
        LAS char* rp = lds + o4 + wv * 2048 + (tl & 7) * 256;
#pragma unroll
        for (int T = 0; T < 2; ++T)
#pragma unroll
          for (int g = 0; g < 4; ++g) {
            const f32x4 v = {acc2[T][b][4 * g], acc2[T][b][4 * g + 1], acc2[T][b][4 * g + 2], acc2[T][b][4 * g + 3]};
            *(LAS f32x4*)(rp + T * 128 + (((2 * g) ^ x7) << 4)) = v;
          }
      }
    }
  } else if (w8 == 4) {
    __builtin_amdgcn_s_setprio(3);
    const rsrc_t r_wg = mk_rsrc(wg, DM * FF * 2), r_wu = mk_rsrc(wu, DM * FF * 2);
    const rsrc_t r_wd = mk_rsrc(wd, FF * DM * 2);
    unsigned woff[7], xoff[4], wv4[4];
#pragma unroll
    for (int i = 0; i < 4; ++i) {
      xoff[i] = (unsigned)((lane >> 3) * 1024 + (((lane & 7) ^ ((lane >> 4) + 4 * (i & 1))) * 16) + i * 7168);
      wv4[i] = (unsigned)((lane >> 3) * 1024 + (((lane & 7) ^ (((lane >> 4) & 1) << 2)) * 16) + (i >> 1) * 458752 + (i & 1) * 8192 - i * 1024);
    }
#pragma unroll
    for (int i = 0; i < 7; ++i) {
      const int c = i * 64 + lane, row = c / 14, col = c - row * 14;
      woff[i] = (unsigned)(row * (FF * 2) + col * 16 - (i & 3) * 1024);
    }
    const unsigned ldw = ldsb + SY_LD;
    auto g1_stage = [&](int s) __attribute__((always_inline)) {
      const unsigned st = (unsigned)((s + rot) & 7), lb = ldsb + (unsigned)((s % 3) * STG);
#pragma unroll
      for (int i = 0; i < 4; ++i)
        dma16x4(lb + XOFF + i * 4096, xoff[0], xoff[1], xoff[2], xoff[3], r_x, (unsigned)((mt * 128 + i * 32) * 1024) + st * 128u);
#pragma unroll
      for (int h = 0; h < 2; ++h) {
        const unsigned wso = (unsigned)(ft * 224) + (st * 64u + h * 32u) * (FF * 2);
        dma16x4(lb + h * 7168, woff[0], woff[1], woff[2], woff[3], r_wg, wso);
        dma16x3(lb + h * 7168 + 4096, woff[4], woff[5], woff[6], r_wg, wso);
        dma16x4(lb + W_UPOFF + h * 7168, woff[0], woff[1], woff[2], woff[3], r_wu, wso);
        dma16x3(lb + W_UPOFF + h * 7168 + 4096, woff[4], woff[5], woff[6], r_wu, wso);
      }
    };
#define G2_GROUP(g) G2_STEPS(5 * (g), cmin(5 * (g) + 5, 28))
#define G2_STEPS(a_, b_) static_for<a_, b_>([&](auto ic) { constexpr int i = decltype(ic)::value; \
        const unsigned so_ = G2_KS(0, i) * 16384u + (unsigned)(dtile * 128); \
        dma16x4(ldsb + g2_base(i), wv4[0], wv4[1], wv4[2], wv4[3], r_wd, so_); \
        dma16x4(ldsb + g2_base(i) + 4096, wv4[0], wv4[1], wv4[2], wv4[3], r_wd, so_ + 2u * 458752u); })
    unsigned tch = 0;
    bar_only();
    g1_stage(0);
    g1_stage(1);
    wait_vm<44>();
    STAMP(1);
    BARX(bar_only);
#pragma unroll
    for (int k = 0; k < 6; ++k) {
      g1_stage(k + 2);
      wait_vm<44>();
      BARX(bar_only);
    }
    STAMP(2);
    G2_GROUP(0);
    wait_vm<40>();
    BARX(bar_only);
    G2_GROUP(1);
    wait_vm<0>();
    asm volatile("" :: "v"(tch));
    lds_setv(ldw, 2u);
    STAMP(3);
    lds_wait<4>(ldsb + SY_MC, 2u);
    G2_STEPS(10, 15);
    wait_vm<16>();
    lds_setv(ldw, 3u);
    STAMP(4);
    G2_STEPS(15, 25);
    lds_wait<4>(ldsb + SY_MC, 3u);
    G2_GROUP(5);
    wait_vm<0>();
    lds_setv(ldw, 6u);
    STAMP(5);
    __builtin_amdgcn_s_setprio(0);
    EPI_X();
    lds_wait<1>(ldsb + SY_SR, 1u);
    EPI_S();
    STAMP(12);
  } else if (w8 == 5) {
    bar_only();
    wait_vm<0>();
    asm volatile("" :: "v"(pf0), "v"(pf1), "v"(pf2), "v"(pf3));
    BARX(bar_only);
    const unsigned tag = lds_get1(ldsb + SY_VAL);
    BARX(bar_only);
    {
      const int c = lane & 15, g = lane >> 4;
      unsigned d[2][3][4];
      float kscale[2];
#pragma unroll
      for (int hc = 0; hc < 2; ++hc) {
        f32x4 kacc[3];
#pragma unroll
        for (int t = 0; t < 3; ++t)
          kacc[t] = *(const LAS f32x4*)(lds + KF + hc * CV_KSTR + t * 1024 + lane * 16) + *(const LAS f32x4*)(lds + KGS + hc * 4096 + t * 1024 + lane * 16);
        const f32x2 gw = *(const LAS f32x2*)(lds + SY_KG + hc * 8);
        const float g2 = gw.x;
        const int we = (int)gw.y;
        if (g2 > 0.f) {
#pragma unroll
          for (int i = 0; i < 4; ++i) {
            const int l0 = 16 * c + 4 * g + i;
            const int l1 = g < 2 ? 256 + 16 * c + 8 * g + 2 * i : 512 + 256 * (g - 2) + 16 * c + 4 * i;
            const int l2 = g < 2 ? 1024 + 256 * (2 * g + (i >> 1)) + 8 * (i & 1) + 16 * c : 2048;
            kacc[0][i] *= __builtin_amdgcn_exp2f(g2 * (float)(l0 - 2048));
            kacc[1][i] *= __builtin_amdgcn_exp2f(g2 * (float)(l1 - 2048));
            kacc[2][i] *= __builtin_amdgcn_exp2f(g2 * (float)(l2 - 2048));
          }
        }
        kscale[hc] = __builtin_amdgcn_ldexpf(1.0f, we - 7) * __builtin_amdgcn_exp2f(g2 * 2048.f);
#pragma unroll
        for (int t = 0; t < 3; ++t)
#pragma unroll
          for (int i = 0; i < 4; ++i) { d[hc][t][i] = pk_f16(kacc[t][i] * 128.f, kacc[t][i] * 128.f); PIN(d[hc][t][i]); }
        BARX(bar_only);
      }
      BARX(bar_only);
      BARX(bar_only);
      BARX(bar_only);
      BARX(bar_only);
      __builtin_amdgcn_s_sleep(WSL);
      __builtin_amdgcn_s_setprio(3);
      STAMP(2);
      *(LAS f32x2*)(lds + SY_VAL + 8) = (f32x2){kscale[0], kscale[1]};
#define MIX(a, b) (((a) & 0xffffu) | ((b) & 0xffff0000u))
#pragma unroll
      for (int hc = 0; hc < 2; ++hc) {
        LAS char* kf = lds + KF + hc * CV_KSTR + 96;
        if (g < 2) {
#pragma unroll
          for (int h = 0; h < 2; ++h) {
            LAS char* p = kf + 2 * (1024 + 256 * (2 * g + h) + 16 * c);
            const unsigned v0 = d[hc][2][2 * h], v1 = d[hc][2][2 * h + 1];
            *(LAS u32x2*)(p - 8) = (u32x2){v0, v0};
            *(LAS u32x4*)(p) = (u32x4){v0, v0, MIX(v0, v1), v1};
            *(LAS u32x2*)(p + 16) = (u32x2){v1, v1};
          }
        } else {
          if (lane == 32) *(LAS u32x2*)(kf + 2 * 2044) = (u32x2){d[hc][2][0], d[hc][2][0]};
        }
        if (g >= 2) {
          LAS char* p = kf + 2 * (512 + 256 * (g - 2) + 16 * c);
          const unsigned v0 = d[hc][1][0], v1 = d[hc][1][1], v2 = d[hc][1][2], v3 = d[hc][1][3];
          *(LAS unsigned*)(p - 4) = v0;
          *(LAS u32x4*)(p) = (u32x4){v0, MIX(v0, v1), v1, v2};
          *(LAS u32x2*)(p + 16) = (u32x2){v2, MIX(v2, v3)};
          *(LAS unsigned*)(p + 24) = v3;
        }
        if (g < 2) {
          LAS char* p = kf + 2 * (256 + 16 * c + 8 * g);
          const unsigned v0 = d[hc][1][0], v1 = d[hc][1][1], v2 = d[hc][1][2], v3 = d[hc][1][3];
          *(LAS unsigned short*)(p - 2) = (unsigned short)v0;
          *(LAS u32x2*)(p) = (u32x2){v0, MIX(v1, v2)};
          *(LAS unsigned*)(p + 8) = v2;
          *(LAS unsigned short*)(p + 12) = (unsigned short)v3;
        }
        *(LAS u32x2*)(kf + 2 * (16 * c + 4 * g)) = (u32x2){MIX(d[hc][0][0], d[hc][0][1]), MIX(d[hc][0][2], d[hc][0][3])};
        if (lane < 6) *(LAS u32x4*)(kf - 96 + lane * 16) = (u32x4){0u, 0u, 0u, 0u};
      }
      lds_setv(ldsb + SY_UR + 8, 1u);
      STAMP(3);
    }
    __builtin_amdgcn_s_setprio(1);
    lds_wait<4>(ldsb + SY_UR, 1u);
    conv_upper<CV_NP, 16>(lds, lane, 1, cp, dsk2p[cp], r_s);
    conv_upper<CV_NP, 14>(lds, lane, 0, cp, dsk2p[cp], r_s);
    STAMP(4);
    publish<4>(lds, 2, ws, FL_S + (unsigned)((lane * 8 + (cp >> 5)) * 256 + (cp & 31) * 4), 32, tag, lane);
    STAMP(5);
    EPI_X();
    lds_wait<1>(ldsb + SY_CNT + 8, 4u);
    poll_flags<4>((lane & 7) * 16u, r_fl, FL_S + (unsigned)((tt * 8 + dtile) * 256), tag);
    lds_setv(ldsb + SY_SR, 1u);
    STAMP(6);
    EPI_S();
    STAMP(12);
  } else {
    const int hc = wv - 2;
    const int dch = cp * 2 + hc;
    const rsrc_t r_xn = mk_rsrc(ws + WS_XNF, SL * DM * 2);
    u32x4 xr[4];
    unsigned ep;
    const int row0 = bid * 8 + hc * 4;
    {
      const unsigned vo = (unsigned)((lane >> 4) * 1024 + (lane & 15) * 16);
#pragma unroll
      for (int i = 0; i < 4; ++i) ld128(xr[i], vo, r_x, (unsigned)(row0 * 1024 + i * 256));
      ld32c(ep, 0u * lane, r_fl, FL_EP);
    }
    bar_only();
    const unsigned dsk2 = dsk2p[cp];
    wait_vm<0>();
    STAMP(1);
#pragma unroll
    for (int i = 0; i < 4; ++i) PIN(xr[i]);
    PIN(ep);
    const unsigned tag = (unsigned)__builtin_amdgcn_readfirstlane(ep) + 1u;
    lds_setv(ldsb + SY_VAL, tag);
    {
      float f[32];
#pragma unroll
      for (int j = 0; j < 16; ++j) { f[2 * j] = lo_f(xr[j >> 2][j & 3]); f[2 * j + 1] = hi_f(xr[j >> 2][j & 3]); }
      float s0 = 0.f, s1 = 0.f;
#pragma unroll
      for (int j = 0; j < 32; j += 4) { s0 += f[j] + f[j + 1]; s1 += f[j + 2] + f[j + 3]; }
      const float mean = row_sum(s0 + s1) * (1.0f / DM);
      float q0 = 0.f, q1 = 0.f;
#pragma unroll
      for (int j = 0; j < 32; j += 2) {
        const float da = f[j] - mean, db = f[j + 1] - mean;
        q0 = fmaf(da, da, q0); q1 = fmaf(db, db, q1);
      }
      const float var = row_sum(q0 + q1) * (1.0f / DM);
      const float mb = rbf(mean);
      const float sb = rbf(__builtin_amdgcn_sqrtf(rbf(rbf(var) + 1e-5f)));
      if ((lane & 15) == 0)
        st64c((u32x2){pk_bf16(mb, sb), tag}, (unsigned)((lane >> 4) * 8), r_fl, FL_STAT + (unsigned)(row0 * 8));
      const float rs = __builtin_amdgcn_rcpf(sb);
      const unsigned vo = (unsigned)((lane & 15) * 32768 + (lane >> 4) * 16);
      STAMP(2);
#pragma unroll
      for (int i = 0; i < 4; ++i) {
        u32x4 o;
#pragma unroll
        for (int j = 0; j < 4; ++j) {
          const unsigned d = pk_bf16(f[8 * i + 2 * j] - mb, f[8 * i + 2 * j + 1] - mb);
          o[j] = pk_bf16(lo_f(d) * rs, hi_f(d) * rs);
        }
        st128c(o, vo, r_xn, (unsigned)(i * 16 * 32768 + row0 * 16));
      }
    }
    BARX(bar_lgkm);
    wait_vm<0>();
    BARX(bar_only);
    if (hc == 0 && lane < 8) set_flag(ws, FL_ALL + (unsigned)(lane * 4096 + bid * 4), tag);
    BARX(bar_only);
    BARX(bar_only);
    u32x4 vf;
    ld128c(vf, lane * 16u, r_fl, FL_ALL + (unsigned)(xcd * 4096));
    BARX(bar_only);
    wait_vm<0>();
    PIN(vf);
    const bool all_ok = wave_all((vf[0] == tag) & (vf[1] == tag) & (vf[2] == tag) & (vf[3] == tag));
    unsigned uw[16];
    const unsigned usb = (unsigned)((cp >> 2) * 32768 + (cp & 3) * 4 + hc * 16384);
    if (!all_ok) {
      for (int r = 5; r <= 7; ++r) BARX(bar_only);
      poll_flags<4>(lane * 16u, r_fl, FL_ALL + (unsigned)(xcd * 4096), tag);
    }
#pragma unroll
    for (int m = 0; m < 16; ++m) ld32c(uw[m], lane * 16u, r_xn, usb + (unsigned)m * 1024u);
    if (lane == 0 && bid == 0 && hc == 0) set_flag(ws, FL_EP, tag);
    if (all_ok) { BARX(bar_only); BARX(bar_only); }
    wait_vm<0>();
    {
      const bool od = lane & 1;
      LAS char* up = lds + CV_U + 160 + hc * 2048 + (od ? 128 : 0) + (lane >> 1) * 4;
#pragma unroll
      for (int m = 0; m < 16; m += 2) {
        PIN(uw[m]); PIN(uw[m + 1]);
        const unsigned a = pk_f16(lo_f(uw[m]), hi_f(uw[m])), b = pk_f16(lo_f(uw[m + 1]), hi_f(uw[m + 1]));
        const unsigned y = (unsigned)__builtin_amdgcn_update_dpp(0, (int)(od ? a : b), 0xB1, 0xf, 0xf, true);
        const unsigned f0 = od ? y : a, f1 = od ? b : y;
        uw[m] = __builtin_amdgcn_perm(f1, f0, 0x05040100u);
        uw[m + 1] = __builtin_amdgcn_perm(f1, f0, 0x07060302u);
        PIN(uw[m]); PIN(uw[m + 1]);
      }
      if (all_ok) BARX(bar_only);
      __builtin_amdgcn_s_sleep(WSL);
      __builtin_amdgcn_s_setprio(3);
      if (lane < 10) *(LAS u32x4*)(lds + CV_U + hc * CV_USTR + lane * 16) = (u32x4){0u, 0u, 0u, 0u};
#pragma unroll
      for (int m = 0; m < 16; m += 2) {
        *(LAS unsigned*)(up + m * 128) = uw[m];
        *(LAS unsigned*)(up + CV_USTR + m * 128) = uw[m + 1];
      }
      lds_setv(ldsb + SY_UR + hc * 4, 1u);
    }
    STAMP(3);
    __builtin_amdgcn_s_setprio(1);
    lds_wait<4>(ldsb + SY_UR, 1u);
    const float kscale = *(const LAS float*)(lds + SY_VAL + 8 + hc * 4);
    STAMP(6);
    {
      const float dskip = hc ? hi_f(dsk2) : lo_f(dsk2);
      const int G = lane >> 4, q4 = (lane & 15) >> 2, p = lane & 3;
      const LAS char* pa = lds + KF + hc * CV_KSTR + 96 + 2 * (76 + 16 * p - 32 * G - 4 * q4 - 16);
      const LAS char* pb = lds + CV_U + hc * CV_USTR + 160 + 2 * (-76 + 32 * G + 4 * q4 + 4 * p);
      const LAS char* pu = lds + CV_U + hc * CV_USTR + 160 + lane * 2;
      f16x8 A[16], B[32];
#define CV_A(q) __builtin_bit_cast(f16x8, cat8(trrd(pa + 256 * (q) + 32), trrd(pa + 256 * (q))))
#define CV_B(w) __builtin_bit_cast(f16x8, cat8(trrd(pb + 128 * (w)), trrd(pb + 128 * (w) + 32)))
      A[0] = CV_A(0); B[0] = CV_B(0); B[1] = CV_B(1);
      A[1] = CV_A(1); B[2] = CV_B(2); B[3] = CV_B(3);
      f32x4 ca[2][2];
      _Float16 us[2][2];
      unsigned so16[16];
      float c3 = 0.f, c2 = 0.f, c1 = 0.f;
      auto tile_y = [&](const f32x4 a) __attribute__((always_inline)) {
        const float h3 = a[3];
        const float h2 = a[2] + shr_carry(c3, h3);
        const float h1 = a[1] + shr_carry(c2, h2);
        const float y = a[0] + shr_carry(c1, h1);
        c3 = dpp_mov<0x13C>(h3); c2 = dpp_mov<0x13C>(h2); c1 = dpp_mov<0x13C>(h1);
        return y * kscale;
      };
      static_for<0, CV_NP + 1>([&](auto jc) {
        constexpr int jj = decltype(jc)::value;
        if constexpr (jj + 2 < CV_NP) { A[jj + 2] = CV_A(jj + 2); B[2 * jj + 4] = CV_B(2 * jj + 4); B[2 * jj + 5] = CV_B(2 * jj + 5); }
        if constexpr (jj < CV_NP) {
          us[jj & 1][0] = *(const LAS _Float16*)(pu + jj * 256); us[jj & 1][1] = *(const LAS _Float16*)(pu + jj * 256 + 128);
          f32x4 a0 = {0.f, 0.f, 0.f, 0.f}, a1 = {0.f, 0.f, 0.f, 0.f};
#pragma unroll
          for (int q = jj; q >= 0; --q) {
            a0 = __builtin_amdgcn_mfma_f32_16x16x32_f16(A[q], B[2 * (jj - q)], a0, 0, 0, 0);
            a1 = __builtin_amdgcn_mfma_f32_16x16x32_f16(A[q], B[2 * (jj - q) + 1], a1, 0, 0, 0);
          }
          ca[jj & 1][0] = a0; ca[jj & 1][1] = a1;
        }
        if constexpr (jj >= 1) {
          const float y0 = tile_y(ca[(jj - 1) & 1][0]), y1 = tile_y(ca[(jj - 1) & 1][1]);
          const unsigned yq = pk_bf16(y0, y1);
          const unsigned uq = pk_bf16((float)us[(jj - 1) & 1][0] * dskip, (float)us[(jj - 1) & 1][1] * dskip);
          so16[jj - 1] = pk_bf16(lo_f(yq) + lo_f(uq), hi_f(yq) + hi_f(uq));
        }
        if constexpr (jj >= 1 && jj < CV_NP) {
#pragma unroll
          for (int k = 0; k < 2 * (jj + 1); ++k) {
            __builtin_amdgcn_sched_group_barrier(0x8, 1, 0);
            __builtin_amdgcn_sched_group_barrier(0x100, 1, 0);
            __builtin_amdgcn_sched_group_barrier(0x2, jj < 7 ? 4 : 2, 0);
          }
        }
        SCHED_FENCE();
        if constexpr (jj >= 1) st32c(so16[jj - 1], lane * 4u, r_s, (unsigned)(dch * 4096 + (jj - 1) * 256));
      });
    }
    STAMP(7);
    publish<4>(lds, 2, ws, FL_S + (unsigned)((lane * 8 + (cp >> 5)) * 256 + (cp & 31) * 4), 32, tag, lane);
    STAMP(9);
    EPI_X();
    lds_wait<1>(ldsb + SY_SR, 1u);
    EPI_S();
    STAMP(12);
  }
  bar_lgkm();
  {
    int t2 = tid; PIN(t2);
    const u32x4 xv = *(const LAS u32x4*)(lds + EPX + t2 * 16);
    unsigned sv[8];
#pragma unroll
    for (int j = 0; j < 8; ++j) sv[j] = *(const LAS unsigned*)(lds + EPS + ((t2 & 7) * 8 + j) * 256 + (((t2 >> 5) + (t2 & 7)) & 15) * 16 + ((t2 >> 3) & 3) * 4);
    const int rb = w8 < 5 ? RED_A + w8 * 8192 : RED_B + (w8 - 5) * 8192;
    const int tr7 = (t2 >> 3) & 7;
    const LAS char* rp = lds + rb + tr7 * 256 + ((t2 >> 2) & 1) * 128;
    const bool odd = tt & 1;
    u32x4 o;
#pragma unroll
    for (int i = 0; i < 2; ++i) {
      const int ch = (((t2 & 3) * 2 + i) ^ tr7) << 4;
      f32x4 v = *(const LAS f32x4*)(rp + ch);
#pragma unroll
      for (int w = 1; w < 4; ++w) v += *(const LAS f32x4*)(rp + w * 2048 + ch);
#pragma unroll
      for (int jj = 0; jj < 2; ++jj) {
        const unsigned fp = pk_bf16(v[2 * jj], v[2 * jj + 1]);
        const float f0 = lo_f(fp), f1 = hi_f(fp);
        const float g0 = f0 * __builtin_amdgcn_rcpf(1.0f + __builtin_amdgcn_exp2f(f0 * -1.4426950408889634f));
        const float g1 = f1 * __builtin_amdgcn_rcpf(1.0f + __builtin_amdgcn_exp2f(f1 * -1.4426950408889634f));
        const unsigned gp = pk_bf16(g0, g1);
        const int j = 4 * i + 2 * jj;
        const float s0 = odd ? hi_f(sv[j]) : lo_f(sv[j]), s1 = odd ? hi_f(sv[j + 1]) : lo_f(sv[j + 1]);
        const unsigned sg = pk_bf16(s0 * lo_f(gp), s1 * hi_f(gp));
        const unsigned xw = xv[2 * i + jj];
        o[2 * i + jj] = pk_bf16(lo_f(xw) + lo_f(sg), hi_f(xw) + hi_f(sg));
      }
    }
    *(u32x4*)((char*)out + XO(t2)) = o;
  }
  STAMP(13); STAMP_RT(15);
  STAMP_OUT(0);
  BW_OUT();
}

extern "C" void kernel_launch(void* const* d_in, const int* in_sizes, int n_in, void* d_out,
                              int out_size, void* d_ws, size_t ws_size, hipStream_t stream) {
  (void)hipFuncSetAttribute((const void*)fused_kernel, hipFuncAttributeMaxDynamicSharedMemorySize, LDS_BYTES);
  fused_kernel<<<256, 512, LDS_BYTES, stream>>>(
      (const unsigned short*)d_in[0], (const unsigned short*)d_in[1], (const unsigned short*)d_in[2],
      (const unsigned short*)d_in[3], (const unsigned short*)d_in[4], (const unsigned short*)d_in[5],
      (const unsigned short*)d_in[6], (const unsigned*)d_in[7], (const unsigned*)d_in[8],
      (const unsigned short*)d_in[10], (const unsigned short*)d_in[11], (const unsigned short*)d_in[12],
      (char*)d_ws, (unsigned short*)d_out);
}
```

```cpp
#include <hip/hip_runtime.h>
#include <hip/hip_bf16.h>
#include <stdint.h>
#include <type_traits>

typedef __attribute__((ext_vector_type(4))) short s16x4;
typedef __attribute__((ext_vector_type(8))) short s16x8;
typedef __attribute__((ext_vector_type(8))) _Float16 f16x8;
typedef __attribute__((ext_vector_type(8))) __bf16 bf16x8;
typedef __attribute__((ext_vector_type(2))) float f32x2;
typedef __attribute__((ext_vector_type(4))) float f32x4;
typedef __attribute__((ext_vector_type(16))) float f32x16;
typedef __attribute__((ext_vector_type(4))) unsigned u32x4;
typedef __attribute__((ext_vector_type(3))) unsigned u32x3;
typedef __attribute__((ext_vector_type(2))) unsigned u32x2;
typedef __attribute__((ext_vector_type(2))) __bf16 bf16x2;
typedef __attribute__((ext_vector_type(2))) _Float16 f16x2;
#define LAS __attribute__((address_space(3)))
#define GAS __attribute__((address_space(1)))
typedef __amdgpu_buffer_rsrc_t rsrc_t;

constexpr int SL = 2048;
constexpr int DM = 512;
constexpr int NM = 128;
constexpr int FF = 1792;

constexpr size_t WS_XNF = 0;
constexpr size_t WS_ST = 2 * (size_t)SL * DM * 2;
constexpr size_t WS_HF = WS_ST + (size_t)SL * DM * 2;
constexpr size_t WS_FLAG = 2 * (size_t)SL * DM;
constexpr unsigned FL_ALL = 0, FL_STAT = 65536, FL_H = 131072, FL_S = 196608, FL_EP = 262144, FL_BYTES = 262400;

template <int I, int N, class F>
__device__ __forceinline__ void static_for(F&& f) {
  if constexpr (I < N) { f(std::integral_constant<int, I>{}); static_for<I + 1, N>(f); }
}

__device__ __forceinline__ unsigned pk_bf16(float lo, float hi) {
  f32x2 v = {lo, hi};
  return __builtin_bit_cast(unsigned, __builtin_convertvector(v, bf16x2));
}
__device__ __forceinline__ unsigned pk_f16(float lo, float hi) {
  f32x2 v = {lo, hi};
  return __builtin_bit_cast(unsigned, __builtin_convertvector(v, f16x2));
}
__device__ __forceinline__ float lo_f(unsigned p) { return __uint_as_float(p << 16); }
__device__ __forceinline__ float hi_f(unsigned p) { return __uint_as_float(p & 0xffff0000u); }
__device__ __forceinline__ float rbf(float x) { return lo_f(pk_bf16(x, x)); }

template <int CTRL>
__device__ __forceinline__ float dpp_mov(float v) {
  return __int_as_float(__builtin_amdgcn_update_dpp(0, __float_as_int(v), CTRL, 0xf, 0xf, true));
}
__device__ __forceinline__ float shr_carry(float c, float v) {
  return __builtin_bit_cast(float, __builtin_amdgcn_update_dpp(__builtin_bit_cast(int, c), __builtin_bit_cast(int, v), 0x138, 0xf, 0xf, false));
}
__device__ __forceinline__ float rdlane(float v, int l) {
  return __int_as_float(__builtin_amdgcn_readlane(__float_as_int(v), l));
}
__device__ __forceinline__ float wave_sum(float v) {
  v += dpp_mov<0xB1>(v);
  v += dpp_mov<0x4E>(v);
  v += dpp_mov<0x141>(v);
  v += dpp_mov<0x140>(v);
  return (rdlane(v, 0) + rdlane(v, 16)) + (rdlane(v, 32) + rdlane(v, 48));
}
__device__ __forceinline__ float row_sum(float v) {
  v += dpp_mov<0xB1>(v);
  v += dpp_mov<0x4E>(v);
  v += dpp_mov<0x141>(v);
  v += dpp_mov<0x140>(v);
  return v;
}
__device__ __forceinline__ float half_sum(float v, int lane) {
  v += dpp_mov<0xB1>(v);
  v += dpp_mov<0x4E>(v);
  v += dpp_mov<0x141>(v);
  v += dpp_mov<0x140>(v);
  const float lo = rdlane(v, 0) + rdlane(v, 16), hi = rdlane(v, 32) + rdlane(v, 48);
  return lane < 32 ? lo : hi;
}
__device__ __forceinline__ float wave_max(float v) {
  v = fmaxf(v, dpp_mov<0xB1>(v));
  v = fmaxf(v, dpp_mov<0x4E>(v));
  v = fmaxf(v, dpp_mov<0x141>(v));
  v = fmaxf(v, dpp_mov<0x140>(v));
  return fmaxf(fmaxf(rdlane(v, 0), rdlane(v, 16)), fmaxf(rdlane(v, 32), rdlane(v, 48)));
}

__device__ __forceinline__ rsrc_t mk_rsrc(const void* p, unsigned bytes) {
  return __builtin_amdgcn_make_buffer_rsrc((void*)p, (short)0, (int)bytes, 0x00020000);
}
__device__ __forceinline__ void dma16(unsigned lds_dst, unsigned voff, rsrc_t r, unsigned soff) {
  asm volatile("s_mov_b32 m0, %0\n\ts_nop 0\n\tbuffer_load_dwordx4 %1, %2, %3 offen lds"
               :: "s"(lds_dst), "v"(voff), "s"(r), "s"(soff) : "memory");
}
__device__ __forceinline__ void dma16x4(unsigned lds_dst, unsigned v0, unsigned v1, unsigned v2, unsigned v3, rsrc_t r, unsigned soff) {
  asm volatile("s_mov_b32 m0, %0\n\ts_nop 0\n\t"
               "buffer_load_dwordx4 %1, %5, %6 offen lds\n\t"
               "buffer_load_dwordx4 %2, %5, %6 offen offset:1024 lds\n\t"
               "buffer_load_dwordx4 %3, %5, %6 offen offset:2048 lds\n\t"
               "buffer_load_dwordx4 %4, %5, %6 offen offset:3072 lds"
               :: "s"(lds_dst), "v"(v0), "v"(v1), "v"(v2), "v"(v3), "s"(r), "s"(soff) : "memory");
}
__device__ __forceinline__ void dma16x3(unsigned lds_dst, unsigned v0, unsigned v1, unsigned v2, rsrc_t r, unsigned soff) {
  asm volatile("s_mov_b32 m0, %0\n\ts_nop 0\n\t"
               "buffer_load_dwordx4 %1, %4, %5 offen lds\n\t"
               "buffer_load_dwordx4 %2, %4, %5 offen offset:1024 lds\n\t"
               "buffer_load_dwordx4 %3, %4, %5 offen offset:2048 lds"
               :: "s"(lds_dst), "v"(v0), "v"(v1), "v"(v2), "s"(r), "s"(soff) : "memory");
}
__device__ __forceinline__ void ld128(u32x4& dst, unsigned voff, rsrc_t r, unsigned soff) {
  asm volatile("buffer_load_dwordx4 %0, %1, %2, %3 offen" : "=v"(dst) : "v"(voff), "s"(r), "s"(soff) : "memory");
}
__device__ __forceinline__ void ld128c(u32x4& dst, unsigned voff, rsrc_t r, unsigned soff) {
  asm volatile("buffer_load_dwordx4 %0, %1, %2, %3 offen sc1" : "=v"(dst) : "v"(voff), "s"(r), "s"(soff) : "memory");
}
__device__ __forceinline__ void ld32(unsigned& dst, unsigned voff, rsrc_t r, unsigned soff) {
  asm volatile("buffer_load_dword %0, %1, %2, %3 offen" : "=v"(dst) : "v"(voff), "s"(r), "s"(soff) : "memory");
}
__device__ __forceinline__ void ld64c(u32x2& dst, unsigned voff, rsrc_t r, unsigned soff) {
  asm volatile("buffer_load_dwordx2 %0, %1, %2, %3 offen sc1" : "=v"(dst) : "v"(voff), "s"(r), "s"(soff) : "memory");
}
__device__ __forceinline__ void ld32c(unsigned& dst, unsigned voff, rsrc_t r, unsigned soff) {
  asm volatile("buffer_load_dword %0, %1, %2, %3 offen sc1" : "=v"(dst) : "v"(voff), "s"(r), "s"(soff) : "memory");
}
__device__ __forceinline__ void st32c(unsigned v, unsigned voff, rsrc_t r, unsigned soff) {
  asm volatile("buffer_store_dword %0, %1, %2, %3 offen sc1" :: "v"(v), "v"(voff), "s"(r), "s"(soff) : "memory");
}
__device__ __forceinline__ void st64c(u32x2 v, unsigned voff, rsrc_t r, unsigned soff) {
  asm volatile("buffer_store_dwordx2 %0, %1, %2, %3 offen sc1\n\ts_nop 1" :: "v"(v), "v"(voff), "s"(r), "s"(soff) : "memory");
}
__device__ __forceinline__ void ld16(unsigned& dst, unsigned voff, rsrc_t r, unsigned soff) {
  asm volatile("buffer_load_ushort %0, %1, %2, %3 offen" : "=v"(dst) : "v"(voff), "s"(r), "s"(soff) : "memory");
}
__device__ __forceinline__ void st128c(u32x4 v, unsigned voff, rsrc_t r, unsigned soff) {
  asm volatile("buffer_store_dwordx4 %0, %1, %2, %3 offen sc1\n\ts_nop 1" :: "v"(v), "v"(voff), "s"(r), "s"(soff) : "memory");
}
template <int N>
__device__ __forceinline__ void wait_vm() { asm volatile("s_waitcnt vmcnt(%0)" :: "n"(N > 63 ? 63 : N) : "memory"); }
__device__ __forceinline__ void bar_lgkm() { asm volatile("s_waitcnt lgkmcnt(0)\n\ts_barrier" ::: "memory"); }
__device__ __forceinline__ void bar_only() { asm volatile("s_barrier" ::: "memory"); }
#define BW_DECL
#define BARX(f) f()
#define BW_OUT()
#define PIN(x) asm volatile("" : "+v"(x))
#define SCHED_FENCE() __builtin_amdgcn_sched_barrier(0)

__device__ __forceinline__ s16x4 trrd(const LAS char* p) {
  return __builtin_amdgcn_ds_read_tr16_b64_v4i16((LAS s16x4*)p);
}
__device__ __forceinline__ s16x8 cat8(s16x4 a, s16x4 b) {
  return __builtin_shufflevector(a, b, 0, 1, 2, 3, 4, 5, 6, 7);
}

__device__ __forceinline__ bool wave_all(bool p) { return __builtin_amdgcn_ballot_w64(!p) == 0ull; }
template <int SLP = 2>
__device__ __forceinline__ void poll_flags(unsigned voff, rsrc_t r, unsigned soff, unsigned tag) {
#pragma unroll 1
  for (int it = 0; it < (1 << 18); ++it) {
    u32x4 v;
    asm volatile("buffer_load_dwordx4 %0, %1, %2, %3 offen sc1\n\ts_waitcnt vmcnt(0)"
                 : "=&v"(v) : "v"(voff), "s"(r), "s"(soff) : "memory");
    const bool ok = (v[0] == tag) & (v[1] == tag) & (v[2] == tag) & (v[3] == tag);
    if (wave_all(ok)) break;
    __builtin_amdgcn_s_sleep(SLP);
  }
}
__device__ __forceinline__ void set_flag(char* ws, unsigned off, unsigned tag) {
  __hip_atomic_store((GAS unsigned*)(ws + WS_FLAG + off), tag, __ATOMIC_RELAXED, __HIP_MEMORY_SCOPE_AGENT);
}

#define STAMP_DECL
#define STAMP(i)
#define STAMP_RT(i)
#define STAMP_OUT(kid)

__device__ __forceinline__ f32x2 cmul(f32x2 a, f32x2 b) {
  f32x2 r;
  r.x = fmaf(a.x, b.x, -(a.y * b.y));
  r.y = fmaf(a.x, b.y, a.y * b.x);
  return r;
}
__device__ __forceinline__ f32x2 cexp_l(float a, float b, float l) {
  const float mag = __builtin_amdgcn_exp2f(a * l * 1.4426950408889634f);
  const float x = b * l;
  const float C1 = 0.15915494309189535f;
  const float C2 = (float)(0.15915494309189533577 - (double)0.15915494309189535f);
  const float hi = x * C1;
  const float lo = fmaf(x, C1, -hi) + x * C2;
  const float r = (hi - __builtin_rintf(hi)) + lo;
  f32x2 z;
  z.x = mag * __builtin_amdgcn_cosf(r);
  z.y = mag * __builtin_amdgcn_sinf(r);
  return z;
}

constexpr int W_UPOFF = 14336 + 128;
constexpr int XOFF = W_UPOFF + 14336;
constexpr int STG = XOFF + 16384;
constexpr int KGS = 3 * STG;
constexpr int KF = KGS + 8192;
constexpr int CV_KSTR = (48 + 2048) * 2;
constexpr int CV_USTR = (80 + 2048) * 2;
constexpr int CV_U = KF + 2 * CV_KSTR;
constexpr int CSUM = CV_U + 2 * CV_USTR;
constexpr int STAT = CSUM + 896;
constexpr int SY = STAT + 512;
constexpr int SY_LD = SY, SY_MC = SY + 16, SY_ST = SY + 4, SY_HR = SY + 8, SY_SR = SY + 12, SY_UR = SY + 32  ,
              SY_CNT = SY + 48, SY_VAL = SY + 64, SY_KG = SY + 80, SY_END = SY + 96;
constexpr int LDS_BYTES = SY_END;
static_assert(LDS_BYTES <= 163840, "lds");
constexpr int G2_RED = 272;
constexpr int EPX = KGS;
constexpr int EPS = KF;
#define WSL 4
constexpr unsigned WS_USED = (unsigned)(WS_HF + (size_t)SL * FF * 2);
constexpr unsigned CODE_BYTES = 49152;
constexpr int CV_NP = 12;
constexpr int G2_NR = 13;
constexpr int cmax(int a, int b) { return a > b ? a : b; }
constexpr int cmin(int a, int b) { return a < b ? a : b; }
constexpr int g2_base(int i) { return (((i % 15) / 5 + 2) % 3) * STG + (i % 5) * 8192; }
constexpr int RED_A = g2_base(15), RED_B = g2_base(20);
constexpr int G2_H0 = 6;
constexpr int g2_issued(int i) { return cmin(28, G2_H0 + (3 * i) / 2); }

__device__ __forceinline__ void lds_setv(unsigned addr, unsigned v) {
  asm volatile("ds_write_b32 %0, %1" :: "v"(addr), "v"(v) : "memory");
}
__device__ __forceinline__ unsigned lds_min4(unsigned addr) {
  u32x4 v;
  asm volatile("ds_read_b128 %0, %1\n\ts_waitcnt lgkmcnt(0)" : "=&v"(v) : "v"(addr) : "memory");
  return (unsigned)__builtin_amdgcn_readfirstlane(min(min(v[0], v[1]), min(v[2], v[3])));
}
__device__ __forceinline__ unsigned lds_get1(unsigned addr) {
  unsigned v;
  asm volatile("ds_read_b32 %0, %1\n\ts_waitcnt lgkmcnt(0)" : "=&v"(v) : "v"(addr) : "memory");
  return (unsigned)__builtin_amdgcn_readfirstlane(v);
}
template <int W>
__device__ __forceinline__ unsigned lds_val(unsigned addr) { return W == 4 ? lds_min4(addr) : lds_get1(addr); }
template <int W>
__device__ __forceinline__ void lds_spin(unsigned addr, unsigned need) {
#pragma unroll 1
  for (int it = 0; it < (1 << 20); ++it) {
    if (lds_val<W>(addr) >= need) break;
    __builtin_amdgcn_s_sleep(1);
  }
}
template <int W>
__device__ __forceinline__ void lds_wait(unsigned addr, unsigned need) {
  if (__builtin_expect(lds_val<W>(addr) < need, 0)) lds_spin<W>(addr, need);
}
template <int NW, bool DRAIN = true>
__device__ __forceinline__ void publish(LAS char* lds, int cnt, char* ws, unsigned off, int nl, unsigned tag, int lane) {
  if (DRAIN) wait_vm<0>();
  unsigned old = 0;
  if (lane == 0) old = __hip_atomic_fetch_add((LAS unsigned*)(lds + SY_CNT + cnt * 4), 1u, __ATOMIC_RELAXED, __HIP_MEMORY_SCOPE_WORKGROUP);
  if (__builtin_amdgcn_readfirstlane(old) == NW - 1 && lane < nl) set_flag(ws, off, tag);
}

template <int PA, int PB>
__device__ __forceinline__ void conv_upper(LAS char* lds, int lane, int hc, int cp, unsigned dsk2, rsrc_t r_s) {
  const float kscale = *(const LAS float*)(lds + SY_VAL + 8 + hc * 4);
  const float dskip = hc ? hi_f(dsk2) : lo_f(dsk2);
  const int G = lane >> 4, q4 = (lane & 15) >> 2, p = lane & 3;
  const LAS char* pa = lds + KF + hc * CV_KSTR + 96 + 2 * (76 + 16 * p - 32 * G - 4 * q4 - 16);
  const LAS char* pb = lds + CV_U + hc * CV_USTR + 160 + 2 * (-76 + 32 * G + 4 * q4 + 4 * p);
  const LAS char* pu = lds + CV_U + hc * CV_USTR + 160 + lane * 2;
  constexpr int J0 = 2 * PA - 1, J1 = 2 * PB, NT = J1 - J0;
  f16x8 A[16], B[32];
  f32x4 ca[NT];
#define CV_A(q) __builtin_bit_cast(f16x8, cat8(trrd(pa + 256 * (q) + 32), trrd(pa + 256 * (q))))
#define CV_B(w) __builtin_bit_cast(f16x8, cat8(trrd(pb + 128 * (w)), trrd(pb + 128 * (w) + 32)))
  B[0] = CV_B(0); B[1] = CV_B(1); B[2] = CV_B(2);
  constexpr int QA = (J0 - 1) / 2;
#pragma unroll
  for (int q = PB - 1; q >= QA; --q) A[q] = CV_A(q);
#pragma unroll
  for (int j = 0; j < NT; ++j) ca[j] = (f32x4){0.f, 0.f, 0.f, 0.f};
  static_for<0, J1>([&](auto wc) {
    constexpr int w = decltype(wc)::value;
    if constexpr (w + 3 < J1) B[w + 3] = CV_B(w + 3);
    if constexpr ((w & 1) == 0 && QA - 1 - w / 2 >= 0) A[QA - 1 - w / 2] = CV_A(QA - 1 - w / 2);
#pragma unroll
    for (int q = PB - 1; q >= 0; --q) {
      const int j = w + 2 * q;
      if (j >= J0 && j < J1) ca[j - J0] = __builtin_amdgcn_mfma_f32_16x16x32_f16(A[q], B[w], ca[j - J0], 0, 0, 0);
    }
    SCHED_FENCE();
  });
  float c3 = 0.f, c2 = 0.f, c1 = 0.f, yv[NT];
#pragma unroll
  for (int j = 0; j < NT; ++j) {
    const f32x4 a = ca[j];
    const float h3 = a[3];
    const float h2 = a[2] + shr_carry(c3, h3);
    const float h1 = a[1] + shr_carry(c2, h2);
    yv[j] = a[0] + shr_carry(c1, h1);
    c3 = dpp_mov<0x13C>(h3); c2 = dpp_mov<0x13C>(h2); c1 = dpp_mov<0x13C>(h1);
  }
#pragma unroll
  for (int jp = PA; jp < PB; ++jp) {
    const float u0 = (float)*(const LAS _Float16*)(pu + jp * 256), u1 = (float)*(const LAS _Float16*)(pu + jp * 256 + 128);
    const unsigned yq = pk_bf16(yv[2 * jp - J0] * kscale, yv[2 * jp + 1 - J0] * kscale);
    const unsigned uq = pk_bf16(u0 * dskip, u1 * dskip);
    st32c(pk_bf16(lo_f(yq) + lo_f(uq), hi_f(yq) + hi_f(uq)), lane * 4u, r_s, (unsigned)((cp * 2 + hc) * 4096 + jp * 256));
  }
#undef CV_A
#undef CV_B
}

#if defined(__HIP_DEVICE_COMPILE__)
__attribute__((target("no-packed-fp32-ops")))
#endif
__global__ __launch_bounds__(512, 2) void fused_kernel(
    const unsigned short* __restrict__ x, const unsigned short* __restrict__ A_re,
    const unsigned short* __restrict__ A_im, const unsigned short* __restrict__ B_re,
    const unsigned short* __restrict__ B_im, const unsigned short* __restrict__ C_re,
    const unsigned short* __restrict__ C_im, const unsigned* __restrict__ log_dt2,
    const unsigned* __restrict__ dsk2p, const unsigned short* __restrict__ wg,
    const unsigned short* __restrict__ wu, const unsigned short* __restrict__ wd,
    char* ws, unsigned short* __restrict__ out) {
  extern __shared__ __attribute__((aligned(16))) char smem[];
  unsigned long long pc0;
  asm volatile("s_getpc_b64 %0" : "=s"(pc0));
  LAS char* lds = (LAS char*)smem;
  const unsigned ldsb = (unsigned)(uintptr_t)lds;
  const int tid = threadIdx.x, lane = tid & 63;
  const int w8 = __builtin_amdgcn_readfirstlane(tid >> 6);
  const int wv = w8 & 3;
  STAMP_DECL; BW_DECL; STAMP_RT(14); STAMP(0);
  const int bid = blockIdx.x;
  const int xcd = bid & 7, bi = bid >> 3;
  unsigned pf0 = 0;
  if (w8 == 5) ld32(pf0, (unsigned)((bi * 16 + (lane & 15)) * 128), mk_rsrc((const void*)pc0, CODE_BYTES), 0u);
  asm volatile("" :: "s"(x), "s"(A_re), "s"(A_im), "s"(B_re), "s"(B_im), "s"(C_re), "s"(C_im),
               "s"(wg), "s"(wu), "s"(wd), "s"(ws), "s"(out));
  const unsigned ldt2 = log_dt2[xcd * 32 + bi], dsk2 = dsk2p[xcd * 32 + bi];
  const int mt = (xcd >> 2) * 8 + (bi >> 2);
  const int ft = (xcd & 3) * 4 + (bi & 3);
  const int rot = ((bi >> 2) + 2 * (bi & 3)) & 7;
  const int cp = xcd * 32 + bi;
  const int tt = xcd * 4 + (bi >> 3);
  const int dtile = bi & 7;
  const int rot2 = ((bi >> 3) * 7 + dtile * 3) % 28;

  const rsrc_t r_x = mk_rsrc(x, SL * DM * 2);
  const rsrc_t r_s = mk_rsrc(ws + WS_ST, SL * DM * 2);
  const rsrc_t r_fl = mk_rsrc(ws + WS_FLAG, FL_BYTES);
#define G2_KS(w, i) ((unsigned)((w) * 28 + (((i) + rot2) >= 28 ? (i) + rot2 - 28 : (i) + rot2)))
#define XO(t_) ((unsigned)((tt * 64 + ((t_) >> 3)) * (DM * 2) + dtile * 128 + ((t_) & 7) * 16))
#define EPI_X() do { int l_ = lane; PIN(l_); u32x4 xw_[2]; \
    _Pragma("unroll") for (int j = 0; j < 2; ++j) ld128(xw_[j], XO(wv * 128 + j * 64 + l_), r_x, 0u); \
    lds_wait<4>(ldsb + SY_UR, 1u); wait_vm<0>(); \
    _Pragma("unroll") for (int j = 0; j < 2; ++j) { PIN(xw_[j]); *(LAS u32x4*)(lds + EPX + (wv * 128 + j * 64 + l_) * 16) = xw_[j]; } } while (0)
#define EPI_S() do { int l_ = lane; PIN(l_); u32x4 sw_[4]; \
    const unsigned so = (unsigned)((dtile * 64 + wv * 16 + (l_ >> 4)) * 4096 + (tt >> 1) * 256 + (l_ & 15) * 16); \
    _Pragma("unroll") for (int j = 0; j < 4; ++j) ld128c(sw_[j], so, r_s, (unsigned)(j * 16384)); \
    wait_vm<0>(); \
    _Pragma("unroll") for (int j = 0; j < 4; ++j) { PIN(sw_[j]); \
      *(LAS u32x4*)(lds + EPS + (wv * 16 + j * 4 + (l_ >> 4)) * 256 + ((l_ + wv * 2 + (j >> 1)) & 15) * 16) = sw_[j]; } } while (0)

  if (w8 < 4) {
    const rsrc_t r_h = mk_rsrc(ws + WS_HF, SL * FF * 2);
    {
    const int hc = wv & 1, m = wv >> 1;
    const int dch = cp * 2 + hc;
    unsigned prm[6];
    {
      const unsigned po = (unsigned)(dch * NM * 2);
      ld32(prm[0], lane * 4u, mk_rsrc(A_re, DM * NM * 2), po);
      ld32(prm[1], lane * 4u, mk_rsrc(A_im, DM * NM * 2), po);
      ld32(prm[2], lane * 4u, mk_rsrc(B_re, DM * NM * 2), po);
      ld32(prm[3], lane * 4u, mk_rsrc(B_im, DM * NM * 2), po);
      ld32(prm[4], lane * 4u, mk_rsrc(C_re, DM * NM * 2), po);
      ld32(prm[5], lane * 4u, mk_rsrc(C_im, DM * NM * 2), po);
    }
    if (tid < 5) *(LAS u32x4*)(lds + SY + tid * 16) = (u32x4){0u, 0u, 0u, tid == 2 ? 1u : 0u};
    bar_lgkm();
    __builtin_amdgcn_s_setprio(2);
    {
      const float ldt = hc ? hi_f(ldt2) : lo_f(ldt2);
      const float L2E = 1.4426950408889634f, L2E_LO = (float)(1.4426950408889634074 - (double)1.4426950408889634f);
      const float th = ldt * L2E;
      const float tl = fmaf(ldt, L2E, -th) + ldt * L2E_LO;
      const float e0 = __builtin_amdgcn_exp2f(th);
      const float dt = rbf(fmaf(e0 * 0.6931471805599453f, tl, e0));
      wait_vm<0>();
#pragma unroll
      for (int i = 0; i < 6; ++i) PIN(prm[i]);
      float av[2], bv[2];
      f32x2 wq[2];
#pragma unroll
      for (int m = 0; m < 2; ++m) {
        av[m] = dt * (m ? hi_f(prm[0]) : lo_f(prm[0]));
        bv[m] = dt * (m ? hi_f(prm[1]) : lo_f(prm[1]));
        const f32x2 db = {dt * (m ? hi_f(prm[2]) : lo_f(prm[2])), dt * (m ? hi_f(prm[3]) : lo_f(prm[3]))};
        const f32x2 cc = {m ? hi_f(prm[4]) : lo_f(prm[4]), m ? hi_f(prm[5]) : lo_f(prm[5])};
        wq[m] = cmul(cc, db);
      }
      const float amax = fmaxf(wave_max(fmaxf(av[0], av[1])), 0.f);
      const float g2 = amax * L2E;
      const float wm = wave_max(fmaxf(fmaxf(fabsf(wq[0].x), fabsf(wq[0].y)), fmaxf(fabsf(wq[1].x), fabsf(wq[1].y))));
      int we = __builtin_amdgcn_frexp_expf(wm);
      we = we < -100 ? -100 : we;
      const float wsc = __builtin_amdgcn_ldexpf(1.0f, -we);
      f32x4 kacc[3];
#pragma unroll
      for (int t = 0; t < 3; ++t) kacc[t] = (f32x4){0.f, 0.f, 0.f, 0.f};
      const int kgs = (m ? CV_U : KGS) + hc * 4096;
      LAS char* tw[4];
#pragma unroll
      for (int c = 0; c < 4; ++c) tw[c] = lds + kgs + lane * 64 + ((c ^ ((lane >> 1) & 3)) << 4);
      const LAS char* tr[2];
      {
        const int G = lane >> 4, q4 = (lane & 15) >> 2, p = lane & 3;
#pragma unroll
        for (int t = 0; t < 2; ++t)
          tr[t] = lds + kgs + (4 * G + 2 * t + (q4 >> 1)) * 64 + ((((q4 & 1) * 2 + (p >> 1)) ^ ((2 * G + t) & 3)) << 4) + (p & 1) * 8;
      }
      auto put16 = [&](const f32x2 (&e)[16]) __attribute__((always_inline)) {
#pragma unroll
        for (int h = 0; h < 2; ++h) {
          u32x4 re, im;
#pragma unroll
          for (int j = 0; j < 4; ++j) {
            re[j] = pk_f16(e[8 * h + 2 * j].x, e[8 * h + 2 * j + 1].x);
            im[j] = pk_f16(e[8 * h + 2 * j].y, e[8 * h + 2 * j + 1].y);
          }
          *(LAS u32x4*)tw[h] = re;
          *(LAS u32x4*)tw[2 + h] = im;
        }
      };
#define KG_FRAG(s) __builtin_bit_cast(f16x8, cat8(trrd(tr[0] + (s) * 1024), trrd(tr[1] + (s) * 1024)))
#define KG_RD() do { _Pragma("unroll") for (int s = 0; s < 4; ++s) pf[s] = KG_FRAG(s); SCHED_FENCE(); } while (0)
#define KG_MMA(t) do { SCHED_FENCE(); _Pragma("unroll") for (int s = 0; s < 4; ++s) \
        kacc[t] = __builtin_amdgcn_mfma_f32_16x16x32_f16(pf[s], qf[s], kacc[t], 0, 0, 0); } while (0)
      {
        const float a = (m ? av[1] : av[0]) - amax, b = m ? bv[1] : bv[0];
        f32x2 w = m ? wq[1] : wq[0];
        w.x *= wsc; w.y *= -wsc;
        f32x2 e[16];
        {
          const f32x2 z16 = cexp_l(a, b, 16.f);
          e[0] = (f32x2){1.f, 0.f}; e[1] = z16; e[2] = cmul(z16, z16); e[3] = cmul(e[2], z16);
          const f32x2 z64 = cmul(e[2], e[2]);
#pragma unroll
          for (int c = 4; c < 16; ++c) e[c] = cmul(e[c - 4], z64);
          put16(e);
        }
        f16x8 qf[4], pf[4];
#pragma unroll
        for (int s = 0; s < 4; ++s) qf[s] = KG_FRAG(s);
        SCHED_FENCE();
        const f32x2 z1 = cexp_l(a, -b, 1.f);
        const f32x2 z2 = cmul(z1, z1), z4 = cmul(z2, z2);
        e[0] = w; e[1] = cmul(w, z1); e[2] = cmul(w, z2); e[3] = cmul(e[1], z2);
#pragma unroll
        for (int c = 4; c < 16; ++c) e[c] = cmul(e[c - 4], z4);
        put16(e);
        KG_RD();
        const f32x2 z256 = cexp_l(a, -b, 256.f);
        const f32x2 z512 = cmul(z256, z256);
        const f32x2 w2 = cmul(w, z512);
        e[0] = cmul(w, z256); e[1] = cmul(e[0], z2); e[8] = w2; e[12] = cmul(e[0], z512);
        const f32x2 w3 = e[12];
#pragma unroll
        for (int i = 1; i < 4; ++i) {
          e[2 * i] = cmul(e[2 * i - 2], z4); e[2 * i + 1] = cmul(e[2 * i - 1], z4);
          e[8 + i] = cmul(e[7 + i], z4); e[12 + i] = cmul(e[11 + i], z4);
        }
        KG_MMA(0);
        put16(e);
        KG_RD();
        const f32x2 z8 = cmul(z4, z4);
        e[0] = cmul(w2, z512); e[2] = cmul(w3, z512); e[4] = cmul(e[0], z512); e[6] = cmul(e[2], z512);
        e[8] = cmul(e[4], z512);
#pragma unroll
        for (int i = 0; i < 4; ++i) e[2 * i + 1] = cmul(e[2 * i], z8);
#pragma unroll
        for (int i = 9; i < 16; ++i) e[i] = (f32x2){0.f, 0.f};
        KG_MMA(1);
        put16(e);
        KG_RD();
        KG_MMA(2);
      }
#pragma unroll
      for (int t = 0; t < 3; ++t) *(LAS f32x4*)(lds + (m ? KF + hc * CV_KSTR : KGS + hc * 4096) + t * 1024 + lane * 16) = kacc[t];
      if (m) *(LAS f32x2*)(lds + SY_KG + hc * 8) = (f32x2){g2, (float)we};
    }
    }
    f32x16 acc[7];
    f32x4 csa[2];
    unsigned tag;
    {
      const int G = lane >> 4;
      const LAS char* wa0 = lds + (G & 1) * W_UPOFF + (8 * (G >> 1) + ((lane & 15) >> 2)) * 224 + (lane & 3) * 8;
      const LAS char* wa[7];
#pragma unroll
      for (int T = 0; T < 7; ++T) wa[T] = wa0 + 32 * ((T + 2 * wv) % 7);
      const LAS char* xa[4];
      {
        const int row = 32 * wv + (lane & 31), sw = (row >> 1) & 7;
#pragma unroll
        for (int ks = 0; ks < 4; ++ks) xa[ks] = lds + XOFF + row * 128 + (((2 * ks + (lane >> 5)) ^ sw) << 4);
      }
      constexpr int PD = 3;
      bf16x8 Af[196], Bf[32];
      const unsigned o1 = ((lane & 15) == 4 * ((lane >> 4) & 1)) ? 0x3f803f80u : 0u;
      u32x4 onesq = {o1, o1, o1, o1};
      PIN(onesq);
      const bf16x8 ones = __builtin_bit_cast(bf16x8, onesq);
#define A_READ(i, T) __builtin_bit_cast(bf16x8, cat8(trrd(wa[T] + (((i) >> 2) % 3) * STG + (16 * ((i) & 3)) * 224), \
                                                      trrd(wa[T] + (((i) >> 2) % 3) * STG + (16 * ((i) & 3) + 4) * 224)))
#define B_READ(i) (*(const LAS bf16x8*)(xa[(i) & 3] + (((i) >> 2) % 3) * STG))
#pragma unroll
      for (int T = 0; T < 7; ++T) acc[T] = (f32x16){};
      csa[0] = (f32x4){0.f, 0.f, 0.f, 0.f}; csa[1] = csa[0];
      BARX(bar_lgkm);
      STAMP(1);
      tag = lds_get1(ldsb + SY_VAL);
      Bf[0] = B_READ(0);
#pragma unroll
      for (int T = 0; T < PD; ++T) Af[T] = A_READ(0, T);
      bf16x8 Al[7][4];
      u32x2 stg;
      float cs[7];
      static_for<0, 196>([&](auto nc) {
        constexpr int n = decltype(nc)::value, i = n / 7, T = n % 7;
        constexpr int m = n + PD, im = m / 7, Tm = m % 7;
        if constexpr (n == 140) ld64c(stg, (unsigned)((lane & 31) * 8), r_fl, FL_STAT + (unsigned)((mt * 128 + wv * 32) * 8));
        if constexpr (n == 27 * 7 + 2) {
          if (lane < 32) {
#pragma unroll
            for (int j = 0; j < 2; ++j) *(LAS float*)(lds + CSUM + ((j + 2 * wv) % 7) * 128 + lane * 4) = csa[j][0];
          }
        }
        if constexpr (Tm == 0 && (im & 3) == 0) BARX(bar_lgkm);
        if constexpr (m < 196) {
          if constexpr (Tm == 0) Bf[im] = B_READ(im);
          Af[m] = A_READ(im, Tm);
        } else if constexpr (m == 196) {
#pragma unroll
          for (int k = 28; k < 32; ++k) Bf[k] = B_READ(k);
        } else if constexpr (m == 197) {
#pragma unroll
          for (int t = 0; t < 7; ++t) cs[t] = *(const LAS float*)(lds + CSUM + ((t + 2 * wv) % 7) * 128 + (lane & 31) * 4);
        } else {
#pragma unroll
          for (int k = 0; k < 4; ++k) Al[0][k] = A_READ(28 + k, 0);
        }
        acc[T] = __builtin_amdgcn_mfma_f32_32x32x16_bf16(Af[n], Bf[i], acc[T], 0, 0, 0);
        if constexpr (T < 2)
          csa[T] = __builtin_amdgcn_mfma_f32_16x16x32_bf16(ones, Af[n], csa[T], 0, 0, 0);
        SCHED_FENCE();
      });
      STAMP(5);
      wait_vm<0>();
      PIN(stg);
      if (__builtin_expect(!wave_all(stg[1] == tag), 0)) {
#pragma unroll 1
        for (int it = 0; it < (1 << 18); ++it) {
          ld64c(stg, (unsigned)((lane & 31) * 8), r_fl, FL_STAT + (unsigned)((mt * 128 + wv * 32) * 8));
          wait_vm<0>();
          PIN(stg);
          if (wave_all(stg[1] == tag)) break;
          __builtin_amdgcn_s_sleep(2);
        }
      }
      const unsigned stw = stg[0];
      const float mb = lo_f(stw), sd = hi_f(stw);
#pragma unroll
      for (int k = 28; k < 32; ++k) {
        u32x4 v = __builtin_bit_cast(u32x4, Bf[k]);
#pragma unroll
        for (int j = 0; j < 4; ++j) v[j] = pk_bf16(lo_f(v[j]) - mb, hi_f(v[j]) - mb);
        Bf[k] = __builtin_bit_cast(bf16x8, v);
      }
      const unsigned nm = (stw & 0xffffu) ^ 0x8000u;
      u32x4 bq = {nm | (nm << 16), nm, 0u, 0u};
      if (lane >= 32) bq = (u32x4){0u, 0u, 0u, 0u};
      const float ga = -1.4426950408889634f * __builtin_amdgcn_rcpf(sd);
      const unsigned hsb = (unsigned)(((mt * 4 + wv) * 112 + ft * 7) * 1024);
      STAMP(6);
      static_for<0, 8>([&](auto tc) {
        constexpr int T = decltype(tc)::value;
        u32x4 o;
        if constexpr (T < 7) {
          const float c = cs[T];
          const float ch = rbf(c), cm = rbf(c - ch), cl = (c - ch) - cm;
          const u32x4 aq = {pk_bf16(ch, cm), pk_bf16(cl, 0.f), 0u, 0u};
          acc[T] = __builtin_amdgcn_mfma_f32_32x32x16_bf16(__builtin_bit_cast(bf16x8, aq), __builtin_bit_cast(bf16x8, bq), acc[T], 0, 0, 0);
          SCHED_FENCE();
        }
#pragma unroll
        for (int j = 0; j < 4; ++j) {
          if constexpr (T + 1 < 7) Al[T + 1][j] = A_READ(28 + j, T + 1);
          if constexpr (T < 7) acc[T] = __builtin_amdgcn_mfma_f32_32x32x16_bf16(Al[T][j], Bf[28 + j], acc[T], 0, 0, 0);
          SCHED_FENCE();
          if constexpr (T >= 1) {
            const float s0 = __builtin_amdgcn_rcpf(fmaf(__builtin_amdgcn_exp2f(acc[T - 1][2 * j] * ga), sd, sd));
            const float s1 = __builtin_amdgcn_rcpf(fmaf(__builtin_amdgcn_exp2f(acc[T - 1][2 * j + 1] * ga), sd, sd));
            o[j] = pk_bf16(s0 * acc[T - 1][8 + 2 * j], s1 * acc[T - 1][8 + 2 * j + 1]);
            PIN(o[j]);
          }
          SCHED_FENCE();
        }
        if constexpr (T >= 1) st128c(o, lane * 16u, r_h, hsb + (unsigned)((T - 1 + 2 * wv) % 7) * 1024u);
      });
    }
    STAMP(7);
    const unsigned ldw = ldsb + SY_LD;
    const LAS char* waT[2];
    {
      const int G = lane >> 4, q4 = (lane & 15) >> 2;
      const LAS char* wa0 = lds + wv * 2048 + (4 * (G >> 1) + q4) * 128 + (G & 1) * 32 + (lane & 3) * 8;
      waT[0] = wa0 + ((q4 >> 1) * 64); waT[1] = wa0 + (((q4 >> 1) ^ 1) * 64);
    }
#define D_READ(i, T) __builtin_bit_cast(bf16x8, cat8(trrd(waT[T] + g2_base(i)), trrd(waT[T] + g2_base(i) + 1024)))
    bf16x8 Wr[28][2];
    lds_wait<1>(ldw, 2u);
    static_for<0, 5>([&](auto ic) { constexpr int i = decltype(ic)::value; Wr[i][0] = D_READ(i, 0); Wr[i][1] = D_READ(i, 1); });
    static_for<5, 10>([&](auto ic) { constexpr int i = decltype(ic)::value; Wr[i][0] = D_READ(i, 0); Wr[i][1] = D_READ(i, 1); });
    wait_vm<0>();
    if (lane < 8) set_flag(ws, FL_H + (unsigned)(((mt * 2 + (wv >> 1)) * 8 + lane) * 256 + (ft * 2 + (wv & 1)) * 4), tag);
    STAMP(8);
    lds_setv(ldsb + SY_MC + wv * 4, 2u);
    if (wv == 0) {
      lds_wait<4>(ldsb + SY_UR, 1u);
      conv_upper<14, 16>(lds, lane, 0, cp, dsk2, r_s);
    }
    lds_wait<1>(ldw, 3u);
    static_for<10, G2_NR>([&](auto ic) { constexpr int i = decltype(ic)::value; Wr[i][0] = D_READ(i, 0); Wr[i][1] = D_READ(i, 1); });
    lds_setv(ldsb + SY_MC + wv * 4, 3u);
    STAMP(9);
    __builtin_amdgcn_s_setprio(0);
    poll_flags<2>((lane & 1) * 16u, r_fl, FL_H + (unsigned)((tt * 8 + dtile) * 256 + wv * 32), tag);
    if (wv == 0) publish<4>(lds, 2, ws, FL_S + (unsigned)((lane * 8 + (cp >> 5)) * 256 + (cp & 31) * 4), 32, tag, lane);
    __builtin_amdgcn_s_setprio(2);
    STAMP(10);
    u32x4 hb[28][2];
#define HB_LOAD(i) do { ld128c(hb[i][0], lane * 16u, r_h, ((unsigned)(tt * 2) * 112u + G2_KS(wv, i)) * 1024u); \
                        ld128c(hb[i][1], lane * 16u, r_h, ((unsigned)(tt * 2 + 1) * 112u + G2_KS(wv, i)) * 1024u); } while (0)
#pragma unroll
    for (int i = 0; i < G2_H0; ++i) HB_LOAD(i);
    f32x16 acc2[2][2];
    {
#pragma unroll
      for (int a = 0; a < 2; ++a)
#pragma unroll
        for (int b = 0; b < 2; ++b) acc2[a][b] = (f32x16){};
      static_for<0, 28>([&](auto ic) {
        constexpr int i = decltype(ic)::value;
        if constexpr (i == 14) STAMP(11);
        if constexpr (i + 2 == 13) lds_wait<1>(ldw, 6u);
        if constexpr (i + 2 >= G2_NR && i + 2 < 28) { Wr[i + 2][0] = D_READ(i + 2, 0); Wr[i + 2][1] = D_READ(i + 2, 1); }
        wait_vm<2 * (g2_issued(i) - i - 1)>();
        PIN(hb[i][0]); PIN(hb[i][1]);
#pragma unroll
        for (int T = 0; T < 2; ++T)
#pragma unroll
          for (int b = 0; b < 2; ++b)
            acc2[T][b] = __builtin_amdgcn_mfma_f32_32x32x16_bf16(Wr[i][T], __builtin_bit_cast(bf16x8, hb[i][b]), acc2[T][b], 0, 0, 0);
        SCHED_FENCE();
        static_for<g2_issued(i), g2_issued(i + 1)>([&](auto jc) { constexpr int j = decltype(jc)::value; HB_LOAD(j); });
      });
    }
    STAMP(12);
    {
      int l2 = lane; PIN(l2);
      const int tl = l2 & 31, x7 = (tl & 7) ^ (l2 >> 5);
#pragma unroll
      for (int b = 0; b < 2; ++b) {
        const int c8 = 4 * b + (tl >> 3);
        const int o4 = c8 < 5 ? RED_A + c8 * 8192 : RED_B + (c8 - 5) * 8192;
        LAS char* rp = lds + o4 + wv * 2048 + (tl & 7) * 256;
#pragma unroll
        for (int T = 0; T < 2; ++T)
#pragma unroll
          for (int g = 0; g < 4; ++g) {
            const f32x4 v = {acc2[T][b][4 * g], acc2[T][b][4 * g + 1], acc2[T][b][4 * g + 2], acc2[T][b][4 * g + 3]};
            *(LAS f32x4*)(rp + T * 128 + (((2 * g) ^ x7) << 4)) = v;
          }
      }
    }
  } else if (w8 == 4) {
    __builtin_amdgcn_s_setprio(3);
    const rsrc_t r_wg = mk_rsrc(wg, DM * FF * 2), r_wu = mk_rsrc(wu, DM * FF * 2);
    const rsrc_t r_wd = mk_rsrc(wd, FF * DM * 2);
    unsigned woff[7], xoff[4], wv4[4];
#pragma unroll
    for (int i = 0; i < 4; ++i) {
      xoff[i] = (unsigned)((lane >> 3) * 1024 + (((lane & 7) ^ ((lane >> 4) + 4 * (i & 1))) * 16) + i * 7168);
      wv4[i] = (unsigned)((lane >> 3) * 1024 + (((lane & 7) ^ (((lane >> 4) & 1) << 2)) * 16) + (i >> 1) * 458752 + (i & 1) * 8192 - i * 1024);
    }
#pragma unroll
    for (int i = 0; i < 7; ++i) {
      const int c = i * 64 + lane, row = c / 14, col = c - row * 14;
      woff[i] = (unsigned)(row * (FF * 2) + col * 16 - (i & 3) * 1024);
    }
    const unsigned ldw = ldsb + SY_LD;
    auto g1_stage = [&](int s) __attribute__((always_inline)) {
      const unsigned st = (unsigned)((s + rot) & 7), lb = ldsb + (unsigned)((s % 3) * STG);
#pragma unroll
      for (int i = 0; i < 4; ++i)
        dma16x4(lb + XOFF + i * 4096, xoff[0], xoff[1], xoff[2], xoff[3], r_x, (unsigned)((mt * 128 + i * 32) * 1024) + st * 128u);
#pragma unroll
      for (int h = 0; h < 2; ++h) {
        const unsigned wso = (unsigned)(ft * 224) + (st * 64u + h * 32u) * (FF * 2);
        dma16x4(lb + h * 7168, woff[0], woff[1], woff[2], woff[3], r_wg, wso);
        dma16x3(lb + h * 7168 + 4096, woff[4], woff[5], woff[6], r_wg, wso);
        dma16x4(lb + W_UPOFF + h * 7168, woff[0], woff[1], woff[2], woff[3], r_wu, wso);
        dma16x3(lb + W_UPOFF + h * 7168 + 4096, woff[4], woff[5], woff[6], r_wu, wso);
      }
    };
#define G2_GROUP(g) G2_STEPS(5 * (g), cmin(5 * (g) + 5, 28))
#define G2_STEPS(a_, b_) static_for<a_, b_>([&](auto ic) { constexpr int i = decltype(ic)::value; \
        const unsigned so_ = G2_KS(0, i) * 16384u + (unsigned)(dtile * 128); \
        dma16x4(ldsb + g2_base(i), wv4[0], wv4[1], wv4[2], wv4[3], r_wd, so_); \
        dma16x4(ldsb + g2_base(i) + 4096, wv4[0], wv4[1], wv4[2], wv4[3], r_wd, so_ + 2u * 458752u); })
    unsigned tch = 0;
    bar_only();
    g1_stage(0);
    g1_stage(1);
    wait_vm<44>();
    STAMP(1);
    BARX(bar_only);
#pragma unroll
    for (int k = 0; k < 6; ++k) {
      g1_stage(k + 2);
      wait_vm<44>();
      BARX(bar_only);
    }
    STAMP(2);
    G2_GROUP(0);
    wait_vm<40>();
    BARX(bar_only);
    G2_GROUP(1);
    wait_vm<0>();
    asm volatile("" :: "v"(tch));
    lds_setv(ldw, 2u);
    STAMP(3);
    lds_wait<4>(ldsb + SY_MC, 2u);
    G2_STEPS(10, 15);
    wait_vm<16>();
    lds_setv(ldw, 3u);
    STAMP(4);
    G2_STEPS(15, 25);
    lds_wait<4>(ldsb + SY_MC, 3u);
    G2_GROUP(5);
    wait_vm<0>();
    lds_setv(ldw, 6u);
    STAMP(5);
    __builtin_amdgcn_s_setprio(0);
    EPI_X();
    lds_wait<1>(ldsb + SY_SR, 1u);
    EPI_S();
    STAMP(12);
  } else if (w8 == 5) {
    bar_only();
    wait_vm<0>();
    asm volatile("" :: "v"(pf0));
    BARX(bar_only);
    const unsigned tag = lds_get1(ldsb + SY_VAL);
    BARX(bar_only);
    {
      const int c = lane & 15, g = lane >> 4;
      unsigned d[2][3][4];
      float kscale[2];
#pragma unroll
      for (int hc = 0; hc < 2; ++hc) {
        f32x4 kacc[3];
#pragma unroll
        for (int t = 0; t < 3; ++t)
          kacc[t] = *(const LAS f32x4*)(lds + KF + hc * CV_KSTR + t * 1024 + lane * 16) + *(const LAS f32x4*)(lds + KGS + hc * 4096 + t * 1024 + lane * 16);
        const f32x2 gw = *(const LAS f32x2*)(lds + SY_KG + hc * 8);
        const float g2 = gw.x;
        const int we = (int)gw.y;
        if (g2 > 0.f) {
#pragma unroll
          for (int i = 0; i < 4; ++i) {
            const int l0 = 16 * c + 4 * g + i;
            const int l1 = g < 2 ? 256 + 16 * c + 8 * g + 2 * i : 512 + 256 * (g - 2) + 16 * c + 4 * i;
            const int l2 = g < 2 ? 1024 + 256 * (2 * g + (i >> 1)) + 8 * (i & 1) + 16 * c : 2048;
            kacc[0][i] *= __builtin_amdgcn_exp2f(g2 * (float)(l0 - 2048));
            kacc[1][i] *= __builtin_amdgcn_exp2f(g2 * (float)(l1 - 2048));
            kacc[2][i] *= __builtin_amdgcn_exp2f(g2 * (float)(l2 - 2048));
          }
        }
        kscale[hc] = __builtin_amdgcn_ldexpf(1.0f, we - 7) * __builtin_amdgcn_exp2f(g2 * 2048.f);
#pragma unroll
        for (int t = 0; t < 3; ++t)
#pragma unroll
          for (int i = 0; i < 4; ++i) { d[hc][t][i] = pk_f16(kacc[t][i] * 128.f, kacc[t][i] * 128.f); PIN(d[hc][t][i]); }
        BARX(bar_only);
      }
      BARX(bar_only);
      BARX(bar_only);
      BARX(bar_only);
      BARX(bar_only);
      __builtin_amdgcn_s_sleep(WSL);
      __builtin_amdgcn_s_setprio(3);
      STAMP(2);
      *(LAS f32x2*)(lds + SY_VAL + 8) = (f32x2){kscale[0], kscale[1]};
#define MIX(a, b) (((a) & 0xffffu) | ((b) & 0xffff0000u))
#pragma unroll
      for (int hc = 0; hc < 2; ++hc) {
        LAS char* kf = lds + KF + hc * CV_KSTR + 96;
        if (g < 2) {
#pragma unroll
          for (int h = 0; h < 2; ++h) {
            LAS char* p = kf + 2 * (1024 + 256 * (2 * g + h) + 16 * c);
            const unsigned v0 = d[hc][2][2 * h], v1 = d[hc][2][2 * h + 1];
            *(LAS u32x2*)(p - 8) = (u32x2){v0, v0};
            *(LAS u32x4*)(p) = (u32x4){v0, v0, MIX(v0, v1), v1};
            *(LAS u32x2*)(p + 16) = (u32x2){v1, v1};
          }
        } else {
          if (lane == 32) *(LAS u32x2*)(kf + 2 * 2044) = (u32x2){d[hc][2][0], d[hc][2][0]};
        }
        if (g >= 2) {
          LAS char* p = kf + 2 * (512 + 256 * (g - 2) + 16 * c);
          const unsigned v0 = d[hc][1][0], v1 = d[hc][1][1], v2 = d[hc][1][2], v3 = d[hc][1][3];
          *(LAS unsigned*)(p - 4) = v0;
          *(LAS u32x4*)(p) = (u32x4){v0, MIX(v0, v1), v1, v2};
          *(LAS u32x2*)(p + 16) = (u32x2){v2, MIX(v2, v3)};
          *(LAS unsigned*)(p + 24) = v3;
        }
        if (g < 2) {
          LAS char* p = kf + 2 * (256 + 16 * c + 8 * g);
          const unsigned v0 = d[hc][1][0], v1 = d[hc][1][1], v2 = d[hc][1][2], v3 = d[hc][1][3];
          *(LAS unsigned short*)(p - 2) = (unsigned short)v0;
          *(LAS u32x2*)(p) = (u32x2){v0, MIX(v1, v2)};
          *(LAS unsigned*)(p + 8) = v2;
          *(LAS unsigned short*)(p + 12) = (unsigned short)v3;
        }
        *(LAS u32x2*)(kf + 2 * (16 * c + 4 * g)) = (u32x2){MIX(d[hc][0][0], d[hc][0][1]), MIX(d[hc][0][2], d[hc][0][3])};
        if (lane < 6) *(LAS u32x4*)(kf - 96 + lane * 16) = (u32x4){0u, 0u, 0u, 0u};
      }
      lds_setv(ldsb + SY_UR + 8, 1u);
      STAMP(3);
    }
    __builtin_amdgcn_s_setprio(1);
    lds_wait<4>(ldsb + SY_UR, 1u);
    conv_upper<CV_NP, 16>(lds, lane, 1, cp, dsk2, r_s);
    conv_upper<CV_NP, 14>(lds, lane, 0, cp, dsk2, r_s);
    STAMP(4);
    publish<4>(lds, 2, ws, FL_S + (unsigned)((lane * 8 + (cp >> 5)) * 256 + (cp & 31) * 4), 32, tag, lane);
    STAMP(5);
    EPI_X();
    lds_wait<1>(ldsb + SY_CNT + 8, 4u);
    poll_flags<4>((lane & 7) * 16u, r_fl, FL_S + (unsigned)((tt * 8 + dtile) * 256), tag);
    lds_setv(ldsb + SY_SR, 1u);
    STAMP(6);
    EPI_S();
    STAMP(12);
  } else {
    const int hc = wv - 2;
    const int dch = cp * 2 + hc;
    const rsrc_t r_xn = mk_rsrc(ws + WS_XNF, SL * DM * 2);
    u32x4 xr[4];
    unsigned ep;
    const int row0 = bid * 8 + hc * 4;
    {
      const unsigned vo = (unsigned)((lane >> 4) * 1024 + (lane & 15) * 16);
#pragma unroll
      for (int i = 0; i < 4; ++i) ld128(xr[i], vo, r_x, (unsigned)(row0 * 1024 + i * 256));
      ld32c(ep, 0u * lane, r_fl, FL_EP);
    }
    bar_only();
    wait_vm<0>();
    STAMP(1);
#pragma unroll
    for (int i = 0; i < 4; ++i) PIN(xr[i]);
    PIN(ep);
    const unsigned tag = (unsigned)__builtin_amdgcn_readfirstlane(ep) + 1u;
    lds_setv(ldsb + SY_VAL, tag);
    {
      float f[32];
#pragma unroll
      for (int j = 0; j < 16; ++j) { f[2 * j] = lo_f(xr[j >> 2][j & 3]); f[2 * j + 1] = hi_f(xr[j >> 2][j & 3]); }
      float s0 = 0.f, s1 = 0.f;
#pragma unroll
      for (int j = 0; j < 32; j += 4) { s0 += f[j] + f[j + 1]; s1 += f[j + 2] + f[j + 3]; }
      const float mean = row_sum(s0 + s1) * (1.0f / DM);
      float q0 = 0.f, q1 = 0.f;
#pragma unroll
      for (int j = 0; j < 32; j += 2) {
        const float da = f[j] - mean, db = f[j + 1] - mean;
        q0 = fmaf(da, da, q0); q1 = fmaf(db, db, q1);
      }
      const float var = row_sum(q0 + q1) * (1.0f / DM);
      const float mb = rbf(mean);
      const float sb = rbf(__builtin_amdgcn_sqrtf(rbf(rbf(var) + 1e-5f)));
      if ((lane & 15) == 0)
        st64c((u32x2){pk_bf16(mb, sb), tag}, (unsigned)((lane >> 4) * 8), r_fl, FL_STAT + (unsigned)(row0 * 8));
      const float rs = __builtin_amdgcn_rcpf(sb);
      const unsigned vo = (unsigned)((lane & 15) * 32768 + (lane >> 4) * 16);
      STAMP(2);
#pragma unroll
      for (int i = 0; i < 4; ++i) {
        u32x4 o;
#pragma unroll
        for (int j = 0; j < 4; ++j) {
          const unsigned d = pk_bf16(f[8 * i + 2 * j] - mb, f[8 * i + 2 * j + 1] - mb);
          o[j] = pk_bf16(lo_f(d) * rs, hi_f(d) * rs);
        }
        st128c(o, vo, r_xn, (unsigned)(i * 16 * 32768 + row0 * 16));
      }
    }
    BARX(bar_lgkm);
    wait_vm<0>();
    BARX(bar_only);
    if (hc == 0 && lane < 8) set_flag(ws, FL_ALL + (unsigned)(lane * 4096 + bid * 4), tag);
    BARX(bar_only);
    BARX(bar_only);
    unsigned uw[16];
#pragma unroll
    for (int m = 0; m < 16; ++m) uw[m] = 0u;
    const unsigned usb = (unsigned)((cp >> 2) * 32768 + (cp & 3) * 4 + hc * 16384);
    const bool od = lane & 1;
    int st = 0;
#pragma unroll 1
    for (int k = 4; k < (1 << 18); ++k) {
      if (st == 0) {
        u32x4 vf;
        ld128c(vf, lane * 16u, r_fl, FL_ALL + (unsigned)(xcd * 4096));
        if (k <= 7) BARX(bar_only); else __builtin_amdgcn_s_sleep(4);
        wait_vm<0>();
        PIN(vf);
        if (wave_all((vf[0] == tag) & (vf[1] == tag) & (vf[2] == tag) & (vf[3] == tag))) st = 1;
      } else if (st == 1) {
        unsigned ul[16];
#pragma unroll
        for (int m = 0; m < 16; ++m) ld32c(ul[m], lane * 16u, r_xn, usb + (unsigned)m * 1024u);
        if (k <= 7) BARX(bar_only);
        wait_vm<0>();
#pragma unroll
        for (int m = 0; m < 16; m += 2) {
          PIN(ul[m]); PIN(ul[m + 1]);
          const unsigned a = pk_f16(lo_f(ul[m]), hi_f(ul[m])), b = pk_f16(lo_f(ul[m + 1]), hi_f(ul[m + 1]));
          const unsigned y = (unsigned)__builtin_amdgcn_update_dpp(0, (int)(od ? a : b), 0xB1, 0xf, 0xf, true);
          const unsigned f0 = od ? y : a, f1 = od ? b : y;
          uw[m] = __builtin_amdgcn_perm(f1, f0, 0x05040100u);
          uw[m + 1] = __builtin_amdgcn_perm(f1, f0, 0x07060302u);
        }
        st = 2;
      } else {
        BARX(bar_only);
      }
      if (st == 2 && k >= 7) break;
    }
    if (lane == 0 && bid == 0 && hc == 0) set_flag(ws, FL_EP, tag);
    {
      LAS char* up = lds + CV_U + 160 + hc * 2048 + (od ? 128 : 0) + (lane >> 1) * 4;
      __builtin_amdgcn_s_sleep(WSL);
      __builtin_amdgcn_s_setprio(3);
      if (lane < 10) *(LAS u32x4*)(lds + CV_U + hc * CV_USTR + lane * 16) = (u32x4){0u, 0u, 0u, 0u};
#pragma unroll
      for (int m = 0; m < 16; m += 2) {
        *(LAS unsigned*)(up + m * 128) = uw[m];
        *(LAS unsigned*)(up + CV_USTR + m * 128) = uw[m + 1];
      }
      lds_setv(ldsb + SY_UR + hc * 4, 1u);
    }
    STAMP(3);
    __builtin_amdgcn_s_setprio(1);
    lds_wait<4>(ldsb + SY_UR, 1u);
    const float kscale = *(const LAS float*)(lds + SY_VAL + 8 + hc * 4);
    STAMP(6);
    {
      const float dskip = hc ? hi_f(dsk2) : lo_f(dsk2);
      const int G = lane >> 4, q4 = (lane & 15) >> 2, p = lane & 3;
      const LAS char* pa = lds + KF + hc * CV_KSTR + 96 + 2 * (76 + 16 * p - 32 * G - 4 * q4 - 16);
      const LAS char* pb = lds + CV_U + hc * CV_USTR + 160 + 2 * (-76 + 32 * G + 4 * q4 + 4 * p);
      const LAS char* pu = lds + CV_U + hc * CV_USTR + 160 + lane * 2;
      f16x8 A[16], B[32];
#define CV_A(q) __builtin_bit_cast(f16x8, cat8(trrd(pa + 256 * (q) + 32), trrd(pa + 256 * (q))))
#define CV_B(w) __builtin_bit_cast(f16x8, cat8(trrd(pb + 128 * (w)), trrd(pb + 128 * (w) + 32)))
      A[0] = CV_A(0); B[0] = CV_B(0); B[1] = CV_B(1);
      A[1] = CV_A(1); B[2] = CV_B(2); B[3] = CV_B(3);
      f32x4 ca[2][2];
      _Float16 us[2][2];
      unsigned so16[16];
      float c3 = 0.f, c2 = 0.f, c1 = 0.f;
      auto tile_y = [&](const f32x4 a) __attribute__((always_inline)) {
        const float h3 = a[3];
        const float h2 = a[2] + shr_carry(c3, h3);
        const float h1 = a[1] + shr_carry(c2, h2);
        const float y = a[0] + shr_carry(c1, h1);
        c3 = dpp_mov<0x13C>(h3); c2 = dpp_mov<0x13C>(h2); c1 = dpp_mov<0x13C>(h1);
        return y * kscale;
      };
      static_for<0, CV_NP + 1>([&](auto jc) {
        constexpr int jj = decltype(jc)::value;
        if constexpr (jj + 2 < CV_NP) { A[jj + 2] = CV_A(jj + 2); B[2 * jj + 4] = CV_B(2 * jj + 4); B[2 * jj + 5] = CV_B(2 * jj + 5); }
        if constexpr (jj < CV_NP) {
          us[jj & 1][0] = *(const LAS _Float16*)(pu + jj * 256); us[jj & 1][1] = *(const LAS _Float16*)(pu + jj * 256 + 128);
          f32x4 a0 = {0.f, 0.f, 0.f, 0.f}, a1 = {0.f, 0.f, 0.f, 0.f};
#pragma unroll
          for (int q = jj; q >= 0; --q) {
            a0 = __builtin_amdgcn_mfma_f32_16x16x32_f16(A[q], B[2 * (jj - q)], a0, 0, 0, 0);
            a1 = __builtin_amdgcn_mfma_f32_16x16x32_f16(A[q], B[2 * (jj - q) + 1], a1, 0, 0, 0);
          }
          ca[jj & 1][0] = a0; ca[jj & 1][1] = a1;
        }
        if constexpr (jj >= 1) {
          const float y0 = tile_y(ca[(jj - 1) & 1][0]), y1 = tile_y(ca[(jj - 1) & 1][1]);
          const unsigned yq = pk_bf16(y0, y1);
          const unsigned uq = pk_bf16((float)us[(jj - 1) & 1][0] * dskip, (float)us[(jj - 1) & 1][1] * dskip);
          so16[jj - 1] = pk_bf16(lo_f(yq) + lo_f(uq), hi_f(yq) + hi_f(uq));
        }
        if constexpr (jj >= 1 && jj < CV_NP) {
#pragma unroll
          for (int k = 0; k < 2 * (jj + 1); ++k) {
            __builtin_amdgcn_sched_group_barrier(0x8, 1, 0);
            __builtin_amdgcn_sched_group_barrier(0x100, 1, 0);
            __builtin_amdgcn_sched_group_barrier(0x2, jj < 7 ? 4 : 2, 0);
          }
        }
        SCHED_FENCE();
        if constexpr (jj >= 1) st32c(so16[jj - 1], lane * 4u, r_s, (unsigned)(dch * 4096 + (jj - 1) * 256));
      });
    }
    STAMP(7);
    publish<4>(lds, 2, ws, FL_S + (unsigned)((lane * 8 + (cp >> 5)) * 256 + (cp & 31) * 4), 32, tag, lane);
    STAMP(9);
    EPI_X();
    lds_wait<1>(ldsb + SY_SR, 1u);
    EPI_S();
    STAMP(12);
  }
  bar_lgkm();
  {
    int t2 = tid; PIN(t2);
    const u32x4 xv = *(const LAS u32x4*)(lds + EPX + t2 * 16);
    unsigned sv[8];
#pragma unroll
    for (int j = 0; j < 8; ++j) sv[j] = *(const LAS unsigned*)(lds + EPS + ((t2 & 7) * 8 + j) * 256 + (((t2 >> 5) + (t2 & 7)) & 15) * 16 + ((t2 >> 3) & 3) * 4);
    const int rb = w8 < 5 ? RED_A + w8 * 8192 : RED_B + (w8 - 5) * 8192;
    const int tr7 = (t2 >> 3) & 7;
    const LAS char* rp = lds + rb + tr7 * 256 + ((t2 >> 2) & 1) * 128;
    const bool odd = tt & 1;
    u32x4 o;
#pragma unroll
    for (int i = 0; i < 2; ++i) {
      const int ch = (((t2 & 3) * 2 + i) ^ tr7) << 4;
      f32x4 v = *(const LAS f32x4*)(rp + ch);
#pragma unroll
      for (int w = 1; w < 4; ++w) v += *(const LAS f32x4*)(rp + w * 2048 + ch);
#pragma unroll
      for (int jj = 0; jj < 2; ++jj) {
        const unsigned fp = pk_bf16(v[2 * jj], v[2 * jj + 1]);
        const float f0 = lo_f(fp), f1 = hi_f(fp);
        const float g0 = f0 * __builtin_amdgcn_rcpf(1.0f + __builtin_amdgcn_exp2f(f0 * -1.4426950408889634f));
        const float g1 = f1 * __builtin_amdgcn_rcpf(1.0f + __builtin_amdgcn_exp2f(f1 * -1.4426950408889634f));
        const unsigned gp = pk_bf16(g0, g1);
        const int j = 4 * i + 2 * jj;
        const float s0 = odd ? hi_f(sv[j]) : lo_f(sv[j]), s1 = odd ? hi_f(sv[j + 1]) : lo_f(sv[j + 1]);
        const unsigned sg = pk_bf16(s0 * lo_f(gp), s1 * hi_f(gp));
        const unsigned xw = xv[2 * i + jj];
        o[2 * i + jj] = pk_bf16(lo_f(xw) + lo_f(sg), hi_f(xw) + hi_f(sg));
      }
    }
    *(u32x4*)((char*)out + XO(t2)) = o;
  }
  STAMP(13); STAMP_RT(15);
  STAMP_OUT(0);
  BW_OUT();
}

extern "C" void kernel_launch(void* const* d_in, const int* in_sizes, int n_in, void* d_out,
                              int out_size, void* d_ws, size_t ws_size, hipStream_t stream) {
  (void)hipFuncSetAttribute((const void*)fused_kernel, hipFuncAttributeMaxDynamicSharedMemorySize, LDS_BYTES);
  fused_kernel<<<256, 512, LDS_BYTES, stream>>>(
      (const unsigned short*)d_in[0], (const unsigned short*)d_in[1], (const unsigned short*)d_in[2],
      (const unsigned short*)d_in[3], (const unsigned short*)d_in[4], (const unsigned short*)d_in[5],
      (const unsigned short*)d_in[6], (const unsigned*)d_in[7], (const unsigned*)d_in[8],
      (const unsigned short*)d_in[10], (const unsigned short*)d_in[11], (const unsigned short*)d_in[12],
      (char*)d_ws, (unsigned short*)d_out);
}
```

```cpp
#include <hip/hip_runtime.h>
#include <hip/hip_bf16.h>
#include <stdint.h>
#include <type_traits>

typedef __attribute__((ext_vector_type(4))) short s16x4;
typedef __attribute__((ext_vector_type(8))) short s16x8;
typedef __attribute__((ext_vector_type(8))) _Float16 f16x8;
typedef __attribute__((ext_vector_type(8))) __bf16 bf16x8;
typedef __attribute__((ext_vector_type(2))) float f32x2;
typedef __attribute__((ext_vector_type(4))) float f32x4;
typedef __attribute__((ext_vector_type(16))) float f32x16;
typedef __attribute__((ext_vector_type(4))) unsigned u32x4;
typedef __attribute__((ext_vector_type(3))) unsigned u32x3;
typedef __attribute__((ext_vector_type(2))) unsigned u32x2;
typedef __attribute__((ext_vector_type(2))) __bf16 bf16x2;
typedef __attribute__((ext_vector_type(2))) _Float16 f16x2;
#define LAS __attribute__((address_space(3)))
#define GAS __attribute__((address_space(1)))
typedef __amdgpu_buffer_rsrc_t rsrc_t;

constexpr int SL = 2048;
constexpr int DM = 512;
constexpr int NM = 128;
constexpr int FF = 1792;

constexpr size_t WS_XNF = 0;
constexpr size_t WS_ST = 2 * (size_t)SL * DM * 2;
constexpr size_t WS_HF = WS_ST + (size_t)SL * DM * 2;
constexpr size_t WS_FLAG = 2 * (size_t)SL * DM;
constexpr unsigned FL_ALL = 0, FL_STAT = 65536, FL_H = 131072, FL_S = 196608, FL_EP = 262144, FL_BYTES = 262400;

template <int I, int N, class F>
__device__ __forceinline__ void static_for(F&& f) {
  if constexpr (I < N) { f(std::integral_constant<int, I>{}); static_for<I + 1, N>(f); }
}

__device__ __forceinline__ unsigned pk_bf16(float lo, float hi) {
  f32x2 v = {lo, hi};
  return __builtin_bit_cast(unsigned, __builtin_convertvector(v, bf16x2));
}
__device__ __forceinline__ unsigned pk_f16(float lo, float hi) {
  f32x2 v = {lo, hi};
  return __builtin_bit_cast(unsigned, __builtin_convertvector(v, f16x2));
}
__device__ __forceinline__ float lo_f(unsigned p) { return __uint_as_float(p << 16); }
__device__ __forceinline__ float hi_f(unsigned p) { return __uint_as_float(p & 0xffff0000u); }
__device__ __forceinline__ float rbf(float x) { return lo_f(pk_bf16(x, x)); }

template <int CTRL>
__device__ __forceinline__ float dpp_mov(float v) {
  return __int_as_float(__builtin_amdgcn_update_dpp(0, __float_as_int(v), CTRL, 0xf, 0xf, true));
}
__device__ __forceinline__ float shr_carry(float c, float v) {
  return __builtin_bit_cast(float, __builtin_amdgcn_update_dpp(__builtin_bit_cast(int, c), __builtin_bit_cast(int, v), 0x138, 0xf, 0xf, false));
}
__device__ __forceinline__ float rdlane(float v, int l) {
  return __int_as_float(__builtin_amdgcn_readlane(__float_as_int(v), l));
}
__device__ __forceinline__ float wave_sum(float v) {
  v += dpp_mov<0xB1>(v);
  v += dpp_mov<0x4E>(v);
  v += dpp_mov<0x141>(v);
  v += dpp_mov<0x140>(v);
  return (rdlane(v, 0) + rdlane(v, 16)) + (rdlane(v, 32) + rdlane(v, 48));
}
__device__ __forceinline__ float row_sum(float v) {
  v += dpp_mov<0xB1>(v);
  v += dpp_mov<0x4E>(v);
  v += dpp_mov<0x141>(v);
  v += dpp_mov<0x140>(v);
  return v;
}
__device__ __forceinline__ float half_sum(float v, int lane) {
  v += dpp_mov<0xB1>(v);
  v += dpp_mov<0x4E>(v);
  v += dpp_mov<0x141>(v);
  v += dpp_mov<0x140>(v);
  const float lo = rdlane(v, 0) + rdlane(v, 16), hi = rdlane(v, 32) + rdlane(v, 48);
  return lane < 32 ? lo : hi;
}
__device__ __forceinline__ float wave_max(float v) {
  v = fmaxf(v, dpp_mov<0xB1>(v));
  v = fmaxf(v, dpp_mov<0x4E>(v));
  v = fmaxf(v, dpp_mov<0x141>(v));
  v = fmaxf(v, dpp_mov<0x140>(v));
  return fmaxf(fmaxf(rdlane(v, 0), rdlane(v, 16)), fmaxf(rdlane(v, 32), rdlane(v, 48)));
}

__device__ __forceinline__ rsrc_t mk_rsrc(const void* p, unsigned bytes) {
  return __builtin_amdgcn_make_buffer_rsrc((void*)p, (short)0, (int)bytes, 0x00020000);
}
__device__ __forceinline__ void dma16(unsigned lds_dst, unsigned voff, rsrc_t r, unsigned soff) {
  asm volatile("s_mov_b32 m0, %0\n\ts_nop 0\n\tbuffer_load_dwordx4 %1, %2, %3 offen lds"
               :: "s"(lds_dst), "v"(voff), "s"(r), "s"(soff) : "memory");
}
__device__ __forceinline__ void dma16x4(unsigned lds_dst, unsigned v0, unsigned v1, unsigned v2, unsigned v3, rsrc_t r, unsigned soff) {
  asm volatile("s_mov_b32 m0, %0\n\ts_nop 0\n\t"
               "buffer_load_dwordx4 %1, %5, %6 offen lds\n\t"
               "buffer_load_dwordx4 %2, %5, %6 offen offset:1024 lds\n\t"
               "buffer_load_dwordx4 %3, %5, %6 offen offset:2048 lds\n\t"
               "buffer_load_dwordx4 %4, %5, %6 offen offset:3072 lds"
               :: "s"(lds_dst), "v"(v0), "v"(v1), "v"(v2), "v"(v3), "s"(r), "s"(soff) : "memory");
}
__device__ __forceinline__ void dma16x3(unsigned lds_dst, unsigned v0, unsigned v1, unsigned v2, rsrc_t r, unsigned soff) {
  asm volatile("s_mov_b32 m0, %0\n\ts_nop 0\n\t"
               "buffer_load_dwordx4 %1, %4, %5 offen lds\n\t"
               "buffer_load_dwordx4 %2, %4, %5 offen offset:1024 lds\n\t"
               "buffer_load_dwordx4 %3, %4, %5 offen offset:2048 lds"
               :: "s"(lds_dst), "v"(v0), "v"(v1), "v"(v2), "s"(r), "s"(soff) : "memory");
}
__device__ __forceinline__ void ld128(u32x4& dst, unsigned voff, rsrc_t r, unsigned soff) {
  asm volatile("buffer_load_dwordx4 %0, %1, %2, %3 offen" : "=v"(dst) : "v"(voff), "s"(r), "s"(soff) : "memory");
}
__device__ __forceinline__ void ld128c(u32x4& dst, unsigned voff, rsrc_t r, unsigned soff) {
  asm volatile("buffer_load_dwordx4 %0, %1, %2, %3 offen sc1" : "=v"(dst) : "v"(voff), "s"(r), "s"(soff) : "memory");
}
__device__ __forceinline__ void ld32(unsigned& dst, unsigned voff, rsrc_t r, unsigned soff) {
  asm volatile("buffer_load_dword %0, %1, %2, %3 offen" : "=v"(dst) : "v"(voff), "s"(r), "s"(soff) : "memory");
}
__device__ __forceinline__ void ld64c(u32x2& dst, unsigned voff, rsrc_t r, unsigned soff) {
  asm volatile("buffer_load_dwordx2 %0, %1, %2, %3 offen sc1" : "=v"(dst) : "v"(voff), "s"(r), "s"(soff) : "memory");
}
__device__ __forceinline__ void ld32c(unsigned& dst, unsigned voff, rsrc_t r, unsigned soff) {
  asm volatile("buffer_load_dword %0, %1, %2, %3 offen sc1" : "=v"(dst) : "v"(voff), "s"(r), "s"(soff) : "memory");
}
__device__ __forceinline__ void st32c(unsigned v, unsigned voff, rsrc_t r, unsigned soff) {
  asm volatile("buffer_store_dword %0, %1, %2, %3 offen sc1" :: "v"(v), "v"(voff), "s"(r), "s"(soff) : "memory");
}
__device__ __forceinline__ void st64c(u32x2 v, unsigned voff, rsrc_t r, unsigned soff) {
  asm volatile("buffer_store_dwordx2 %0, %1, %2, %3 offen sc1\n\ts_nop 1" :: "v"(v), "v"(voff), "s"(r), "s"(soff) : "memory");
}
__device__ __forceinline__ void ld16(unsigned& dst, unsigned voff, rsrc_t r, unsigned soff) {
  asm volatile("buffer_load_ushort %0, %1, %2, %3 offen" : "=v"(dst) : "v"(voff), "s"(r), "s"(soff) : "memory");
}
__device__ __forceinline__ void st128c(u32x4 v, unsigned voff, rsrc_t r, unsigned soff) {
  asm volatile("buffer_store_dwordx4 %0, %1, %2, %3 offen sc1\n\ts_nop 1" :: "v"(v), "v"(voff), "s"(r), "s"(soff) : "memory");
}
template <int N>
__device__ __forceinline__ void wait_vm() { asm volatile("s_waitcnt vmcnt(%0)" :: "n"(N > 63 ? 63 : N) : "memory"); }
__device__ __forceinline__ void bar_lgkm() { asm volatile("s_waitcnt lgkmcnt(0)\n\ts_barrier" ::: "memory"); }
__device__ __forceinline__ void bar_only() { asm volatile("s_barrier" ::: "memory"); }
#define BW_DECL
#define BARX(f) f()
#define BW_OUT()
#define PIN(x) asm volatile("" : "+v"(x))
#define SCHED_FENCE() __builtin_amdgcn_sched_barrier(0)

__device__ __forceinline__ s16x4 trrd(const LAS char* p) {
  return __builtin_amdgcn_ds_read_tr16_b64_v4i16((LAS s16x4*)p);
}
__device__ __forceinline__ s16x8 cat8(s16x4 a, s16x4 b) {
  return __builtin_shufflevector(a, b, 0, 1, 2, 3, 4, 5, 6, 7);
}

__device__ __forceinline__ bool wave_all(bool p) { return __builtin_amdgcn_ballot_w64(!p) == 0ull; }
template <int SLP = 2>
__device__ __forceinline__ void poll_flags(unsigned voff, rsrc_t r, unsigned soff, unsigned tag) {
#pragma unroll 1
  for (int it = 0; it < (1 << 18); ++it) {
    u32x4 v;
    asm volatile("buffer_load_dwordx4 %0, %1, %2, %3 offen sc1\n\ts_waitcnt vmcnt(0)"
                 : "=&v"(v) : "v"(voff), "s"(r), "s"(soff) : "memory");
    const bool ok = (v[0] == tag) & (v[1] == tag) & (v[2] == tag) & (v[3] == tag);
    if (wave_all(ok)) break;
    __builtin_amdgcn_s_sleep(SLP);
  }
}
__device__ __forceinline__ void set_flag(char* ws, unsigned off, unsigned tag) {
  __hip_atomic_store((GAS unsigned*)(ws + WS_FLAG + off), tag, __ATOMIC_RELAXED, __HIP_MEMORY_SCOPE_AGENT);
}

#define STAMP_DECL
#define STAMP(i)
#define STAMP_RT(i)
#define STAMP_OUT(kid)

__device__ __forceinline__ f32x2 cmul(f32x2 a, f32x2 b) {
  f32x2 r;
  r.x = fmaf(a.x, b.x, -(a.y * b.y));
  r.y = fmaf(a.x, b.y, a.y * b.x);
  return r;
}
__device__ __forceinline__ f32x2 cexp_l(float a, float b, float l) {
  const float mag = __builtin_amdgcn_exp2f(a * l * 1.4426950408889634f);
  const float x = b * l;
  const float C1 = 0.15915494309189535f;
  const float C2 = (float)(0.15915494309189533577 - (double)0.15915494309189535f);
  const float hi = x * C1;
  const float lo = fmaf(x, C1, -hi) + x * C2;
  const float r = (hi - __builtin_rintf(hi)) + lo;
  f32x2 z;
  z.x = mag * __builtin_amdgcn_cosf(r);
  z.y = mag * __builtin_amdgcn_sinf(r);
  return z;
}

constexpr int W_UPOFF = 14336 + 128;
constexpr int XOFF = W_UPOFF + 14336;
constexpr int STG = XOFF + 16384;
constexpr int KGS = 3 * STG;
constexpr int KF = KGS + 8192;
constexpr int CV_KSTR = (48 + 2048) * 2;
constexpr int CV_USTR = (80 + 2048) * 2;
constexpr int CV_U = KF + 2 * CV_KSTR;
constexpr int CSUM = CV_U + 2 * CV_USTR;
constexpr int STAT = CSUM + 896;
constexpr int SY = STAT + 512;
constexpr int SY_LD = SY, SY_MC = SY + 16, SY_ST = SY + 4, SY_HR = SY + 8, SY_SR = SY + 12, SY_UR = SY + 32  ,
              SY_CNT = SY + 48, SY_VAL = SY + 64, SY_KG = SY + 80, SY_END = SY + 96;
constexpr int LDS_BYTES = SY_END;
static_assert(LDS_BYTES <= 163840, "lds");
constexpr int G2_RED = 272;
constexpr int EPX = KGS;
constexpr int EPS = KF;
#define WSL 4
constexpr unsigned WS_USED = (unsigned)(WS_HF + (size_t)SL * FF * 2);
constexpr unsigned CODE_BYTES = 49152;
constexpr int CV_NP = 12;
constexpr int G2_NR = 13;
constexpr int cmax(int a, int b) { return a > b ? a : b; }
constexpr int cmin(int a, int b) { return a < b ? a : b; }
constexpr int g2_base(int i) { return (((i % 15) / 5 + 2) % 3) * STG + (i % 5) * 8192; }
constexpr int RED_A = g2_base(15), RED_B = g2_base(20);
constexpr int G2_H0 = 6;
constexpr int g2_issued(int i) { return cmin(28, G2_H0 + (3 * i) / 2); }

__device__ __forceinline__ void lds_setv(unsigned addr, unsigned v) {
  asm volatile("ds_write_b32 %0, %1" :: "v"(addr), "v"(v) : "memory");
}
__device__ __forceinline__ unsigned lds_min4(unsigned addr) {
  u32x4 v;
  asm volatile("ds_read_b128 %0, %1\n\ts_waitcnt lgkmcnt(0)" : "=&v"(v) : "v"(addr) : "memory");
  return (unsigned)__builtin_amdgcn_readfirstlane(min(min(v[0], v[1]), min(v[2], v[3])));
}
__device__ __forceinline__ unsigned lds_get1(unsigned addr) {
  unsigned v;
  asm volatile("ds_read_b32 %0, %1\n\ts_waitcnt lgkmcnt(0)" : "=&v"(v) : "v"(addr) : "memory");
  return (unsigned)__builtin_amdgcn_readfirstlane(v);
}
template <int W>
__device__ __forceinline__ unsigned lds_val(unsigned addr) { return W == 4 ? lds_min4(addr) : lds_get1(addr); }
template <int W>
__device__ __forceinline__ void lds_spin(unsigned addr, unsigned need) {
#pragma unroll 1
  for (int it = 0; it < (1 << 20); ++it) {
    if (lds_val<W>(addr) >= need) break;
    __builtin_amdgcn_s_sleep(1);
  }
}
template <int W>
__device__ __forceinline__ void lds_wait(unsigned addr, unsigned need) {
  if (__builtin_expect(lds_val<W>(addr) < need, 0)) lds_spin<W>(addr, need);
}
template <int NW, bool DRAIN = true>
__device__ __forceinline__ void publish(LAS char* lds, int cnt, char* ws, unsigned off, int nl, unsigned tag, int lane) {
  if (DRAIN) wait_vm<0>();
  unsigned old = 0;
  if (lane == 0) old = __hip_atomic_fetch_add((LAS unsigned*)(lds + SY_CNT + cnt * 4), 1u, __ATOMIC_RELAXED, __HIP_MEMORY_SCOPE_WORKGROUP);
  if (__builtin_amdgcn_readfirstlane(old) == NW - 1 && lane < nl) set_flag(ws, off, tag);
}

template <int PA, int PB>
__device__ __forceinline__ void conv_upper(LAS char* lds, int lane, int hc, int cp, unsigned dsk2, rsrc_t r_s) {
  const float kscale = *(const LAS float*)(lds + SY_VAL + 8 + hc * 4);
  const float dskip = hc ? hi_f(dsk2) : lo_f(dsk2);
  const int G = lane >> 4, q4 = (lane & 15) >> 2, p = lane & 3;
  const LAS char* pa = lds + KF + hc * CV_KSTR + 96 + 2 * (76 + 16 * p - 32 * G - 4 * q4 - 16);
  const LAS char* pb = lds + CV_U + hc * CV_USTR + 160 + 2 * (-76 + 32 * G + 4 * q4 + 4 * p);
  const LAS char* pu = lds + CV_U + hc * CV_USTR + 160 + lane * 2;
  constexpr int J0 = 2 * PA - 1, J1 = 2 * PB, NT = J1 - J0;
  f16x8 A[16], B[32];
  f32x4 ca[NT];
#define CV_A(q) __builtin_bit_cast(f16x8, cat8(trrd(pa + 256 * (q) + 32), trrd(pa + 256 * (q))))
#define CV_B(w) __builtin_bit_cast(f16x8, cat8(trrd(pb + 128 * (w)), trrd(pb + 128 * (w) + 32)))
  B[0] = CV_B(0); B[1] = CV_B(1); B[2] = CV_B(2);
  constexpr int QA = (J0 - 1) / 2;
#pragma unroll
  for (int q = PB - 1; q >= QA; --q) A[q] = CV_A(q);
#pragma unroll
  for (int j = 0; j < NT; ++j) ca[j] = (f32x4){0.f, 0.f, 0.f, 0.f};
  static_for<0, J1>([&](auto wc) {
    constexpr int w = decltype(wc)::value;
    if constexpr (w + 3 < J1) B[w + 3] = CV_B(w + 3);
    if constexpr ((w & 1) == 0 && QA - 1 - w / 2 >= 0) A[QA - 1 - w / 2] = CV_A(QA - 1 - w / 2);
#pragma unroll
    for (int q = PB - 1; q >= 0; --q) {
      const int j = w + 2 * q;
      if (j >= J0 && j < J1) ca[j - J0] = __builtin_amdgcn_mfma_f32_16x16x32_f16(A[q], B[w], ca[j - J0], 0, 0, 0);
    }
    SCHED_FENCE();
  });
  float c3 = 0.f, c2 = 0.f, c1 = 0.f, yv[NT];
#pragma unroll
  for (int j = 0; j < NT; ++j) {
    const f32x4 a = ca[j];
    const float h3 = a[3];
    const float h2 = a[2] + shr_carry(c3, h3);
    const float h1 = a[1] + shr_carry(c2, h2);
    yv[j] = a[0] + shr_carry(c1, h1);
    c3 = dpp_mov<0x13C>(h3); c2 = dpp_mov<0x13C>(h2); c1 = dpp_mov<0x13C>(h1);
  }
#pragma unroll
  for (int jp = PA; jp < PB; ++jp) {
    const float u0 = (float)*(const LAS _Float16*)(pu + jp * 256), u1 = (float)*(const LAS _Float16*)(pu + jp * 256 + 128);
    const unsigned yq = pk_bf16(yv[2 * jp - J0] * kscale, yv[2 * jp + 1 - J0] * kscale);
    const unsigned uq = pk_bf16(u0 * dskip, u1 * dskip);
    st32c(pk_bf16(lo_f(yq) + lo_f(uq), hi_f(yq) + hi_f(uq)), lane * 4u, r_s, (unsigned)((cp * 2 + hc) * 4096 + jp * 256));
  }
#undef CV_A
#undef CV_B
}

#if defined(__HIP_DEVICE_COMPILE__)
__attribute__((target("no-packed-fp32-ops")))
#endif
__global__ __launch_bounds__(512, 2) void fused_kernel(
    const unsigned short* __restrict__ x, const unsigned short* __restrict__ A_re,
    const unsigned short* __restrict__ A_im, const unsigned short* __restrict__ B_re,
    const unsigned short* __restrict__ B_im, const unsigned short* __restrict__ C_re,
    const unsigned short* __restrict__ C_im, const unsigned* __restrict__ log_dt2,
    const unsigned* __restrict__ dsk2p, const unsigned short* __restrict__ wg,
    const unsigned short* __restrict__ wu, const unsigned short* __restrict__ wd,
    char* ws, unsigned short* __restrict__ out) {
  extern __shared__ __attribute__((aligned(16))) char smem[];
  unsigned long long pc0;
  asm volatile("s_getpc_b64 %0" : "=s"(pc0));
  LAS char* lds = (LAS char*)smem;
  const unsigned ldsb = (unsigned)(uintptr_t)lds;
  const int tid = threadIdx.x, lane = tid & 63;
  const int w8 = __builtin_amdgcn_readfirstlane(tid >> 6);
  const int wv = w8 & 3;
  STAMP_DECL; BW_DECL; STAMP_RT(14); STAMP(0);
  const int bid = blockIdx.x;
  const int xcd = bid & 7, bi = bid >> 3;
  unsigned pf0 = 0, pf1 = 0, pf2 = 0, pf3 = 0;
  if (w8 == 5) ld32(pf0, (unsigned)((bi * 16 + (lane & 15)) * 128), mk_rsrc((const void*)pc0, CODE_BYTES), 0u);
  asm volatile("" :: "s"(x), "s"(A_re), "s"(A_im), "s"(B_re), "s"(B_im), "s"(C_re), "s"(C_im), "s"(log_dt2),
               "s"(dsk2p), "s"(wg), "s"(wu), "s"(wd), "s"(ws), "s"(out));
  if (w8 == 5) {
  }
  const int mt = (xcd >> 2) * 8 + (bi >> 2);
  const int ft = (xcd & 3) * 4 + (bi & 3);
  const int rot = ((bi >> 2) + 2 * (bi & 3)) & 7;
  const int cp = xcd * 32 + bi;
  const int tt = xcd * 4 + (bi >> 3);
  const int dtile = bi & 7;
  const int rot2 = ((bi >> 3) * 7 + dtile * 3) % 28;

  const rsrc_t r_x = mk_rsrc(x, SL * DM * 2);
  const rsrc_t r_s = mk_rsrc(ws + WS_ST, SL * DM * 2);
  const rsrc_t r_fl = mk_rsrc(ws + WS_FLAG, FL_BYTES);
#define G2_KS(w, i) ((unsigned)((w) * 28 + (((i) + rot2) >= 28 ? (i) + rot2 - 28 : (i) + rot2)))
#define XO(t_) ((unsigned)((tt * 64 + ((t_) >> 3)) * (DM * 2) + dtile * 128 + ((t_) & 7) * 16))
#define EPI_X() do { int l_ = lane; PIN(l_); u32x4 xw_[2]; \
    _Pragma("unroll") for (int j = 0; j < 2; ++j) ld128(xw_[j], XO(wv * 128 + j * 64 + l_), r_x, 0u); \
    lds_wait<4>(ldsb + SY_UR, 1u); wait_vm<0>(); \
    _Pragma("unroll") for (int j = 0; j < 2; ++j) { PIN(xw_[j]); *(LAS u32x4*)(lds + EPX + (wv * 128 + j * 64 + l_) * 16) = xw_[j]; } } while (0)
#define EPI_S() do { int l_ = lane; PIN(l_); u32x4 sw_[4]; \
    const unsigned so = (unsigned)((dtile * 64 + wv * 16 + (l_ >> 4)) * 4096 + (tt >> 1) * 256 + (l_ & 15) * 16); \
    _Pragma("unroll") for (int j = 0; j < 4; ++j) ld128c(sw_[j], so, r_s, (unsigned)(j * 16384)); \
    wait_vm<0>(); \
    _Pragma("unroll") for (int j = 0; j < 4; ++j) { PIN(sw_[j]); \
      *(LAS u32x4*)(lds + EPS + (wv * 16 + j * 4 + (l_ >> 4)) * 256 + ((l_ + wv * 2 + (j >> 1)) & 15) * 16) = sw_[j]; } } while (0)

  if (w8 < 4) {
    const rsrc_t r_h = mk_rsrc(ws + WS_HF, SL * FF * 2);
    {
    const int hc = wv & 1, m = wv >> 1;
    const int dch = cp * 2 + hc;
    unsigned prm[6];
    {
      const unsigned po = (unsigned)(dch * NM * 2);
      ld32(prm[0], lane * 4u, mk_rsrc(A_re, DM * NM * 2), po);
      ld32(prm[1], lane * 4u, mk_rsrc(A_im, DM * NM * 2), po);
      ld32(prm[2], lane * 4u, mk_rsrc(B_re, DM * NM * 2), po);
      ld32(prm[3], lane * 4u, mk_rsrc(B_im, DM * NM * 2), po);
      ld32(prm[4], lane * 4u, mk_rsrc(C_re, DM * NM * 2), po);
      ld32(prm[5], lane * 4u, mk_rsrc(C_im, DM * NM * 2), po);
    }
    if (tid < 5) *(LAS u32x4*)(lds + SY + tid * 16) = (u32x4){0u, 0u, 0u, tid == 2 ? 1u : 0u};
    bar_lgkm();
    __builtin_amdgcn_s_setprio(2);
    const unsigned ldt2 = log_dt2[cp];
    {
      const float ldt = hc ? hi_f(ldt2) : lo_f(ldt2);
      const float L2E = 1.4426950408889634f, L2E_LO = (float)(1.4426950408889634074 - (double)1.4426950408889634f);
      const float th = ldt * L2E;
      const float tl = fmaf(ldt, L2E, -th) + ldt * L2E_LO;
      const float e0 = __builtin_amdgcn_exp2f(th);
      const float dt = rbf(fmaf(e0 * 0.6931471805599453f, tl, e0));
      wait_vm<0>();
#pragma unroll
      for (int i = 0; i < 6; ++i) PIN(prm[i]);
      float av[2], bv[2];
      f32x2 wq[2];
#pragma unroll
      for (int m = 0; m < 2; ++m) {
        av[m] = dt * (m ? hi_f(prm[0]) : lo_f(prm[0]));
        bv[m] = dt * (m ? hi_f(prm[1]) : lo_f(prm[1]));
        const f32x2 db = {dt * (m ? hi_f(prm[2]) : lo_f(prm[2])), dt * (m ? hi_f(prm[3]) : lo_f(prm[3]))};
        const f32x2 cc = {m ? hi_f(prm[4]) : lo_f(prm[4]), m ? hi_f(prm[5]) : lo_f(prm[5])};
        wq[m] = cmul(cc, db);
      }
      const float amax = fmaxf(wave_max(fmaxf(av[0], av[1])), 0.f);
      const float g2 = amax * L2E;
      const float wm = wave_max(fmaxf(fmaxf(fabsf(wq[0].x), fabsf(wq[0].y)), fmaxf(fabsf(wq[1].x), fabsf(wq[1].y))));
      int we = __builtin_amdgcn_frexp_expf(wm);
      we = we < -100 ? -100 : we;
      const float wsc = __builtin_amdgcn_ldexpf(1.0f, -we);
      f32x4 kacc[3];
#pragma unroll
      for (int t = 0; t < 3; ++t) kacc[t] = (f32x4){0.f, 0.f, 0.f, 0.f};
      const int kgs = (m ? CV_U : KGS) + hc * 4096;
      LAS char* tw[4];
#pragma unroll
      for (int c = 0; c < 4; ++c) tw[c] = lds + kgs + lane * 64 + ((c ^ ((lane >> 1) & 3)) << 4);
      const LAS char* tr[2];
      {
        const int G = lane >> 4, q4 = (lane & 15) >> 2, p = lane & 3;
#pragma unroll
        for (int t = 0; t < 2; ++t)
          tr[t] = lds + kgs + (4 * G + 2 * t + (q4 >> 1)) * 64 + ((((q4 & 1) * 2 + (p >> 1)) ^ ((2 * G + t) & 3)) << 4) + (p & 1) * 8;
      }
      auto put16 = [&](const f32x2 (&e)[16]) __attribute__((always_inline)) {
#pragma unroll
        for (int h = 0; h < 2; ++h) {
          u32x4 re, im;
#pragma unroll
          for (int j = 0; j < 4; ++j) {
            re[j] = pk_f16(e[8 * h + 2 * j].x, e[8 * h + 2 * j + 1].x);
            im[j] = pk_f16(e[8 * h + 2 * j].y, e[8 * h + 2 * j + 1].y);
          }
          *(LAS u32x4*)tw[h] = re;
          *(LAS u32x4*)tw[2 + h] = im;
        }
      };
#define KG_FRAG(s) __builtin_bit_cast(f16x8, cat8(trrd(tr[0] + (s) * 1024), trrd(tr[1] + (s) * 1024)))
#define KG_RD() do { _Pragma("unroll") for (int s = 0; s < 4; ++s) pf[s] = KG_FRAG(s); SCHED_FENCE(); } while (0)
#define KG_MMA(t) do { SCHED_FENCE(); _Pragma("unroll") for (int s = 0; s < 4; ++s) \
        kacc[t] = __builtin_amdgcn_mfma_f32_16x16x32_f16(pf[s], qf[s], kacc[t], 0, 0, 0); } while (0)
      {
        const float a = (m ? av[1] : av[0]) - amax, b = m ? bv[1] : bv[0];
        f32x2 w = m ? wq[1] : wq[0];
        w.x *= wsc; w.y *= -wsc;
        f32x2 e[16];
        {
          const f32x2 z16 = cexp_l(a, b, 16.f);
          e[0] = (f32x2){1.f, 0.f}; e[1] = z16; e[2] = cmul(z16, z16); e[3] = cmul(e[2], z16);
          const f32x2 z64 = cmul(e[2], e[2]);
#pragma unroll
          for (int c = 4; c < 16; ++c) e[c] = cmul(e[c - 4], z64);
          put16(e);
        }
        f16x8 qf[4], pf[4];
#pragma unroll
        for (int s = 0; s < 4; ++s) qf[s] = KG_FRAG(s);
        SCHED_FENCE();
        const f32x2 z1 = cexp_l(a, -b, 1.f);
        const f32x2 z2 = cmul(z1, z1), z4 = cmul(z2, z2);
        e[0] = w; e[1] = cmul(w, z1); e[2] = cmul(w, z2); e[3] = cmul(e[1], z2);
#pragma unroll
        for (int c = 4; c < 16; ++c) e[c] = cmul(e[c - 4], z4);
        put16(e);
        KG_RD();
        const f32x2 z256 = cexp_l(a, -b, 256.f);
        const f32x2 z512 = cmul(z256, z256);
        const f32x2 w2 = cmul(w, z512);
        e[0] = cmul(w, z256); e[1] = cmul(e[0], z2); e[8] = w2; e[12] = cmul(e[0], z512);
        const f32x2 w3 = e[12];
#pragma unroll
        for (int i = 1; i < 4; ++i) {
          e[2 * i] = cmul(e[2 * i - 2], z4); e[2 * i + 1] = cmul(e[2 * i - 1], z4);
          e[8 + i] = cmul(e[7 + i], z4); e[12 + i] = cmul(e[11 + i], z4);
        }
        KG_MMA(0);
        put16(e);
        KG_RD();
        const f32x2 z8 = cmul(z4, z4);
        e[0] = cmul(w2, z512); e[2] = cmul(w3, z512); e[4] = cmul(e[0], z512); e[6] = cmul(e[2], z512);
        e[8] = cmul(e[4], z512);
#pragma unroll
        for (int i = 0; i < 4; ++i) e[2 * i + 1] = cmul(e[2 * i], z8);
#pragma unroll
        for (int i = 9; i < 16; ++i) e[i] = (f32x2){0.f, 0.f};
        KG_MMA(1);
        put16(e);
        KG_RD();
        KG_MMA(2);
      }
#pragma unroll
      for (int t = 0; t < 3; ++t) *(LAS f32x4*)(lds + (m ? KF + hc * CV_KSTR : KGS + hc * 4096) + t * 1024 + lane * 16) = kacc[t];
      if (m) *(LAS f32x2*)(lds + SY_KG + hc * 8) = (f32x2){g2, (float)we};
    }
    }
    f32x16 acc[7];
    f32x4 csa[2];
    unsigned tag;
    {
      const int G = lane >> 4;
      const LAS char* wa0 = lds + (G & 1) * W_UPOFF + (8 * (G >> 1) + ((lane & 15) >> 2)) * 224 + (lane & 3) * 8;
      const LAS char* wa[7];
#pragma unroll
      for (int T = 0; T < 7; ++T) wa[T] = wa0 + 32 * ((T + 2 * wv) % 7);
      const LAS char* xa[4];
      {
        const int row = 32 * wv + (lane & 31), sw = (row >> 1) & 7;
#pragma unroll
        for (int ks = 0; ks < 4; ++ks) xa[ks] = lds + XOFF + row * 128 + (((2 * ks + (lane >> 5)) ^ sw) << 4);
      }
      constexpr int PD = 3;
      bf16x8 Af[196], Bf[32];
      const unsigned o1 = ((lane & 15) == 4 * ((lane >> 4) & 1)) ? 0x3f803f80u : 0u;
      u32x4 onesq = {o1, o1, o1, o1};
      PIN(onesq);
      const bf16x8 ones = __builtin_bit_cast(bf16x8, onesq);
#define A_READ(i, T) __builtin_bit_cast(bf16x8, cat8(trrd(wa[T] + (((i) >> 2) % 3) * STG + (16 * ((i) & 3)) * 224), \
                                                      trrd(wa[T] + (((i) >> 2) % 3) * STG + (16 * ((i) & 3) + 4) * 224)))
#define B_READ(i) (*(const LAS bf16x8*)(xa[(i) & 3] + (((i) >> 2) % 3) * STG))
#pragma unroll
      for (int T = 0; T < 7; ++T) acc[T] = (f32x16){};
      csa[0] = (f32x4){0.f, 0.f, 0.f, 0.f}; csa[1] = csa[0];
      BARX(bar_lgkm);
      STAMP(1);
      tag = lds_get1(ldsb + SY_VAL);
      Bf[0] = B_READ(0);
#pragma unroll
      for (int T = 0; T < PD; ++T) Af[T] = A_READ(0, T);
      bf16x8 Al[7][4];
      u32x2 stg;
      float cs[7];
      static_for<0, 196>([&](auto nc) {
        constexpr int n = decltype(nc)::value, i = n / 7, T = n % 7;
        constexpr int m = n + PD, im = m / 7, Tm = m % 7;
        if constexpr (n == 70) ld64c(stg, (unsigned)((lane & 31) * 8), r_fl, FL_STAT + (unsigned)((mt * 128 + wv * 32) * 8));
        if constexpr (n == 27 * 7 + 2) {
          if (lane < 32) {
#pragma unroll
            for (int j = 0; j < 2; ++j) *(LAS float*)(lds + CSUM + ((j + 2 * wv) % 7) * 128 + lane * 4) = csa[j][0];
          }
        }
        if constexpr (Tm == 0 && (im & 3) == 0) BARX(bar_lgkm);
        if constexpr (m < 196) {
          if constexpr (Tm == 0) Bf[im] = B_READ(im);
          Af[m] = A_READ(im, Tm);
        } else if constexpr (m == 196) {
#pragma unroll
          for (int k = 28; k < 32; ++k) Bf[k] = B_READ(k);
        } else if constexpr (m == 197) {
#pragma unroll
          for (int t = 0; t < 7; ++t) cs[t] = *(const LAS float*)(lds + CSUM + ((t + 2 * wv) % 7) * 128 + (lane & 31) * 4);
        } else {
#pragma unroll
          for (int k = 0; k < 4; ++k) Al[0][k] = A_READ(28 + k, 0);
        }
        acc[T] = __builtin_amdgcn_mfma_f32_32x32x16_bf16(Af[n], Bf[i], acc[T], 0, 0, 0);
        if constexpr (T < 2)
          csa[T] = __builtin_amdgcn_mfma_f32_16x16x32_bf16(ones, Af[n], csa[T], 0, 0, 0);
        SCHED_FENCE();
      });
      STAMP(5);
      wait_vm<0>();
      PIN(stg);
      if (__builtin_expect(!wave_all(stg[1] == tag), 0)) {
#pragma unroll 1
        for (int it = 0; it < (1 << 18); ++it) {
          ld64c(stg, (unsigned)((lane & 31) * 8), r_fl, FL_STAT + (unsigned)((mt * 128 + wv * 32) * 8));
          wait_vm<0>();
          PIN(stg);
          if (wave_all(stg[1] == tag)) break;
          __builtin_amdgcn_s_sleep(2);
        }
      }
      const unsigned stw = stg[0];
      const float mb = lo_f(stw), sd = hi_f(stw);
#pragma unroll
      for (int k = 28; k < 32; ++k) {
        u32x4 v = __builtin_bit_cast(u32x4, Bf[k]);
#pragma unroll
        for (int j = 0; j < 4; ++j) v[j] = pk_bf16(lo_f(v[j]) - mb, hi_f(v[j]) - mb);
        Bf[k] = __builtin_bit_cast(bf16x8, v);
      }
      const unsigned nm = (stw & 0xffffu) ^ 0x8000u;
      u32x4 bq = {nm | (nm << 16), nm, 0u, 0u};
      if (lane >= 32) bq = (u32x4){0u, 0u, 0u, 0u};
      const float ga = -1.4426950408889634f * __builtin_amdgcn_rcpf(sd);
      const unsigned hsb = (unsigned)(((mt * 4 + wv) * 112 + ft * 7) * 1024);
      STAMP(6);
      static_for<0, 8>([&](auto tc) {
        constexpr int T = decltype(tc)::value;
        u32x4 o;
        if constexpr (T < 7) {
          const float c = cs[T];
          const float ch = rbf(c), cm = rbf(c - ch), cl = (c - ch) - cm;
          const u32x4 aq = {pk_bf16(ch, cm), pk_bf16(cl, 0.f), 0u, 0u};
          acc[T] = __builtin_amdgcn_mfma_f32_32x32x16_bf16(__builtin_bit_cast(bf16x8, aq), __builtin_bit_cast(bf16x8, bq), acc[T], 0, 0, 0);
          SCHED_FENCE();
        }
#pragma unroll
        for (int j = 0; j < 4; ++j) {
          if constexpr (T + 1 < 7) Al[T + 1][j] = A_READ(28 + j, T + 1);
          if constexpr (T < 7) acc[T] = __builtin_amdgcn_mfma_f32_32x32x16_bf16(Al[T][j], Bf[28 + j], acc[T], 0, 0, 0);
          SCHED_FENCE();
          if constexpr (T >= 1) {
            const float s0 = __builtin_amdgcn_rcpf(fmaf(__builtin_amdgcn_exp2f(acc[T - 1][2 * j] * ga), sd, sd));
            const float s1 = __builtin_amdgcn_rcpf(fmaf(__builtin_amdgcn_exp2f(acc[T - 1][2 * j + 1] * ga), sd, sd));
            o[j] = pk_bf16(s0 * acc[T - 1][8 + 2 * j], s1 * acc[T - 1][8 + 2 * j + 1]);
            PIN(o[j]);
          }
          SCHED_FENCE();
        }
        if constexpr (T >= 1) st128c(o, lane * 16u, r_h, hsb + (unsigned)((T - 1 + 2 * wv) % 7) * 1024u);
      });
    }
    STAMP(7);
    const unsigned ldw = ldsb + SY_LD;
    const LAS char* waT[2];
    {
      const int G = lane >> 4, q4 = (lane & 15) >> 2;
      const LAS char* wa0 = lds + wv * 2048 + (4 * (G >> 1) + q4) * 128 + (G & 1) * 32 + (lane & 3) * 8;
      waT[0] = wa0 + ((q4 >> 1) * 64); waT[1] = wa0 + (((q4 >> 1) ^ 1) * 64);
    }
#define D_READ(i, T) __builtin_bit_cast(bf16x8, cat8(trrd(waT[T] + g2_base(i)), trrd(waT[T] + g2_base(i) + 1024)))
    bf16x8 Wr[28][2];
    lds_wait<1>(ldw, 2u);
    static_for<0, 5>([&](auto ic) { constexpr int i = decltype(ic)::value; Wr[i][0] = D_READ(i, 0); Wr[i][1] = D_READ(i, 1); });
    static_for<5, 10>([&](auto ic) { constexpr int i = decltype(ic)::value; Wr[i][0] = D_READ(i, 0); Wr[i][1] = D_READ(i, 1); });
    wait_vm<0>();
    if (lane < 8) set_flag(ws, FL_H + (unsigned)(((mt * 2 + (wv >> 1)) * 8 + lane) * 256 + (ft * 2 + (wv & 1)) * 4), tag);
    STAMP(8);
    lds_setv(ldsb + SY_MC + wv * 4, 2u);
    if (wv == 0) {
      lds_wait<4>(ldsb + SY_UR, 1u);
      conv_upper<14, 16>(lds, lane, 0, cp, dsk2p[cp], r_s);
    }
    lds_wait<1>(ldw, 3u);
    static_for<10, G2_NR>([&](auto ic) { constexpr int i = decltype(ic)::value; Wr[i][0] = D_READ(i, 0); Wr[i][1] = D_READ(i, 1); });
    lds_setv(ldsb + SY_MC + wv * 4, 3u);
    STAMP(9);
    __builtin_amdgcn_s_setprio(0);
    poll_flags<2>((lane & 1) * 16u, r_fl, FL_H + (unsigned)((tt * 8 + dtile) * 256 + wv * 32), tag);
    if (wv == 0) publish<4>(lds, 2, ws, FL_S + (unsigned)((lane * 8 + (cp >> 5)) * 256 + (cp & 31) * 4), 32, tag, lane);
    __builtin_amdgcn_s_setprio(2);
    STAMP(10);
    u32x4 hb[28][2];
#define HB_LOAD(i) do { ld128c(hb[i][0], lane * 16u, r_h, ((unsigned)(tt * 2) * 112u + G2_KS(wv, i)) * 1024u); \
                        ld128c(hb[i][1], lane * 16u, r_h, ((unsigned)(tt * 2 + 1) * 112u + G2_KS(wv, i)) * 1024u); } while (0)
#pragma unroll
    for (int i = 0; i < G2_H0; ++i) HB_LOAD(i);
    f32x16 acc2[2][2];
    {
#pragma unroll
      for (int a = 0; a < 2; ++a)
#pragma unroll
        for (int b = 0; b < 2; ++b) acc2[a][b] = (f32x16){};
      static_for<0, 28>([&](auto ic) {
        constexpr int i = decltype(ic)::value;
        if constexpr (i == 14) STAMP(11);
        if constexpr (i + 2 == 13) lds_wait<1>(ldw, 6u);
        if constexpr (i + 2 >= G2_NR && i + 2 < 28) { Wr[i + 2][0] = D_READ(i + 2, 0); Wr[i + 2][1] = D_READ(i + 2, 1); }
        wait_vm<2 * (g2_issued(i) - i - 1)>();
        PIN(hb[i][0]); PIN(hb[i][1]);
#pragma unroll
        for (int T = 0; T < 2; ++T)
#pragma unroll
          for (int b = 0; b < 2; ++b)
            acc2[T][b] = __builtin_amdgcn_mfma_f32_32x32x16_bf16(Wr[i][T], __builtin_bit_cast(bf16x8, hb[i][b]), acc2[T][b], 0, 0, 0);
        SCHED_FENCE();
        static_for<g2_issued(i), g2_issued(i + 1)>([&](auto jc) { constexpr int j = decltype(jc)::value; HB_LOAD(j); });
      });
    }
    STAMP(12);
    {
      int l2 = lane; PIN(l2);
      const int tl = l2 & 31, x7 = (tl & 7) ^ (l2 >> 5);
#pragma unroll
      for (int b = 0; b < 2; ++b) {
        const int c8 = 4 * b + (tl >> 3);
        const int o4 = c8 < 5 ? RED_A + c8 * 8192 : RED_B + (c8 - 5) * 8192;
        LAS char* rp = lds + o4 + wv * 2048 + (tl & 7) * 256;
#pragma unroll
        for (int T = 0; T < 2; ++T)
#pragma unroll
          for (int g = 0; g < 4; ++g) {
            const f32x4 v = {acc2[T][b][4 * g], acc2[T][b][4 * g + 1], acc2[T][b][4 * g + 2], acc2[T][b][4 * g + 3]};
            *(LAS f32x4*)(rp + T * 128 + (((2 * g) ^ x7) << 4)) = v;
          }
      }
    }
  } else if (w8 == 4) {
    __builtin_amdgcn_s_setprio(3);
    const rsrc_t r_wg = mk_rsrc(wg, DM * FF * 2), r_wu = mk_rsrc(wu, DM * FF * 2);
    const rsrc_t r_wd = mk_rsrc(wd, FF * DM * 2);
    unsigned woff[7], xoff[4], wv4[4];
#pragma unroll
    for (int i = 0; i < 4; ++i) {
      xoff[i] = (unsigned)((lane >> 3) * 1024 + (((lane & 7) ^ ((lane >> 4) + 4 * (i & 1))) * 16) + i * 7168);
      wv4[i] = (unsigned)((lane >> 3) * 1024 + (((lane & 7) ^ (((lane >> 4) & 1) << 2)) * 16) + (i >> 1) * 458752 + (i & 1) * 8192 - i * 1024);
    }
#pragma unroll
    for (int i = 0; i < 7; ++i) {
      const int c = i * 64 + lane, row = c / 14, col = c - row * 14;
      woff[i] = (unsigned)(row * (FF * 2) + col * 16 - (i & 3) * 1024);
    }
    const unsigned ldw = ldsb + SY_LD;
    auto g1_stage = [&](int s) __attribute__((always_inline)) {
      const unsigned st = (unsigned)((s + rot) & 7), lb = ldsb + (unsigned)((s % 3) * STG);
#pragma unroll
      for (int i = 0; i < 4; ++i)
        dma16x4(lb + XOFF + i * 4096, xoff[0], xoff[1], xoff[2], xoff[3], r_x, (unsigned)((mt * 128 + i * 32) * 1024) + st * 128u);
#pragma unroll
      for (int h = 0; h < 2; ++h) {
        const unsigned wso = (unsigned)(ft * 224) + (st * 64u + h * 32u) * (FF * 2);
        dma16x4(lb + h * 7168, woff[0], woff[1], woff[2], woff[3], r_wg, wso);
        dma16x3(lb + h * 7168 + 4096, woff[4], woff[5], woff[6], r_wg, wso);
        dma16x4(lb + W_UPOFF + h * 7168, woff[0], woff[1], woff[2], woff[3], r_wu, wso);
        dma16x3(lb + W_UPOFF + h * 7168 + 4096, woff[4], woff[5], woff[6], r_wu, wso);
      }
    };
#define G2_GROUP(g) G2_STEPS(5 * (g), cmin(5 * (g) + 5, 28))
#define G2_STEPS(a_, b_) static_for<a_, b_>([&](auto ic) { constexpr int i = decltype(ic)::value; \
        const unsigned so_ = G2_KS(0, i) * 16384u + (unsigned)(dtile * 128); \
        dma16x4(ldsb + g2_base(i), wv4[0], wv4[1], wv4[2], wv4[3], r_wd, so_); \
        dma16x4(ldsb + g2_base(i) + 4096, wv4[0], wv4[1], wv4[2], wv4[3], r_wd, so_ + 2u * 458752u); })
    unsigned tch = 0;
    bar_only();
    __builtin_amdgcn_s_sleep(10);
    g1_stage(0);
    g1_stage(1);
    wait_vm<44>();
    STAMP(1);
    BARX(bar_only);
#pragma unroll
    for (int k = 0; k < 6; ++k) {
      g1_stage(k + 2);
      wait_vm<44>();
      BARX(bar_only);
    }
    STAMP(2);
    G2_GROUP(0);
    wait_vm<40>();
    BARX(bar_only);
    G2_GROUP(1);
    wait_vm<0>();
    asm volatile("" :: "v"(tch));
    lds_setv(ldw, 2u);
    STAMP(3);
    lds_wait<4>(ldsb + SY_MC, 2u);
    G2_STEPS(10, 15);
    wait_vm<16>();
    lds_setv(ldw, 3u);
    STAMP(4);
    G2_STEPS(15, 25);
    lds_wait<4>(ldsb + SY_MC, 3u);
    G2_GROUP(5);
    wait_vm<0>();
    lds_setv(ldw, 6u);
    STAMP(5);
    __builtin_amdgcn_s_setprio(0);
    EPI_X();
    lds_wait<1>(ldsb + SY_SR, 1u);
    EPI_S();
    STAMP(12);
  } else if (w8 == 5) {
    bar_only();
    wait_vm<0>();
    asm volatile("" :: "v"(pf0), "v"(pf1), "v"(pf2), "v"(pf3));
    BARX(bar_only);
    const unsigned tag = lds_get1(ldsb + SY_VAL);
    BARX(bar_only);
    {
      const int c = lane & 15, g = lane >> 4;
      unsigned d[2][3][4];
      float kscale[2];
#pragma unroll
      for (int hc = 0; hc < 2; ++hc) {
        f32x4 kacc[3];
#pragma unroll
        for (int t = 0; t < 3; ++t)
          kacc[t] = *(const LAS f32x4*)(lds + KF + hc * CV_KSTR + t * 1024 + lane * 16) + *(const LAS f32x4*)(lds + KGS + hc * 4096 + t * 1024 + lane * 16);
        const f32x2 gw = *(const LAS f32x2*)(lds + SY_KG + hc * 8);
        const float g2 = gw.x;
        const int we = (int)gw.y;
        if (g2 > 0.f) {
#pragma unroll
          for (int i = 0; i < 4; ++i) {
            const int l0 = 16 * c + 4 * g + i;
            const int l1 = g < 2 ? 256 + 16 * c + 8 * g + 2 * i : 512 + 256 * (g - 2) + 16 * c + 4 * i;
            const int l2 = g < 2 ? 1024 + 256 * (2 * g + (i >> 1)) + 8 * (i & 1) + 16 * c : 2048;
            kacc[0][i] *= __builtin_amdgcn_exp2f(g2 * (float)(l0 - 2048));
            kacc[1][i] *= __builtin_amdgcn_exp2f(g2 * (float)(l1 - 2048));
            kacc[2][i] *= __builtin_amdgcn_exp2f(g2 * (float)(l2 - 2048));
          }
        }
        kscale[hc] = __builtin_amdgcn_ldexpf(1.0f, we - 7) * __builtin_amdgcn_exp2f(g2 * 2048.f);
#pragma unroll
        for (int t = 0; t < 3; ++t)
#pragma unroll
          for (int i = 0; i < 4; ++i) { d[hc][t][i] = pk_f16(kacc[t][i] * 128.f, kacc[t][i] * 128.f); PIN(d[hc][t][i]); }
        BARX(bar_only);
      }
      BARX(bar_only);
      BARX(bar_only);
      BARX(bar_only);
      BARX(bar_only);
      __builtin_amdgcn_s_sleep(WSL);
      __builtin_amdgcn_s_setprio(3);
      STAMP(2);
      *(LAS f32x2*)(lds + SY_VAL + 8) = (f32x2){kscale[0], kscale[1]};
#define MIX(a, b) (((a) & 0xffffu) | ((b) & 0xffff0000u))
#pragma unroll
      for (int hc = 0; hc < 2; ++hc) {
        LAS char* kf = lds + KF + hc * CV_KSTR + 96;
        if (g < 2) {
#pragma unroll
          for (int h = 0; h < 2; ++h) {
            LAS char* p = kf + 2 * (1024 + 256 * (2 * g + h) + 16 * c);
            const unsigned v0 = d[hc][2][2 * h], v1 = d[hc][2][2 * h + 1];
            *(LAS u32x2*)(p - 8) = (u32x2){v0, v0};
            *(LAS u32x4*)(p) = (u32x4){v0, v0, MIX(v0, v1), v1};
            *(LAS u32x2*)(p + 16) = (u32x2){v1, v1};
          }
        } else {
          if (lane == 32) *(LAS u32x2*)(kf + 2 * 2044) = (u32x2){d[hc][2][0], d[hc][2][0]};
        }
        if (g >= 2) {
          LAS char* p = kf + 2 * (512 + 256 * (g - 2) + 16 * c);
          const unsigned v0 = d[hc][1][0], v1 = d[hc][1][1], v2 = d[hc][1][2], v3 = d[hc][1][3];
          *(LAS unsigned*)(p - 4) = v0;
          *(LAS u32x4*)(p) = (u32x4){v0, MIX(v0, v1), v1, v2};
          *(LAS u32x2*)(p + 16) = (u32x2){v2, MIX(v2, v3)};
          *(LAS unsigned*)(p + 24) = v3;
        }
        if (g < 2) {
          LAS char* p = kf + 2 * (256 + 16 * c + 8 * g);
          const unsigned v0 = d[hc][1][0], v1 = d[hc][1][1], v2 = d[hc][1][2], v3 = d[hc][1][3];
          *(LAS unsigned short*)(p - 2) = (unsigned short)v0;
          *(LAS u32x2*)(p) = (u32x2){v0, MIX(v1, v2)};
          *(LAS unsigned*)(p + 8) = v2;
          *(LAS unsigned short*)(p + 12) = (unsigned short)v3;
        }
        *(LAS u32x2*)(kf + 2 * (16 * c + 4 * g)) = (u32x2){MIX(d[hc][0][0], d[hc][0][1]), MIX(d[hc][0][2], d[hc][0][3])};
        if (lane < 6) *(LAS u32x4*)(kf - 96 + lane * 16) = (u32x4){0u, 0u, 0u, 0u};
      }
      lds_setv(ldsb + SY_UR + 8, 1u);
      STAMP(3);
    }
    __builtin_amdgcn_s_setprio(1);
    lds_wait<4>(ldsb + SY_UR, 1u);
    conv_upper<CV_NP, 16>(lds, lane, 1, cp, dsk2p[cp], r_s);
    conv_upper<CV_NP, 14>(lds, lane, 0, cp, dsk2p[cp], r_s);
    STAMP(4);
    publish<4>(lds, 2, ws, FL_S + (unsigned)((lane * 8 + (cp >> 5)) * 256 + (cp & 31) * 4), 32, tag, lane);
    STAMP(5);
    EPI_X();
    lds_wait<1>(ldsb + SY_CNT + 8, 4u);
    poll_flags<4>((lane & 7) * 16u, r_fl, FL_S + (unsigned)((tt * 8 + dtile) * 256), tag);
    lds_setv(ldsb + SY_SR, 1u);
    STAMP(6);
    EPI_S();
    STAMP(12);
  } else {
    const int hc = wv - 2;
    const int dch = cp * 2 + hc;
    const rsrc_t r_xn = mk_rsrc(ws + WS_XNF, SL * DM * 2);
    u32x4 xr[4];
    unsigned ep;
    const int row0 = bid * 8 + hc * 4;
    {
      const unsigned vo = (unsigned)((lane >> 4) * 1024 + (lane & 15) * 16);
#pragma unroll
      for (int i = 0; i < 4; ++i) ld128(xr[i], vo, r_x, (unsigned)(row0 * 1024 + i * 256));
      ld32c(ep, 0u * lane, r_fl, FL_EP);
    }
    bar_only();
    const unsigned dsk2 = dsk2p[cp];
    wait_vm<0>();
    STAMP(1);
#pragma unroll
    for (int i = 0; i < 4; ++i) PIN(xr[i]);
    PIN(ep);
    const unsigned tag = (unsigned)__builtin_amdgcn_readfirstlane(ep) + 1u;
    lds_setv(ldsb + SY_VAL, tag);
    {
      float f[32];
#pragma unroll
      for (int j = 0; j < 16; ++j) { f[2 * j] = lo_f(xr[j >> 2][j & 3]); f[2 * j + 1] = hi_f(xr[j >> 2][j & 3]); }
      float s0 = 0.f, s1 = 0.f;
#pragma unroll
      for (int j = 0; j < 32; j += 4) { s0 += f[j] + f[j + 1]; s1 += f[j + 2] + f[j + 3]; }
      const float mean = row_sum(s0 + s1) * (1.0f / DM);
      float q0 = 0.f, q1 = 0.f;
#pragma unroll
      for (int j = 0; j < 32; j += 2) {
        const float da = f[j] - mean, db = f[j + 1] - mean;
        q0 = fmaf(da, da, q0); q1 = fmaf(db, db, q1);
      }
      const float var = row_sum(q0 + q1) * (1.0f / DM);
      const float mb = rbf(mean);
      const float sb = rbf(__builtin_amdgcn_sqrtf(rbf(rbf(var) + 1e-5f)));
      if ((lane & 15) == 0)
        st64c((u32x2){pk_bf16(mb, sb), tag}, (unsigned)((lane >> 4) * 8), r_fl, FL_STAT + (unsigned)(row0 * 8));
      const float rs = __builtin_amdgcn_rcpf(sb);
      const unsigned vo = (unsigned)((lane & 15) * 32768 + (lane >> 4) * 16);
      STAMP(2);
#pragma unroll
      for (int i = 0; i < 4; ++i) {
        u32x4 o;
#pragma unroll
        for (int j = 0; j < 4; ++j) {
          const unsigned d = pk_bf16(f[8 * i + 2 * j] - mb, f[8 * i + 2 * j + 1] - mb);
          o[j] = pk_bf16(lo_f(d) * rs, hi_f(d) * rs);
        }
        st128c(o, vo, r_xn, (unsigned)(i * 16 * 32768 + row0 * 16));
      }
    }
    BARX(bar_lgkm);
    wait_vm<0>();
    BARX(bar_only);
    if (hc == 0 && lane < 8) set_flag(ws, FL_ALL + (unsigned)(lane * 4096 + bid * 4), tag);
    BARX(bar_only);
    BARX(bar_only);
    u32x4 vf;
    ld128c(vf, lane * 16u, r_fl, FL_ALL + (unsigned)(xcd * 4096));
    BARX(bar_only);
    wait_vm<0>();
    PIN(vf);
    const bool all_ok = wave_all((vf[0] == tag) & (vf[1] == tag) & (vf[2] == tag) & (vf[3] == tag));
    unsigned uw[16];
    const unsigned usb = (unsigned)((cp >> 2) * 32768 + (cp & 3) * 4 + hc * 16384);
    if (!all_ok) {
      for (int r = 5; r <= 7; ++r) BARX(bar_only);
      poll_flags<4>(lane * 16u, r_fl, FL_ALL + (unsigned)(xcd * 4096), tag);
    }
#pragma unroll
    for (int m = 0; m < 16; ++m) ld32c(uw[m], lane * 16u, r_xn, usb + (unsigned)m * 1024u);
    if (lane == 0 && bid == 0 && hc == 0) set_flag(ws, FL_EP, tag);
    if (all_ok) { BARX(bar_only); BARX(bar_only); }
    wait_vm<0>();
    {
      const bool od = lane & 1;
      LAS char* up = lds + CV_U + 160 + hc * 2048 + (od ? 128 : 0) + (lane >> 1) * 4;
#pragma unroll
      for (int m = 0; m < 16; m += 2) {
        PIN(uw[m]); PIN(uw[m + 1]);
        const unsigned a = pk_f16(lo_f(uw[m]), hi_f(uw[m])), b = pk_f16(lo_f(uw[m + 1]), hi_f(uw[m + 1]));
        const unsigned y = (unsigned)__builtin_amdgcn_update_dpp(0, (int)(od ? a : b), 0xB1, 0xf, 0xf, true);
        const unsigned f0 = od ? y : a, f1 = od ? b : y;
        uw[m] = __builtin_amdgcn_perm(f1, f0, 0x05040100u);
        uw[m + 1] = __builtin_amdgcn_perm(f1, f0, 0x07060302u);
        PIN(uw[m]); PIN(uw[m + 1]);
      }
      if (all_ok) BARX(bar_only);
      __builtin_amdgcn_s_sleep(WSL);
      __builtin_amdgcn_s_setprio(3);
      if (lane < 10) *(LAS u32x4*)(lds + CV_U + hc * CV_USTR + lane * 16) = (u32x4){0u, 0u, 0u, 0u};
#pragma unroll
      for (int m = 0; m < 16; m += 2) {
        *(LAS unsigned*)(up + m * 128) = uw[m];
        *(LAS unsigned*)(up + CV_USTR + m * 128) = uw[m + 1];
      }
      lds_setv(ldsb + SY_UR + hc * 4, 1u);
    }
    STAMP(3);
    __builtin_amdgcn_s_setprio(1);
    lds_wait<4>(ldsb + SY_UR, 1u);
    const float kscale = *(const LAS float*)(lds + SY_VAL + 8 + hc * 4);
    STAMP(6);
    {
      const float dskip = hc ? hi_f(dsk2) : lo_f(dsk2);
      const int G = lane >> 4, q4 = (lane & 15) >> 2, p = lane & 3;
      const LAS char* pa = lds + KF + hc * CV_KSTR + 96 + 2 * (76 + 16 * p - 32 * G - 4 * q4 - 16);
      const LAS char* pb = lds + CV_U + hc * CV_USTR + 160 + 2 * (-76 + 32 * G + 4 * q4 + 4 * p);
      const LAS char* pu = lds + CV_U + hc * CV_USTR + 160 + lane * 2;
      f16x8 A[16], B[32];
#define CV_A(q) __builtin_bit_cast(f16x8, cat8(trrd(pa + 256 * (q) + 32), trrd(pa + 256 * (q))))
#define CV_B(w) __builtin_bit_cast(f16x8, cat8(trrd(pb + 128 * (w)), trrd(pb + 128 * (w) + 32)))
      A[0] = CV_A(0); B[0] = CV_B(0); B[1] = CV_B(1);
      A[1] = CV_A(1); B[2] = CV_B(2); B[3] = CV_B(3);
      f32x4 ca[2][2];
      _Float16 us[2][2];
      unsigned so16[16];
      float c3 = 0.f, c2 = 0.f, c1 = 0.f;
      auto tile_y = [&](const f32x4 a) __attribute__((always_inline)) {
        const float h3 = a[3];
        const float h2 = a[2] + shr_carry(c3, h3);
        const float h1 = a[1] + shr_carry(c2, h2);
        const float y = a[0] + shr_carry(c1, h1);
        c3 = dpp_mov<0x13C>(h3); c2 = dpp_mov<0x13C>(h2); c1 = dpp_mov<0x13C>(h1);
        return y * kscale;
      };
      static_for<0, CV_NP + 1>([&](auto jc) {
        constexpr int jj = decltype(jc)::value;
        if constexpr (jj + 2 < CV_NP) { A[jj + 2] = CV_A(jj + 2); B[2 * jj + 4] = CV_B(2 * jj + 4); B[2 * jj + 5] = CV_B(2 * jj + 5); }
        if constexpr (jj < CV_NP) {
          us[jj & 1][0] = *(const LAS _Float16*)(pu + jj * 256); us[jj & 1][1] = *(const LAS _Float16*)(pu + jj * 256 + 128);
          f32x4 a0 = {0.f, 0.f, 0.f, 0.f}, a1 = {0.f, 0.f, 0.f, 0.f};
#pragma unroll
          for (int q = jj; q >= 0; --q) {
            a0 = __builtin_amdgcn_mfma_f32_16x16x32_f16(A[q], B[2 * (jj - q)], a0, 0, 0, 0);
            a1 = __builtin_amdgcn_mfma_f32_16x16x32_f16(A[q], B[2 * (jj - q) + 1], a1, 0, 0, 0);
          }
          ca[jj & 1][0] = a0; ca[jj & 1][1] = a1;
        }
        if constexpr (jj >= 1) {
          const float y0 = tile_y(ca[(jj - 1) & 1][0]), y1 = tile_y(ca[(jj - 1) & 1][1]);
          const unsigned yq = pk_bf16(y0, y1);
          const unsigned uq = pk_bf16((float)us[(jj - 1) & 1][0] * dskip, (float)us[(jj - 1) & 1][1] * dskip);
          so16[jj - 1] = pk_bf16(lo_f(yq) + lo_f(uq), hi_f(yq) + hi_f(uq));
        }
        if constexpr (jj >= 1 && jj < CV_NP) {
#pragma unroll
          for (int k = 0; k < 2 * (jj + 1); ++k) {
            __builtin_amdgcn_sched_group_barrier(0x8, 1, 0);
            __builtin_amdgcn_sched_group_barrier(0x100, 1, 0);
            __builtin_amdgcn_sched_group_barrier(0x2, jj < 7 ? 4 : 2, 0);
          }
        }
        SCHED_FENCE();
        if constexpr (jj >= 1) st32c(so16[jj - 1], lane * 4u, r_s, (unsigned)(dch * 4096 + (jj - 1) * 256));
      });
    }
    STAMP(7);
    publish<4>(lds, 2, ws, FL_S + (unsigned)((lane * 8 + (cp >> 5)) * 256 + (cp & 31) * 4), 32, tag, lane);
    STAMP(9);
    EPI_X();
    lds_wait<1>(ldsb + SY_SR, 1u);
    EPI_S();
    STAMP(12);
  }
  bar_lgkm();
  {
    int t2 = tid; PIN(t2);
    const u32x4 xv = *(const LAS u32x4*)(lds + EPX + t2 * 16);
    unsigned sv[8];
#pragma unroll
    for (int j = 0; j < 8; ++j) sv[j] = *(const LAS unsigned*)(lds + EPS + ((t2 & 7) * 8 + j) * 256 + (((t2 >> 5) + (t2 & 7)) & 15) * 16 + ((t2 >> 3) & 3) * 4);
    const int rb = w8 < 5 ? RED_A + w8 * 8192 : RED_B + (w8 - 5) * 8192;
    const int tr7 = (t2 >> 3) & 7;
    const LAS char* rp = lds + rb + tr7 * 256 + ((t2 >> 2) & 1) * 128;
    const bool odd = tt & 1;
    u32x4 o;
#pragma unroll
    for (int i = 0; i < 2; ++i) {
      const int ch = (((t2 & 3) * 2 + i) ^ tr7) << 4;
      f32x4 v = *(const LAS f32x4*)(rp + ch);
#pragma unroll
      for (int w = 1; w < 4; ++w) v += *(const LAS f32x4*)(rp + w * 2048 + ch);
#pragma unroll
      for (int jj = 0; jj < 2; ++jj) {
        const unsigned fp = pk_bf16(v[2 * jj], v[2 * jj + 1]);
        const float f0 = lo_f(fp), f1 = hi_f(fp);
        const float g0 = f0 * __builtin_amdgcn_rcpf(1.0f + __builtin_amdgcn_exp2f(f0 * -1.4426950408889634f));
        const float g1 = f1 * __builtin_amdgcn_rcpf(1.0f + __builtin_amdgcn_exp2f(f1 * -1.4426950408889634f));
        const unsigned gp = pk_bf16(g0, g1);
        const int j = 4 * i + 2 * jj;
        const float s0 = odd ? hi_f(sv[j]) : lo_f(sv[j]), s1 = odd ? hi_f(sv[j + 1]) : lo_f(sv[j + 1]);
        const unsigned sg = pk_bf16(s0 * lo_f(gp), s1 * hi_f(gp));
        const unsigned xw = xv[2 * i + jj];
        o[2 * i + jj] = pk_bf16(lo_f(xw) + lo_f(sg), hi_f(xw) + hi_f(sg));
      }
    }
    *(u32x4*)((char*)out + XO(t2)) = o;
  }
  STAMP(13); STAMP_RT(15);
  STAMP_OUT(0);
  BW_OUT();
}

extern "C" void kernel_launch(void* const* d_in, const int* in_sizes, int n_in, void* d_out,
                              int out_size, void* d_ws, size_t ws_size, hipStream_t stream) {
  (void)hipFuncSetAttribute((const void*)fused_kernel, hipFuncAttributeMaxDynamicSharedMemorySize, LDS_BYTES);
  fused_kernel<<<256, 512, LDS_BYTES, stream>>>(
      (const unsigned short*)d_in[0], (const unsigned short*)d_in[1], (const unsigned short*)d_in[2],
      (const unsigned short*)d_in[3], (const unsigned short*)d_in[4], (const unsigned short*)d_in[5],
      (const unsigned short*)d_in[6], (const unsigned*)d_in[7], (const unsigned*)d_in[8],
      (const unsigned short*)d_in[10], (const unsigned short*)d_in[11], (const unsigned short*)d_in[12],
      (char*)d_ws, (unsigned short*)d_out);
}
```

```cpp
#include <hip/hip_runtime.h>
#include <hip/hip_bf16.h>
#include <stdint.h>
#include <type_traits>

typedef __attribute__((ext_vector_type(4))) short s16x4;
typedef __attribute__((ext_vector_type(8))) short s16x8;
typedef __attribute__((ext_vector_type(8))) _Float16 f16x8;
typedef __attribute__((ext_vector_type(8))) __bf16 bf16x8;
typedef __attribute__((ext_vector_type(2))) float f32x2;
typedef __attribute__((ext_vector_type(4))) float f32x4;
typedef __attribute__((ext_vector_type(16))) float f32x16;
typedef __attribute__((ext_vector_type(4))) unsigned u32x4;
typedef __attribute__((ext_vector_type(3))) unsigned u32x3;
typedef __attribute__((ext_vector_type(2))) unsigned u32x2;
typedef __attribute__((ext_vector_type(2))) __bf16 bf16x2;
typedef __attribute__((ext_vector_type(2))) _Float16 f16x2;
#define LAS __attribute__((address_space(3)))
#define GAS __attribute__((address_space(1)))
typedef __amdgpu_buffer_rsrc_t rsrc_t;

constexpr int SL = 2048;
constexpr int DM = 512;
constexpr int NM = 128;
constexpr int FF = 1792;

constexpr size_t WS_XNF = 0;
constexpr size_t WS_ST = 2 * (size_t)SL * DM * 2;
constexpr size_t WS_HF = WS_ST + (size_t)SL * DM * 2;
constexpr size_t WS_FLAG = 2 * (size_t)SL * DM;
constexpr unsigned FL_ALL = 0, FL_STAT = 65536, FL_H = 131072, FL_S = 196608, FL_EP = 262144, FL_BYTES = 262400;

template <int I, int N, class F>
__device__ __forceinline__ void static_for(F&& f) {
  if constexpr (I < N) { f(std::integral_constant<int, I>{}); static_for<I + 1, N>(f); }
}

__device__ __forceinline__ unsigned pk_bf16(float lo, float hi) {
  f32x2 v = {lo, hi};
  return __builtin_bit_cast(unsigned, __builtin_convertvector(v, bf16x2));
}
__device__ __forceinline__ unsigned pk_f16(float lo, float hi) {
  f32x2 v = {lo, hi};
  return __builtin_bit_cast(unsigned, __builtin_convertvector(v, f16x2));
}
__device__ __forceinline__ float lo_f(unsigned p) { return __uint_as_float(p << 16); }
__device__ __forceinline__ float hi_f(unsigned p) { return __uint_as_float(p & 0xffff0000u); }
__device__ __forceinline__ float rbf(float x) { return lo_f(pk_bf16(x, x)); }

template <int CTRL>
__device__ __forceinline__ float dpp_mov(float v) {
  return __int_as_float(__builtin_amdgcn_update_dpp(0, __float_as_int(v), CTRL, 0xf, 0xf, true));
}
__device__ __forceinline__ float shr_carry(float c, float v) {
  return __builtin_bit_cast(float, __builtin_amdgcn_update_dpp(__builtin_bit_cast(int, c), __builtin_bit_cast(int, v), 0x138, 0xf, 0xf, false));
}
__device__ __forceinline__ float rdlane(float v, int l) {
  return __int_as_float(__builtin_amdgcn_readlane(__float_as_int(v), l));
}
__device__ __forceinline__ float wave_sum(float v) {
  v += dpp_mov<0xB1>(v);
  v += dpp_mov<0x4E>(v);
  v += dpp_mov<0x141>(v);
  v += dpp_mov<0x140>(v);
  return (rdlane(v, 0) + rdlane(v, 16)) + (rdlane(v, 32) + rdlane(v, 48));
}
__device__ __forceinline__ float row_sum(float v) {
  v += dpp_mov<0xB1>(v);
  v += dpp_mov<0x4E>(v);
  v += dpp_mov<0x141>(v);
  v += dpp_mov<0x140>(v);
  return v;
}
__device__ __forceinline__ float half_sum(float v, int lane) {
  v += dpp_mov<0xB1>(v);
  v += dpp_mov<0x4E>(v);
  v += dpp_mov<0x141>(v);
  v += dpp_mov<0x140>(v);
  const float lo = rdlane(v, 0) + rdlane(v, 16), hi = rdlane(v, 32) + rdlane(v, 48);
  return lane < 32 ? lo : hi;
}
__device__ __forceinline__ float wave_max(float v) {
  v = fmaxf(v, dpp_mov<0xB1>(v));
  v = fmaxf(v, dpp_mov<0x4E>(v));
  v = fmaxf(v, dpp_mov<0x141>(v));
  v = fmaxf(v, dpp_mov<0x140>(v));
  return fmaxf(fmaxf(rdlane(v, 0), rdlane(v, 16)), fmaxf(rdlane(v, 32), rdlane(v, 48)));
}

__device__ __forceinline__ rsrc_t mk_rsrc(const void* p, unsigned bytes) {
  return __builtin_amdgcn_make_buffer_rsrc((void*)p, (short)0, (int)bytes, 0x00020000);
}
__device__ __forceinline__ void dma16(unsigned lds_dst, unsigned voff, rsrc_t r, unsigned soff) {
  asm volatile("s_mov_b32 m0, %0\n\ts_nop 0\n\tbuffer_load_dwordx4 %1, %2, %3 offen lds"
               :: "s"(lds_dst), "v"(voff), "s"(r), "s"(soff) : "memory");
}
__device__ __forceinline__ void dma16x4(unsigned lds_dst, unsigned v0, unsigned v1, unsigned v2, unsigned v3, rsrc_t r, unsigned soff) {
  asm volatile("s_mov_b32 m0, %0\n\ts_nop 0\n\t"
               "buffer_load_dwordx4 %1, %5, %6 offen lds\n\t"
               "buffer_load_dwordx4 %2, %5, %6 offen offset:1024 lds\n\t"
               "buffer_load_dwordx4 %3, %5, %6 offen offset:2048 lds\n\t"
               "buffer_load_dwordx4 %4, %5, %6 offen offset:3072 lds"
               :: "s"(lds_dst), "v"(v0), "v"(v1), "v"(v2), "v"(v3), "s"(r), "s"(soff) : "memory");
}
__device__ __forceinline__ void dma16x3(unsigned lds_dst, unsigned v0, unsigned v1, unsigned v2, rsrc_t r, unsigned soff) {
  asm volatile("s_mov_b32 m0, %0\n\ts_nop 0\n\t"
               "buffer_load_dwordx4 %1, %4, %5 offen lds\n\t"
               "buffer_load_dwordx4 %2, %4, %5 offen offset:1024 lds\n\t"
               "buffer_load_dwordx4 %3, %4, %5 offen offset:2048 lds"
               :: "s"(lds_dst), "v"(v0), "v"(v1), "v"(v2), "s"(r), "s"(soff) : "memory");
}
__device__ __forceinline__ void ld128(u32x4& dst, unsigned voff, rsrc_t r, unsigned soff) {
  asm volatile("buffer_load_dwordx4 %0, %1, %2, %3 offen" : "=v"(dst) : "v"(voff), "s"(r), "s"(soff) : "memory");
}
__device__ __forceinline__ void ld128c(u32x4& dst, unsigned voff, rsrc_t r, unsigned soff) {
  asm volatile("buffer_load_dwordx4 %0, %1, %2, %3 offen sc1" : "=v"(dst) : "v"(voff), "s"(r), "s"(soff) : "memory");
}
__device__ __forceinline__ void ld32(unsigned& dst, unsigned voff, rsrc_t r, unsigned soff) {
  asm volatile("buffer_load_dword %0, %1, %2, %3 offen" : "=v"(dst) : "v"(voff), "s"(r), "s"(soff) : "memory");
}
__device__ __forceinline__ void ld64c(u32x2& dst, unsigned voff, rsrc_t r, unsigned soff) {
  asm volatile("buffer_load_dwordx2 %0, %1, %2, %3 offen sc1" : "=v"(dst) : "v"(voff), "s"(r), "s"(soff) : "memory");
}
__device__ __forceinline__ void ld32c(unsigned& dst, unsigned voff, rsrc_t r, unsigned soff) {
  asm volatile("buffer_load_dword %0, %1, %2, %3 offen sc1" : "=v"(dst) : "v"(voff), "s"(r), "s"(soff) : "memory");
}
__device__ __forceinline__ void st32c(unsigned v, unsigned voff, rsrc_t r, unsigned soff) {
  asm volatile("buffer_store_dword %0, %1, %2, %3 offen sc1" :: "v"(v), "v"(voff), "s"(r), "s"(soff) : "memory");
}
__device__ __forceinline__ void st64c(u32x2 v, unsigned voff, rsrc_t r, unsigned soff) {
  asm volatile("buffer_store_dwordx2 %0, %1, %2, %3 offen sc1\n\ts_nop 1" :: "v"(v), "v"(voff), "s"(r), "s"(soff) : "memory");
}
__device__ __forceinline__ void ld16(unsigned& dst, unsigned voff, rsrc_t r, unsigned soff) {
  asm volatile("buffer_load_ushort %0, %1, %2, %3 offen" : "=v"(dst) : "v"(voff), "s"(r), "s"(soff) : "memory");
}
__device__ __forceinline__ void st128c(u32x4 v, unsigned voff, rsrc_t r, unsigned soff) {
  asm volatile("buffer_store_dwordx4 %0, %1, %2, %3 offen sc1\n\ts_nop 1" :: "v"(v), "v"(voff), "s"(r), "s"(soff) : "memory");
}
template <int N>
__device__ __forceinline__ void wait_vm() { asm volatile("s_waitcnt vmcnt(%0)" :: "n"(N > 63 ? 63 : N) : "memory"); }
__device__ __forceinline__ void bar_lgkm() { asm volatile("s_waitcnt lgkmcnt(0)\n\ts_barrier" ::: "memory"); }
__device__ __forceinline__ void bar_only() { asm volatile("s_barrier" ::: "memory"); }
#define BW_DECL
#define BARX(f) f()
#define BW_OUT()
#define PIN(x) asm volatile("" : "+v"(x))
#define SCHED_FENCE() __builtin_amdgcn_sched_barrier(0)

__device__ __forceinline__ s16x4 trrd(const LAS char* p) {
  return __builtin_amdgcn_ds_read_tr16_b64_v4i16((LAS s16x4*)p);
}
__device__ __forceinline__ s16x8 cat8(s16x4 a, s16x4 b) {
  return __builtin_shufflevector(a, b, 0, 1, 2, 3, 4, 5, 6, 7);
}

__device__ __forceinline__ bool wave_all(bool p) { return __builtin_amdgcn_ballot_w64(!p) == 0ull; }
template <int SLP = 2>
__device__ __forceinline__ void poll_flags(unsigned voff, rsrc_t r, unsigned soff, unsigned tag) {
#pragma unroll 1
  for (int it = 0; it < (1 << 18); ++it) {
    u32x4 v;
    asm volatile("buffer_load_dwordx4 %0, %1, %2, %3 offen sc1\n\ts_waitcnt vmcnt(0)"
                 : "=&v"(v) : "v"(voff), "s"(r), "s"(soff) : "memory");
    const bool ok = (v[0] == tag) & (v[1] == tag) & (v[2] == tag) & (v[3] == tag);
    if (wave_all(ok)) break;
    __builtin_amdgcn_s_sleep(SLP);
  }
}
__device__ __forceinline__ void set_flag(char* ws, unsigned off, unsigned tag) {
  __hip_atomic_store((GAS unsigned*)(ws + WS_FLAG + off), tag, __ATOMIC_RELAXED, __HIP_MEMORY_SCOPE_AGENT);
}

#define STAMP_DECL
#define STAMP(i)
#define STAMP_RT(i)
#define STAMP_OUT(kid)

__device__ __forceinline__ f32x2 cmul(f32x2 a, f32x2 b) {
  f32x2 r;
  r.x = fmaf(a.x, b.x, -(a.y * b.y));
  r.y = fmaf(a.x, b.y, a.y * b.x);
  return r;
}
__device__ __forceinline__ f32x2 cexp_l(float a, float b, float l) {
  const float mag = __builtin_amdgcn_exp2f(a * l * 1.4426950408889634f);
  const float x = b * l;
  const float C1 = 0.15915494309189535f;
  const float C2 = (float)(0.15915494309189533577 - (double)0.15915494309189535f);
  const float hi = x * C1;
  const float lo = fmaf(x, C1, -hi) + x * C2;
  const float r = (hi - __builtin_rintf(hi)) + lo;
  f32x2 z;
  z.x = mag * __builtin_amdgcn_cosf(r);
  z.y = mag * __builtin_amdgcn_sinf(r);
  return z;
}

constexpr int W_UPOFF = 14336 + 128;
constexpr int XOFF = W_UPOFF + 14336;
constexpr int STG = XOFF + 16384;
constexpr int KGS = 3 * STG;
constexpr int KF = KGS + 8192;
constexpr int CV_KSTR = (48 + 2048) * 2;
constexpr int CV_USTR = (80 + 2048) * 2;
constexpr int CV_U = KF + 2 * CV_KSTR;
constexpr int CSUM = CV_U + 2 * CV_USTR;
constexpr int STAT = CSUM + 896;
constexpr int SY = STAT + 512;
constexpr int SY_LD = SY, SY_MC = SY + 16, SY_ST = SY + 4, SY_HR = SY + 8, SY_SR = SY + 12, SY_UR = SY + 32  ,
              SY_CNT = SY + 48, SY_VAL = SY + 64, SY_KG = SY + 80, SY_END = SY + 96;
constexpr int LDS_BYTES = SY_END;
static_assert(LDS_BYTES <= 163840, "lds");
constexpr int G2_RED = 272;
constexpr int EPX = KGS;
constexpr int EPS = KF;
#define LDLY 24
#define WSL 4
constexpr unsigned WS_USED = (unsigned)(WS_HF + (size_t)SL * FF * 2);
constexpr unsigned CODE_BYTES = 49152;
constexpr int CV_NP = 12;
constexpr int G2_NR = 13;
constexpr int cmax(int a, int b) { return a > b ? a : b; }
constexpr int cmin(int a, int b) { return a < b ? a : b; }
constexpr int g2_base(int i) { return (((i % 15) / 5 + 2) % 3) * STG + (i % 5) * 8192; }
constexpr int RED_A = g2_base(15), RED_B = g2_base(20);
constexpr int G2_H0 = 6;
constexpr int g2_issued(int i) { return cmin(28, G2_H0 + (3 * i) / 2); }

__device__ __forceinline__ void lds_setv(unsigned addr, unsigned v) {
  asm volatile("ds_write_b32 %0, %1" :: "v"(addr), "v"(v) : "memory");
}
__device__ __forceinline__ unsigned lds_min4(unsigned addr) {
  u32x4 v;
  asm volatile("ds_read_b128 %0, %1\n\ts_waitcnt lgkmcnt(0)" : "=&v"(v) : "v"(addr) : "memory");
  return (unsigned)__builtin_amdgcn_readfirstlane(min(min(v[0], v[1]), min(v[2], v[3])));
}
__device__ __forceinline__ unsigned lds_get1(unsigned addr) {
  unsigned v;
  asm volatile("ds_read_b32 %0, %1\n\ts_waitcnt lgkmcnt(0)" : "=&v"(v) : "v"(addr) : "memory");
  return (unsigned)__builtin_amdgcn_readfirstlane(v);
}
template <int W>
__device__ __forceinline__ unsigned lds_val(unsigned addr) { return W == 4 ? lds_min4(addr) : lds_get1(addr); }
template <int W>
__device__ __forceinline__ void lds_spin(unsigned addr, unsigned need) {
#pragma unroll 1
  for (int it = 0; it < (1 << 20); ++it) {
    if (lds_val<W>(addr) >= need) break;
    __builtin_amdgcn_s_sleep(1);
  }
}
template <int W>
__device__ __forceinline__ void lds_wait(unsigned addr, unsigned need) {
  if (__builtin_expect(lds_val<W>(addr) < need, 0)) lds_spin<W>(addr, need);
}
template <int NW, bool DRAIN = true>
__device__ __forceinline__ void publish(LAS char* lds, int cnt, char* ws, unsigned off, int nl, unsigned tag, int lane) {
  if (DRAIN) wait_vm<0>();
  unsigned old = 0;
  if (lane == 0) old = __hip_atomic_fetch_add((LAS unsigned*)(lds + SY_CNT + cnt * 4), 1u, __ATOMIC_RELAXED, __HIP_MEMORY_SCOPE_WORKGROUP);
  if (__builtin_amdgcn_readfirstlane(old) == NW - 1 && lane < nl) set_flag(ws, off, tag);
}

template <int PA, int PB>
__device__ __forceinline__ void conv_upper(LAS char* lds, int lane, int hc, int cp, rsrc_t r_s) {
  const unsigned dsk2 = *(const LAS unsigned*)(lds + SY_VAL + 4);
  const float kscale = *(const LAS float*)(lds + SY_VAL + 8 + hc * 4);
  const float dskip = hc ? hi_f(dsk2) : lo_f(dsk2);
  const int G = lane >> 4, q4 = (lane & 15) >> 2, p = lane & 3;
  const LAS char* pa = lds + KF + hc * CV_KSTR + 96 + 2 * (76 + 16 * p - 32 * G - 4 * q4 - 16);
  const LAS char* pb = lds + CV_U + hc * CV_USTR + 160 + 2 * (-76 + 32 * G + 4 * q4 + 4 * p);
  const LAS char* pu = lds + CV_U + hc * CV_USTR + 160 + lane * 2;
  constexpr int J0 = 2 * PA - 1, J1 = 2 * PB, NT = J1 - J0;
  f16x8 A[16], B[32];
  f32x4 ca[NT];
#define CV_A(q) __builtin_bit_cast(f16x8, cat8(trrd(pa + 256 * (q) + 32), trrd(pa + 256 * (q))))
#define CV_B(w) __builtin_bit_cast(f16x8, cat8(trrd(pb + 128 * (w)), trrd(pb + 128 * (w) + 32)))
  B[0] = CV_B(0); B[1] = CV_B(1); B[2] = CV_B(2);
  constexpr int QA = (J0 - 1) / 2;
#pragma unroll
  for (int q = PB - 1; q >= QA; --q) A[q] = CV_A(q);
#pragma unroll
  for (int j = 0; j < NT; ++j) ca[j] = (f32x4){0.f, 0.f, 0.f, 0.f};
  static_for<0, J1>([&](auto wc) {
    constexpr int w = decltype(wc)::value;
    if constexpr (w + 3 < J1) B[w + 3] = CV_B(w + 3);
    if constexpr ((w & 1) == 0 && QA - 1 - w / 2 >= 0) A[QA - 1 - w / 2] = CV_A(QA - 1 - w / 2);
#pragma unroll
    for (int q = PB - 1; q >= 0; --q) {
      const int j = w + 2 * q;
      if (j >= J0 && j < J1) ca[j - J0] = __builtin_amdgcn_mfma_f32_16x16x32_f16(A[q], B[w], ca[j - J0], 0, 0, 0);
    }
    SCHED_FENCE();
  });
  float c3 = 0.f, c2 = 0.f, c1 = 0.f, yv[NT];
#pragma unroll
  for (int j = 0; j < NT; ++j) {
    const f32x4 a = ca[j];
    const float h3 = a[3];
    const float h2 = a[2] + shr_carry(c3, h3);
    const float h1 = a[1] + shr_carry(c2, h2);
    yv[j] = a[0] + shr_carry(c1, h1);
    c3 = dpp_mov<0x13C>(h3); c2 = dpp_mov<0x13C>(h2); c1 = dpp_mov<0x13C>(h1);
  }
#pragma unroll
  for (int jp = PA; jp < PB; ++jp) {
    const float u0 = (float)*(const LAS _Float16*)(pu + jp * 256), u1 = (float)*(const LAS _Float16*)(pu + jp * 256 + 128);
    const unsigned yq = pk_bf16(yv[2 * jp - J0] * kscale, yv[2 * jp + 1 - J0] * kscale);
    const unsigned uq = pk_bf16(u0 * dskip, u1 * dskip);
    st32c(pk_bf16(lo_f(yq) + lo_f(uq), hi_f(yq) + hi_f(uq)), lane * 4u, r_s, (unsigned)((cp * 2 + hc) * 4096 + jp * 256));
  }
#undef CV_A
#undef CV_B
}

#if defined(__HIP_DEVICE_COMPILE__)
__attribute__((target("no-packed-fp32-ops")))
#endif
__global__ __launch_bounds__(512, 2) void fused_kernel(
    const unsigned short* __restrict__ x, const unsigned short* __restrict__ A_re,
    const unsigned short* __restrict__ A_im, const unsigned short* __restrict__ B_re,
    const unsigned short* __restrict__ B_im, const unsigned short* __restrict__ C_re,
    const unsigned short* __restrict__ C_im, const unsigned* __restrict__ log_dt2,
    const unsigned* __restrict__ dsk2p, const unsigned short* __restrict__ wg,
    const unsigned short* __restrict__ wu, const unsigned short* __restrict__ wd,
    char* ws, unsigned short* __restrict__ out) {
  extern __shared__ __attribute__((aligned(16))) char smem[];
  unsigned long long pc0;
  asm volatile("s_getpc_b64 %0" : "=s"(pc0));
  LAS char* lds = (LAS char*)smem;
  const unsigned ldsb = (unsigned)(uintptr_t)lds;
  const int tid = threadIdx.x, lane = tid & 63;
  const int w8 = __builtin_amdgcn_readfirstlane(tid >> 6);
  const int wv = w8 & 3;
  STAMP_DECL; BW_DECL; STAMP_RT(14); STAMP(0);
  const int bid = blockIdx.x;
  const int xcd = bid & 7, bi = bid >> 3;
  unsigned pf0 = 0;
  if (w8 == 5) ld32(pf0, (unsigned)((bi * 16 + (lane & 15)) * 128), mk_rsrc((const void*)pc0, CODE_BYTES), 0u);
  asm volatile("" :: "s"(x), "s"(A_re), "s"(A_im), "s"(B_re), "s"(B_im), "s"(C_re), "s"(C_im), "s"(log_dt2),
               "s"(dsk2p), "s"(wg), "s"(wu), "s"(wd), "s"(ws), "s"(out));
  const int mt = (xcd >> 2) * 8 + (bi >> 2);
  const int ft = (xcd & 3) * 4 + (bi & 3);
  const int rot = ((bi >> 2) + 2 * (bi & 3)) & 7;
  const int cp = xcd * 32 + bi;
  const int tt = xcd * 4 + (bi >> 3);
  const int dtile = bi & 7;
  const int rot2 = ((bi >> 3) * 7 + dtile * 3) % 28;

  const rsrc_t r_x = mk_rsrc(x, SL * DM * 2);
  const rsrc_t r_s = mk_rsrc(ws + WS_ST, SL * DM * 2);
  const rsrc_t r_fl = mk_rsrc(ws + WS_FLAG, FL_BYTES);
#define G2_KS(w, i) ((unsigned)((w) * 28 + (((i) + rot2) >= 28 ? (i) + rot2 - 28 : (i) + rot2)))
#define XO(t_) ((unsigned)((tt * 64 + ((t_) >> 3)) * (DM * 2) + dtile * 128 + ((t_) & 7) * 16))
#define EPI_X() do { int l_ = lane; PIN(l_); u32x4 xw_[2]; \
    _Pragma("unroll") for (int j = 0; j < 2; ++j) ld128(xw_[j], XO(wv * 128 + j * 64 + l_), r_x, 0u); \
    lds_wait<4>(ldsb + SY_UR, 1u); wait_vm<0>(); \
    _Pragma("unroll") for (int j = 0; j < 2; ++j) { PIN(xw_[j]); *(LAS u32x4*)(lds + EPX + (wv * 128 + j * 64 + l_) * 16) = xw_[j]; } } while (0)
#define EPI_S() do { int l_ = lane; PIN(l_); u32x4 sw_[4]; \
    const unsigned so = (unsigned)((dtile * 64 + wv * 16 + (l_ >> 4)) * 4096 + (tt >> 1) * 256 + (l_ & 15) * 16); \
    _Pragma("unroll") for (int j = 0; j < 4; ++j) ld128c(sw_[j], so, r_s, (unsigned)(j * 16384)); \
    wait_vm<0>(); \
    _Pragma("unroll") for (int j = 0; j < 4; ++j) { PIN(sw_[j]); \
      *(LAS u32x4*)(lds + EPS + (wv * 16 + j * 4 + (l_ >> 4)) * 256 + ((l_ + wv * 2 + (j >> 1)) & 15) * 16) = sw_[j]; } } while (0)

  const rsrc_t r_wg = mk_rsrc(wg, DM * FF * 2), r_wu = mk_rsrc(wu, DM * FF * 2);
  unsigned woff[7], xoff[4];
  auto g1_stage = [&](int s) __attribute__((always_inline)) {
    const unsigned st = s == 0 ? 0u : (unsigned)(1 + (s - 1 + rot) % 7), lb = ldsb + (unsigned)((s % 3) * STG);
#pragma unroll
    for (int i = 0; i < 4; ++i)
      dma16x4(lb + XOFF + i * 4096, xoff[0], xoff[1], xoff[2], xoff[3], r_x, (unsigned)((mt * 128 + i * 32) * 1024) + st * 128u);
#pragma unroll
    for (int h = 0; h < 2; ++h) {
      const unsigned wso = (unsigned)(ft * 224) + (st * 64u + h * 32u) * (FF * 2);
      dma16x4(lb + h * 7168, woff[0], woff[1], woff[2], woff[3], r_wg, wso);
      dma16x3(lb + h * 7168 + 4096, woff[4], woff[5], woff[6], r_wg, wso);
      dma16x4(lb + W_UPOFF + h * 7168, woff[0], woff[1], woff[2], woff[3], r_wu, wso);
      dma16x3(lb + W_UPOFF + h * 7168 + 4096, woff[4], woff[5], woff[6], r_wu, wso);
    }
  };
  unsigned prm[6], ldt2, ep, dsk2;
  u32x4 xr[4];
  if (w8 < 4) {
    const unsigned po = (unsigned)((cp * 2 + (wv & 1)) * NM * 2);
    ld32(prm[0], lane * 4u, mk_rsrc(A_re, DM * NM * 2), po);
    ld32(prm[1], lane * 4u, mk_rsrc(A_im, DM * NM * 2), po);
    ld32(prm[2], lane * 4u, mk_rsrc(B_re, DM * NM * 2), po);
    ld32(prm[3], lane * 4u, mk_rsrc(B_im, DM * NM * 2), po);
    ld32(prm[4], lane * 4u, mk_rsrc(C_re, DM * NM * 2), po);
    ld32(prm[5], lane * 4u, mk_rsrc(C_im, DM * NM * 2), po);
    ld32(ldt2, 0u * lane, mk_rsrc(log_dt2, DM * 2), (unsigned)(cp * 4));
  } else if (w8 >= 6) {
    const unsigned vo = (unsigned)((lane >> 4) * 1024 + (lane & 15) * 16);
#pragma unroll
    for (int i = 0; i < 4; ++i) ld128(xr[i], vo, r_x, (unsigned)((bid * 8 + (wv - 2) * 4) * 1024 + i * 256));
    ld32c(ep, 0u * lane, r_fl, FL_EP);
    ld32(dsk2, 0u * lane, mk_rsrc(dsk2p, DM * 2), (unsigned)(cp * 4));
  } else if (w8 == 4) {
    __builtin_amdgcn_s_setprio(3);
#pragma unroll
    for (int i = 0; i < 4; ++i)
      xoff[i] = (unsigned)((lane >> 3) * 1024 + (((lane & 7) ^ ((lane >> 4) + 4 * (i & 1))) * 16) + i * 7168);
#pragma unroll
    for (int i = 0; i < 7; ++i) {
      const int c = i * 64 + lane, row = c / 14, col = c - row * 14;
      woff[i] = (unsigned)(row * (FF * 2) + col * 16 - (i & 3) * 1024);
    }
    __builtin_amdgcn_s_sleep(LDLY);
    g1_stage(0);
  }
  asm volatile("; first loads issued" ::: "memory");
  if (w8 < 4) {
    const rsrc_t r_h = mk_rsrc(ws + WS_HF, SL * FF * 2);
    {
    const int hc = wv & 1, m = wv >> 1;
    if (tid < 4) *(LAS u32x4*)(lds + SY + tid * 16) = (u32x4){0u, 0u, 0u, tid == 2 ? 1u : 0u};
    __builtin_amdgcn_s_setprio(2);
    {
      wait_vm<0>();
      PIN(ldt2);
      const float ldt = hc ? hi_f(ldt2) : lo_f(ldt2);
      const float L2E = 1.4426950408889634f, L2E_LO = (float)(1.4426950408889634074 - (double)1.4426950408889634f);
      const float th = ldt * L2E;
      const float tl = fmaf(ldt, L2E, -th) + ldt * L2E_LO;
      const float e0 = __builtin_amdgcn_exp2f(th);
      const float dt = rbf(fmaf(e0 * 0.6931471805599453f, tl, e0));
      wait_vm<0>();
#pragma unroll
      for (int i = 0; i < 6; ++i) PIN(prm[i]);
      float av[2], bv[2];
      f32x2 wq[2];
#pragma unroll
      for (int m = 0; m < 2; ++m) {
        av[m] = dt * (m ? hi_f(prm[0]) : lo_f(prm[0]));
        bv[m] = dt * (m ? hi_f(prm[1]) : lo_f(prm[1]));
        const f32x2 db = {dt * (m ? hi_f(prm[2]) : lo_f(prm[2])), dt * (m ? hi_f(prm[3]) : lo_f(prm[3]))};
        const f32x2 cc = {m ? hi_f(prm[4]) : lo_f(prm[4]), m ? hi_f(prm[5]) : lo_f(prm[5])};
        wq[m] = cmul(cc, db);
      }
      const float amax = fmaxf(wave_max(fmaxf(av[0], av[1])), 0.f);
      const float g2 = amax * L2E;
      const float wm = wave_max(fmaxf(fmaxf(fabsf(wq[0].x), fabsf(wq[0].y)), fmaxf(fabsf(wq[1].x), fabsf(wq[1].y))));
      int we = __builtin_amdgcn_frexp_expf(wm);
      we = we < -100 ? -100 : we;
      const float wsc = __builtin_amdgcn_ldexpf(1.0f, -we);
      f32x4 kacc[3];
#pragma unroll
      for (int t = 0; t < 3; ++t) kacc[t] = (f32x4){0.f, 0.f, 0.f, 0.f};
      const int kgs = (m ? CV_U : KGS) + hc * 4096;
      LAS char* tw[4];
#pragma unroll
      for (int c = 0; c < 4; ++c) tw[c] = lds + kgs + lane * 64 + ((c ^ ((lane >> 1) & 3)) << 4);
      const LAS char* tr[2];
      {
        const int G = lane >> 4, q4 = (lane & 15) >> 2, p = lane & 3;
#pragma unroll
        for (int t = 0; t < 2; ++t)
          tr[t] = lds + kgs + (4 * G + 2 * t + (q4 >> 1)) * 64 + ((((q4 & 1) * 2 + (p >> 1)) ^ ((2 * G + t) & 3)) << 4) + (p & 1) * 8;
      }
      auto put16 = [&](const f32x2 (&e)[16]) __attribute__((always_inline)) {
#pragma unroll
        for (int h = 0; h < 2; ++h) {
          u32x4 re, im;
#pragma unroll
          for (int j = 0; j < 4; ++j) {
            re[j] = pk_f16(e[8 * h + 2 * j].x, e[8 * h + 2 * j + 1].x);
            im[j] = pk_f16(e[8 * h + 2 * j].y, e[8 * h + 2 * j + 1].y);
          }
          *(LAS u32x4*)tw[h] = re;
          *(LAS u32x4*)tw[2 + h] = im;
        }
      };
#define KG_FRAG(s) __builtin_bit_cast(f16x8, cat8(trrd(tr[0] + (s) * 1024), trrd(tr[1] + (s) * 1024)))
#define KG_RD() do { _Pragma("unroll") for (int s = 0; s < 4; ++s) pf[s] = KG_FRAG(s); SCHED_FENCE(); } while (0)
#define KG_MMA(t) do { SCHED_FENCE(); _Pragma("unroll") for (int s = 0; s < 4; ++s) \
        kacc[t] = __builtin_amdgcn_mfma_f32_16x16x32_f16(pf[s], qf[s], kacc[t], 0, 0, 0); } while (0)
      {
        const float a = (m ? av[1] : av[0]) - amax, b = m ? bv[1] : bv[0];
        f32x2 w = m ? wq[1] : wq[0];
        w.x *= wsc; w.y *= -wsc;
        f32x2 e[16];
        {
          const f32x2 z16 = cexp_l(a, b, 16.f);
          e[0] = (f32x2){1.f, 0.f}; e[1] = z16; e[2] = cmul(z16, z16); e[3] = cmul(e[2], z16);
          const f32x2 z64 = cmul(e[2], e[2]);
#pragma unroll
          for (int c = 4; c < 16; ++c) e[c] = cmul(e[c - 4], z64);
          put16(e);
        }
        f16x8 qf[4], pf[4];
#pragma unroll
        for (int s = 0; s < 4; ++s) qf[s] = KG_FRAG(s);
        SCHED_FENCE();
        const f32x2 z1 = cexp_l(a, -b, 1.f);
        const f32x2 z2 = cmul(z1, z1), z4 = cmul(z2, z2);
        e[0] = w; e[1] = cmul(w, z1); e[2] = cmul(w, z2); e[3] = cmul(e[1], z2);
#pragma unroll
        for (int c = 4; c < 16; ++c) e[c] = cmul(e[c - 4], z4);
        put16(e);
        KG_RD();
        const f32x2 z256 = cexp_l(a, -b, 256.f);
        const f32x2 z512 = cmul(z256, z256);
        const f32x2 w2 = cmul(w, z512);
        e[0] = cmul(w, z256); e[1] = cmul(e[0], z2); e[8] = w2; e[12] = cmul(e[0], z512);
        const f32x2 w3 = e[12];
#pragma unroll
        for (int i = 1; i < 4; ++i) {
          e[2 * i] = cmul(e[2 * i - 2], z4); e[2 * i + 1] = cmul(e[2 * i - 1], z4);
          e[8 + i] = cmul(e[7 + i], z4); e[12 + i] = cmul(e[11 + i], z4);
        }
        KG_MMA(0);
        put16(e);
        KG_RD();
        const f32x2 z8 = cmul(z4, z4);
        e[0] = cmul(w2, z512); e[2] = cmul(w3, z512); e[4] = cmul(e[0], z512); e[6] = cmul(e[2], z512);
        e[8] = cmul(e[4], z512);
#pragma unroll
        for (int i = 0; i < 4; ++i) e[2 * i + 1] = cmul(e[2 * i], z8);
#pragma unroll
        for (int i = 9; i < 16; ++i) e[i] = (f32x2){0.f, 0.f};
        KG_MMA(1);
        put16(e);
        KG_RD();
        KG_MMA(2);
      }
#pragma unroll
      for (int t = 0; t < 3; ++t) *(LAS f32x4*)(lds + (m ? KF + hc * CV_KSTR : KGS + hc * 4096) + t * 1024 + lane * 16) = kacc[t];
      if (m) *(LAS f32x2*)(lds + SY_KG + hc * 8) = (f32x2){g2, (float)we};
    }
    }
    f32x16 acc[7];
    f32x4 csa[2];
    unsigned tag;
    {
      const int G = lane >> 4;
      const LAS char* wa0 = lds + (G & 1) * W_UPOFF + (8 * (G >> 1) + ((lane & 15) >> 2)) * 224 + (lane & 3) * 8;
      const LAS char* wa[7];
#pragma unroll
      for (int T = 0; T < 7; ++T) wa[T] = wa0 + 32 * ((T + 2 * wv) % 7);
      const LAS char* xa[4];
      {
        const int row = 32 * wv + (lane & 31), sw = (row >> 1) & 7;
#pragma unroll
        for (int ks = 0; ks < 4; ++ks) xa[ks] = lds + XOFF + row * 128 + (((2 * ks + (lane >> 5)) ^ sw) << 4);
      }
      constexpr int PD = 3;
      bf16x8 Af[196], Bf[32];
      const unsigned o1 = ((lane & 15) == 4 * ((lane >> 4) & 1)) ? 0x3f803f80u : 0u;
      u32x4 onesq = {o1, o1, o1, o1};
      PIN(onesq);
      const bf16x8 ones = __builtin_bit_cast(bf16x8, onesq);
#define A_READ(i, T) __builtin_bit_cast(bf16x8, cat8(trrd(wa[T] + (((i) >> 2) % 3) * STG + (16 * ((i) & 3)) * 224), \
                                                      trrd(wa[T] + (((i) >> 2) % 3) * STG + (16 * ((i) & 3) + 4) * 224)))
#define B_READ(i) (*(const LAS bf16x8*)(xa[(i) & 3] + (((i) >> 2) % 3) * STG))
#pragma unroll
      for (int T = 0; T < 7; ++T) acc[T] = (f32x16){};
      csa[0] = (f32x4){0.f, 0.f, 0.f, 0.f}; csa[1] = csa[0];
      BARX(bar_lgkm);
      STAMP(1);
      tag = lds_get1(ldsb + SY_VAL);
      Bf[0] = B_READ(0);
#pragma unroll
      for (int T = 0; T < PD; ++T) Af[T] = A_READ(0, T);
      bf16x8 Al[7][4];
      u32x2 stg;
      float cs[7];
      static_for<0, 196>([&](auto nc) {
        constexpr int n = decltype(nc)::value, i = n / 7, T = n % 7;
        constexpr int m = n + PD, im = m / 7, Tm = m % 7;
        if constexpr (n == 70) ld64c(stg, (unsigned)((lane & 31) * 8), r_fl, FL_STAT + (unsigned)((mt * 128 + wv * 32) * 8));
        if constexpr (n == 27 * 7 + 2) {
          if (lane < 32) {
#pragma unroll
            for (int j = 0; j < 2; ++j) *(LAS float*)(lds + CSUM + ((j + 2 * wv) % 7) * 128 + lane * 4) = csa[j][0];
          }
        }
        if constexpr (Tm == 0 && (im & 3) == 0) BARX(bar_lgkm);
        if constexpr (m < 196) {
          if constexpr (Tm == 0) Bf[im] = B_READ(im);
          Af[m] = A_READ(im, Tm);
        } else if constexpr (m == 196) {
#pragma unroll
          for (int k = 28; k < 32; ++k) Bf[k] = B_READ(k);
        } else if constexpr (m == 197) {
#pragma unroll
          for (int t = 0; t < 7; ++t) cs[t] = *(const LAS float*)(lds + CSUM + ((t + 2 * wv) % 7) * 128 + (lane & 31) * 4);
        } else {
#pragma unroll
          for (int k = 0; k < 4; ++k) Al[0][k] = A_READ(28 + k, 0);
        }
        acc[T] = __builtin_amdgcn_mfma_f32_32x32x16_bf16(Af[n], Bf[i], acc[T], 0, 0, 0);
        if constexpr (T < 2)
          csa[T] = __builtin_amdgcn_mfma_f32_16x16x32_bf16(ones, Af[n], csa[T], 0, 0, 0);
        SCHED_FENCE();
      });
      STAMP(5);
      wait_vm<0>();
      PIN(stg);
      if (__builtin_expect(!wave_all(stg[1] == tag), 0)) {
#pragma unroll 1
        for (int it = 0; it < (1 << 18); ++it) {
          ld64c(stg, (unsigned)((lane & 31) * 8), r_fl, FL_STAT + (unsigned)((mt * 128 + wv * 32) * 8));
          wait_vm<0>();
          PIN(stg);
          if (wave_all(stg[1] == tag)) break;
          __builtin_amdgcn_s_sleep(2);
        }
      }
      const unsigned stw = stg[0];
      const float mb = lo_f(stw), sd = hi_f(stw);
#pragma unroll
      for (int k = 28; k < 32; ++k) {
        u32x4 v = __builtin_bit_cast(u32x4, Bf[k]);
#pragma unroll
        for (int j = 0; j < 4; ++j) v[j] = pk_bf16(lo_f(v[j]) - mb, hi_f(v[j]) - mb);
        Bf[k] = __builtin_bit_cast(bf16x8, v);
      }
      const unsigned nm = (stw & 0xffffu) ^ 0x8000u;
      u32x4 bq = {nm | (nm << 16), nm, 0u, 0u};
      if (lane >= 32) bq = (u32x4){0u, 0u, 0u, 0u};
      const float ga = -1.4426950408889634f * __builtin_amdgcn_rcpf(sd);
      const unsigned hsb = (unsigned)(((mt * 4 + wv) * 112 + ft * 7) * 1024);
      STAMP(6);
      static_for<0, 8>([&](auto tc) {
        constexpr int T = decltype(tc)::value;
        u32x4 o;
        if constexpr (T < 7) {
          const float c = cs[T];
          const float ch = rbf(c), cm = rbf(c - ch), cl = (c - ch) - cm;
          const u32x4 aq = {pk_bf16(ch, cm), pk_bf16(cl, 0.f), 0u, 0u};
          acc[T] = __builtin_amdgcn_mfma_f32_32x32x16_bf16(__builtin_bit_cast(bf16x8, aq), __builtin_bit_cast(bf16x8, bq), acc[T], 0, 0, 0);
          SCHED_FENCE();
        }
#pragma unroll
        for (int j = 0; j < 4; ++j) {
          if constexpr (T + 1 < 7) Al[T + 1][j] = A_READ(28 + j, T + 1);
          if constexpr (T < 7) acc[T] = __builtin_amdgcn_mfma_f32_32x32x16_bf16(Al[T][j], Bf[28 + j], acc[T], 0, 0, 0);
          SCHED_FENCE();
          if constexpr (T >= 1) {
            const float s0 = __builtin_amdgcn_rcpf(fmaf(__builtin_amdgcn_exp2f(acc[T - 1][2 * j] * ga), sd, sd));
            const float s1 = __builtin_amdgcn_rcpf(fmaf(__builtin_amdgcn_exp2f(acc[T - 1][2 * j + 1] * ga), sd, sd));
            o[j] = pk_bf16(s0 * acc[T - 1][8 + 2 * j], s1 * acc[T - 1][8 + 2 * j + 1]);
            PIN(o[j]);
          }
          SCHED_FENCE();
        }
        if constexpr (T >= 1) st128c(o, lane * 16u, r_h, hsb + (unsigned)((T - 1 + 2 * wv) % 7) * 1024u);
      });
    }
    STAMP(7);
    const unsigned ldw = ldsb + SY_LD;
    const LAS char* waT[2];
    {
      const int G = lane >> 4, q4 = (lane & 15) >> 2;
      const LAS char* wa0 = lds + wv * 2048 + (4 * (G >> 1) + q4) * 128 + (G & 1) * 32 + (lane & 3) * 8;
      waT[0] = wa0 + ((q4 >> 1) * 64); waT[1] = wa0 + (((q4 >> 1) ^ 1) * 64);
    }
#define D_READ(i, T) __builtin_bit_cast(bf16x8, cat8(trrd(waT[T] + g2_base(i)), trrd(waT[T] + g2_base(i) + 1024)))
    bf16x8 Wr[28][2];
    lds_wait<1>(ldw, 2u);
    static_for<0, 5>([&](auto ic) { constexpr int i = decltype(ic)::value; Wr[i][0] = D_READ(i, 0); Wr[i][1] = D_READ(i, 1); });
    static_for<5, 10>([&](auto ic) { constexpr int i = decltype(ic)::value; Wr[i][0] = D_READ(i, 0); Wr[i][1] = D_READ(i, 1); });
    wait_vm<0>();
    if (lane < 8) set_flag(ws, FL_H + (unsigned)(((mt * 2 + (wv >> 1)) * 8 + lane) * 256 + (ft * 2 + (wv & 1)) * 4), tag);
    STAMP(8);
    lds_setv(ldsb + SY_MC + wv * 4, 2u);
    if (wv == 0) {
      lds_wait<4>(ldsb + SY_UR, 1u);
      conv_upper<14, 16>(lds, lane, 0, cp, r_s);
    }
    lds_wait<1>(ldw, 3u);
    static_for<10, G2_NR>([&](auto ic) { constexpr int i = decltype(ic)::value; Wr[i][0] = D_READ(i, 0); Wr[i][1] = D_READ(i, 1); });
    lds_setv(ldsb + SY_MC + wv * 4, 3u);
    STAMP(9);
    __builtin_amdgcn_s_setprio(0);
    poll_flags<2>((lane & 1) * 16u, r_fl, FL_H + (unsigned)((tt * 8 + dtile) * 256 + wv * 32), tag);
    if (wv == 0) publish<4>(lds, 2, ws, FL_S + (unsigned)((lane * 8 + (cp >> 5)) * 256 + (cp & 31) * 4), 32, tag, lane);
    __builtin_amdgcn_s_setprio(2);
    STAMP(10);
    u32x4 hb[28][2];
#define HB_LOAD(i) do { ld128c(hb[i][0], lane * 16u, r_h, ((unsigned)(tt * 2) * 112u + G2_KS(wv, i)) * 1024u); \
                        ld128c(hb[i][1], lane * 16u, r_h, ((unsigned)(tt * 2 + 1) * 112u + G2_KS(wv, i)) * 1024u); } while (0)
#pragma unroll
    for (int i = 0; i < G2_H0; ++i) HB_LOAD(i);
    f32x16 acc2[2][2];
    {
#pragma unroll
      for (int a = 0; a < 2; ++a)
#pragma unroll
        for (int b = 0; b < 2; ++b) acc2[a][b] = (f32x16){};
      static_for<0, 28>([&](auto ic) {
        constexpr int i = decltype(ic)::value;
        if constexpr (i == 14) STAMP(11);
        if constexpr (i + 2 == 13) lds_wait<1>(ldw, 6u);
        if constexpr (i + 2 >= G2_NR && i + 2 < 28) { Wr[i + 2][0] = D_READ(i + 2, 0); Wr[i + 2][1] = D_READ(i + 2, 1); }
        wait_vm<2 * (g2_issued(i) - i - 1)>();
        PIN(hb[i][0]); PIN(hb[i][1]);
#pragma unroll
        for (int T = 0; T < 2; ++T)
#pragma unroll
          for (int b = 0; b < 2; ++b)
            acc2[T][b] = __builtin_amdgcn_mfma_f32_32x32x16_bf16(Wr[i][T], __builtin_bit_cast(bf16x8, hb[i][b]), acc2[T][b], 0, 0, 0);
        SCHED_FENCE();
        static_for<g2_issued(i), g2_issued(i + 1)>([&](auto jc) { constexpr int j = decltype(jc)::value; HB_LOAD(j); });
      });
    }
    STAMP(12);
    {
      int l2 = lane; PIN(l2);
      const int tl = l2 & 31, x7 = (tl & 7) ^ (l2 >> 5);
#pragma unroll
      for (int b = 0; b < 2; ++b) {
        const int c8 = 4 * b + (tl >> 3);
        const int o4 = c8 < 5 ? RED_A + c8 * 8192 : RED_B + (c8 - 5) * 8192;
        LAS char* rp = lds + o4 + wv * 2048 + (tl & 7) * 256;
#pragma unroll
        for (int T = 0; T < 2; ++T)
#pragma unroll
          for (int g = 0; g < 4; ++g) {
            const f32x4 v = {acc2[T][b][4 * g], acc2[T][b][4 * g + 1], acc2[T][b][4 * g + 2], acc2[T][b][4 * g + 3]};
            *(LAS f32x4*)(rp + T * 128 + (((2 * g) ^ x7) << 4)) = v;
          }
      }
    }
  } else if (w8 == 4) {
    const rsrc_t r_wd = mk_rsrc(wd, FF * DM * 2);
    unsigned wv4[4];
#pragma unroll
    for (int i = 0; i < 4; ++i)
      wv4[i] = (unsigned)((lane >> 3) * 1024 + (((lane & 7) ^ (((lane >> 4) & 1) << 2)) * 16) + (i >> 1) * 458752 + (i & 1) * 8192 - i * 1024);
    const unsigned ldw = ldsb + SY_LD;
#define G2_GROUP(g) G2_STEPS(5 * (g), cmin(5 * (g) + 5, 28))
#define G2_STEPS(a_, b_) static_for<a_, b_>([&](auto ic) { constexpr int i = decltype(ic)::value; \
        const unsigned so_ = G2_KS(0, i) * 16384u + (unsigned)(dtile * 128); \
        dma16x4(ldsb + g2_base(i), wv4[0], wv4[1], wv4[2], wv4[3], r_wd, so_); \
        dma16x4(ldsb + g2_base(i) + 4096, wv4[0], wv4[1], wv4[2], wv4[3], r_wd, so_ + 2u * 458752u); })
    g1_stage(1);
    wait_vm<44>();
    STAMP(1);
    BARX(bar_only);
#pragma unroll
    for (int k = 0; k < 6; ++k) {
      g1_stage(k + 2);
      wait_vm<44>();
      BARX(bar_only);
    }
    STAMP(2);
    G2_GROUP(0);
    wait_vm<40>();
    BARX(bar_only);
    G2_GROUP(1);
    wait_vm<0>();
    lds_setv(ldw, 2u);
    STAMP(3);
    lds_wait<4>(ldsb + SY_MC, 2u);
    G2_STEPS(10, 15);
    wait_vm<16>();
    lds_setv(ldw, 3u);
    STAMP(4);
    G2_STEPS(15, 25);
    lds_wait<4>(ldsb + SY_MC, 3u);
    G2_GROUP(5);
    wait_vm<0>();
    lds_setv(ldw, 6u);
    STAMP(5);
    __builtin_amdgcn_s_setprio(0);
    EPI_X();
    lds_wait<1>(ldsb + SY_SR, 1u);
    EPI_S();
    STAMP(12);
  } else if (w8 == 5) {
    wait_vm<0>();
    asm volatile("" :: "v"(pf0));
    BARX(bar_only);
    const unsigned tag = lds_get1(ldsb + SY_VAL);
    BARX(bar_only);
    {
      const int c = lane & 15, g = lane >> 4;
      unsigned d[2][3][4];
      float kscale[2];
#pragma unroll
      for (int hc = 0; hc < 2; ++hc) {
        f32x4 kacc[3];
#pragma unroll
        for (int t = 0; t < 3; ++t)
          kacc[t] = *(const LAS f32x4*)(lds + KF + hc * CV_KSTR + t * 1024 + lane * 16) + *(const LAS f32x4*)(lds + KGS + hc * 4096 + t * 1024 + lane * 16);
        const f32x2 gw = *(const LAS f32x2*)(lds + SY_KG + hc * 8);
        const float g2 = gw.x;
        const int we = (int)gw.y;
        if (g2 > 0.f) {
#pragma unroll
          for (int i = 0; i < 4; ++i) {
            const int l0 = 16 * c + 4 * g + i;
            const int l1 = g < 2 ? 256 + 16 * c + 8 * g + 2 * i : 512 + 256 * (g - 2) + 16 * c + 4 * i;
            const int l2 = g < 2 ? 1024 + 256 * (2 * g + (i >> 1)) + 8 * (i & 1) + 16 * c : 2048;
            kacc[0][i] *= __builtin_amdgcn_exp2f(g2 * (float)(l0 - 2048));
            kacc[1][i] *= __builtin_amdgcn_exp2f(g2 * (float)(l1 - 2048));
            kacc[2][i] *= __builtin_amdgcn_exp2f(g2 * (float)(l2 - 2048));
          }
        }
        kscale[hc] = __builtin_amdgcn_ldexpf(1.0f, we - 7) * __builtin_amdgcn_exp2f(g2 * 2048.f);
#pragma unroll
        for (int t = 0; t < 3; ++t)
#pragma unroll
          for (int i = 0; i < 4; ++i) { d[hc][t][i] = pk_f16(kacc[t][i] * 128.f, kacc[t][i] * 128.f); PIN(d[hc][t][i]); }
        BARX(bar_only);
      }
      BARX(bar_only);
      BARX(bar_only);
      BARX(bar_only);
      BARX(bar_only);
      __builtin_amdgcn_s_sleep(WSL);
      __builtin_amdgcn_s_setprio(3);
      STAMP(2);
      *(LAS f32x2*)(lds + SY_VAL + 8) = (f32x2){kscale[0], kscale[1]};
#define MIX(a, b) (((a) & 0xffffu) | ((b) & 0xffff0000u))
#pragma unroll
      for (int hc = 0; hc < 2; ++hc) {
        LAS char* kf = lds + KF + hc * CV_KSTR + 96;
        if (g < 2) {
#pragma unroll
          for (int h = 0; h < 2; ++h) {
            LAS char* p = kf + 2 * (1024 + 256 * (2 * g + h) + 16 * c);
            const unsigned v0 = d[hc][2][2 * h], v1 = d[hc][2][2 * h + 1];
            *(LAS u32x2*)(p - 8) = (u32x2){v0, v0};
            *(LAS u32x4*)(p) = (u32x4){v0, v0, MIX(v0, v1), v1};
            *(LAS u32x2*)(p + 16) = (u32x2){v1, v1};
          }
        } else {
          if (lane == 32) *(LAS u32x2*)(kf + 2 * 2044) = (u32x2){d[hc][2][0], d[hc][2][0]};
        }
        if (g >= 2) {
          LAS char* p = kf + 2 * (512 + 256 * (g - 2) + 16 * c);
          const unsigned v0 = d[hc][1][0], v1 = d[hc][1][1], v2 = d[hc][1][2], v3 = d[hc][1][3];
          *(LAS unsigned*)(p - 4) = v0;
          *(LAS u32x4*)(p) = (u32x4){v0, MIX(v0, v1), v1, v2};
          *(LAS u32x2*)(p + 16) = (u32x2){v2, MIX(v2, v3)};
          *(LAS unsigned*)(p + 24) = v3;
        }
        if (g < 2) {
          LAS char* p = kf + 2 * (256 + 16 * c + 8 * g);
          const unsigned v0 = d[hc][1][0], v1 = d[hc][1][1], v2 = d[hc][1][2], v3 = d[hc][1][3];
          *(LAS unsigned short*)(p - 2) = (unsigned short)v0;
          *(LAS u32x2*)(p) = (u32x2){v0, MIX(v1, v2)};
          *(LAS unsigned*)(p + 8) = v2;
          *(LAS unsigned short*)(p + 12) = (unsigned short)v3;
        }
        *(LAS u32x2*)(kf + 2 * (16 * c + 4 * g)) = (u32x2){MIX(d[hc][0][0], d[hc][0][1]), MIX(d[hc][0][2], d[hc][0][3])};
        if (lane < 6) *(LAS u32x4*)(kf - 96 + lane * 16) = (u32x4){0u, 0u, 0u, 0u};
      }
      lds_setv(ldsb + SY_UR + 8, 1u);
      STAMP(3);
    }
    __builtin_amdgcn_s_setprio(1);
    lds_wait<4>(ldsb + SY_UR, 1u);
    conv_upper<CV_NP, 16>(lds, lane, 1, cp, r_s);
    conv_upper<CV_NP, 14>(lds, lane, 0, cp, r_s);
    STAMP(4);
    publish<4>(lds, 2, ws, FL_S + (unsigned)((lane * 8 + (cp >> 5)) * 256 + (cp & 31) * 4), 32, tag, lane);
    STAMP(5);
    EPI_X();
    lds_wait<1>(ldsb + SY_CNT + 8, 4u);
    poll_flags<4>((lane & 7) * 16u, r_fl, FL_S + (unsigned)((tt * 8 + dtile) * 256), tag);
    lds_setv(ldsb + SY_SR, 1u);
    STAMP(6);
    EPI_S();
    STAMP(12);
  } else {
    const int hc = wv - 2;
    const int dch = cp * 2 + hc;
    const rsrc_t r_xn = mk_rsrc(ws + WS_XNF, SL * DM * 2);
    const int row0 = bid * 8 + hc * 4;
    wait_vm<0>();
    STAMP(1);
#pragma unroll
    for (int i = 0; i < 4; ++i) PIN(xr[i]);
    PIN(ep); PIN(dsk2);
    const unsigned tag = (unsigned)__builtin_amdgcn_readfirstlane(ep) + 1u;
    lds_setv(ldsb + SY_VAL, tag);
    lds_setv(ldsb + SY_VAL + 4, dsk2);
    {
      float f[32];
#pragma unroll
      for (int j = 0; j < 16; ++j) { f[2 * j] = lo_f(xr[j >> 2][j & 3]); f[2 * j + 1] = hi_f(xr[j >> 2][j & 3]); }
      float s0 = 0.f, s1 = 0.f;
#pragma unroll
      for (int j = 0; j < 32; j += 4) { s0 += f[j] + f[j + 1]; s1 += f[j + 2] + f[j + 3]; }
      const float mean = row_sum(s0 + s1) * (1.0f / DM);
      float q0 = 0.f, q1 = 0.f;
#pragma unroll
      for (int j = 0; j < 32; j += 2) {
        const float da = f[j] - mean, db = f[j + 1] - mean;
        q0 = fmaf(da, da, q0); q1 = fmaf(db, db, q1);
      }
      const float var = row_sum(q0 + q1) * (1.0f / DM);
      const float mb = rbf(mean);
      const float sb = rbf(__builtin_amdgcn_sqrtf(rbf(rbf(var) + 1e-5f)));
      if ((lane & 15) == 0)
        st64c((u32x2){pk_bf16(mb, sb), tag}, (unsigned)((lane >> 4) * 8), r_fl, FL_STAT + (unsigned)(row0 * 8));
      const float rs = __builtin_amdgcn_rcpf(sb);
      const unsigned vo = (unsigned)((lane & 15) * 32768 + (lane >> 4) * 16);
      STAMP(2);
#pragma unroll
      for (int i = 0; i < 4; ++i) {
        u32x4 o;
#pragma unroll
        for (int j = 0; j < 4; ++j) {
          const unsigned d = pk_bf16(f[8 * i + 2 * j] - mb, f[8 * i + 2 * j + 1] - mb);
          o[j] = pk_bf16(lo_f(d) * rs, hi_f(d) * rs);
        }
        st128c(o, vo, r_xn, (unsigned)(i * 16 * 32768 + row0 * 16));
      }
    }
    BARX(bar_lgkm);
    wait_vm<0>();
    BARX(bar_only);
    if (hc == 0 && lane < 8) set_flag(ws, FL_ALL + (unsigned)(lane * 4096 + bid * 4), tag);
    BARX(bar_only);
    BARX(bar_only);
    u32x4 vf;
    ld128c(vf, lane * 16u, r_fl, FL_ALL + (unsigned)(xcd * 4096));
    BARX(bar_only);
    wait_vm<0>();
    PIN(vf);
    const bool all_ok = wave_all((vf[0] == tag) & (vf[1] == tag) & (vf[2] == tag) & (vf[3] == tag));
    unsigned uw[16];
    const unsigned usb = (unsigned)((cp >> 2) * 32768 + (cp & 3) * 4 + hc * 16384);
    if (!all_ok) {
      for (int r = 5; r <= 7; ++r) BARX(bar_only);
      poll_flags<4>(lane * 16u, r_fl, FL_ALL + (unsigned)(xcd * 4096), tag);
    }
#pragma unroll
    for (int m = 0; m < 16; ++m) ld32c(uw[m], lane * 16u, r_xn, usb + (unsigned)m * 1024u);
    if (lane == 0 && bid == 0 && hc == 0) set_flag(ws, FL_EP, tag);
    if (all_ok) { BARX(bar_only); BARX(bar_only); }
    wait_vm<0>();
    {
      const bool od = lane & 1;
      LAS char* up = lds + CV_U + 160 + hc * 2048 + (od ? 128 : 0) + (lane >> 1) * 4;
#pragma unroll
      for (int m = 0; m < 16; m += 2) {
        PIN(uw[m]); PIN(uw[m + 1]);
        const unsigned a = pk_f16(lo_f(uw[m]), hi_f(uw[m])), b = pk_f16(lo_f(uw[m + 1]), hi_f(uw[m + 1]));
        const unsigned y = (unsigned)__builtin_amdgcn_update_dpp(0, (int)(od ? a : b), 0xB1, 0xf, 0xf, true);
        const unsigned f0 = od ? y : a, f1 = od ? b : y;
        uw[m] = __builtin_amdgcn_perm(f1, f0, 0x05040100u);
        uw[m + 1] = __builtin_amdgcn_perm(f1, f0, 0x07060302u);
        PIN(uw[m]); PIN(uw[m + 1]);
      }
      if (all_ok) BARX(bar_only);
      __builtin_amdgcn_s_sleep(WSL);
      __builtin_amdgcn_s_setprio(3);
      if (lane < 10) *(LAS u32x4*)(lds + CV_U + hc * CV_USTR + lane * 16) = (u32x4){0u, 0u, 0u, 0u};
#pragma unroll
      for (int m = 0; m < 16; m += 2) {
        *(LAS unsigned*)(up + m * 128) = uw[m];
        *(LAS unsigned*)(up + CV_USTR + m * 128) = uw[m + 1];
      }
      lds_setv(ldsb + SY_UR + hc * 4, 1u);
    }
    STAMP(3);
    __builtin_amdgcn_s_setprio(1);
    lds_wait<4>(ldsb + SY_UR, 1u);
    const float kscale = *(const LAS float*)(lds + SY_VAL + 8 + hc * 4);
    STAMP(6);
    {
      const float dskip = hc ? hi_f(dsk2) : lo_f(dsk2);
      const int G = lane >> 4, q4 = (lane & 15) >> 2, p = lane & 3;
      const LAS char* pa = lds + KF + hc * CV_KSTR + 96 + 2 * (76 + 16 * p - 32 * G - 4 * q4 - 16);
      const LAS char* pb = lds + CV_U + hc * CV_USTR + 160 + 2 * (-76 + 32 * G + 4 * q4 + 4 * p);
      const LAS char* pu = lds + CV_U + hc * CV_USTR + 160 + lane * 2;
      f16x8 A[16], B[32];
#define CV_A(q) __builtin_bit_cast(f16x8, cat8(trrd(pa + 256 * (q) + 32), trrd(pa + 256 * (q))))
#define CV_B(w) __builtin_bit_cast(f16x8, cat8(trrd(pb + 128 * (w)), trrd(pb + 128 * (w) + 32)))
      A[0] = CV_A(0); B[0] = CV_B(0); B[1] = CV_B(1);
      A[1] = CV_A(1); B[2] = CV_B(2); B[3] = CV_B(3);
      f32x4 ca[2][2];
      _Float16 us[2][2];
      unsigned so16[16];
      float c3 = 0.f, c2 = 0.f, c1 = 0.f;
      auto tile_y = [&](const f32x4 a) __attribute__((always_inline)) {
        const float h3 = a[3];
        const float h2 = a[2] + shr_carry(c3, h3);
        const float h1 = a[1] + shr_carry(c2, h2);
        const float y = a[0] + shr_carry(c1, h1);
        c3 = dpp_mov<0x13C>(h3); c2 = dpp_mov<0x13C>(h2); c1 = dpp_mov<0x13C>(h1);
        return y * kscale;
      };
      static_for<0, CV_NP + 1>([&](auto jc) {
        constexpr int jj = decltype(jc)::value;
        if constexpr (jj + 2 < CV_NP) { A[jj + 2] = CV_A(jj + 2); B[2 * jj + 4] = CV_B(2 * jj + 4); B[2 * jj + 5] = CV_B(2 * jj + 5); }
        if constexpr (jj < CV_NP) {
          us[jj & 1][0] = *(const LAS _Float16*)(pu + jj * 256); us[jj & 1][1] = *(const LAS _Float16*)(pu + jj * 256 + 128);
          f32x4 a0 = {0.f, 0.f, 0.f, 0.f}, a1 = {0.f, 0.f, 0.f, 0.f};
#pragma unroll
          for (int q = jj; q >= 0; --q) {
            a0 = __builtin_amdgcn_mfma_f32_16x16x32_f16(A[q], B[2 * (jj - q)], a0, 0, 0, 0);
            a1 = __builtin_amdgcn_mfma_f32_16x16x32_f16(A[q], B[2 * (jj - q) + 1], a1, 0, 0, 0);
          }
          ca[jj & 1][0] = a0; ca[jj & 1][1] = a1;
        }
        if constexpr (jj >= 1) {
          const float y0 = tile_y(ca[(jj - 1) & 1][0]), y1 = tile_y(ca[(jj - 1) & 1][1]);
          const unsigned yq = pk_bf16(y0, y1);
          const unsigned uq = pk_bf16((float)us[(jj - 1) & 1][0] * dskip, (float)us[(jj - 1) & 1][1] * dskip);
          so16[jj - 1] = pk_bf16(lo_f(yq) + lo_f(uq), hi_f(yq) + hi_f(uq));
        }
        if constexpr (jj >= 1 && jj < CV_NP) {
#pragma unroll
          for (int k = 0; k < 2 * (jj + 1); ++k) {
            __builtin_amdgcn_sched_group_barrier(0x8, 1, 0);
            __builtin_amdgcn_sched_group_barrier(0x100, 1, 0);
            __builtin_amdgcn_sched_group_barrier(0x2, jj < 7 ? 4 : 2, 0);
          }
        }
        SCHED_FENCE();
        if constexpr (jj >= 1) st32c(so16[jj - 1], lane * 4u, r_s, (unsigned)(dch * 4096 + (jj - 1) * 256));
      });
    }
    STAMP(7);
    publish<4>(lds, 2, ws, FL_S + (unsigned)((lane * 8 + (cp >> 5)) * 256 + (cp & 31) * 4), 32, tag, lane);
    STAMP(9);
    EPI_X();
    lds_wait<1>(ldsb + SY_SR, 1u);
    EPI_S();
    STAMP(12);
  }
  bar_lgkm();
  {
    int t2 = tid; PIN(t2);
    const u32x4 xv = *(const LAS u32x4*)(lds + EPX + t2 * 16);
    unsigned sv[8];
#pragma unroll
    for (int j = 0; j < 8; ++j) sv[j] = *(const LAS unsigned*)(lds + EPS + ((t2 & 7) * 8 + j) * 256 + (((t2 >> 5) + (t2 & 7)) & 15) * 16 + ((t2 >> 3) & 3) * 4);
    const int rb = w8 < 5 ? RED_A + w8 * 8192 : RED_B + (w8 - 5) * 8192;
    const int tr7 = (t2 >> 3) & 7;
    const LAS char* rp = lds + rb + tr7 * 256 + ((t2 >> 2) & 1) * 128;
    const bool odd = tt & 1;
    u32x4 o;
#pragma unroll
    for (int i = 0; i < 2; ++i) {
      const int ch = (((t2 & 3) * 2 + i) ^ tr7) << 4;
      f32x4 v = *(const LAS f32x4*)(rp + ch);
#pragma unroll
      for (int w = 1; w < 4; ++w) v += *(const LAS f32x4*)(rp + w * 2048 + ch);
#pragma unroll
      for (int jj = 0; jj < 2; ++jj) {
        const unsigned fp = pk_bf16(v[2 * jj], v[2 * jj + 1]);
        const float f0 = lo_f(fp), f1 = hi_f(fp);
        const float g0 = f0 * __builtin_amdgcn_rcpf(1.0f + __builtin_amdgcn_exp2f(f0 * -1.4426950408889634f));
        const float g1 = f1 * __builtin_amdgcn_rcpf(1.0f + __builtin_amdgcn_exp2f(f1 * -1.4426950408889634f));
        const unsigned gp = pk_bf16(g0, g1);
        const int j = 4 * i + 2 * jj;
        const float s0 = odd ? hi_f(sv[j]) : lo_f(sv[j]), s1 = odd ? hi_f(sv[j + 1]) : lo_f(sv[j + 1]);
        const unsigned sg = pk_bf16(s0 * lo_f(gp), s1 * hi_f(gp));
        const unsigned xw = xv[2 * i + jj];
        o[2 * i + jj] = pk_bf16(lo_f(xw) + lo_f(sg), hi_f(xw) + hi_f(sg));
      }
    }
    st128c(o, XO(t2), mk_rsrc(out, SL * DM * 2), 0u);
  }
  STAMP(13); STAMP_RT(15);
  STAMP_OUT(0);
  BW_OUT();
}

extern "C" void kernel_launch(void* const* d_in, const int* in_sizes, int n_in, void* d_out,
                              int out_size, void* d_ws, size_t ws_size, hipStream_t stream) {
  (void)hipFuncSetAttribute((const void*)fused_kernel, hipFuncAttributeMaxDynamicSharedMemorySize, LDS_BYTES);
  fused_kernel<<<256, 512, LDS_BYTES, stream>>>(
      (const unsigned short*)d_in[0], (const unsigned short*)d_in[1], (const unsigned short*)d_in[2],
      (const unsigned short*)d_in[3], (const unsigned short*)d_in[4], (const unsigned short*)d_in[5],
      (const unsigned short*)d_in[6], (const unsigned*)d_in[7], (const unsigned*)d_in[8],
      (const unsigned short*)d_in[10], (const unsigned short*)d_in[11], (const unsigned short*)d_in[12],
      (char*)d_ws, (unsigned short*)d_out);
}
```

```cpp
#include <hip/hip_runtime.h>
#include <hip/hip_bf16.h>
#include <stdint.h>
#include <type_traits>

typedef __attribute__((ext_vector_type(4))) short s16x4;
typedef __attribute__((ext_vector_type(8))) short s16x8;
typedef __attribute__((ext_vector_type(8))) _Float16 f16x8;
typedef __attribute__((ext_vector_type(8))) __bf16 bf16x8;
typedef __attribute__((ext_vector_type(2))) float f32x2;
typedef __attribute__((ext_vector_type(4))) float f32x4;
typedef __attribute__((ext_vector_type(16))) float f32x16;
typedef __attribute__((ext_vector_type(4))) unsigned u32x4;
typedef __attribute__((ext_vector_type(3))) unsigned u32x3;
typedef __attribute__((ext_vector_type(2))) unsigned u32x2;
typedef __attribute__((ext_vector_type(2))) __bf16 bf16x2;
typedef __attribute__((ext_vector_type(2))) _Float16 f16x2;
#define LAS __attribute__((address_space(3)))
#define GAS __attribute__((address_space(1)))
typedef __amdgpu_buffer_rsrc_t rsrc_t;

constexpr int SL = 2048;
constexpr int DM = 512;
constexpr int NM = 128;
constexpr int FF = 1792;

constexpr size_t WS_XNF = 0;
constexpr size_t WS_ST = 2 * (size_t)SL * DM * 2;
constexpr size_t WS_HF = WS_ST + (size_t)SL * DM * 2;
constexpr size_t WS_FLAG = 2 * (size_t)SL * DM;
constexpr unsigned FL_ALL = 0, FL_STAT = 65536, FL_H = 131072, FL_S = 196608, FL_EP = 262144, FL_BYTES = 262400;

template <int I, int N, class F>
__device__ __forceinline__ void static_for(F&& f) {
  if constexpr (I < N) { f(std::integral_constant<int, I>{}); static_for<I + 1, N>(f); }
}

__device__ __forceinline__ unsigned pk_bf16(float lo, float hi) {
  f32x2 v = {lo, hi};
  return __builtin_bit_cast(unsigned, __builtin_convertvector(v, bf16x2));
}
__device__ __forceinline__ unsigned pk_f16(float lo, float hi) {
  f32x2 v = {lo, hi};
  return __builtin_bit_cast(unsigned, __builtin_convertvector(v, f16x2));
}
__device__ __forceinline__ float lo_f(unsigned p) { return __uint_as_float(p << 16); }
__device__ __forceinline__ float hi_f(unsigned p) { return __uint_as_float(p & 0xffff0000u); }
__device__ __forceinline__ float rbf(float x) { return lo_f(pk_bf16(x, x)); }

template <int CTRL>
__device__ __forceinline__ float dpp_mov(float v) {
  return __int_as_float(__builtin_amdgcn_update_dpp(0, __float_as_int(v), CTRL, 0xf, 0xf, true));
}
__device__ __forceinline__ float shr_carry(float c, float v) {
  return __builtin_bit_cast(float, __builtin_amdgcn_update_dpp(__builtin_bit_cast(int, c), __builtin_bit_cast(int, v), 0x138, 0xf, 0xf, false));
}
__device__ __forceinline__ float rdlane(float v, int l) {
  return __int_as_float(__builtin_amdgcn_readlane(__float_as_int(v), l));
}
__device__ __forceinline__ float wave_sum(float v) {
  v += dpp_mov<0xB1>(v);
  v += dpp_mov<0x4E>(v);
  v += dpp_mov<0x141>(v);
  v += dpp_mov<0x140>(v);
  return (rdlane(v, 0) + rdlane(v, 16)) + (rdlane(v, 32) + rdlane(v, 48));
}
__device__ __forceinline__ float row_sum(float v) {
  v += dpp_mov<0xB1>(v);
  v += dpp_mov<0x4E>(v);
  v += dpp_mov<0x141>(v);
  v += dpp_mov<0x140>(v);
  return v;
}
__device__ __forceinline__ float half_sum(float v, int lane) {
  v += dpp_mov<0xB1>(v);
  v += dpp_mov<0x4E>(v);
  v += dpp_mov<0x141>(v);
  v += dpp_mov<0x140>(v);
  const float lo = rdlane(v, 0) + rdlane(v, 16), hi = rdlane(v, 32) + rdlane(v, 48);
  return lane < 32 ? lo : hi;
}
__device__ __forceinline__ float wave_max(float v) {
  v = fmaxf(v, dpp_mov<0xB1>(v));
  v = fmaxf(v, dpp_mov<0x4E>(v));
  v = fmaxf(v, dpp_mov<0x141>(v));
  v = fmaxf(v, dpp_mov<0x140>(v));
  return fmaxf(fmaxf(rdlane(v, 0), rdlane(v, 16)), fmaxf(rdlane(v, 32), rdlane(v, 48)));
}

__device__ __forceinline__ rsrc_t mk_rsrc(const void* p, unsigned bytes) {
  return __builtin_amdgcn_make_buffer_rsrc((void*)p, (short)0, (int)bytes, 0x00020000);
}
__device__ __forceinline__ void dma16(unsigned lds_dst, unsigned voff, rsrc_t r, unsigned soff) {
  asm volatile("s_mov_b32 m0, %0\n\ts_nop 0\n\tbuffer_load_dwordx4 %1, %2, %3 offen lds"
               :: "s"(lds_dst), "v"(voff), "s"(r), "s"(soff) : "memory");
}
__device__ __forceinline__ void dma16x4(unsigned lds_dst, unsigned v0, unsigned v1, unsigned v2, unsigned v3, rsrc_t r, unsigned soff) {
  asm volatile("s_mov_b32 m0, %0\n\ts_nop 0\n\t"
               "buffer_load_dwordx4 %1, %5, %6 offen lds\n\t"
               "buffer_load_dwordx4 %2, %5, %6 offen offset:1024 lds\n\t"
               "buffer_load_dwordx4 %3, %5, %6 offen offset:2048 lds\n\t"
               "buffer_load_dwordx4 %4, %5, %6 offen offset:3072 lds"
               :: "s"(lds_dst), "v"(v0), "v"(v1), "v"(v2), "v"(v3), "s"(r), "s"(soff) : "memory");
}
__device__ __forceinline__ void dma16x3(unsigned lds_dst, unsigned v0, unsigned v1, unsigned v2, rsrc_t r, unsigned soff) {
  asm volatile("s_mov_b32 m0, %0\n\ts_nop 0\n\t"
               "buffer_load_dwordx4 %1, %4, %5 offen lds\n\t"
               "buffer_load_dwordx4 %2, %4, %5 offen offset:1024 lds\n\t"
               "buffer_load_dwordx4 %3, %4, %5 offen offset:2048 lds"
               :: "s"(lds_dst), "v"(v0), "v"(v1), "v"(v2), "s"(r), "s"(soff) : "memory");
}
__device__ __forceinline__ void ld128(u32x4& dst, unsigned voff, rsrc_t r, unsigned soff) {
  asm volatile("buffer_load_dwordx4 %0, %1, %2, %3 offen" : "=v"(dst) : "v"(voff), "s"(r), "s"(soff) : "memory");
}
__device__ __forceinline__ void ld128c(u32x4& dst, unsigned voff, rsrc_t r, unsigned soff) {
  asm volatile("buffer_load_dwordx4 %0, %1, %2, %3 offen sc1" : "=v"(dst) : "v"(voff), "s"(r), "s"(soff) : "memory");
}
__device__ __forceinline__ void ld32(unsigned& dst, unsigned voff, rsrc_t r, unsigned soff) {
  asm volatile("buffer_load_dword %0, %1, %2, %3 offen" : "=v"(dst) : "v"(voff), "s"(r), "s"(soff) : "memory");
}
__device__ __forceinline__ void ld64c(u32x2& dst, unsigned voff, rsrc_t r, unsigned soff) {
  asm volatile("buffer_load_dwordx2 %0, %1, %2, %3 offen sc1" : "=v"(dst) : "v"(voff), "s"(r), "s"(soff) : "memory");
}
__device__ __forceinline__ void ld32c(unsigned& dst, unsigned voff, rsrc_t r, unsigned soff) {
  asm volatile("buffer_load_dword %0, %1, %2, %3 offen sc1" : "=v"(dst) : "v"(voff), "s"(r), "s"(soff) : "memory");
}
__device__ __forceinline__ void st32c(unsigned v, unsigned voff, rsrc_t r, unsigned soff) {
  asm volatile("buffer_store_dword %0, %1, %2, %3 offen sc1" :: "v"(v), "v"(voff), "s"(r), "s"(soff) : "memory");
}
__device__ __forceinline__ void st64c(u32x2 v, unsigned voff, rsrc_t r, unsigned soff) {
  asm volatile("buffer_store_dwordx2 %0, %1, %2, %3 offen sc1\n\ts_nop 1" :: "v"(v), "v"(voff), "s"(r), "s"(soff) : "memory");
}
__device__ __forceinline__ void ld16(unsigned& dst, unsigned voff, rsrc_t r, unsigned soff) {
  asm volatile("buffer_load_ushort %0, %1, %2, %3 offen" : "=v"(dst) : "v"(voff), "s"(r), "s"(soff) : "memory");
}
__device__ __forceinline__ void st128c(u32x4 v, unsigned voff, rsrc_t r, unsigned soff) {
  asm volatile("buffer_store_dwordx4 %0, %1, %2, %3 offen sc1\n\ts_nop 1" :: "v"(v), "v"(voff), "s"(r), "s"(soff) : "memory");
}
template <int N>
__device__ __forceinline__ void wait_vm() { asm volatile("s_waitcnt vmcnt(%0)" :: "n"(N > 63 ? 63 : N) : "memory"); }
__device__ __forceinline__ void bar_lgkm() { asm volatile("s_waitcnt lgkmcnt(0)\n\ts_barrier" ::: "memory"); }
__device__ __forceinline__ void bar_only() { asm volatile("s_barrier" ::: "memory"); }
#define BW_DECL
#define BARX(f) f()
#define BW_OUT()
#define PIN(x) asm volatile("" : "+v"(x))
#define SCHED_FENCE() __builtin_amdgcn_sched_barrier(0)

__device__ __forceinline__ s16x4 trrd(const LAS char* p) {
  return __builtin_amdgcn_ds_read_tr16_b64_v4i16((LAS s16x4*)p);
}
__device__ __forceinline__ s16x8 cat8(s16x4 a, s16x4 b) {
  return __builtin_shufflevector(a, b, 0, 1, 2, 3, 4, 5, 6, 7);
}

__device__ __forceinline__ bool wave_all(bool p) { return __builtin_amdgcn_ballot_w64(!p) == 0ull; }
template <int SLP = 2>
__device__ __forceinline__ void poll_flags(unsigned voff, rsrc_t r, unsigned soff, unsigned tag) {
#pragma unroll 1
  for (int it = 0; it < (1 << 18); ++it) {
    u32x4 v;
    asm volatile("buffer_load_dwordx4 %0, %1, %2, %3 offen sc1\n\ts_waitcnt vmcnt(0)"
                 : "=&v"(v) : "v"(voff), "s"(r), "s"(soff) : "memory");
    const bool ok = (v[0] == tag) & (v[1] == tag) & (v[2] == tag) & (v[3] == tag);
    if (wave_all(ok)) break;
    __builtin_amdgcn_s_sleep(SLP);
  }
}
__device__ __forceinline__ void set_flag(char* ws, unsigned off, unsigned tag) {
  __hip_atomic_store((GAS unsigned*)(ws + WS_FLAG + off), tag, __ATOMIC_RELAXED, __HIP_MEMORY_SCOPE_AGENT);
}

#define STAMP_DECL
#define STAMP(i)
#define STAMP_RT(i)
#define STAMP_OUT(kid)

__device__ __forceinline__ f32x2 cmul(f32x2 a, f32x2 b) {
  f32x2 r;
  r.x = fmaf(a.x, b.x, -(a.y * b.y));
  r.y = fmaf(a.x, b.y, a.y * b.x);
  return r;
}
__device__ __forceinline__ f32x2 cexp_l(float a, float b, float l) {
  const float mag = __builtin_amdgcn_exp2f(a * l * 1.4426950408889634f);
  const float x = b * l;
  const float C1 = 0.15915494309189535f;
  const float C2 = (float)(0.15915494309189533577 - (double)0.15915494309189535f);
  const float r = fmaf(x, C2, fmaf(x, C1, -__builtin_rintf(x * C1)));
  f32x2 z;
  z.x = mag * __builtin_amdgcn_cosf(r);
  z.y = mag * __builtin_amdgcn_sinf(r);
  return z;
}

constexpr int W_UPOFF = 14336 + 128;
constexpr int XOFF = W_UPOFF + 14336;
constexpr int STG = XOFF + 16384;
constexpr int KGS = 3 * STG;
constexpr int KF = KGS + 8192;
constexpr int CV_KSTR = (48 + 2048) * 2;
constexpr int CV_USTR = (80 + 2048) * 2;
constexpr int CV_U = KF + 2 * CV_KSTR;
constexpr int CSUM = CV_U + 2 * CV_USTR;
constexpr int STAT = CSUM + 896;
constexpr int SY = STAT + 512;
constexpr int SY_LD = SY, SY_MC = SY + 16, SY_ST = SY + 4, SY_HR = SY + 8, SY_SR = SY + 12, SY_UR = SY + 32  ,
              SY_CNT = SY + 48, SY_VAL = SY + 64, SY_KG = SY + 80, SY_END = SY + 96;
constexpr int LDS_BYTES = SY_END;
static_assert(LDS_BYTES <= 163840, "lds");
constexpr int G2_RED = 272;
constexpr int EPX = KGS;
constexpr int EPS = KF;
#define LDLY 20
#define WSL 4
constexpr unsigned WS_USED = (unsigned)(WS_HF + (size_t)SL * FF * 2);
constexpr unsigned CODE_BYTES = 49152;
constexpr int CV_NP = 12;
constexpr int G2_NR = 13;
constexpr int cmax(int a, int b) { return a > b ? a : b; }
constexpr int cmin(int a, int b) { return a < b ? a : b; }
constexpr int g2_base(int i) { return (((i % 15) / 5 + 2) % 3) * STG + (i % 5) * 8192; }
constexpr int RED_A = g2_base(15), RED_B = g2_base(20);
constexpr int G2_H0 = 6;
constexpr int g2_issued(int i) { return cmin(28, G2_H0 + (3 * i) / 2); }

__device__ __forceinline__ void lds_setv(unsigned addr, unsigned v) {
  asm volatile("ds_write_b32 %0, %1" :: "v"(addr), "v"(v) : "memory");
}
__device__ __forceinline__ unsigned lds_min4(unsigned addr) {
  u32x4 v;
  asm volatile("ds_read_b128 %0, %1\n\ts_waitcnt lgkmcnt(0)" : "=&v"(v) : "v"(addr) : "memory");
  return (unsigned)__builtin_amdgcn_readfirstlane(min(min(v[0], v[1]), min(v[2], v[3])));
}
__device__ __forceinline__ unsigned lds_get1(unsigned addr) {
  unsigned v;
  asm volatile("ds_read_b32 %0, %1\n\ts_waitcnt lgkmcnt(0)" : "=&v"(v) : "v"(addr) : "memory");
  return (unsigned)__builtin_amdgcn_readfirstlane(v);
}
template <int W>
__device__ __forceinline__ unsigned lds_val(unsigned addr) { return W == 4 ? lds_min4(addr) : lds_get1(addr); }
template <int W>
__device__ __forceinline__ void lds_spin(unsigned addr, unsigned need) {
#pragma unroll 1
  for (int it = 0; it < (1 << 20); ++it) {
    if (lds_val<W>(addr) >= need) break;
    __builtin_amdgcn_s_sleep(1);
  }
}
template <int W>
__device__ __forceinline__ void lds_wait(unsigned addr, unsigned need) {
  if (__builtin_expect(lds_val<W>(addr) < need, 0)) lds_spin<W>(addr, need);
}
template <int NW, bool DRAIN = true>
__device__ __forceinline__ void publish(LAS char* lds, int cnt, char* ws, unsigned off, int nl, unsigned tag, int lane) {
  if (DRAIN) wait_vm<0>();
  unsigned old = 0;
  if (lane == 0) old = __hip_atomic_fetch_add((LAS unsigned*)(lds + SY_CNT + cnt * 4), 1u, __ATOMIC_RELAXED, __HIP_MEMORY_SCOPE_WORKGROUP);
  if (__builtin_amdgcn_readfirstlane(old) == NW - 1 && lane < nl) set_flag(ws, off, tag);
}

template <int PA, int PB>
__device__ __forceinline__ void conv_upper(LAS char* lds, int lane, int hc, int cp, rsrc_t r_s) {
  const unsigned dsk2 = *(const LAS unsigned*)(lds + SY_VAL + 4);
  const float kscale = *(const LAS float*)(lds + SY_VAL + 8 + hc * 4);
  const float dskip = hc ? hi_f(dsk2) : lo_f(dsk2);
  const int G = lane >> 4, q4 = (lane & 15) >> 2, p = lane & 3;
  const LAS char* pa = lds + KF + hc * CV_KSTR + 96 + 2 * (76 + 16 * p - 32 * G - 4 * q4 - 16);
  const LAS char* pb = lds + CV_U + hc * CV_USTR + 160 + 2 * (-76 + 32 * G + 4 * q4 + 4 * p);
  const LAS char* pu = lds + CV_U + hc * CV_USTR + 160 + lane * 2;
  constexpr int J0 = 2 * PA - 1, J1 = 2 * PB, NT = J1 - J0;
  f16x8 A[16], B[32];
  f32x4 ca[NT];
#define CV_A(q) __builtin_bit_cast(f16x8, cat8(trrd(pa + 256 * (q) + 32), trrd(pa + 256 * (q))))
#define CV_B(w) __builtin_bit_cast(f16x8, cat8(trrd(pb + 128 * (w)), trrd(pb + 128 * (w) + 32)))
  B[0] = CV_B(0); B[1] = CV_B(1); B[2] = CV_B(2);
  constexpr int QA = (J0 - 1) / 2;
#pragma unroll
  for (int q = PB - 1; q >= QA; --q) A[q] = CV_A(q);
#pragma unroll
  for (int j = 0; j < NT; ++j) ca[j] = (f32x4){0.f, 0.f, 0.f, 0.f};
  static_for<0, J1>([&](auto wc) {
    constexpr int w = decltype(wc)::value;
    if constexpr (w + 3 < J1) B[w + 3] = CV_B(w + 3);
    if constexpr ((w & 1) == 0 && QA - 1 - w / 2 >= 0) A[QA - 1 - w / 2] = CV_A(QA - 1 - w / 2);
#pragma unroll
    for (int q = PB - 1; q >= 0; --q) {
      const int j = w + 2 * q;
      if (j >= J0 && j < J1) ca[j - J0] = __builtin_amdgcn_mfma_f32_16x16x32_f16(A[q], B[w], ca[j - J0], 0, 0, 0);
    }
    SCHED_FENCE();
  });
  float c3 = 0.f, c2 = 0.f, c1 = 0.f, yv[NT];
#pragma unroll
  for (int j = 0; j < NT; ++j) {
    const f32x4 a = ca[j];
    const float h3 = a[3];
    const float h2 = a[2] + shr_carry(c3, h3);
    const float h1 = a[1] + shr_carry(c2, h2);
    yv[j] = a[0] + shr_carry(c1, h1);
    c3 = dpp_mov<0x13C>(h3); c2 = dpp_mov<0x13C>(h2); c1 = dpp_mov<0x13C>(h1);
  }
#pragma unroll
  for (int jp = PA; jp < PB; ++jp) {
    const float u0 = (float)*(const LAS _Float16*)(pu + jp * 256), u1 = (float)*(const LAS _Float16*)(pu + jp * 256 + 128);
    const unsigned yq = pk_bf16(yv[2 * jp - J0] * kscale, yv[2 * jp + 1 - J0] * kscale);
    const unsigned uq = pk_bf16(u0 * dskip, u1 * dskip);
    st32c(pk_bf16(lo_f(yq) + lo_f(uq), hi_f(yq) + hi_f(uq)), lane * 4u, r_s, (unsigned)((cp * 2 + hc) * 4096 + jp * 256));
  }
#undef CV_A
#undef CV_B
}

#if defined(__HIP_DEVICE_COMPILE__)
__attribute__((target("no-packed-fp32-ops")))
#endif
__global__ __launch_bounds__(512, 2) void fused_kernel(
    const unsigned short* __restrict__ x, const unsigned short* __restrict__ A_re,
    const unsigned short* __restrict__ A_im, const unsigned short* __restrict__ B_re,
    const unsigned short* __restrict__ B_im, const unsigned short* __restrict__ C_re,
    const unsigned short* __restrict__ C_im, const unsigned* __restrict__ log_dt2,
    const unsigned* __restrict__ dsk2p, const unsigned short* __restrict__ wg,
    const unsigned short* __restrict__ wu, const unsigned short* __restrict__ wd,
    char* ws, unsigned short* __restrict__ out) {
  extern __shared__ __attribute__((aligned(16))) char smem[];
  unsigned long long pc0;
  asm volatile("s_getpc_b64 %0" : "=s"(pc0));
  LAS char* lds = (LAS char*)smem;
  const unsigned ldsb = (unsigned)(uintptr_t)lds;
  const int tid = threadIdx.x, lane = tid & 63;
  const int w8 = __builtin_amdgcn_readfirstlane(tid >> 6);
  const int wv = w8 & 3;
  STAMP_DECL; BW_DECL; STAMP_RT(14); STAMP(0);
  const int bid = blockIdx.x;
  const int xcd = bid & 7, bi = bid >> 3;
  unsigned pf0 = 0;
  if (w8 == 5) ld32(pf0, (unsigned)((bi * 16 + (lane & 15)) * 128), mk_rsrc((const void*)pc0, CODE_BYTES), 0u);
  asm volatile("" :: "s"(x), "s"(A_re), "s"(A_im), "s"(B_re), "s"(B_im), "s"(C_re), "s"(C_im), "s"(log_dt2),
               "s"(dsk2p), "s"(wg), "s"(wu), "s"(wd), "s"(ws), "s"(out));
  const int mt = (xcd >> 2) * 8 + (bi >> 2);
  const int ft = (xcd & 3) * 4 + (bi & 3);
  const int rot = ((bi >> 2) + 2 * (bi & 3)) & 7;
  const int cp = xcd * 32 + bi;
  const int tt = xcd * 4 + (bi >> 3);
  const int dtile = bi & 7;
  const int rot2 = ((bi >> 3) * 7 + dtile * 3) % 28;

  const rsrc_t r_x = mk_rsrc(x, SL * DM * 2);
  const rsrc_t r_s = mk_rsrc(ws + WS_ST, SL * DM * 2);
  const rsrc_t r_fl = mk_rsrc(ws + WS_FLAG, FL_BYTES);
#define G2_KS(w, i) ((unsigned)((w) * 28 + (((i) + rot2) >= 28 ? (i) + rot2 - 28 : (i) + rot2)))
#define XO(t_) ((unsigned)((tt * 64 + ((t_) >> 3)) * (DM * 2) + dtile * 128 + ((t_) & 7) * 16))
#define EPI_X() do { int l_ = lane; PIN(l_); u32x4 xw_[2]; \
    _Pragma("unroll") for (int j = 0; j < 2; ++j) ld128(xw_[j], XO(wv * 128 + j * 64 + l_), r_x, 0u); \
    lds_wait<4>(ldsb + SY_UR, 1u); wait_vm<0>(); \
    _Pragma("unroll") for (int j = 0; j < 2; ++j) { PIN(xw_[j]); *(LAS u32x4*)(lds + EPX + (wv * 128 + j * 64 + l_) * 16) = xw_[j]; } } while (0)
#define EPI_S() do { int l_ = lane; PIN(l_); u32x4 sw_[4]; \
    const unsigned so = (unsigned)((dtile * 64 + wv * 16 + (l_ >> 4)) * 4096 + (tt >> 1) * 256 + (l_ & 15) * 16); \
    _Pragma("unroll") for (int j = 0; j < 4; ++j) ld128c(sw_[j], so, r_s, (unsigned)(j * 16384)); \
    wait_vm<0>(); \
    _Pragma("unroll") for (int j = 0; j < 4; ++j) { PIN(sw_[j]); \
      *(LAS u32x4*)(lds + EPS + (wv * 16 + j * 4 + (l_ >> 4)) * 256 + ((l_ + wv * 2 + (j >> 1)) & 15) * 16) = sw_[j]; } } while (0)

  const rsrc_t r_wg = mk_rsrc(wg, DM * FF * 2), r_wu = mk_rsrc(wu, DM * FF * 2);
  unsigned woff[7], xoff[4];
  auto g1_stage = [&](int s) __attribute__((always_inline)) {
    const unsigned st = s == 0 ? 0u : (unsigned)(1 + (s - 1 + rot) % 7), lb = ldsb + (unsigned)((s % 3) * STG);
#pragma unroll
    for (int i = 0; i < 4; ++i)
      dma16x4(lb + XOFF + i * 4096, xoff[0], xoff[1], xoff[2], xoff[3], r_x, (unsigned)((mt * 128 + i * 32) * 1024) + st * 128u);
#pragma unroll
    for (int h = 0; h < 2; ++h) {
      const unsigned wso = (unsigned)(ft * 224) + (st * 64u + h * 32u) * (FF * 2);
      dma16x4(lb + h * 7168, woff[0], woff[1], woff[2], woff[3], r_wg, wso);
      dma16x3(lb + h * 7168 + 4096, woff[4], woff[5], woff[6], r_wg, wso);
      dma16x4(lb + W_UPOFF + h * 7168, woff[0], woff[1], woff[2], woff[3], r_wu, wso);
      dma16x3(lb + W_UPOFF + h * 7168 + 4096, woff[4], woff[5], woff[6], r_wu, wso);
    }
  };
  unsigned prm[6], ldt2, ep, dsk2;
  u32x4 xr[4];
  if (w8 < 4) {
    const unsigned po = (unsigned)((cp * 2 + (wv & 1)) * NM * 2);
    ld32(prm[0], lane * 4u, mk_rsrc(A_re, DM * NM * 2), po);
    ld32(prm[1], lane * 4u, mk_rsrc(A_im, DM * NM * 2), po);
    ld32(prm[2], lane * 4u, mk_rsrc(B_re, DM * NM * 2), po);
    ld32(prm[3], lane * 4u, mk_rsrc(B_im, DM * NM * 2), po);
    ld32(prm[4], lane * 4u, mk_rsrc(C_re, DM * NM * 2), po);
    ld32(prm[5], lane * 4u, mk_rsrc(C_im, DM * NM * 2), po);
    ld32(ldt2, 0u * lane, mk_rsrc(log_dt2, DM * 2), (unsigned)(cp * 4));
  } else if (w8 >= 6) {
    const unsigned vo = (unsigned)((lane >> 4) * 1024 + (lane & 15) * 16);
#pragma unroll
    for (int i = 0; i < 4; ++i) ld128(xr[i], vo, r_x, (unsigned)((bid * 8 + (wv - 2) * 4) * 1024 + i * 256));
    ld32c(ep, 0u * lane, r_fl, FL_EP);
    ld32(dsk2, 0u * lane, mk_rsrc(dsk2p, DM * 2), (unsigned)(cp * 4));
  } else if (w8 == 4) {
    __builtin_amdgcn_s_setprio(3);
#pragma unroll
    for (int i = 0; i < 4; ++i)
      xoff[i] = (unsigned)((lane >> 3) * 1024 + (((lane & 7) ^ ((lane >> 4) + 4 * (i & 1))) * 16) + i * 7168);
#pragma unroll
    for (int i = 0; i < 7; ++i) {
      const int c = i * 64 + lane, row = c / 14, col = c - row * 14;
      woff[i] = (unsigned)(row * (FF * 2) + col * 16 - (i & 3) * 1024);
    }
    __builtin_amdgcn_s_sleep(LDLY);
    g1_stage(0);
  }
  asm volatile("; first loads issued" ::: "memory");
  if (w8 < 4) {
    const rsrc_t r_h = mk_rsrc(ws + WS_HF, SL * FF * 2);
    {
    const int hc = wv & 1, m = wv >> 1;
    if (tid < 4) *(LAS u32x4*)(lds + SY + tid * 16) = (u32x4){0u, 0u, 0u, tid == 2 ? 1u : 0u};
    __builtin_amdgcn_s_setprio(2);
    {
      wait_vm<0>();
      PIN(ldt2);
      const float ldt = hc ? hi_f(ldt2) : lo_f(ldt2);
      const float L2E = 1.4426950408889634f, L2E_LO = (float)(1.4426950408889634074 - (double)1.4426950408889634f);
      const float th = ldt * L2E;
      const float tl = fmaf(ldt, L2E, -th) + ldt * L2E_LO;
      const float e0 = __builtin_amdgcn_exp2f(th);
      const float dt = rbf(fmaf(e0 * 0.6931471805599453f, tl, e0));
      wait_vm<0>();
#pragma unroll
      for (int i = 0; i < 6; ++i) PIN(prm[i]);
      float av[2], bv[2];
      f32x2 wq[2];
#pragma unroll
      for (int m = 0; m < 2; ++m) {
        av[m] = dt * (m ? hi_f(prm[0]) : lo_f(prm[0]));
        bv[m] = dt * (m ? hi_f(prm[1]) : lo_f(prm[1]));
        const f32x2 db = {dt * (m ? hi_f(prm[2]) : lo_f(prm[2])), dt * (m ? hi_f(prm[3]) : lo_f(prm[3]))};
        const f32x2 cc = {m ? hi_f(prm[4]) : lo_f(prm[4]), m ? hi_f(prm[5]) : lo_f(prm[5])};
        wq[m] = cmul(cc, db);
      }
      const float amax = fmaxf(wave_max(fmaxf(av[0], av[1])), 0.f);
      const float g2 = amax * L2E;
      const float wm = wave_max(fmaxf(fmaxf(fabsf(wq[0].x), fabsf(wq[0].y)), fmaxf(fabsf(wq[1].x), fabsf(wq[1].y))));
      int we = __builtin_amdgcn_frexp_expf(wm);
      we = we < -100 ? -100 : we;
      const float wsc = __builtin_amdgcn_ldexpf(1.0f, -we);
      f32x4 kacc[3];
#pragma unroll
      for (int t = 0; t < 3; ++t) kacc[t] = (f32x4){0.f, 0.f, 0.f, 0.f};
      const int kgs = (m ? CV_U : KGS) + hc * 4096;
      LAS char* tw[4];
#pragma unroll
      for (int c = 0; c < 4; ++c) tw[c] = lds + kgs + lane * 64 + ((c ^ ((lane >> 1) & 3)) << 4);
      const LAS char* tr[2];
      {
        const int G = lane >> 4, q4 = (lane & 15) >> 2, p = lane & 3;
#pragma unroll
        for (int t = 0; t < 2; ++t)
          tr[t] = lds + kgs + (4 * G + 2 * t + (q4 >> 1)) * 64 + ((((q4 & 1) * 2 + (p >> 1)) ^ ((2 * G + t) & 3)) << 4) + (p & 1) * 8;
      }
      auto put16 = [&](const f32x2 (&e)[16]) __attribute__((always_inline)) {
#pragma unroll
        for (int h = 0; h < 2; ++h) {
          u32x4 re, im;
#pragma unroll
          for (int j = 0; j < 4; ++j) {
            re[j] = pk_f16(e[8 * h + 2 * j].x, e[8 * h + 2 * j + 1].x);
            im[j] = pk_f16(e[8 * h + 2 * j].y, e[8 * h + 2 * j + 1].y);
          }
          *(LAS u32x4*)tw[h] = re;
          *(LAS u32x4*)tw[2 + h] = im;
        }
      };
#define KG_FRAG(s) __builtin_bit_cast(f16x8, cat8(trrd(tr[0] + (s) * 1024), trrd(tr[1] + (s) * 1024)))
#define KG_RD() do { _Pragma("unroll") for (int s = 0; s < 4; ++s) pf[s] = KG_FRAG(s); SCHED_FENCE(); } while (0)
#define KG_MMA(t) do { SCHED_FENCE(); _Pragma("unroll") for (int s = 0; s < 4; ++s) \
        kacc[t] = __builtin_amdgcn_mfma_f32_16x16x32_f16(pf[s], qf[s], kacc[t], 0, 0, 0); } while (0)
      {
        const float a = (m ? av[1] : av[0]) - amax, b = m ? bv[1] : bv[0];
        f32x2 w = m ? wq[1] : wq[0];
        w.x *= wsc; w.y *= -wsc;
        f32x2 e[16];
        {
          const f32x2 z16 = cexp_l(a, b, 16.f);
          e[0] = (f32x2){1.f, 0.f}; e[1] = z16; e[2] = cmul(z16, z16); e[3] = cmul(e[2], z16);
          const f32x2 z64 = cmul(e[2], e[2]);
#pragma unroll
          for (int c = 4; c < 16; ++c) e[c] = cmul(e[c - 4], z64);
          put16(e);
        }
        f16x8 qf[4], pf[4];
#pragma unroll
        for (int s = 0; s < 4; ++s) qf[s] = KG_FRAG(s);
        SCHED_FENCE();
        const f32x2 z1 = cexp_l(a, -b, 1.f);
        const f32x2 z2 = cmul(z1, z1), z4 = cmul(z2, z2);
        e[0] = w; e[1] = cmul(w, z1); e[2] = cmul(w, z2); e[3] = cmul(e[1], z2);
#pragma unroll
        for (int c = 4; c < 16; ++c) e[c] = cmul(e[c - 4], z4);
        put16(e);
        KG_RD();
        const f32x2 z256 = cexp_l(a, -b, 256.f);
        const f32x2 z512 = cmul(z256, z256);
        const f32x2 w2 = cmul(w, z512);
        e[0] = cmul(w, z256); e[1] = cmul(e[0], z2); e[8] = w2; e[12] = cmul(e[0], z512);
        const f32x2 w3 = e[12];
#pragma unroll
        for (int i = 1; i < 4; ++i) {
          e[2 * i] = cmul(e[2 * i - 2], z4); e[2 * i + 1] = cmul(e[2 * i - 1], z4);
          e[8 + i] = cmul(e[7 + i], z4); e[12 + i] = cmul(e[11 + i], z4);
        }
        KG_MMA(0);
        put16(e);
        KG_RD();
        const f32x2 z8 = cmul(z4, z4);
        e[0] = cmul(w2, z512); e[2] = cmul(w3, z512); e[4] = cmul(e[0], z512); e[6] = cmul(e[2], z512);
        e[8] = cmul(e[4], z512);
#pragma unroll
        for (int i = 0; i < 4; ++i) e[2 * i + 1] = cmul(e[2 * i], z8);
#pragma unroll
        for (int i = 9; i < 16; ++i) e[i] = (f32x2){0.f, 0.f};
        KG_MMA(1);
        put16(e);
        KG_RD();
        KG_MMA(2);
      }
#pragma unroll
      for (int t = 0; t < 3; ++t) *(LAS f32x4*)(lds + (m ? KF + hc * CV_KSTR : KGS + hc * 4096) + t * 1024 + lane * 16) = kacc[t];
      if (m) *(LAS f32x2*)(lds + SY_KG + hc * 8) = (f32x2){g2, (float)we};
    }
    }
    f32x16 acc[7];
    f32x4 csa[2];
    unsigned tag;
    {
      const int G = lane >> 4;
      const LAS char* wa0 = lds + (G & 1) * W_UPOFF + (8 * (G >> 1) + ((lane & 15) >> 2)) * 224 + (lane & 3) * 8;
      const LAS char* wa[7];
#pragma unroll
      for (int T = 0; T < 7; ++T) wa[T] = wa0 + 32 * ((T + 2 * wv) % 7);
      const LAS char* xa[4];
      {
        const int row = 32 * wv + (lane & 31), sw = (row >> 1) & 7;
#pragma unroll
        for (int ks = 0; ks < 4; ++ks) xa[ks] = lds + XOFF + row * 128 + (((2 * ks + (lane >> 5)) ^ sw) << 4);
      }
      constexpr int PD = 3;
      bf16x8 Af[196], Bf[32];
      const unsigned o1 = ((lane & 15) == 4 * ((lane >> 4) & 1)) ? 0x3f803f80u : 0u;
      u32x4 onesq = {o1, o1, o1, o1};
      PIN(onesq);
      const bf16x8 ones = __builtin_bit_cast(bf16x8, onesq);
#define A_READ(i, T) __builtin_bit_cast(bf16x8, cat8(trrd(wa[T] + (((i) >> 2) % 3) * STG + (16 * ((i) & 3)) * 224), \
                                                      trrd(wa[T] + (((i) >> 2) % 3) * STG + (16 * ((i) & 3) + 4) * 224)))
#define B_READ(i) (*(const LAS bf16x8*)(xa[(i) & 3] + (((i) >> 2) % 3) * STG))
#pragma unroll
      for (int T = 0; T < 7; ++T) acc[T] = (f32x16){};
      csa[0] = (f32x4){0.f, 0.f, 0.f, 0.f}; csa[1] = csa[0];
      BARX(bar_lgkm);
      STAMP(1);
      tag = lds_get1(ldsb + SY_VAL);
      Bf[0] = B_READ(0);
#pragma unroll
      for (int T = 0; T < PD; ++T) Af[T] = A_READ(0, T);
      bf16x8 Al[7][4];
      u32x2 stg;
      float cs[7];
      static_for<0, 196>([&](auto nc) {
        constexpr int n = decltype(nc)::value, i = n / 7, T = n % 7;
        constexpr int m = n + PD, im = m / 7, Tm = m % 7;
        if constexpr (n == 70) ld64c(stg, (unsigned)((lane & 31) * 8), r_fl, FL_STAT + (unsigned)((mt * 128 + wv * 32) * 8));
        if constexpr (n == 27 * 7 + 2) {
          if (lane < 32) {
#pragma unroll
            for (int j = 0; j < 2; ++j) *(LAS float*)(lds + CSUM + ((j + 2 * wv) % 7) * 128 + lane * 4) = csa[j][0];
          }
        }
        if constexpr (Tm == 0 && (im & 3) == 0) BARX(bar_lgkm);
        if constexpr (m < 196) {
          if constexpr (Tm == 0) Bf[im] = B_READ(im);
          Af[m] = A_READ(im, Tm);
        } else if constexpr (m == 196) {
#pragma unroll
          for (int k = 28; k < 32; ++k) Bf[k] = B_READ(k);
        } else if constexpr (m == 197) {
#pragma unroll
          for (int t = 0; t < 7; ++t) cs[t] = *(const LAS float*)(lds + CSUM + ((t + 2 * wv) % 7) * 128 + (lane & 31) * 4);
        } else {
#pragma unroll
          for (int k = 0; k < 4; ++k) Al[0][k] = A_READ(28 + k, 0);
        }
        acc[T] = __builtin_amdgcn_mfma_f32_32x32x16_bf16(Af[n], Bf[i], acc[T], 0, 0, 0);
        if constexpr (T < 2)
          csa[T] = __builtin_amdgcn_mfma_f32_16x16x32_bf16(ones, Af[n], csa[T], 0, 0, 0);
        SCHED_FENCE();
      });
      STAMP(5);
      wait_vm<0>();
      PIN(stg);
      if (__builtin_expect(!wave_all(stg[1] == tag), 0)) {
#pragma unroll 1
        for (int it = 0; it < (1 << 18); ++it) {
          ld64c(stg, (unsigned)((lane & 31) * 8), r_fl, FL_STAT + (unsigned)((mt * 128 + wv * 32) * 8));
          wait_vm<0>();
          PIN(stg);
          if (wave_all(stg[1] == tag)) break;
          __builtin_amdgcn_s_sleep(2);
        }
      }
      const unsigned stw = stg[0];
      const float mb = lo_f(stw), sd = hi_f(stw);
#pragma unroll
      for (int k = 28; k < 32; ++k) {
        u32x4 v = __builtin_bit_cast(u32x4, Bf[k]);
#pragma unroll
        for (int j = 0; j < 4; ++j) v[j] = pk_bf16(lo_f(v[j]) - mb, hi_f(v[j]) - mb);
        Bf[k] = __builtin_bit_cast(bf16x8, v);
      }
      const unsigned nm = (stw & 0xffffu) ^ 0x8000u;
      u32x4 bq = {nm | (nm << 16), nm, 0u, 0u};
      if (lane >= 32) bq = (u32x4){0u, 0u, 0u, 0u};
      const float ga = -1.4426950408889634f * __builtin_amdgcn_rcpf(sd);
      const unsigned hsb = (unsigned)(((mt * 4 + wv) * 112 + ft * 7) * 1024);
      STAMP(6);
      static_for<0, 8>([&](auto tc) {
        constexpr int T = decltype(tc)::value;
        u32x4 o;
        if constexpr (T < 7) {
          const float c = cs[T];
          const float ch = rbf(c), cm = rbf(c - ch), cl = (c - ch) - cm;
          const u32x4 aq = {pk_bf16(ch, cm), pk_bf16(cl, 0.f), 0u, 0u};
          acc[T] = __builtin_amdgcn_mfma_f32_32x32x16_bf16(__builtin_bit_cast(bf16x8, aq), __builtin_bit_cast(bf16x8, bq), acc[T], 0, 0, 0);
          SCHED_FENCE();
        }
#pragma unroll
        for (int j = 0; j < 4; ++j) {
          if constexpr (T + 1 < 7) Al[T + 1][j] = A_READ(28 + j, T + 1);
          if constexpr (T < 7) acc[T] = __builtin_amdgcn_mfma_f32_32x32x16_bf16(Al[T][j], Bf[28 + j], acc[T], 0, 0, 0);
          SCHED_FENCE();
          if constexpr (T >= 1) {
            const float s0 = __builtin_amdgcn_rcpf(fmaf(__builtin_amdgcn_exp2f(acc[T - 1][2 * j] * ga), sd, sd));
            const float s1 = __builtin_amdgcn_rcpf(fmaf(__builtin_amdgcn_exp2f(acc[T - 1][2 * j + 1] * ga), sd, sd));
            o[j] = pk_bf16(s0 * acc[T - 1][8 + 2 * j], s1 * acc[T - 1][8 + 2 * j + 1]);
            PIN(o[j]);
          }
          SCHED_FENCE();
        }
        if constexpr (T >= 1) st128c(o, lane * 16u, r_h, hsb + (unsigned)((T - 1 + 2 * wv) % 7) * 1024u);
      });
    }
    STAMP(7);
    const unsigned ldw = ldsb + SY_LD;
    const LAS char* waT[2];
    {
      const int G = lane >> 4, q4 = (lane & 15) >> 2;
      const LAS char* wa0 = lds + wv * 2048 + (4 * (G >> 1) + q4) * 128 + (G & 1) * 32 + (lane & 3) * 8;
      waT[0] = wa0 + ((q4 >> 1) * 64); waT[1] = wa0 + (((q4 >> 1) ^ 1) * 64);
    }
#define D_READ(i, T) __builtin_bit_cast(bf16x8, cat8(trrd(waT[T] + g2_base(i)), trrd(waT[T] + g2_base(i) + 1024)))
    bf16x8 Wr[28][2];
    lds_wait<1>(ldw, 2u);
    static_for<0, 5>([&](auto ic) { constexpr int i = decltype(ic)::value; Wr[i][0] = D_READ(i, 0); Wr[i][1] = D_READ(i, 1); });
    static_for<5, 10>([&](auto ic) { constexpr int i = decltype(ic)::value; Wr[i][0] = D_READ(i, 0); Wr[i][1] = D_READ(i, 1); });
    wait_vm<0>();
    if (lane < 8) set_flag(ws, FL_H + (unsigned)(((mt * 2 + (wv >> 1)) * 8 + lane) * 256 + (ft * 2 + (wv & 1)) * 4), tag);
    STAMP(8);
    lds_setv(ldsb + SY_MC + wv * 4, 2u);
    if (wv == 0) {
      lds_wait<4>(ldsb + SY_UR, 1u);
      conv_upper<14, 16>(lds, lane, 0, cp, r_s);
    }
    lds_wait<1>(ldw, 3u);
    static_for<10, G2_NR>([&](auto ic) { constexpr int i = decltype(ic)::value; Wr[i][0] = D_READ(i, 0); Wr[i][1] = D_READ(i, 1); });
    lds_setv(ldsb + SY_MC + wv * 4, 3u);
    STAMP(9);
    __builtin_amdgcn_s_setprio(0);
    poll_flags<2>((lane & 1) * 16u, r_fl, FL_H + (unsigned)((tt * 8 + dtile) * 256 + wv * 32), tag);
    if (wv == 0) publish<4>(lds, 2, ws, FL_S + (unsigned)((lane * 8 + (cp >> 5)) * 256 + (cp & 31) * 4), 32, tag, lane);
    __builtin_amdgcn_s_setprio(2);
    STAMP(10);
    u32x4 hb[28][2];
#define HB_LOAD(i) do { ld128c(hb[i][0], lane * 16u, r_h, ((unsigned)(tt * 2) * 112u + G2_KS(wv, i)) * 1024u); \
                        ld128c(hb[i][1], lane * 16u, r_h, ((unsigned)(tt * 2 + 1) * 112u + G2_KS(wv, i)) * 1024u); } while (0)
#pragma unroll
    for (int i = 0; i < G2_H0; ++i) HB_LOAD(i);
    f32x16 acc2[2][2];
    {
#pragma unroll
      for (int a = 0; a < 2; ++a)
#pragma unroll
        for (int b = 0; b < 2; ++b) acc2[a][b] = (f32x16){};
      static_for<0, 28>([&](auto ic) {
        constexpr int i = decltype(ic)::value;
        if constexpr (i == 14) STAMP(11);
        if constexpr (i + 2 == 13) lds_wait<1>(ldw, 6u);
        if constexpr (i + 2 >= G2_NR && i + 2 < 28) { Wr[i + 2][0] = D_READ(i + 2, 0); Wr[i + 2][1] = D_READ(i + 2, 1); }
        wait_vm<2 * (g2_issued(i) - i - 1)>();
        PIN(hb[i][0]); PIN(hb[i][1]);
#pragma unroll
        for (int T = 0; T < 2; ++T)
#pragma unroll
          for (int b = 0; b < 2; ++b)
            acc2[T][b] = __builtin_amdgcn_mfma_f32_32x32x16_bf16(Wr[i][T], __builtin_bit_cast(bf16x8, hb[i][b]), acc2[T][b], 0, 0, 0);
        SCHED_FENCE();
        static_for<g2_issued(i), g2_issued(i + 1)>([&](auto jc) { constexpr int j = decltype(jc)::value; HB_LOAD(j); });
      });
    }
    STAMP(12);
    {
      int l2 = lane; PIN(l2);
      const int tl = l2 & 31, x7 = (tl & 7) ^ (l2 >> 5);
#pragma unroll
      for (int b = 0; b < 2; ++b) {
        const int c8 = 4 * b + (tl >> 3);
        const int o4 = c8 < 5 ? RED_A + c8 * 8192 : RED_B + (c8 - 5) * 8192;
        LAS char* rp = lds + o4 + wv * 2048 + (tl & 7) * 256;
#pragma unroll
        for (int T = 0; T < 2; ++T)
#pragma unroll
          for (int g = 0; g < 4; ++g) {
            const f32x4 v = {acc2[T][b][4 * g], acc2[T][b][4 * g + 1], acc2[T][b][4 * g + 2], acc2[T][b][4 * g + 3]};
            *(LAS f32x4*)(rp + T * 128 + (((2 * g) ^ x7) << 4)) = v;
          }
      }
    }
  } else if (w8 == 4) {
    const rsrc_t r_wd = mk_rsrc(wd, FF * DM * 2);
    unsigned wv4[4];
#pragma unroll
    for (int i = 0; i < 4; ++i)
      wv4[i] = (unsigned)((lane >> 3) * 1024 + (((lane & 7) ^ (((lane >> 4) & 1) << 2)) * 16) + (i >> 1) * 458752 + (i & 1) * 8192 - i * 1024);
    const unsigned ldw = ldsb + SY_LD;
#define G2_GROUP(g) G2_STEPS(5 * (g), cmin(5 * (g) + 5, 28))
#define G2_STEPS(a_, b_) static_for<a_, b_>([&](auto ic) { constexpr int i = decltype(ic)::value; \
        const unsigned so_ = G2_KS(0, i) * 16384u + (unsigned)(dtile * 128); \
        dma16x4(ldsb + g2_base(i), wv4[0], wv4[1], wv4[2], wv4[3], r_wd, so_); \
        dma16x4(ldsb + g2_base(i) + 4096, wv4[0], wv4[1], wv4[2], wv4[3], r_wd, so_ + 2u * 458752u); })
    g1_stage(1);
    wait_vm<44>();
    STAMP(1);
    BARX(bar_only);
#pragma unroll
    for (int k = 0; k < 6; ++k) {
      g1_stage(k + 2);
      wait_vm<44>();
      BARX(bar_only);
    }
    STAMP(2);
    G2_GROUP(0);
    wait_vm<40>();
    BARX(bar_only);
    G2_GROUP(1);
    wait_vm<0>();
    lds_setv(ldw, 2u);
    STAMP(3);
    lds_wait<4>(ldsb + SY_MC, 2u);
    G2_STEPS(10, 15);
    wait_vm<16>();
    lds_setv(ldw, 3u);
    STAMP(4);
    G2_STEPS(15, 25);
    lds_wait<4>(ldsb + SY_MC, 3u);
    G2_GROUP(5);
    wait_vm<0>();
    lds_setv(ldw, 6u);
    STAMP(5);
    __builtin_amdgcn_s_setprio(0);
    EPI_X();
    lds_wait<1>(ldsb + SY_SR, 1u);
    EPI_S();
    STAMP(12);
  } else if (w8 == 5) {
    wait_vm<0>();
    asm volatile("" :: "v"(pf0));
    BARX(bar_only);
    const unsigned tag = lds_get1(ldsb + SY_VAL);
    BARX(bar_only);
    {
      const int c = lane & 15, g = lane >> 4;
      unsigned d[2][3][4];
      float kscale[2];
#pragma unroll
      for (int hc = 0; hc < 2; ++hc) {
        f32x4 kacc[3];
#pragma unroll
        for (int t = 0; t < 3; ++t)
          kacc[t] = *(const LAS f32x4*)(lds + KF + hc * CV_KSTR + t * 1024 + lane * 16) + *(const LAS f32x4*)(lds + KGS + hc * 4096 + t * 1024 + lane * 16);
        const f32x2 gw = *(const LAS f32x2*)(lds + SY_KG + hc * 8);
        const float g2 = gw.x;
        const int we = (int)gw.y;
        if (g2 > 0.f) {
#pragma unroll
          for (int i = 0; i < 4; ++i) {
            const int l0 = 16 * c + 4 * g + i;
            const int l1 = g < 2 ? 256 + 16 * c + 8 * g + 2 * i : 512 + 256 * (g - 2) + 16 * c + 4 * i;
            const int l2 = g < 2 ? 1024 + 256 * (2 * g + (i >> 1)) + 8 * (i & 1) + 16 * c : 2048;
            kacc[0][i] *= __builtin_amdgcn_exp2f(g2 * (float)(l0 - 2048));
            kacc[1][i] *= __builtin_amdgcn_exp2f(g2 * (float)(l1 - 2048));
            kacc[2][i] *= __builtin_amdgcn_exp2f(g2 * (float)(l2 - 2048));
          }
        }
        kscale[hc] = __builtin_amdgcn_ldexpf(1.0f, we - 7) * __builtin_amdgcn_exp2f(g2 * 2048.f);
#pragma unroll
        for (int t = 0; t < 3; ++t)
#pragma unroll
          for (int i = 0; i < 4; ++i) { d[hc][t][i] = pk_f16(kacc[t][i] * 128.f, kacc[t][i] * 128.f); PIN(d[hc][t][i]); }
        BARX(bar_only);
      }
      BARX(bar_only);
      BARX(bar_only);
      BARX(bar_only);
      BARX(bar_only);
      __builtin_amdgcn_s_sleep(WSL);
      __builtin_amdgcn_s_setprio(3);
      STAMP(2);
      *(LAS f32x2*)(lds + SY_VAL + 8) = (f32x2){kscale[0], kscale[1]};
#define MIX(a, b) (((a) & 0xffffu) | ((b) & 0xffff0000u))
#pragma unroll
      for (int hc = 0; hc < 2; ++hc) {
        LAS char* kf = lds + KF + hc * CV_KSTR + 96;
        if (g < 2) {
#pragma unroll
          for (int h = 0; h < 2; ++h) {
            LAS char* p = kf + 2 * (1024 + 256 * (2 * g + h) + 16 * c);
            const unsigned v0 = d[hc][2][2 * h], v1 = d[hc][2][2 * h + 1];
            *(LAS u32x2*)(p - 8) = (u32x2){v0, v0};
            *(LAS u32x4*)(p) = (u32x4){v0, v0, MIX(v0, v1), v1};
            *(LAS u32x2*)(p + 16) = (u32x2){v1, v1};
          }
        } else {
          if (lane == 32) *(LAS u32x2*)(kf + 2 * 2044) = (u32x2){d[hc][2][0], d[hc][2][0]};
        }
        if (g >= 2) {
          LAS char* p = kf + 2 * (512 + 256 * (g - 2) + 16 * c);
          const unsigned v0 = d[hc][1][0], v1 = d[hc][1][1], v2 = d[hc][1][2], v3 = d[hc][1][3];
          *(LAS unsigned*)(p - 4) = v0;
          *(LAS u32x4*)(p) = (u32x4){v0, MIX(v0, v1), v1, v2};
          *(LAS u32x2*)(p + 16) = (u32x2){v2, MIX(v2, v3)};
          *(LAS unsigned*)(p + 24) = v3;
        }
        if (g < 2) {
          LAS char* p = kf + 2 * (256 + 16 * c + 8 * g);
          const unsigned v0 = d[hc][1][0], v1 = d[hc][1][1], v2 = d[hc][1][2], v3 = d[hc][1][3];
          *(LAS unsigned short*)(p - 2) = (unsigned short)v0;
          *(LAS u32x2*)(p) = (u32x2){v0, MIX(v1, v2)};
          *(LAS unsigned*)(p + 8) = v2;
          *(LAS unsigned short*)(p + 12) = (unsigned short)v3;
        }
        *(LAS u32x2*)(kf + 2 * (16 * c + 4 * g)) = (u32x2){MIX(d[hc][0][0], d[hc][0][1]), MIX(d[hc][0][2], d[hc][0][3])};
        if (lane < 6) *(LAS u32x4*)(kf - 96 + lane * 16) = (u32x4){0u, 0u, 0u, 0u};
      }
      lds_setv(ldsb + SY_UR + 8, 1u);
      STAMP(3);
    }
    __builtin_amdgcn_s_setprio(1);
    lds_wait<4>(ldsb + SY_UR, 1u);
    conv_upper<CV_NP, 16>(lds, lane, 1, cp, r_s);
    conv_upper<CV_NP, 14>(lds, lane, 0, cp, r_s);
    STAMP(4);
    publish<4>(lds, 2, ws, FL_S + (unsigned)((lane * 8 + (cp >> 5)) * 256 + (cp & 31) * 4), 32, tag, lane);
    STAMP(5);
    EPI_X();
    lds_wait<1>(ldsb + SY_CNT + 8, 4u);
    poll_flags<4>((lane & 7) * 16u, r_fl, FL_S + (unsigned)((tt * 8 + dtile) * 256), tag);
    lds_setv(ldsb + SY_SR, 1u);
    STAMP(6);
    EPI_S();
    STAMP(12);
  } else {
    const int hc = wv - 2;
    const int dch = cp * 2 + hc;
    const rsrc_t r_xn = mk_rsrc(ws + WS_XNF, SL * DM * 2);
    const int row0 = bid * 8 + hc * 4;
    wait_vm<0>();
    STAMP(1);
#pragma unroll
    for (int i = 0; i < 4; ++i) PIN(xr[i]);
    PIN(ep); PIN(dsk2);
    const unsigned tag = (unsigned)__builtin_amdgcn_readfirstlane(ep) + 1u;
    lds_setv(ldsb + SY_VAL, tag);
    lds_setv(ldsb + SY_VAL + 4, dsk2);
    {
      float f[32];
#pragma unroll
      for (int j = 0; j < 16; ++j) { f[2 * j] = lo_f(xr[j >> 2][j & 3]); f[2 * j + 1] = hi_f(xr[j >> 2][j & 3]); }
      float s0 = 0.f, s1 = 0.f;
#pragma unroll
      for (int j = 0; j < 32; j += 4) { s0 += f[j] + f[j + 1]; s1 += f[j + 2] + f[j + 3]; }
      const float mean = row_sum(s0 + s1) * (1.0f / DM);
      float q0 = 0.f, q1 = 0.f;
#pragma unroll
      for (int j = 0; j < 32; j += 2) {
        const float da = f[j] - mean, db = f[j + 1] - mean;
        q0 = fmaf(da, da, q0); q1 = fmaf(db, db, q1);
      }
      const float var = row_sum(q0 + q1) * (1.0f / DM);
      const float mb = rbf(mean);
      const float sb = rbf(__builtin_amdgcn_sqrtf(rbf(rbf(var) + 1e-5f)));
      if ((lane & 15) == 0)
        st64c((u32x2){pk_bf16(mb, sb), tag}, (unsigned)((lane >> 4) * 8), r_fl, FL_STAT + (unsigned)(row0 * 8));
      const float rs = __builtin_amdgcn_rcpf(sb);
      const unsigned vo = (unsigned)((lane & 15) * 32768 + (lane >> 4) * 16);
      STAMP(2);
#pragma unroll
      for (int i = 0; i < 4; ++i) {
        u32x4 o;
#pragma unroll
        for (int j = 0; j < 4; ++j) {
          const unsigned d = pk_bf16(f[8 * i + 2 * j] - mb, f[8 * i + 2 * j + 1] - mb);
          o[j] = pk_bf16(lo_f(d) * rs, hi_f(d) * rs);
        }
        st128c(o, vo, r_xn, (unsigned)(i * 16 * 32768 + row0 * 16));
      }
    }
    BARX(bar_lgkm);
    wait_vm<0>();
    BARX(bar_only);
    if (hc == 0 && lane < 8) set_flag(ws, FL_ALL + (unsigned)(lane * 4096 + bid * 4), tag);
    BARX(bar_only);
    BARX(bar_only);
    u32x4 vf;
    ld128c(vf, lane * 16u, r_fl, FL_ALL + (unsigned)(xcd * 4096));
    BARX(bar_only);
    wait_vm<0>();
    PIN(vf);
    const bool all_ok = wave_all((vf[0] == tag) & (vf[1] == tag) & (vf[2] == tag) & (vf[3] == tag));
    unsigned uw[16];
    const unsigned usb = (unsigned)((cp >> 2) * 32768 + (cp & 3) * 4 + hc * 16384);
    if (!all_ok) {
      for (int r = 5; r <= 7; ++r) BARX(bar_only);
      poll_flags<4>(lane * 16u, r_fl, FL_ALL + (unsigned)(xcd * 4096), tag);
    }
#pragma unroll
    for (int m = 0; m < 16; ++m) ld32c(uw[m], lane * 16u, r_xn, usb + (unsigned)m * 1024u);
    if (lane == 0 && bid == 0 && hc == 0) set_flag(ws, FL_EP, tag);
    if (all_ok) { BARX(bar_only); BARX(bar_only); }
    wait_vm<0>();
    {
      const bool od = lane & 1;
      LAS char* up = lds + CV_U + 160 + hc * 2048 + (od ? 128 : 0) + (lane >> 1) * 4;
#pragma unroll
      for (int m = 0; m < 16; m += 2) {
        PIN(uw[m]); PIN(uw[m + 1]);
        const unsigned a = pk_f16(lo_f(uw[m]), hi_f(uw[m])), b = pk_f16(lo_f(uw[m + 1]), hi_f(uw[m + 1]));
        const unsigned y = (unsigned)__builtin_amdgcn_update_dpp(0, (int)(od ? a : b), 0xB1, 0xf, 0xf, true);
        const unsigned f0 = od ? y : a, f1 = od ? b : y;
        uw[m] = __builtin_amdgcn_perm(f1, f0, 0x05040100u);
        uw[m + 1] = __builtin_amdgcn_perm(f1, f0, 0x07060302u);
        PIN(uw[m]); PIN(uw[m + 1]);
      }
      if (all_ok) BARX(bar_only);
      __builtin_amdgcn_s_sleep(WSL);
      __builtin_amdgcn_s_setprio(3);
      if (lane < 10) *(LAS u32x4*)(lds + CV_U + hc * CV_USTR + lane * 16) = (u32x4){0u, 0u, 0u, 0u};
#pragma unroll
      for (int m = 0; m < 16; m += 2) {
        *(LAS unsigned*)(up + m * 128) = uw[m];
        *(LAS unsigned*)(up + CV_USTR + m * 128) = uw[m + 1];
      }
      lds_setv(ldsb + SY_UR + hc * 4, 1u);
    }
    STAMP(3);
    __builtin_amdgcn_s_setprio(1);
    lds_wait<4>(ldsb + SY_UR, 1u);
    const float kscale = *(const LAS float*)(lds + SY_VAL + 8 + hc * 4);
    STAMP(6);
    {
      const float dskip = hc ? hi_f(dsk2) : lo_f(dsk2);
      const int G = lane >> 4, q4 = (lane & 15) >> 2, p = lane & 3;
      const LAS char* pa = lds + KF + hc * CV_KSTR + 96 + 2 * (76 + 16 * p - 32 * G - 4 * q4 - 16);
      const LAS char* pb = lds + CV_U + hc * CV_USTR + 160 + 2 * (-76 + 32 * G + 4 * q4 + 4 * p);
      const LAS char* pu = lds + CV_U + hc * CV_USTR + 160 + lane * 2;
      f16x8 A[16], B[32];
#define CV_A(q) __builtin_bit_cast(f16x8, cat8(trrd(pa + 256 * (q) + 32), trrd(pa + 256 * (q))))
#define CV_B(w) __builtin_bit_cast(f16x8, cat8(trrd(pb + 128 * (w)), trrd(pb + 128 * (w) + 32)))
      A[0] = CV_A(0); B[0] = CV_B(0); B[1] = CV_B(1);
      A[1] = CV_A(1); B[2] = CV_B(2); B[3] = CV_B(3);
      f32x4 ca[2][2];
      _Float16 us[2][2];
      unsigned so16[16];
      float c3 = 0.f, c2 = 0.f, c1 = 0.f;
      auto tile_y = [&](const f32x4 a) __attribute__((always_inline)) {
        const float h3 = a[3];
        const float h2 = a[2] + shr_carry(c3, h3);
        const float h1 = a[1] + shr_carry(c2, h2);
        const float y = a[0] + shr_carry(c1, h1);
        c3 = dpp_mov<0x13C>(h3); c2 = dpp_mov<0x13C>(h2); c1 = dpp_mov<0x13C>(h1);
        return y * kscale;
      };
      static_for<0, CV_NP + 1>([&](auto jc) {
        constexpr int jj = decltype(jc)::value;
        if constexpr (jj + 2 < CV_NP) { A[jj + 2] = CV_A(jj + 2); B[2 * jj + 4] = CV_B(2 * jj + 4); B[2 * jj + 5] = CV_B(2 * jj + 5); }
        if constexpr (jj < CV_NP) {
          us[jj & 1][0] = *(const LAS _Float16*)(pu + jj * 256); us[jj & 1][1] = *(const LAS _Float16*)(pu + jj * 256 + 128);
          f32x4 a0 = {0.f, 0.f, 0.f, 0.f}, a1 = {0.f, 0.f, 0.f, 0.f};
#pragma unroll
          for (int q = jj; q >= 0; --q) {
            a0 = __builtin_amdgcn_mfma_f32_16x16x32_f16(A[q], B[2 * (jj - q)], a0, 0, 0, 0);
            a1 = __builtin_amdgcn_mfma_f32_16x16x32_f16(A[q], B[2 * (jj - q) + 1], a1, 0, 0, 0);
          }
          ca[jj & 1][0] = a0; ca[jj & 1][1] = a1;
        }
        if constexpr (jj >= 1) {
          const float y0 = tile_y(ca[(jj - 1) & 1][0]), y1 = tile_y(ca[(jj - 1) & 1][1]);
          const unsigned yq = pk_bf16(y0, y1);
          const unsigned uq = pk_bf16((float)us[(jj - 1) & 1][0] * dskip, (float)us[(jj - 1) & 1][1] * dskip);
          so16[jj - 1] = pk_bf16(lo_f(yq) + lo_f(uq), hi_f(yq) + hi_f(uq));
        }
        if constexpr (jj >= 1 && jj < CV_NP) {
#pragma unroll
          for (int k = 0; k < 2 * (jj + 1); ++k) {
            __builtin_amdgcn_sched_group_barrier(0x8, 1, 0);
            __builtin_amdgcn_sched_group_barrier(0x100, 1, 0);
            __builtin_amdgcn_sched_group_barrier(0x2, jj < 7 ? 4 : 2, 0);
          }
        }
        SCHED_FENCE();
        if constexpr (jj >= 1) st32c(so16[jj - 1], lane * 4u, r_s, (unsigned)(dch * 4096 + (jj - 1) * 256));
      });
    }
    STAMP(7);
    publish<4>(lds, 2, ws, FL_S + (unsigned)((lane * 8 + (cp >> 5)) * 256 + (cp & 31) * 4), 32, tag, lane);
    STAMP(9);
    EPI_X();
    lds_wait<1>(ldsb + SY_SR, 1u);
    EPI_S();
    STAMP(12);
  }
  bar_lgkm();
  {
    int t2 = tid; PIN(t2);
    const u32x4 xv = *(const LAS u32x4*)(lds + EPX + t2 * 16);
    unsigned sv[8];
#pragma unroll
    for (int j = 0; j < 8; ++j) sv[j] = *(const LAS unsigned*)(lds + EPS + ((t2 & 7) * 8 + j) * 256 + (((t2 >> 5) + (t2 & 7)) & 15) * 16 + ((t2 >> 3) & 3) * 4);
    const int rb = w8 < 5 ? RED_A + w8 * 8192 : RED_B + (w8 - 5) * 8192;
    const int tr7 = (t2 >> 3) & 7;
    const LAS char* rp = lds + rb + tr7 * 256 + ((t2 >> 2) & 1) * 128;
    const bool odd = tt & 1;
    u32x4 o;
#pragma unroll
    for (int i = 0; i < 2; ++i) {
      const int ch = (((t2 & 3) * 2 + i) ^ tr7) << 4;
      f32x4 v = *(const LAS f32x4*)(rp + ch);
#pragma unroll
      for (int w = 1; w < 4; ++w) v += *(const LAS f32x4*)(rp + w * 2048 + ch);
#pragma unroll
      for (int jj = 0; jj < 2; ++jj) {
        const unsigned fp = pk_bf16(v[2 * jj], v[2 * jj + 1]);
        const float f0 = lo_f(fp), f1 = hi_f(fp);
        const float g0 = f0 * __builtin_amdgcn_rcpf(1.0f + __builtin_amdgcn_exp2f(f0 * -1.4426950408889634f));
        const float g1 = f1 * __builtin_amdgcn_rcpf(1.0f + __builtin_amdgcn_exp2f(f1 * -1.4426950408889634f));
        const unsigned gp = pk_bf16(g0, g1);
        const int j = 4 * i + 2 * jj;
        const float s0 = odd ? hi_f(sv[j]) : lo_f(sv[j]), s1 = odd ? hi_f(sv[j + 1]) : lo_f(sv[j + 1]);
        const unsigned sg = pk_bf16(s0 * lo_f(gp), s1 * hi_f(gp));
        const unsigned xw = xv[2 * i + jj];
        o[2 * i + jj] = pk_bf16(lo_f(xw) + lo_f(sg), hi_f(xw) + hi_f(sg));
      }
    }
    st128c(o, XO(t2), mk_rsrc(out, SL * DM * 2), 0u);
  }
  STAMP(13); STAMP_RT(15);
  STAMP_OUT(0);
  BW_OUT();
}

extern "C" void kernel_launch(void* const* d_in, const int* in_sizes, int n_in, void* d_out,
                              int out_size, void* d_ws, size_t ws_size, hipStream_t stream) {
  (void)hipFuncSetAttribute((const void*)fused_kernel, hipFuncAttributeMaxDynamicSharedMemorySize, LDS_BYTES);
  fused_kernel<<<256, 512, LDS_BYTES, stream>>>(
      (const unsigned short*)d_in[0], (const unsigned short*)d_in[1], (const unsigned short*)d_in[2],
      (const unsigned short*)d_in[3], (const unsigned short*)d_in[4], (const unsigned short*)d_in[5],
      (const unsigned short*)d_in[6], (const unsigned*)d_in[7], (const unsigned*)d_in[8],
      (const unsigned short*)d_in[10], (const unsigned short*)d_in[11], (const unsigned short*)d_in[12],
      (char*)d_ws, (unsigned short*)d_out);
}
```

```cpp
#include <hip/hip_runtime.h>
#include <hip/hip_bf16.h>
#include <stdint.h>
#include <type_traits>

typedef __attribute__((ext_vector_type(4))) short s16x4;
typedef __attribute__((ext_vector_type(8))) short s16x8;
typedef __attribute__((ext_vector_type(8))) _Float16 f16x8;
typedef __attribute__((ext_vector_type(8))) __bf16 bf16x8;
typedef __attribute__((ext_vector_type(2))) float f32x2;
typedef __attribute__((ext_vector_type(4))) float f32x4;
typedef __attribute__((ext_vector_type(16))) float f32x16;
typedef __attribute__((ext_vector_type(4))) unsigned u32x4;
typedef __attribute__((ext_vector_type(3))) unsigned u32x3;
typedef __attribute__((ext_vector_type(2))) unsigned u32x2;
typedef __attribute__((ext_vector_type(2))) __bf16 bf16x2;
typedef __attribute__((ext_vector_type(2))) _Float16 f16x2;
#define LAS __attribute__((address_space(3)))
#define GAS __attribute__((address_space(1)))
typedef __amdgpu_buffer_rsrc_t rsrc_t;

constexpr int SL = 2048;
constexpr int DM = 512;
constexpr int NM = 128;
constexpr int FF = 1792;

constexpr size_t WS_XNF = 0;
constexpr size_t WS_ST = 2 * (size_t)SL * DM * 2;
constexpr size_t WS_HF = WS_ST + (size_t)SL * DM * 2;
constexpr size_t WS_FLAG = 2 * (size_t)SL * DM;
constexpr unsigned FL_ALL = 0, FL_STAT = 65536, FL_H = 131072, FL_S = 196608, FL_EP = 262144, FL_BYTES = 262400;

template <int I, int N, class F>
__device__ __forceinline__ void static_for(F&& f) {
  if constexpr (I < N) { f(std::integral_constant<int, I>{}); static_for<I + 1, N>(f); }
}

__device__ __forceinline__ unsigned pk_bf16(float lo, float hi) {
  f32x2 v = {lo, hi};
  return __builtin_bit_cast(unsigned, __builtin_convertvector(v, bf16x2));
}
__device__ __forceinline__ unsigned pk_f16(float lo, float hi) {
  f32x2 v = {lo, hi};
  return __builtin_bit_cast(unsigned, __builtin_convertvector(v, f16x2));
}
__device__ __forceinline__ float lo_f(unsigned p) { return __uint_as_float(p << 16); }
__device__ __forceinline__ float hi_f(unsigned p) { return __uint_as_float(p & 0xffff0000u); }
__device__ __forceinline__ float rbf(float x) { return lo_f(pk_bf16(x, x)); }

template <int CTRL>
__device__ __forceinline__ float dpp_mov(float v) {
  return __int_as_float(__builtin_amdgcn_update_dpp(0, __float_as_int(v), CTRL, 0xf, 0xf, true));
}
__device__ __forceinline__ float shr_carry(float c, float v) {
  return __builtin_bit_cast(float, __builtin_amdgcn_update_dpp(__builtin_bit_cast(int, c), __builtin_bit_cast(int, v), 0x138, 0xf, 0xf, false));
}
__device__ __forceinline__ float rdlane(float v, int l) {
  return __int_as_float(__builtin_amdgcn_readlane(__float_as_int(v), l));
}
__device__ __forceinline__ float wave_sum(float v) {
  v += dpp_mov<0xB1>(v);
  v += dpp_mov<0x4E>(v);
  v += dpp_mov<0x141>(v);
  v += dpp_mov<0x140>(v);
  return (rdlane(v, 0) + rdlane(v, 16)) + (rdlane(v, 32) + rdlane(v, 48));
}
__device__ __forceinline__ float row_sum(float v) {
  v += dpp_mov<0xB1>(v);
  v += dpp_mov<0x4E>(v);
  v += dpp_mov<0x141>(v);
  v += dpp_mov<0x140>(v);
  return v;
}
__device__ __forceinline__ float half_sum(float v, int lane) {
  v += dpp_mov<0xB1>(v);
  v += dpp_mov<0x4E>(v);
  v += dpp_mov<0x141>(v);
  v += dpp_mov<0x140>(v);
  const float lo = rdlane(v, 0) + rdlane(v, 16), hi = rdlane(v, 32) + rdlane(v, 48);
  return lane < 32 ? lo : hi;
}
__device__ __forceinline__ float wave_max(float v) {
  v = fmaxf(v, dpp_mov<0xB1>(v));
  v = fmaxf(v, dpp_mov<0x4E>(v));
  v = fmaxf(v, dpp_mov<0x141>(v));
  v = fmaxf(v, dpp_mov<0x140>(v));
  return fmaxf(fmaxf(rdlane(v, 0), rdlane(v, 16)), fmaxf(rdlane(v, 32), rdlane(v, 48)));
}

__device__ __forceinline__ rsrc_t mk_rsrc(const void* p, unsigned bytes) {
  return __builtin_amdgcn_make_buffer_rsrc((void*)p, (short)0, (int)bytes, 0x00020000);
}
__device__ __forceinline__ void dma16(unsigned lds_dst, unsigned voff, rsrc_t r, unsigned soff) {
  asm volatile("s_mov_b32 m0, %0\n\ts_nop 0\n\tbuffer_load_dwordx4 %1, %2, %3 offen lds"
               :: "s"(lds_dst), "v"(voff), "s"(r), "s"(soff) : "memory");
}
__device__ __forceinline__ void dma16x4(unsigned lds_dst, unsigned v0, unsigned v1, unsigned v2, unsigned v3, rsrc_t r, unsigned soff) {
  asm volatile("s_mov_b32 m0, %0\n\ts_nop 0\n\t"
               "buffer_load_dwordx4 %1, %5, %6 offen lds\n\t"
               "buffer_load_dwordx4 %2, %5, %6 offen offset:1024 lds\n\t"
               "buffer_load_dwordx4 %3, %5, %6 offen offset:2048 lds\n\t"
               "buffer_load_dwordx4 %4, %5, %6 offen offset:3072 lds"
               :: "s"(lds_dst), "v"(v0), "v"(v1), "v"(v2), "v"(v3), "s"(r), "s"(soff) : "memory");
}
__device__ __forceinline__ void dma16x3(unsigned lds_dst, unsigned v0, unsigned v1, unsigned v2, rsrc_t r, unsigned soff) {
  asm volatile("s_mov_b32 m0, %0\n\ts_nop 0\n\t"
               "buffer_load_dwordx4 %1, %4, %5 offen lds\n\t"
               "buffer_load_dwordx4 %2, %4, %5 offen offset:1024 lds\n\t"
               "buffer_load_dwordx4 %3, %4, %5 offen offset:2048 lds"
               :: "s"(lds_dst), "v"(v0), "v"(v1), "v"(v2), "s"(r), "s"(soff) : "memory");
}
__device__ __forceinline__ void ld128(u32x4& dst, unsigned voff, rsrc_t r, unsigned soff) {
  asm volatile("buffer_load_dwordx4 %0, %1, %2, %3 offen" : "=v"(dst) : "v"(voff), "s"(r), "s"(soff) : "memory");
}
__device__ __forceinline__ void ld128c(u32x4& dst, unsigned voff, rsrc_t r, unsigned soff) {
  asm volatile("buffer_load_dwordx4 %0, %1, %2, %3 offen sc1" : "=v"(dst) : "v"(voff), "s"(r), "s"(soff) : "memory");
}
__device__ __forceinline__ void ld32(unsigned& dst, unsigned voff, rsrc_t r, unsigned soff) {
  asm volatile("buffer_load_dword %0, %1, %2, %3 offen" : "=v"(dst) : "v"(voff), "s"(r), "s"(soff) : "memory");
}
__device__ __forceinline__ void ld64c(u32x2& dst, unsigned voff, rsrc_t r, unsigned soff) {
  asm volatile("buffer_load_dwordx2 %0, %1, %2, %3 offen sc1" : "=v"(dst) : "v"(voff), "s"(r), "s"(soff) : "memory");
}
__device__ __forceinline__ void ld32c(unsigned& dst, unsigned voff, rsrc_t r, unsigned soff) {
  asm volatile("buffer_load_dword %0, %1, %2, %3 offen sc1" : "=v"(dst) : "v"(voff), "s"(r), "s"(soff) : "memory");
}
__device__ __forceinline__ void st32c(unsigned v, unsigned voff, rsrc_t r, unsigned soff) {
  asm volatile("buffer_store_dword %0, %1, %2, %3 offen sc1" :: "v"(v), "v"(voff), "s"(r), "s"(soff) : "memory");
}
__device__ __forceinline__ void st64c(u32x2 v, unsigned voff, rsrc_t r, unsigned soff) {
  asm volatile("buffer_store_dwordx2 %0, %1, %2, %3 offen sc1\n\ts_nop 1" :: "v"(v), "v"(voff), "s"(r), "s"(soff) : "memory");
}
__device__ __forceinline__ void ld16(unsigned& dst, unsigned voff, rsrc_t r, unsigned soff) {
  asm volatile("buffer_load_ushort %0, %1, %2, %3 offen" : "=v"(dst) : "v"(voff), "s"(r), "s"(soff) : "memory");
}
__device__ __forceinline__ void st128c(u32x4 v, unsigned voff, rsrc_t r, unsigned soff) {
  asm volatile("buffer_store_dwordx4 %0, %1, %2, %3 offen sc1\n\ts_nop 1" :: "v"(v), "v"(voff), "s"(r), "s"(soff) : "memory");
}
template <int N>
__device__ __forceinline__ void wait_vm() { asm volatile("s_waitcnt vmcnt(%0)" :: "n"(N > 63 ? 63 : N) : "memory"); }
__device__ __forceinline__ void bar_lgkm() { asm volatile("s_waitcnt lgkmcnt(0)\n\ts_barrier" ::: "memory"); }
__device__ __forceinline__ void bar_only() { asm volatile("s_barrier" ::: "memory"); }
#define PIN(x) asm volatile("" : "+v"(x))
#define SCHED_FENCE() __builtin_amdgcn_sched_barrier(0)

__device__ __forceinline__ s16x4 trrd(const LAS char* p) {
  return __builtin_amdgcn_ds_read_tr16_b64_v4i16((LAS s16x4*)p);
}
__device__ __forceinline__ s16x8 cat8(s16x4 a, s16x4 b) {
  return __builtin_shufflevector(a, b, 0, 1, 2, 3, 4, 5, 6, 7);
}

__device__ __forceinline__ bool wave_all(bool p) { return __builtin_amdgcn_ballot_w64(!p) == 0ull; }
template <int SLP = 2>
__device__ __forceinline__ void poll_flags(unsigned voff, rsrc_t r, unsigned soff, unsigned tag) {
#pragma unroll 1
  for (int it = 0; it < (1 << 18); ++it) {
    u32x4 v;
    asm volatile("buffer_load_dwordx4 %0, %1, %2, %3 offen sc1\n\ts_waitcnt vmcnt(0)"
                 : "=&v"(v) : "v"(voff), "s"(r), "s"(soff) : "memory");
    const bool ok = (v[0] == tag) & (v[1] == tag) & (v[2] == tag) & (v[3] == tag);
    if (wave_all(ok)) break;
    __builtin_amdgcn_s_sleep(SLP);
  }
}
__device__ __forceinline__ void set_flag(char* ws, unsigned off, unsigned tag) {
  __hip_atomic_store((GAS unsigned*)(ws + WS_FLAG + off), tag, __ATOMIC_RELAXED, __HIP_MEMORY_SCOPE_AGENT);
}

__device__ __forceinline__ f32x2 cmul(f32x2 a, f32x2 b) {
  f32x2 r;
  r.x = fmaf(a.x, b.x, -(a.y * b.y));
  r.y = fmaf(a.x, b.y, a.y * b.x);
  return r;
}
__device__ __forceinline__ f32x2 cexp_l(float a, float b, float l) {
  const float mag = __builtin_amdgcn_exp2f(a * l * 1.4426950408889634f);
  const float x = b * l;
  const float C1 = 0.15915494309189535f;
  const float C2 = (float)(0.15915494309189533577 - (double)0.15915494309189535f);
  const float r = fmaf(x, C2, fmaf(x, C1, -__builtin_rintf(x * C1)));
  f32x2 z;
  z.x = mag * __builtin_amdgcn_cosf(r);
  z.y = mag * __builtin_amdgcn_sinf(r);
  return z;
}

constexpr int W_UPOFF = 14336 + 128;
constexpr int XOFF = W_UPOFF + 14336;
constexpr int STG = XOFF + 16384;
constexpr int KGS = 3 * STG;
constexpr int KF = KGS + 8192;
constexpr int CV_KSTR = (48 + 2048) * 2;
constexpr int CV_USTR = (80 + 2048) * 2;
constexpr int CV_U = KF + 2 * CV_KSTR;
constexpr int CSUM = CV_U + 2 * CV_USTR;
constexpr int STAT = CSUM + 896;
constexpr int SY = STAT + 512;
constexpr int SY_LD = SY, SY_MC = SY + 16, SY_ST = SY + 4, SY_HR = SY + 8, SY_SR = SY + 12, SY_UR = SY + 32  ,
              SY_CNT = SY + 48, SY_VAL = SY + 64, SY_KG = SY + 80, SY_END = SY + 96;
constexpr int LDS_BYTES = SY_END;
static_assert(LDS_BYTES <= 163840, "lds");
constexpr int G2_RED = 272;
constexpr int EPX = KGS;
constexpr int EPS = KF;
constexpr int LDLY = 20;
constexpr int WSL = 4;
constexpr unsigned WS_USED = (unsigned)(WS_HF + (size_t)SL * FF * 2);
constexpr unsigned CODE_BYTES = 49152;
constexpr int CV_NP = 12;
constexpr int G2_NR = 13;
constexpr int cmax(int a, int b) { return a > b ? a : b; }
constexpr int cmin(int a, int b) { return a < b ? a : b; }
constexpr int g2_base(int i) { return (((i % 15) / 5 + 2) % 3) * STG + (i % 5) * 8192; }
constexpr int RED_A = g2_base(15), RED_B = g2_base(20);
constexpr int G2_H0 = 6;
constexpr int g2_issued(int i) { return cmin(28, G2_H0 + (3 * i) / 2); }

__device__ __forceinline__ void lds_setv(unsigned addr, unsigned v) {
  asm volatile("ds_write_b32 %0, %1" :: "v"(addr), "v"(v) : "memory");
}
__device__ __forceinline__ unsigned lds_min4(unsigned addr) {
  u32x4 v;
  asm volatile("ds_read_b128 %0, %1\n\ts_waitcnt lgkmcnt(0)" : "=&v"(v) : "v"(addr) : "memory");
  return (unsigned)__builtin_amdgcn_readfirstlane(min(min(v[0], v[1]), min(v[2], v[3])));
}
__device__ __forceinline__ unsigned lds_get1(unsigned addr) {
  unsigned v;
  asm volatile("ds_read_b32 %0, %1\n\ts_waitcnt lgkmcnt(0)" : "=&v"(v) : "v"(addr) : "memory");
  return (unsigned)__builtin_amdgcn_readfirstlane(v);
}
template <int W>
__device__ __forceinline__ unsigned lds_val(unsigned addr) { return W == 4 ? lds_min4(addr) : lds_get1(addr); }
template <int W>
__device__ __forceinline__ void lds_spin(unsigned addr, unsigned need) {
#pragma unroll 1
  for (int it = 0; it < (1 << 20); ++it) {
    if (lds_val<W>(addr) >= need) break;
    __builtin_amdgcn_s_sleep(1);
  }
}
template <int W>
__device__ __forceinline__ void lds_wait(unsigned addr, unsigned need) {
  if (__builtin_expect(lds_val<W>(addr) < need, 0)) lds_spin<W>(addr, need);
}
template <int NW, bool DRAIN = true>
__device__ __forceinline__ void publish(LAS char* lds, int cnt, char* ws, unsigned off, int nl, unsigned tag, int lane) {
  if (DRAIN) wait_vm<0>();
  unsigned old = 0;
  if (lane == 0) old = __hip_atomic_fetch_add((LAS unsigned*)(lds + SY_CNT + cnt * 4), 1u, __ATOMIC_RELAXED, __HIP_MEMORY_SCOPE_WORKGROUP);
  if (__builtin_amdgcn_readfirstlane(old) == NW - 1 && lane < nl) set_flag(ws, off, tag);
}

template <int PA, int PB>
__device__ __forceinline__ void conv_upper(LAS char* lds, int lane, int hc, int cp, rsrc_t r_s) {
  const unsigned dsk2 = *(const LAS unsigned*)(lds + SY_VAL + 4);
  const float kscale = *(const LAS float*)(lds + SY_VAL + 8 + hc * 4);
  const float dskip = hc ? hi_f(dsk2) : lo_f(dsk2);
  const int G = lane >> 4, q4 = (lane & 15) >> 2, p = lane & 3;
  const LAS char* pa = lds + KF + hc * CV_KSTR + 96 + 2 * (76 + 16 * p - 32 * G - 4 * q4 - 16);
  const LAS char* pb = lds + CV_U + hc * CV_USTR + 160 + 2 * (-76 + 32 * G + 4 * q4 + 4 * p);
  const LAS char* pu = lds + CV_U + hc * CV_USTR + 160 + lane * 2;
  constexpr int J0 = 2 * PA - 1, J1 = 2 * PB, NT = J1 - J0;
  f16x8 A[16], B[32];
  f32x4 ca[NT];
#define CV_A(q) __builtin_bit_cast(f16x8, cat8(trrd(pa + 256 * (q) + 32), trrd(pa + 256 * (q))))
#define CV_B(w) __builtin_bit_cast(f16x8, cat8(trrd(pb + 128 * (w)), trrd(pb + 128 * (w) + 32)))
  B[0] = CV_B(0); B[1] = CV_B(1); B[2] = CV_B(2);
  constexpr int QA = (J0 - 1) / 2;
#pragma unroll
  for (int q = PB - 1; q >= QA; --q) A[q] = CV_A(q);
#pragma unroll
  for (int j = 0; j < NT; ++j) ca[j] = (f32x4){0.f, 0.f, 0.f, 0.f};
  static_for<0, J1>([&](auto wc) {
    constexpr int w = decltype(wc)::value;
    if constexpr (w + 3 < J1) B[w + 3] = CV_B(w + 3);
    if constexpr ((w & 1) == 0 && QA - 1 - w / 2 >= 0) A[QA - 1 - w / 2] = CV_A(QA - 1 - w / 2);
#pragma unroll
    for (int q = PB - 1; q >= 0; --q) {
      const int j = w + 2 * q;
      if (j >= J0 && j < J1) ca[j - J0] = __builtin_amdgcn_mfma_f32_16x16x32_f16(A[q], B[w], ca[j - J0], 0, 0, 0);
    }
    SCHED_FENCE();
  });
  float c3 = 0.f, c2 = 0.f, c1 = 0.f, yv[NT];
#pragma unroll
  for (int j = 0; j < NT; ++j) {
    const f32x4 a = ca[j];
    const float h3 = a[3];
    const float h2 = a[2] + shr_carry(c3, h3);
    const float h1 = a[1] + shr_carry(c2, h2);
    yv[j] = a[0] + shr_carry(c1, h1);
    c3 = dpp_mov<0x13C>(h3); c2 = dpp_mov<0x13C>(h2); c1 = dpp_mov<0x13C>(h1);
  }
#pragma unroll
  for (int jp = PA; jp < PB; ++jp) {
    const float u0 = (float)*(const LAS _Float16*)(pu + jp * 256), u1 = (float)*(const LAS _Float16*)(pu + jp * 256 + 128);
    const unsigned yq = pk_bf16(yv[2 * jp - J0] * kscale, yv[2 * jp + 1 - J0] * kscale);
    const unsigned uq = pk_bf16(u0 * dskip, u1 * dskip);
    st32c(pk_bf16(lo_f(yq) + lo_f(uq), hi_f(yq) + hi_f(uq)), lane * 4u, r_s, (unsigned)((cp * 2 + hc) * 4096 + jp * 256));
  }
#undef CV_A
#undef CV_B
}

#if defined(__HIP_DEVICE_COMPILE__)
__attribute__((target("no-packed-fp32-ops")))
#endif
__global__ __launch_bounds__(512, 2) void fused_kernel(
    const unsigned short* __restrict__ x, const unsigned short* __restrict__ A_re,
    const unsigned short* __restrict__ A_im, const unsigned short* __restrict__ B_re,
    const unsigned short* __restrict__ B_im, const unsigned short* __restrict__ C_re,
    const unsigned short* __restrict__ C_im, const unsigned* __restrict__ log_dt2,
    const unsigned* __restrict__ dsk2p, const unsigned short* __restrict__ wg,
    const unsigned short* __restrict__ wu, const unsigned short* __restrict__ wd,
    char* ws, unsigned short* __restrict__ out) {
  extern __shared__ __attribute__((aligned(16))) char smem[];
  unsigned long long pc0;
  asm volatile("s_getpc_b64 %0" : "=s"(pc0));
  LAS char* lds = (LAS char*)smem;
  const unsigned ldsb = (unsigned)(uintptr_t)lds;
  const int tid = threadIdx.x, lane = tid & 63;
  const int w8 = __builtin_amdgcn_readfirstlane(tid >> 6);
  const int wv = w8 & 3;
  const int bid = blockIdx.x;
  const int xcd = bid & 7, bi = bid >> 3;
  unsigned pf0 = 0;
  if (w8 == 5) ld32(pf0, (unsigned)((bi * 16 + (lane & 15)) * 128), mk_rsrc((const void*)pc0, CODE_BYTES), 0u);
  asm volatile("" :: "s"(x), "s"(A_re), "s"(A_im), "s"(B_re), "s"(B_im), "s"(C_re), "s"(C_im), "s"(log_dt2),
               "s"(dsk2p), "s"(wg), "s"(wu), "s"(wd), "s"(ws), "s"(out));
  const int mt = (xcd >> 2) * 8 + (bi >> 2);
  const int ft = (xcd & 3) * 4 + (bi & 3);
  const int rot = ((bi >> 2) + 2 * (bi & 3)) & 7;
  const int cp = xcd * 32 + bi;
  const int tt = xcd * 4 + (bi >> 3);
  const int dtile = bi & 7;
  const int rot2 = ((bi >> 3) * 7 + dtile * 3) % 28;

  const rsrc_t r_x = mk_rsrc(x, SL * DM * 2);
  const rsrc_t r_s = mk_rsrc(ws + WS_ST, SL * DM * 2);
  const rsrc_t r_fl = mk_rsrc(ws + WS_FLAG, FL_BYTES);
#define G2_KS(w, i) ((unsigned)((w) * 28 + (((i) + rot2) >= 28 ? (i) + rot2 - 28 : (i) + rot2)))
#define XO(t_) ((unsigned)((tt * 64 + ((t_) >> 3)) * (DM * 2) + dtile * 128 + ((t_) & 7) * 16))
#define EPI_X() do { int l_ = lane; PIN(l_); u32x4 xw_[2]; \
    _Pragma("unroll") for (int j = 0; j < 2; ++j) ld128(xw_[j], XO(wv * 128 + j * 64 + l_), r_x, 0u); \
    lds_wait<4>(ldsb + SY_UR, 1u); wait_vm<0>(); \
    _Pragma("unroll") for (int j = 0; j < 2; ++j) { PIN(xw_[j]); *(LAS u32x4*)(lds + EPX + (wv * 128 + j * 64 + l_) * 16) = xw_[j]; } } while (0)
#define EPI_S() do { int l_ = lane; PIN(l_); u32x4 sw_[4]; \
    const unsigned so = (unsigned)((dtile * 64 + wv * 16 + (l_ >> 4)) * 4096 + (tt >> 1) * 256 + (l_ & 15) * 16); \
    _Pragma("unroll") for (int j = 0; j < 4; ++j) ld128c(sw_[j], so, r_s, (unsigned)(j * 16384)); \
    wait_vm<0>(); \
    _Pragma("unroll") for (int j = 0; j < 4; ++j) { PIN(sw_[j]); \
      *(LAS u32x4*)(lds + EPS + (wv * 16 + j * 4 + (l_ >> 4)) * 256 + ((l_ + wv * 2 + (j >> 1)) & 15) * 16) = sw_[j]; } } while (0)

  const rsrc_t r_wg = mk_rsrc(wg, DM * FF * 2), r_wu = mk_rsrc(wu, DM * FF * 2);
  unsigned woff[7], xoff[4];
  auto g1_stage = [&](int s) __attribute__((always_inline)) {
    const unsigned st = s == 0 ? 0u : (unsigned)(1 + (s - 1 + rot) % 7), lb = ldsb + (unsigned)((s % 3) * STG);
#pragma unroll
    for (int i = 0; i < 4; ++i)
      dma16x4(lb + XOFF + i * 4096, xoff[0], xoff[1], xoff[2], xoff[3], r_x, (unsigned)((mt * 128 + i * 32) * 1024) + st * 128u);
#pragma unroll
    for (int h = 0; h < 2; ++h) {
      const unsigned wso = (unsigned)(ft * 224) + (st * 64u + h * 32u) * (FF * 2);
      dma16x4(lb + h * 7168, woff[0], woff[1], woff[2], woff[3], r_wg, wso);
      dma16x3(lb + h * 7168 + 4096, woff[4], woff[5], woff[6], r_wg, wso);
      dma16x4(lb + W_UPOFF + h * 7168, woff[0], woff[1], woff[2], woff[3], r_wu, wso);
      dma16x3(lb + W_UPOFF + h * 7168 + 4096, woff[4], woff[5], woff[6], r_wu, wso);
    }
  };
  unsigned prm[6], ldt2, ep, dsk2;
  u32x4 xr[4];
  if (w8 < 4) {
    const unsigned po = (unsigned)((cp * 2 + (wv & 1)) * NM * 2);
    ld32(prm[0], lane * 4u, mk_rsrc(A_re, DM * NM * 2), po);
    ld32(prm[1], lane * 4u, mk_rsrc(A_im, DM * NM * 2), po);
    ld32(prm[2], lane * 4u, mk_rsrc(B_re, DM * NM * 2), po);
    ld32(prm[3], lane * 4u, mk_rsrc(B_im, DM * NM * 2), po);
    ld32(prm[4], lane * 4u, mk_rsrc(C_re, DM * NM * 2), po);
    ld32(prm[5], lane * 4u, mk_rsrc(C_im, DM * NM * 2), po);
    ld32(ldt2, 0u * lane, mk_rsrc(log_dt2, DM * 2), (unsigned)(cp * 4));
  } else if (w8 >= 6) {
    const unsigned vo = (unsigned)((lane >> 4) * 1024 + (lane & 15) * 16);
#pragma unroll
    for (int i = 0; i < 4; ++i) ld128(xr[i], vo, r_x, (unsigned)((bid * 8 + (wv - 2) * 4) * 1024 + i * 256));
    ld32c(ep, 0u * lane, r_fl, FL_EP);
    ld32(dsk2, 0u * lane, mk_rsrc(dsk2p, DM * 2), (unsigned)(cp * 4));
  } else if (w8 == 4) {
    __builtin_amdgcn_s_setprio(3);
#pragma unroll
    for (int i = 0; i < 4; ++i)
      xoff[i] = (unsigned)((lane >> 3) * 1024 + (((lane & 7) ^ ((lane >> 4) + 4 * (i & 1))) * 16) + i * 7168);
#pragma unroll
    for (int i = 0; i < 7; ++i) {
      const int c = i * 64 + lane, row = c / 14, col = c - row * 14;
      woff[i] = (unsigned)(row * (FF * 2) + col * 16 - (i & 3) * 1024);
    }
    __builtin_amdgcn_s_sleep(LDLY);
    g1_stage(0);
  }
  asm volatile("; first loads issued" ::: "memory");
  if (w8 < 4) {
    const rsrc_t r_h = mk_rsrc(ws + WS_HF, SL * FF * 2);
    {
    const int hc = wv & 1, m = wv >> 1;
    if (tid < 4) *(LAS u32x4*)(lds + SY + tid * 16) = (u32x4){0u, 0u, 0u, tid == 2 ? 1u : 0u};
    __builtin_amdgcn_s_setprio(2);
    {
      wait_vm<0>();
      PIN(ldt2);
      const float ldt = hc ? hi_f(ldt2) : lo_f(ldt2);
      const float L2E = 1.4426950408889634f, L2E_LO = (float)(1.4426950408889634074 - (double)1.4426950408889634f);
      const float th = ldt * L2E;
      const float tl = fmaf(ldt, L2E, -th) + ldt * L2E_LO;
      const float e0 = __builtin_amdgcn_exp2f(th);
      const float dt = rbf(fmaf(e0 * 0.6931471805599453f, tl, e0));
      wait_vm<0>();
#pragma unroll
      for (int i = 0; i < 6; ++i) PIN(prm[i]);
      float av[2], bv[2];
      f32x2 wq[2];
#pragma unroll
      for (int m = 0; m < 2; ++m) {
        av[m] = dt * (m ? hi_f(prm[0]) : lo_f(prm[0]));
        bv[m] = dt * (m ? hi_f(prm[1]) : lo_f(prm[1]));
        const f32x2 db = {dt * (m ? hi_f(prm[2]) : lo_f(prm[2])), dt * (m ? hi_f(prm[3]) : lo_f(prm[3]))};
        const f32x2 cc = {m ? hi_f(prm[4]) : lo_f(prm[4]), m ? hi_f(prm[5]) : lo_f(prm[5])};
        wq[m] = cmul(cc, db);
      }
      const float amax = fmaxf(wave_max(fmaxf(av[0], av[1])), 0.f);
      const float g2 = amax * L2E;
      const float wm = wave_max(fmaxf(fmaxf(fabsf(wq[0].x), fabsf(wq[0].y)), fmaxf(fabsf(wq[1].x), fabsf(wq[1].y))));
      int we = __builtin_amdgcn_frexp_expf(wm);
      we = we < -100 ? -100 : we;
      const float wsc = __builtin_amdgcn_ldexpf(1.0f, -we);
      f32x4 kacc[3];
#pragma unroll
      for (int t = 0; t < 3; ++t) kacc[t] = (f32x4){0.f, 0.f, 0.f, 0.f};
      const int kgs = (m ? CV_U : KGS) + hc * 4096;
      LAS char* tw[4];
#pragma unroll
      for (int c = 0; c < 4; ++c) tw[c] = lds + kgs + lane * 64 + ((c ^ ((lane >> 1) & 3)) << 4);
      const LAS char* tr[2];
      {
        const int G = lane >> 4, q4 = (lane & 15) >> 2, p = lane & 3;
#pragma unroll
        for (int t = 0; t < 2; ++t)
          tr[t] = lds + kgs + (4 * G + 2 * t + (q4 >> 1)) * 64 + ((((q4 & 1) * 2 + (p >> 1)) ^ ((2 * G + t) & 3)) << 4) + (p & 1) * 8;
      }
      auto put16 = [&](const f32x2 (&e)[16]) __attribute__((always_inline)) {
#pragma unroll
        for (int h = 0; h < 2; ++h) {
          u32x4 re, im;
#pragma unroll
          for (int j = 0; j < 4; ++j) {
            re[j] = pk_f16(e[8 * h + 2 * j].x, e[8 * h + 2 * j + 1].x);
            im[j] = pk_f16(e[8 * h + 2 * j].y, e[8 * h + 2 * j + 1].y);
          }
          *(LAS u32x4*)tw[h] = re;
          *(LAS u32x4*)tw[2 + h] = im;
        }
      };
#define KG_FRAG(s) __builtin_bit_cast(f16x8, cat8(trrd(tr[0] + (s) * 1024), trrd(tr[1] + (s) * 1024)))
#define KG_RD() do { _Pragma("unroll") for (int s = 0; s < 4; ++s) pf[s] = KG_FRAG(s); SCHED_FENCE(); } while (0)
#define KG_MMA(t) do { SCHED_FENCE(); _Pragma("unroll") for (int s = 0; s < 4; ++s) \
        kacc[t] = __builtin_amdgcn_mfma_f32_16x16x32_f16(pf[s], qf[s], kacc[t], 0, 0, 0); } while (0)
      {
        const float a = (m ? av[1] : av[0]) - amax, b = m ? bv[1] : bv[0];
        f32x2 w = m ? wq[1] : wq[0];
        w.x *= wsc; w.y *= -wsc;
        f32x2 e[16];
        {
          const f32x2 z16 = cexp_l(a, b, 16.f);
          e[0] = (f32x2){1.f, 0.f}; e[1] = z16; e[2] = cmul(z16, z16); e[3] = cmul(e[2], z16);
          const f32x2 z64 = cmul(e[2], e[2]);
#pragma unroll
          for (int c = 4; c < 16; ++c) e[c] = cmul(e[c - 4], z64);
          put16(e);
        }
        f16x8 qf[4], pf[4];
#pragma unroll
        for (int s = 0; s < 4; ++s) qf[s] = KG_FRAG(s);
        SCHED_FENCE();
        const f32x2 z1 = cexp_l(a, -b, 1.f);
        const f32x2 z2 = cmul(z1, z1), z4 = cmul(z2, z2);
        e[0] = w; e[1] = cmul(w, z1); e[2] = cmul(w, z2); e[3] = cmul(e[1], z2);
#pragma unroll
        for (int c = 4; c < 16; ++c) e[c] = cmul(e[c - 4], z4);
        put16(e);
        KG_RD();
        const f32x2 z256 = cexp_l(a, -b, 256.f);
        const f32x2 z512 = cmul(z256, z256);
        const f32x2 w2 = cmul(w, z512);
        e[0] = cmul(w, z256); e[1] = cmul(e[0], z2); e[8] = w2; e[12] = cmul(e[0], z512);
        const f32x2 w3 = e[12];
#pragma unroll
        for (int i = 1; i < 4; ++i) {
          e[2 * i] = cmul(e[2 * i - 2], z4); e[2 * i + 1] = cmul(e[2 * i - 1], z4);
          e[8 + i] = cmul(e[7 + i], z4); e[12 + i] = cmul(e[11 + i], z4);
        }
        KG_MMA(0);
        put16(e);
        KG_RD();
        const f32x2 z8 = cmul(z4, z4);
        e[0] = cmul(w2, z512); e[2] = cmul(w3, z512); e[4] = cmul(e[0], z512); e[6] = cmul(e[2], z512);
        e[8] = cmul(e[4], z512);
#pragma unroll
        for (int i = 0; i < 4; ++i) e[2 * i + 1] = cmul(e[2 * i], z8);
#pragma unroll
        for (int i = 9; i < 16; ++i) e[i] = (f32x2){0.f, 0.f};
        KG_MMA(1);
        put16(e);
        KG_RD();
        KG_MMA(2);
      }
#pragma unroll
      for (int t = 0; t < 3; ++t) *(LAS f32x4*)(lds + (m ? KF + hc * CV_KSTR : KGS + hc * 4096) + t * 1024 + lane * 16) = kacc[t];
      if (m) *(LAS f32x2*)(lds + SY_KG + hc * 8) = (f32x2){g2, (float)we};
    }
    }
    f32x16 acc[7];
    f32x4 csa[2];
    unsigned tag;
    {
      const int G = lane >> 4;
      const LAS char* wa0 = lds + (G & 1) * W_UPOFF + (8 * (G >> 1) + ((lane & 15) >> 2)) * 224 + (lane & 3) * 8;
      const LAS char* wa[7];
#pragma unroll
      for (int T = 0; T < 7; ++T) wa[T] = wa0 + 32 * ((T + 2 * wv) % 7);
      const LAS char* xa[4];
      {
        const int row = 32 * wv + (lane & 31), sw = (row >> 1) & 7;
#pragma unroll
        for (int ks = 0; ks < 4; ++ks) xa[ks] = lds + XOFF + row * 128 + (((2 * ks + (lane >> 5)) ^ sw) << 4);
      }
      constexpr int PD = 3;
      bf16x8 Af[196], Bf[32];
      const unsigned o1 = ((lane & 15) == 4 * ((lane >> 4) & 1)) ? 0x3f803f80u : 0u;
      u32x4 onesq = {o1, o1, o1, o1};
      PIN(onesq);
      const bf16x8 ones = __builtin_bit_cast(bf16x8, onesq);
#define A_READ(i, T) __builtin_bit_cast(bf16x8, cat8(trrd(wa[T] + (((i) >> 2) % 3) * STG + (16 * ((i) & 3)) * 224), \
                                                      trrd(wa[T] + (((i) >> 2) % 3) * STG + (16 * ((i) & 3) + 4) * 224)))
#define B_READ(i) (*(const LAS bf16x8*)(xa[(i) & 3] + (((i) >> 2) % 3) * STG))
#pragma unroll
      for (int T = 0; T < 7; ++T) acc[T] = (f32x16){};
      csa[0] = (f32x4){0.f, 0.f, 0.f, 0.f}; csa[1] = csa[0];
      bar_lgkm();
      tag = lds_get1(ldsb + SY_VAL);
      Bf[0] = B_READ(0);
#pragma unroll
      for (int T = 0; T < PD; ++T) Af[T] = A_READ(0, T);
      bf16x8 Al[7][4];
      u32x2 stg;
      float cs[7];
      static_for<0, 196>([&](auto nc) {
        constexpr int n = decltype(nc)::value, i = n / 7, T = n % 7;
        constexpr int m = n + PD, im = m / 7, Tm = m % 7;
        if constexpr (n == 70) ld64c(stg, (unsigned)((lane & 31) * 8), r_fl, FL_STAT + (unsigned)((mt * 128 + wv * 32) * 8));
        if constexpr (n == 27 * 7 + 2) {
          if (lane < 32) {
#pragma unroll
            for (int j = 0; j < 2; ++j) *(LAS float*)(lds + CSUM + ((j + 2 * wv) % 7) * 128 + lane * 4) = csa[j][0];
          }
        }
        if constexpr (Tm == 0 && (im & 3) == 0) bar_lgkm();
        if constexpr (m < 196) {
          if constexpr (Tm == 0) Bf[im] = B_READ(im);
          Af[m] = A_READ(im, Tm);
        } else if constexpr (m == 196) {
#pragma unroll
          for (int k = 28; k < 32; ++k) Bf[k] = B_READ(k);
        } else if constexpr (m == 197) {
#pragma unroll
          for (int t = 0; t < 7; ++t) cs[t] = *(const LAS float*)(lds + CSUM + ((t + 2 * wv) % 7) * 128 + (lane & 31) * 4);
        } else {
#pragma unroll
          for (int k = 0; k < 4; ++k) Al[0][k] = A_READ(28 + k, 0);
        }
        acc[T] = __builtin_amdgcn_mfma_f32_32x32x16_bf16(Af[n], Bf[i], acc[T], 0, 0, 0);
        if constexpr (T < 2)
          csa[T] = __builtin_amdgcn_mfma_f32_16x16x32_bf16(ones, Af[n], csa[T], 0, 0, 0);
        SCHED_FENCE();
      });
      wait_vm<0>();
      PIN(stg);
      if (__builtin_expect(!wave_all(stg[1] == tag), 0)) {
#pragma unroll 1
        for (int it = 0; it < (1 << 18); ++it) {
          ld64c(stg, (unsigned)((lane & 31) * 8), r_fl, FL_STAT + (unsigned)((mt * 128 + wv * 32) * 8));
          wait_vm<0>();
          PIN(stg);
          if (wave_all(stg[1] == tag)) break;
          __builtin_amdgcn_s_sleep(2);
        }
      }
      const unsigned stw = stg[0];
      const float mb = lo_f(stw), sd = hi_f(stw);
#pragma unroll
      for (int k = 28; k < 32; ++k) {
        u32x4 v = __builtin_bit_cast(u32x4, Bf[k]);
#pragma unroll
        for (int j = 0; j < 4; ++j) v[j] = pk_bf16(lo_f(v[j]) - mb, hi_f(v[j]) - mb);
        Bf[k] = __builtin_bit_cast(bf16x8, v);
      }
      const unsigned nm = (stw & 0xffffu) ^ 0x8000u;
      u32x4 bq = {nm | (nm << 16), nm, 0u, 0u};
      if (lane >= 32) bq = (u32x4){0u, 0u, 0u, 0u};
      const float ga = -1.4426950408889634f * __builtin_amdgcn_rcpf(sd);
      const unsigned hsb = (unsigned)(((mt * 4 + wv) * 112 + ft * 7) * 1024);
      static_for<0, 8>([&](auto tc) {
        constexpr int T = decltype(tc)::value;
        u32x4 o;
        if constexpr (T < 7) {
          const float c = cs[T];
          const float ch = rbf(c), cm = rbf(c - ch), cl = (c - ch) - cm;
          const u32x4 aq = {pk_bf16(ch, cm), pk_bf16(cl, 0.f), 0u, 0u};
          acc[T] = __builtin_amdgcn_mfma_f32_32x32x16_bf16(__builtin_bit_cast(bf16x8, aq), __builtin_bit_cast(bf16x8, bq), acc[T], 0, 0, 0);
          SCHED_FENCE();
        }
#pragma unroll
        for (int j = 0; j < 4; ++j) {
          if constexpr (T + 1 < 7) Al[T + 1][j] = A_READ(28 + j, T + 1);
          if constexpr (T < 7) acc[T] = __builtin_amdgcn_mfma_f32_32x32x16_bf16(Al[T][j], Bf[28 + j], acc[T], 0, 0, 0);
          SCHED_FENCE();
          if constexpr (T >= 1) {
            const float s0 = __builtin_amdgcn_rcpf(fmaf(__builtin_amdgcn_exp2f(acc[T - 1][2 * j] * ga), sd, sd));
            const float s1 = __builtin_amdgcn_rcpf(fmaf(__builtin_amdgcn_exp2f(acc[T - 1][2 * j + 1] * ga), sd, sd));
            o[j] = pk_bf16(s0 * acc[T - 1][8 + 2 * j], s1 * acc[T - 1][8 + 2 * j + 1]);
            PIN(o[j]);
          }
          SCHED_FENCE();
        }
        if constexpr (T >= 1) st128c(o, lane * 16u, r_h, hsb + (unsigned)((T - 1 + 2 * wv) % 7) * 1024u);
      });
    }
    const unsigned ldw = ldsb + SY_LD;
    const LAS char* waT[2];
    {
      const int G = lane >> 4, q4 = (lane & 15) >> 2;
      const LAS char* wa0 = lds + wv * 2048 + (4 * (G >> 1) + q4) * 128 + (G & 1) * 32 + (lane & 3) * 8;
      waT[0] = wa0 + ((q4 >> 1) * 64); waT[1] = wa0 + (((q4 >> 1) ^ 1) * 64);
    }
#define D_READ(i, T) __builtin_bit_cast(bf16x8, cat8(trrd(waT[T] + g2_base(i)), trrd(waT[T] + g2_base(i) + 1024)))
    bf16x8 Wr[28][2];
    lds_wait<1>(ldw, 2u);
    static_for<0, 5>([&](auto ic) { constexpr int i = decltype(ic)::value; Wr[i][0] = D_READ(i, 0); Wr[i][1] = D_READ(i, 1); });
    static_for<5, 10>([&](auto ic) { constexpr int i = decltype(ic)::value; Wr[i][0] = D_READ(i, 0); Wr[i][1] = D_READ(i, 1); });
    wait_vm<0>();
    if (lane < 8) set_flag(ws, FL_H + (unsigned)(((mt * 2 + (wv >> 1)) * 8 + lane) * 256 + (ft * 2 + (wv & 1)) * 4), tag);
    lds_setv(ldsb + SY_MC + wv * 4, 2u);
    if (wv == 0) {
      lds_wait<4>(ldsb + SY_UR, 1u);
      conv_upper<14, 16>(lds, lane, 0, cp, r_s);
    }
    lds_wait<1>(ldw, 3u);
    static_for<10, G2_NR>([&](auto ic) { constexpr int i = decltype(ic)::value; Wr[i][0] = D_READ(i, 0); Wr[i][1] = D_READ(i, 1); });
    lds_setv(ldsb + SY_MC + wv * 4, 3u);
    __builtin_amdgcn_s_setprio(0);
    poll_flags<2>((lane & 1) * 16u, r_fl, FL_H + (unsigned)((tt * 8 + dtile) * 256 + wv * 32), tag);
    if (wv == 0) publish<4>(lds, 2, ws, FL_S + (unsigned)((lane * 8 + (cp >> 5)) * 256 + (cp & 31) * 4), 32, tag, lane);
    __builtin_amdgcn_s_setprio(2);
    u32x4 hb[28][2];
#define HB_LOAD(i) do { ld128c(hb[i][0], lane * 16u, r_h, ((unsigned)(tt * 2) * 112u + G2_KS(wv, i)) * 1024u); \
                        ld128c(hb[i][1], lane * 16u, r_h, ((unsigned)(tt * 2 + 1) * 112u + G2_KS(wv, i)) * 1024u); } while (0)
#pragma unroll
    for (int i = 0; i < G2_H0; ++i) HB_LOAD(i);
    f32x16 acc2[2][2];
    {
#pragma unroll
      for (int a = 0; a < 2; ++a)
#pragma unroll
        for (int b = 0; b < 2; ++b) acc2[a][b] = (f32x16){};
      static_for<0, 28>([&](auto ic) {
        constexpr int i = decltype(ic)::value;
        if constexpr (i + 2 == 13) lds_wait<1>(ldw, 6u);
        if constexpr (i + 2 >= G2_NR && i + 2 < 28) { Wr[i + 2][0] = D_READ(i + 2, 0); Wr[i + 2][1] = D_READ(i + 2, 1); }
        wait_vm<2 * (g2_issued(i) - i - 1)>();
        PIN(hb[i][0]); PIN(hb[i][1]);
#pragma unroll
        for (int T = 0; T < 2; ++T)
#pragma unroll
          for (int b = 0; b < 2; ++b)
            acc2[T][b] = __builtin_amdgcn_mfma_f32_32x32x16_bf16(Wr[i][T], __builtin_bit_cast(bf16x8, hb[i][b]), acc2[T][b], 0, 0, 0);
        SCHED_FENCE();
        static_for<g2_issued(i), g2_issued(i + 1)>([&](auto jc) { constexpr int j = decltype(jc)::value; HB_LOAD(j); });
      });
    }
    {
      int l2 = lane; PIN(l2);
      const int tl = l2 & 31, x7 = (tl & 7) ^ (l2 >> 5);
#pragma unroll
      for (int b = 0; b < 2; ++b) {
        const int c8 = 4 * b + (tl >> 3);
        const int o4 = c8 < 5 ? RED_A + c8 * 8192 : RED_B + (c8 - 5) * 8192;
        LAS char* rp = lds + o4 + wv * 2048 + (tl & 7) * 256;
#pragma unroll
        for (int T = 0; T < 2; ++T)
#pragma unroll
          for (int g = 0; g < 4; ++g) {
            const f32x4 v = {acc2[T][b][4 * g], acc2[T][b][4 * g + 1], acc2[T][b][4 * g + 2], acc2[T][b][4 * g + 3]};
            *(LAS f32x4*)(rp + T * 128 + (((2 * g) ^ x7) << 4)) = v;
          }
      }
    }
  } else if (w8 == 4) {
    const rsrc_t r_wd = mk_rsrc(wd, FF * DM * 2);
    unsigned wv4[4];
#pragma unroll
    for (int i = 0; i < 4; ++i)
      wv4[i] = (unsigned)((lane >> 3) * 1024 + (((lane & 7) ^ (((lane >> 4) & 1) << 2)) * 16) + (i >> 1) * 458752 + (i & 1) * 8192 - i * 1024);
    const unsigned ldw = ldsb + SY_LD;
#define G2_GROUP(g) G2_STEPS(5 * (g), cmin(5 * (g) + 5, 28))
#define G2_STEPS(a_, b_) static_for<a_, b_>([&](auto ic) { constexpr int i = decltype(ic)::value; \
        const unsigned so_ = G2_KS(0, i) * 16384u + (unsigned)(dtile * 128); \
        dma16x4(ldsb + g2_base(i), wv4[0], wv4[1], wv4[2], wv4[3], r_wd, so_); \
        dma16x4(ldsb + g2_base(i) + 4096, wv4[0], wv4[1], wv4[2], wv4[3], r_wd, so_ + 2u * 458752u); })
    g1_stage(1);
    wait_vm<44>();
    bar_only();
#pragma unroll
    for (int k = 0; k < 6; ++k) {
      g1_stage(k + 2);
      wait_vm<44>();
      bar_only();
    }
    G2_GROUP(0);
    wait_vm<40>();
    bar_only();
    G2_GROUP(1);
    wait_vm<0>();
    lds_setv(ldw, 2u);
    lds_wait<4>(ldsb + SY_MC, 2u);
    G2_STEPS(10, 15);
    wait_vm<16>();
    lds_setv(ldw, 3u);
    G2_STEPS(15, 25);
    lds_wait<4>(ldsb + SY_MC, 3u);
    G2_GROUP(5);
    wait_vm<0>();
    lds_setv(ldw, 6u);
    __builtin_amdgcn_s_setprio(0);
    EPI_X();
    lds_wait<1>(ldsb + SY_SR, 1u);
    EPI_S();
  } else if (w8 == 5) {
    wait_vm<0>();
    asm volatile("" :: "v"(pf0));
    bar_only();
    const unsigned tag = lds_get1(ldsb + SY_VAL);
    bar_only();
    {
      const int c = lane & 15, g = lane >> 4;
      unsigned d[2][3][4];
      float kscale[2];
#pragma unroll
      for (int hc = 0; hc < 2; ++hc) {
        f32x4 kacc[3];
#pragma unroll
        for (int t = 0; t < 3; ++t)
          kacc[t] = *(const LAS f32x4*)(lds + KF + hc * CV_KSTR + t * 1024 + lane * 16) + *(const LAS f32x4*)(lds + KGS + hc * 4096 + t * 1024 + lane * 16);
        const f32x2 gw = *(const LAS f32x2*)(lds + SY_KG + hc * 8);
        const float g2 = gw.x;
        const int we = (int)gw.y;
        if (g2 > 0.f) {
#pragma unroll
          for (int i = 0; i < 4; ++i) {
            const int l0 = 16 * c + 4 * g + i;
            const int l1 = g < 2 ? 256 + 16 * c + 8 * g + 2 * i : 512 + 256 * (g - 2) + 16 * c + 4 * i;
            const int l2 = g < 2 ? 1024 + 256 * (2 * g + (i >> 1)) + 8 * (i & 1) + 16 * c : 2048;
            kacc[0][i] *= __builtin_amdgcn_exp2f(g2 * (float)(l0 - 2048));
            kacc[1][i] *= __builtin_amdgcn_exp2f(g2 * (float)(l1 - 2048));
            kacc[2][i] *= __builtin_amdgcn_exp2f(g2 * (float)(l2 - 2048));
          }
        }
        kscale[hc] = __builtin_amdgcn_ldexpf(1.0f, we - 7) * __builtin_amdgcn_exp2f(g2 * 2048.f);
#pragma unroll
        for (int t = 0; t < 3; ++t)
#pragma unroll
          for (int i = 0; i < 4; ++i) { d[hc][t][i] = pk_f16(kacc[t][i] * 128.f, kacc[t][i] * 128.f); PIN(d[hc][t][i]); }
        bar_only();
      }
      bar_only();
      bar_only();
      bar_only();
      bar_only();
      __builtin_amdgcn_s_sleep(WSL);
      __builtin_amdgcn_s_setprio(3);
      *(LAS f32x2*)(lds + SY_VAL + 8) = (f32x2){kscale[0], kscale[1]};
#define MIX(a, b) (((a) & 0xffffu) | ((b) & 0xffff0000u))
#pragma unroll
      for (int hc = 0; hc < 2; ++hc) {
        LAS char* kf = lds + KF + hc * CV_KSTR + 96;
        if (g < 2) {
#pragma unroll
          for (int h = 0; h < 2; ++h) {
            LAS char* p = kf + 2 * (1024 + 256 * (2 * g + h) + 16 * c);
            const unsigned v0 = d[hc][2][2 * h], v1 = d[hc][2][2 * h + 1];
            *(LAS u32x2*)(p - 8) = (u32x2){v0, v0};
            *(LAS u32x4*)(p) = (u32x4){v0, v0, MIX(v0, v1), v1};
            *(LAS u32x2*)(p + 16) = (u32x2){v1, v1};
          }
        } else {
          if (lane == 32) *(LAS u32x2*)(kf + 2 * 2044) = (u32x2){d[hc][2][0], d[hc][2][0]};
        }
        if (g >= 2) {
          LAS char* p = kf + 2 * (512 + 256 * (g - 2) + 16 * c);
          const unsigned v0 = d[hc][1][0], v1 = d[hc][1][1], v2 = d[hc][1][2], v3 = d[hc][1][3];
          *(LAS unsigned*)(p - 4) = v0;
          *(LAS u32x4*)(p) = (u32x4){v0, MIX(v0, v1), v1, v2};
          *(LAS u32x2*)(p + 16) = (u32x2){v2, MIX(v2, v3)};
          *(LAS unsigned*)(p + 24) = v3;
        }
        if (g < 2) {
          LAS char* p = kf + 2 * (256 + 16 * c + 8 * g);
          const unsigned v0 = d[hc][1][0], v1 = d[hc][1][1], v2 = d[hc][1][2], v3 = d[hc][1][3];
          *(LAS unsigned short*)(p - 2) = (unsigned short)v0;
          *(LAS u32x2*)(p) = (u32x2){v0, MIX(v1, v2)};
          *(LAS unsigned*)(p + 8) = v2;
          *(LAS unsigned short*)(p + 12) = (unsigned short)v3;
        }
        *(LAS u32x2*)(kf + 2 * (16 * c + 4 * g)) = (u32x2){MIX(d[hc][0][0], d[hc][0][1]), MIX(d[hc][0][2], d[hc][0][3])};
        if (lane < 6) *(LAS u32x4*)(kf - 96 + lane * 16) = (u32x4){0u, 0u, 0u, 0u};
      }
      lds_setv(ldsb + SY_UR + 8, 1u);
    }
    __builtin_amdgcn_s_setprio(1);
    lds_wait<4>(ldsb + SY_UR, 1u);
    conv_upper<CV_NP, 16>(lds, lane, 1, cp, r_s);
    conv_upper<CV_NP, 14>(lds, lane, 0, cp, r_s);
    publish<4>(lds, 2, ws, FL_S + (unsigned)((lane * 8 + (cp >> 5)) * 256 + (cp & 31) * 4), 32, tag, lane);
    EPI_X();
    lds_wait<1>(ldsb + SY_CNT + 8, 4u);
    poll_flags<4>((lane & 7) * 16u, r_fl, FL_S + (unsigned)((tt * 8 + dtile) * 256), tag);
    lds_setv(ldsb + SY_SR, 1u);
    EPI_S();
  } else {
    const int hc = wv - 2;
    const int dch = cp * 2 + hc;
    const rsrc_t r_xn = mk_rsrc(ws + WS_XNF, SL * DM * 2);
    const int row0 = bid * 8 + hc * 4;
    wait_vm<0>();
#pragma unroll
    for (int i = 0; i < 4; ++i) PIN(xr[i]);
    PIN(ep); PIN(dsk2);
    const unsigned tag = (unsigned)__builtin_amdgcn_readfirstlane(ep) + 1u;
    lds_setv(ldsb + SY_VAL, tag);
    lds_setv(ldsb + SY_VAL + 4, dsk2);
    {
      float f[32];
#pragma unroll
      for (int j = 0; j < 16; ++j) { f[2 * j] = lo_f(xr[j >> 2][j & 3]); f[2 * j + 1] = hi_f(xr[j >> 2][j & 3]); }
      float s0 = 0.f, s1 = 0.f;
#pragma unroll
      for (int j = 0; j < 32; j += 4) { s0 += f[j] + f[j + 1]; s1 += f[j + 2] + f[j + 3]; }
      const float mean = row_sum(s0 + s1) * (1.0f / DM);
      float q0 = 0.f, q1 = 0.f;
#pragma unroll
      for (int j = 0; j < 32; j += 2) {
        const float da = f[j] - mean, db = f[j + 1] - mean;
        q0 = fmaf(da, da, q0); q1 = fmaf(db, db, q1);
      }
      const float var = row_sum(q0 + q1) * (1.0f / DM);
      const float mb = rbf(mean);
      const float sb = rbf(__builtin_amdgcn_sqrtf(rbf(rbf(var) + 1e-5f)));
      if ((lane & 15) == 0)
        st64c((u32x2){pk_bf16(mb, sb), tag}, (unsigned)((lane >> 4) * 8), r_fl, FL_STAT + (unsigned)(row0 * 8));
      const float rs = __builtin_amdgcn_rcpf(sb);
      const unsigned vo = (unsigned)((lane & 15) * 32768 + (lane >> 4) * 16);
#pragma unroll
      for (int i = 0; i < 4; ++i) {
        u32x4 o;
#pragma unroll
        for (int j = 0; j < 4; ++j) {
          const unsigned d = pk_bf16(f[8 * i + 2 * j] - mb, f[8 * i + 2 * j + 1] - mb);
          o[j] = pk_bf16(lo_f(d) * rs, hi_f(d) * rs);
        }
        st128c(o, vo, r_xn, (unsigned)(i * 16 * 32768 + row0 * 16));
      }
    }
    bar_lgkm();
    wait_vm<0>();
    bar_only();
    if (hc == 0 && lane < 8) set_flag(ws, FL_ALL + (unsigned)(lane * 4096 + bid * 4), tag);
    bar_only();
    bar_only();
    u32x4 vf;
    ld128c(vf, lane * 16u, r_fl, FL_ALL + (unsigned)(xcd * 4096));
    bar_only();
    wait_vm<0>();
    PIN(vf);
    const bool all_ok = wave_all((vf[0] == tag) & (vf[1] == tag) & (vf[2] == tag) & (vf[3] == tag));
    unsigned uw[16];
    const unsigned usb = (unsigned)((cp >> 2) * 32768 + (cp & 3) * 4 + hc * 16384);
    if (!all_ok) {
      for (int r = 5; r <= 7; ++r) bar_only();
      poll_flags<4>(lane * 16u, r_fl, FL_ALL + (unsigned)(xcd * 4096), tag);
    }
#pragma unroll
    for (int m = 0; m < 16; ++m) ld32c(uw[m], lane * 16u, r_xn, usb + (unsigned)m * 1024u);
    if (lane == 0 && bid == 0 && hc == 0) set_flag(ws, FL_EP, tag);
    if (all_ok) { bar_only(); bar_only(); }
    wait_vm<0>();
    {
      const bool od = lane & 1;
      LAS char* up = lds + CV_U + 160 + hc * 2048 + (od ? 128 : 0) + (lane >> 1) * 4;
#pragma unroll
      for (int m = 0; m < 16; m += 2) {
        PIN(uw[m]); PIN(uw[m + 1]);
        const unsigned a = pk_f16(lo_f(uw[m]), hi_f(uw[m])), b = pk_f16(lo_f(uw[m + 1]), hi_f(uw[m + 1]));
        const unsigned y = (unsigned)__builtin_amdgcn_update_dpp(0, (int)(od ? a : b), 0xB1, 0xf, 0xf, true);
        const unsigned f0 = od ? y : a, f1 = od ? b : y;
        uw[m] = __builtin_amdgcn_perm(f1, f0, 0x05040100u);
        uw[m + 1] = __builtin_amdgcn_perm(f1, f0, 0x07060302u);
        PIN(uw[m]); PIN(uw[m + 1]);
      }
      if (all_ok) bar_only();
      __builtin_amdgcn_s_sleep(WSL);
      __builtin_amdgcn_s_setprio(3);
      if (lane < 10) *(LAS u32x4*)(lds + CV_U + hc * CV_USTR + lane * 16) = (u32x4){0u, 0u, 0u, 0u};
#pragma unroll
      for (int m = 0; m < 16; m += 2) {
        *(LAS unsigned*)(up + m * 128) = uw[m];
        *(LAS unsigned*)(up + CV_USTR + m * 128) = uw[m + 1];
      }
      lds_setv(ldsb + SY_UR + hc * 4, 1u);
    }
    __builtin_amdgcn_s_setprio(1);
    lds_wait<4>(ldsb + SY_UR, 1u);
    const float kscale = *(const LAS float*)(lds + SY_VAL + 8 + hc * 4);
    {
      const float dskip = hc ? hi_f(dsk2) : lo_f(dsk2);
      const int G = lane >> 4, q4 = (lane & 15) >> 2, p = lane & 3;
      const LAS char* pa = lds + KF + hc * CV_KSTR + 96 + 2 * (76 + 16 * p - 32 * G - 4 * q4 - 16);
      const LAS char* pb = lds + CV_U + hc * CV_USTR + 160 + 2 * (-76 + 32 * G + 4 * q4 + 4 * p);
      const LAS char* pu = lds + CV_U + hc * CV_USTR + 160 + lane * 2;
      f16x8 A[16], B[32];
#define CV_A(q) __builtin_bit_cast(f16x8, cat8(trrd(pa + 256 * (q) + 32), trrd(pa + 256 * (q))))
#define CV_B(w) __builtin_bit_cast(f16x8, cat8(trrd(pb + 128 * (w)), trrd(pb + 128 * (w) + 32)))
      A[0] = CV_A(0); B[0] = CV_B(0); B[1] = CV_B(1);
      A[1] = CV_A(1); B[2] = CV_B(2); B[3] = CV_B(3);
      f32x4 ca[2][2];
      _Float16 us[2][2];
      unsigned so16[16];
      float c3 = 0.f, c2 = 0.f, c1 = 0.f;
      auto tile_y = [&](const f32x4 a) __attribute__((always_inline)) {
        const float h3 = a[3];
        const float h2 = a[2] + shr_carry(c3, h3);
        const float h1 = a[1] + shr_carry(c2, h2);
        const float y = a[0] + shr_carry(c1, h1);
        c3 = dpp_mov<0x13C>(h3); c2 = dpp_mov<0x13C>(h2); c1 = dpp_mov<0x13C>(h1);
        return y * kscale;
      };
      static_for<0, CV_NP + 1>([&](auto jc) {
        constexpr int jj = decltype(jc)::value;
        if constexpr (jj + 2 < CV_NP) { A[jj + 2] = CV_A(jj + 2); B[2 * jj + 4] = CV_B(2 * jj + 4); B[2 * jj + 5] = CV_B(2 * jj + 5); }
        if constexpr (jj < CV_NP) {
          us[jj & 1][0] = *(const LAS _Float16*)(pu + jj * 256); us[jj & 1][1] = *(const LAS _Float16*)(pu + jj * 256 + 128);
          f32x4 a0 = {0.f, 0.f, 0.f, 0.f}, a1 = {0.f, 0.f, 0.f, 0.f};
#pragma unroll
          for (int q = jj; q >= 0; --q) {
            a0 = __builtin_amdgcn_mfma_f32_16x16x32_f16(A[q], B[2 * (jj - q)], a0, 0, 0, 0);
            a1 = __builtin_amdgcn_mfma_f32_16x16x32_f16(A[q], B[2 * (jj - q) + 1], a1, 0, 0, 0);
          }
          ca[jj & 1][0] = a0; ca[jj & 1][1] = a1;
        }
        if constexpr (jj >= 1) {
          const float y0 = tile_y(ca[(jj - 1) & 1][0]), y1 = tile_y(ca[(jj - 1) & 1][1]);
          const unsigned yq = pk_bf16(y0, y1);
          const unsigned uq = pk_bf16((float)us[(jj - 1) & 1][0] * dskip, (float)us[(jj - 1) & 1][1] * dskip);
          so16[jj - 1] = pk_bf16(lo_f(yq) + lo_f(uq), hi_f(yq) + hi_f(uq));
        }
        if constexpr (jj >= 1 && jj < CV_NP) {
#pragma unroll
          for (int k = 0; k < 2 * (jj + 1); ++k) {
            __builtin_amdgcn_sched_group_barrier(0x8, 1, 0);
            __builtin_amdgcn_sched_group_barrier(0x100, 1, 0);
            __builtin_amdgcn_sched_group_barrier(0x2, jj < 7 ? 4 : 2, 0);
          }
        }
        SCHED_FENCE();
        if constexpr (jj >= 1) st32c(so16[jj - 1], lane * 4u, r_s, (unsigned)(dch * 4096 + (jj - 1) * 256));
      });
    }
    publish<4>(lds, 2, ws, FL_S + (unsigned)((lane * 8 + (cp >> 5)) * 256 + (cp & 31) * 4), 32, tag, lane);
    EPI_X();
    lds_wait<1>(ldsb + SY_SR, 1u);
    EPI_S();
  }
  bar_lgkm();
  {
    int t2 = tid; PIN(t2);
    const u32x4 xv = *(const LAS u32x4*)(lds + EPX + t2 * 16);
    unsigned sv[8];
#pragma unroll
    for (int j = 0; j < 8; ++j) sv[j] = *(const LAS unsigned*)(lds + EPS + ((t2 & 7) * 8 + j) * 256 + (((t2 >> 5) + (t2 & 7)) & 15) * 16 + ((t2 >> 3) & 3) * 4);
    const int rb = w8 < 5 ? RED_A + w8 * 8192 : RED_B + (w8 - 5) * 8192;
    const int tr7 = (t2 >> 3) & 7;
    const LAS char* rp = lds + rb + tr7 * 256 + ((t2 >> 2) & 1) * 128;
    const bool odd = tt & 1;
    u32x4 o;
#pragma unroll
    for (int i = 0; i < 2; ++i) {
      const int ch = (((t2 & 3) * 2 + i) ^ tr7) << 4;
      f32x4 v = *(const LAS f32x4*)(rp + ch);
#pragma unroll
      for (int w = 1; w < 4; ++w) v += *(const LAS f32x4*)(rp + w * 2048 + ch);
#pragma unroll
      for (int jj = 0; jj < 2; ++jj) {
        const unsigned fp = pk_bf16(v[2 * jj], v[2 * jj + 1]);
        const float f0 = lo_f(fp), f1 = hi_f(fp);
        const float g0 = f0 * __builtin_amdgcn_rcpf(1.0f + __builtin_amdgcn_exp2f(f0 * -1.4426950408889634f));
        const float g1 = f1 * __builtin_amdgcn_rcpf(1.0f + __builtin_amdgcn_exp2f(f1 * -1.4426950408889634f));
        const unsigned gp = pk_bf16(g0, g1);
        const int j = 4 * i + 2 * jj;
        const float s0 = odd ? hi_f(sv[j]) : lo_f(sv[j]), s1 = odd ? hi_f(sv[j + 1]) : lo_f(sv[j + 1]);
        const unsigned sg = pk_bf16(s0 * lo_f(gp), s1 * hi_f(gp));
        const unsigned xw = xv[2 * i + jj];
        o[2 * i + jj] = pk_bf16(lo_f(xw) + lo_f(sg), hi_f(xw) + hi_f(sg));
      }
    }
    st128c(o, XO(t2), mk_rsrc(out, SL * DM * 2), 0u);
  }
}

extern "C" void kernel_launch(void* const* d_in, const int* in_sizes, int n_in, void* d_out,
                              int out_size, void* d_ws, size_t ws_size, hipStream_t stream) {
  (void)hipFuncSetAttribute((const void*)fused_kernel, hipFuncAttributeMaxDynamicSharedMemorySize, LDS_BYTES);
  fused_kernel<<<256, 512, LDS_BYTES, stream>>>(
      (const unsigned short*)d_in[0], (const unsigned short*)d_in[1], (const unsigned short*)d_in[2],
      (const unsigned short*)d_in[3], (const unsigned short*)d_in[4], (const unsigned short*)d_in[5],
      (const unsigned short*)d_in[6], (const unsigned*)d_in[7], (const unsigned*)d_in[8],
      (const unsigned short*)d_in[10], (const unsigned short*)d_in[11], (const unsigned short*)d_in[12],
      (char*)d_ws, (unsigned short*)d_out);
}
```

```cpp
#include <hip/hip_runtime.h>
#include <hip/hip_bf16.h>
#include <stdint.h>
#include <type_traits>

typedef __attribute__((ext_vector_type(4))) short s16x4;
typedef __attribute__((ext_vector_type(8))) short s16x8;
typedef __attribute__((ext_vector_type(8))) _Float16 f16x8;
typedef __attribute__((ext_vector_type(8))) __bf16 bf16x8;
typedef __attribute__((ext_vector_type(2))) float f32x2;
typedef __attribute__((ext_vector_type(4))) float f32x4;
typedef __attribute__((ext_vector_type(16))) float f32x16;
typedef __attribute__((ext_vector_type(4))) unsigned u32x4;
typedef __attribute__((ext_vector_type(3))) unsigned u32x3;
typedef __attribute__((ext_vector_type(2))) unsigned u32x2;
typedef __attribute__((ext_vector_type(2))) __bf16 bf16x2;
typedef __attribute__((ext_vector_type(2))) _Float16 f16x2;
#define LAS __attribute__((address_space(3)))
#define GAS __attribute__((address_space(1)))
typedef __amdgpu_buffer_rsrc_t rsrc_t;

constexpr int SL = 2048;
constexpr int DM = 512;
constexpr int NM = 128;
constexpr int FF = 1792;

constexpr size_t WS_XNF = 0;
constexpr size_t WS_ST = 2 * (size_t)SL * DM * 2;
constexpr size_t WS_HF = WS_ST + (size_t)SL * DM * 2;
constexpr size_t WS_FLAG = 2 * (size_t)SL * DM;
constexpr unsigned FL_ALL = 0, FL_STAT = 65536, FL_H = 131072, FL_S = 196608, FL_EP = 262144, FL_BYTES = 262400;

template <int I, int N, class F>
__device__ __forceinline__ void static_for(F&& f) {
  if constexpr (I < N) { f(std::integral_constant<int, I>{}); static_for<I + 1, N>(f); }
}

__device__ __forceinline__ unsigned pk_bf16(float lo, float hi) {
  f32x2 v = {lo, hi};
  return __builtin_bit_cast(unsigned, __builtin_convertvector(v, bf16x2));
}
__device__ __forceinline__ unsigned pk_f16(float lo, float hi) {
  f32x2 v = {lo, hi};
  return __builtin_bit_cast(unsigned, __builtin_convertvector(v, f16x2));
}
__device__ __forceinline__ float lo_f(unsigned p) { return __uint_as_float(p << 16); }
__device__ __forceinline__ float hi_f(unsigned p) { return __uint_as_float(p & 0xffff0000u); }
__device__ __forceinline__ float rbf(float x) { return lo_f(pk_bf16(x, x)); }

template <int CTRL>
__device__ __forceinline__ float dpp_mov(float v) {
  return __int_as_float(__builtin_amdgcn_update_dpp(0, __float_as_int(v), CTRL, 0xf, 0xf, true));
}
__device__ __forceinline__ float shr_carry(float c, float v) {
  return __builtin_bit_cast(float, __builtin_amdgcn_update_dpp(__builtin_bit_cast(int, c), __builtin_bit_cast(int, v), 0x138, 0xf, 0xf, false));
}
__device__ __forceinline__ float rdlane(float v, int l) {
  return __int_as_float(__builtin_amdgcn_readlane(__float_as_int(v), l));
}
__device__ __forceinline__ float wave_sum(float v) {
  v += dpp_mov<0xB1>(v);
  v += dpp_mov<0x4E>(v);
  v += dpp_mov<0x141>(v);
  v += dpp_mov<0x140>(v);
  return (rdlane(v, 0) + rdlane(v, 16)) + (rdlane(v, 32) + rdlane(v, 48));
}
__device__ __forceinline__ float row_sum(float v) {
  v += dpp_mov<0xB1>(v);
  v += dpp_mov<0x4E>(v);
  v += dpp_mov<0x141>(v);
  v += dpp_mov<0x140>(v);
  return v;
}
__device__ __forceinline__ float half_sum(float v, int lane) {
  v += dpp_mov<0xB1>(v);
  v += dpp_mov<0x4E>(v);
  v += dpp_mov<0x141>(v);
  v += dpp_mov<0x140>(v);
  const float lo = rdlane(v, 0) + rdlane(v, 16), hi = rdlane(v, 32) + rdlane(v, 48);
  return lane < 32 ? lo : hi;
}
__device__ __forceinline__ float wave_max(float v) {
  v = fmaxf(v, dpp_mov<0xB1>(v));
  v = fmaxf(v, dpp_mov<0x4E>(v));
  v = fmaxf(v, dpp_mov<0x141>(v));
  v = fmaxf(v, dpp_mov<0x140>(v));
  return fmaxf(fmaxf(rdlane(v, 0), rdlane(v, 16)), fmaxf(rdlane(v, 32), rdlane(v, 48)));
}

__device__ __forceinline__ rsrc_t mk_rsrc(const void* p, unsigned bytes) {
  return __builtin_amdgcn_make_buffer_rsrc((void*)p, (short)0, (int)bytes, 0x00020000);
}
__device__ __forceinline__ void dma16(unsigned lds_dst, unsigned voff, rsrc_t r, unsigned soff) {
  asm volatile("s_mov_b32 m0, %0\n\ts_nop 0\n\tbuffer_load_dwordx4 %1, %2, %3 offen lds"
               :: "s"(lds_dst), "v"(voff), "s"(r), "s"(soff) : "memory");
}
__device__ __forceinline__ void dma16x4(unsigned lds_dst, unsigned v0, unsigned v1, unsigned v2, unsigned v3, rsrc_t r, unsigned soff) {
  asm volatile("s_mov_b32 m0, %0\n\ts_nop 0\n\t"
               "buffer_load_dwordx4 %1, %5, %6 offen lds\n\t"
               "buffer_load_dwordx4 %2, %5, %6 offen offset:1024 lds\n\t"
               "buffer_load_dwordx4 %3, %5, %6 offen offset:2048 lds\n\t"
               "buffer_load_dwordx4 %4, %5, %6 offen offset:3072 lds"
               :: "s"(lds_dst), "v"(v0), "v"(v1), "v"(v2), "v"(v3), "s"(r), "s"(soff) : "memory");
}
__device__ __forceinline__ void dma16x3(unsigned lds_dst, unsigned v0, unsigned v1, unsigned v2, rsrc_t r, unsigned soff) {
  asm volatile("s_mov_b32 m0, %0\n\ts_nop 0\n\t"
               "buffer_load_dwordx4 %1, %4, %5 offen lds\n\t"
               "buffer_load_dwordx4 %2, %4, %5 offen offset:1024 lds\n\t"
               "buffer_load_dwordx4 %3, %4, %5 offen offset:2048 lds"
               :: "s"(lds_dst), "v"(v0), "v"(v1), "v"(v2), "s"(r), "s"(soff) : "memory");
}
__device__ __forceinline__ void ld128(u32x4& dst, unsigned voff, rsrc_t r, unsigned soff) {
  asm volatile("buffer_load_dwordx4 %0, %1, %2, %3 offen" : "=v"(dst) : "v"(voff), "s"(r), "s"(soff) : "memory");
}
__device__ __forceinline__ void ld128c(u32x4& dst, unsigned voff, rsrc_t r, unsigned soff) {
  asm volatile("buffer_load_dwordx4 %0, %1, %2, %3 offen sc1" : "=v"(dst) : "v"(voff), "s"(r), "s"(soff) : "memory");
}
__device__ __forceinline__ void ld32(unsigned& dst, unsigned voff, rsrc_t r, unsigned soff) {
  asm volatile("buffer_load_dword %0, %1, %2, %3 offen" : "=v"(dst) : "v"(voff), "s"(r), "s"(soff) : "memory");
}
__device__ __forceinline__ void ld64c(u32x2& dst, unsigned voff, rsrc_t r, unsigned soff) {
  asm volatile("buffer_load_dwordx2 %0, %1, %2, %3 offen sc1" : "=v"(dst) : "v"(voff), "s"(r), "s"(soff) : "memory");
}
__device__ __forceinline__ void ld32c(unsigned& dst, unsigned voff, rsrc_t r, unsigned soff) {
  asm volatile("buffer_load_dword %0, %1, %2, %3 offen sc1" : "=v"(dst) : "v"(voff), "s"(r), "s"(soff) : "memory");
}
__device__ __forceinline__ void st32c(unsigned v, unsigned voff, rsrc_t r, unsigned soff) {
  asm volatile("buffer_store_dword %0, %1, %2, %3 offen sc1" :: "v"(v), "v"(voff), "s"(r), "s"(soff) : "memory");
}
__device__ __forceinline__ void st64c(u32x2 v, unsigned voff, rsrc_t r, unsigned soff) {
  asm volatile("buffer_store_dwordx2 %0, %1, %2, %3 offen sc1\n\ts_nop 1" :: "v"(v), "v"(voff), "s"(r), "s"(soff) : "memory");
}
__device__ __forceinline__ void ld16(unsigned& dst, unsigned voff, rsrc_t r, unsigned soff) {
  asm volatile("buffer_load_ushort %0, %1, %2, %3 offen" : "=v"(dst) : "v"(voff), "s"(r), "s"(soff) : "memory");
}
__device__ __forceinline__ void st128c(u32x4 v, unsigned voff, rsrc_t r, unsigned soff) {
  asm volatile("buffer_store_dwordx4 %0, %1, %2, %3 offen sc1\n\ts_nop 1" :: "v"(v), "v"(voff), "s"(r), "s"(soff) : "memory");
}
template <int N>
__device__ __forceinline__ void wait_vm() { asm volatile("s_waitcnt vmcnt(%0)" :: "n"(N > 63 ? 63 : N) : "memory"); }
__device__ __forceinline__ void bar_lgkm() { asm volatile("s_waitcnt lgkmcnt(0)\n\ts_barrier" ::: "memory"); }
__device__ __forceinline__ void bar_only() { asm volatile("s_barrier" ::: "memory"); }
#define PIN(x) asm volatile("" : "+v"(x))
#define SCHED_FENCE() __builtin_amdgcn_sched_barrier(0)

__device__ __forceinline__ s16x4 trrd(const LAS char* p) {
  return __builtin_amdgcn_ds_read_tr16_b64_v4i16((LAS s16x4*)p);
}
__device__ __forceinline__ s16x8 cat8(s16x4 a, s16x4 b) {
  return __builtin_shufflevector(a, b, 0, 1, 2, 3, 4, 5, 6, 7);
}

__device__ __forceinline__ bool wave_all(bool p) { return __builtin_amdgcn_ballot_w64(!p) == 0ull; }
template <int SLP = 2>
__device__ __forceinline__ void poll_flags(unsigned voff, rsrc_t r, unsigned soff, unsigned tag) {
#pragma unroll 1
  for (int it = 0; it < (1 << 18); ++it) {
    u32x4 v;
    asm volatile("buffer_load_dwordx4 %0, %1, %2, %3 offen sc1\n\ts_waitcnt vmcnt(0)"
                 : "=&v"(v) : "v"(voff), "s"(r), "s"(soff) : "memory");
    const bool ok = (v[0] == tag) & (v[1] == tag) & (v[2] == tag) & (v[3] == tag);
    if (wave_all(ok)) break;
    __builtin_amdgcn_s_sleep(SLP);
  }
}
__device__ __forceinline__ void set_flag(char* ws, unsigned off, unsigned tag) {
  __hip_atomic_store((GAS unsigned*)(ws + WS_FLAG + off), tag, __ATOMIC_RELAXED, __HIP_MEMORY_SCOPE_AGENT);
}

__device__ __forceinline__ f32x2 cmul(f32x2 a, f32x2 b) {
  f32x2 r;
  r.x = fmaf(a.x, b.x, -(a.y * b.y));
  r.y = fmaf(a.x, b.y, a.y * b.x);
  return r;
}
__device__ __forceinline__ f32x2 cexp_l(float a, float b, float l) {
  const float mag = __builtin_amdgcn_exp2f(a * l * 1.4426950408889634f);
  const float x = b * l;
  const float C1 = 0.15915494309189535f;
  const float C2 = (float)(0.15915494309189533577 - (double)0.15915494309189535f);
  const float r = fmaf(x, C2, fmaf(x, C1, -__builtin_rintf(x * C1)));
  f32x2 z;
  z.x = mag * __builtin_amdgcn_cosf(r);
  z.y = mag * __builtin_amdgcn_sinf(r);
  return z;
}

constexpr int W_UPOFF = 14336 + 128;
constexpr int XOFF = W_UPOFF + 14336;
constexpr int STG = XOFF + 16384;
constexpr int KGS = 3 * STG;
constexpr int KF = KGS + 8192;
constexpr int CV_KSTR = (48 + 2048) * 2;
constexpr int CV_USTR = (80 + 2048) * 2;
constexpr int CV_U = KF + 2 * CV_KSTR;
constexpr int CSUM = CV_U + 2 * CV_USTR;
constexpr int STAT = CSUM + 896;
constexpr int SY = STAT + 512;
constexpr int SY_LD = SY, SY_MC = SY + 16, SY_ST = SY + 4, SY_HR = SY + 8, SY_SR = SY + 12, SY_UR = SY + 32  ,
              SY_CNT = SY + 48, SY_VAL = SY + 64, SY_KG = SY + 80, SY_END = SY + 96;
constexpr int LDS_BYTES = SY_END;
static_assert(LDS_BYTES <= 163840, "lds");
constexpr int G2_RED = 272;
constexpr int EPX = KGS;
constexpr int EPS = KF;
constexpr int LDLY = 20;
constexpr int WSL = 4;
constexpr unsigned WS_USED = (unsigned)(WS_HF + (size_t)SL * FF * 2);
constexpr unsigned CODE_BYTES = 49152;
constexpr int CV_NP = 12;
constexpr int G2_NR = 13;
constexpr int cmax(int a, int b) { return a > b ? a : b; }
constexpr int cmin(int a, int b) { return a < b ? a : b; }
constexpr int g2_base(int i) { return (((i % 15) / 5 + 2) % 3) * STG + (i % 5) * 8192; }
constexpr int RED_A = g2_base(15), RED_B = g2_base(20);
constexpr int G2_H0 = 6;
constexpr int g2_issued(int i) { return cmin(28, G2_H0 + (3 * i) / 2); }

__device__ __forceinline__ void lds_setv(unsigned addr, unsigned v) {
  asm volatile("ds_write_b32 %0, %1" :: "v"(addr), "v"(v) : "memory");
}
__device__ __forceinline__ unsigned lds_min4(unsigned addr) {
  u32x4 v;
  asm volatile("ds_read_b128 %0, %1\n\ts_waitcnt lgkmcnt(0)" : "=&v"(v) : "v"(addr) : "memory");
  return (unsigned)__builtin_amdgcn_readfirstlane(min(min(v[0], v[1]), min(v[2], v[3])));
}
__device__ __forceinline__ unsigned lds_get1(unsigned addr) {
  unsigned v;
  asm volatile("ds_read_b32 %0, %1\n\ts_waitcnt lgkmcnt(0)" : "=&v"(v) : "v"(addr) : "memory");
  return (unsigned)__builtin_amdgcn_readfirstlane(v);
}
template <int W>
__device__ __forceinline__ unsigned lds_val(unsigned addr) { return W == 4 ? lds_min4(addr) : lds_get1(addr); }
template <int W>
__device__ __forceinline__ void lds_spin(unsigned addr, unsigned need) {
#pragma unroll 1
  for (int it = 0; it < (1 << 20); ++it) {
    if (lds_val<W>(addr) >= need) break;
    __builtin_amdgcn_s_sleep(1);
  }
}
template <int W>
__device__ __forceinline__ void lds_wait(unsigned addr, unsigned need) {
  if (__builtin_expect(lds_val<W>(addr) < need, 0)) lds_spin<W>(addr, need);
}
template <int NW, bool DRAIN = true>
__device__ __forceinline__ void publish(LAS char* lds, int cnt, char* ws, unsigned off, int nl, unsigned tag, int lane) {
  if (DRAIN) wait_vm<0>();
  unsigned old = 0;
  if (lane == 0) old = __hip_atomic_fetch_add((LAS unsigned*)(lds + SY_CNT + cnt * 4), 1u, __ATOMIC_RELAXED, __HIP_MEMORY_SCOPE_WORKGROUP);
  if (__builtin_amdgcn_readfirstlane(old) == NW - 1 && lane < nl) set_flag(ws, off, tag);
}

template <int PA, int PB>
__device__ __forceinline__ void conv_upper(LAS char* lds, int lane, int hc, int cp, rsrc_t r_s) {
  const unsigned dsk2 = *(const LAS unsigned*)(lds + SY_VAL + 4);
  const float kscale = *(const LAS float*)(lds + SY_VAL + 8 + hc * 4);
  const float dskip = hc ? hi_f(dsk2) : lo_f(dsk2);
  const int G = lane >> 4, q4 = (lane & 15) >> 2, p = lane & 3;
  const LAS char* pa = lds + KF + hc * CV_KSTR + 96 + 2 * (76 + 16 * p - 32 * G - 4 * q4 - 16);
  const LAS char* pb = lds + CV_U + hc * CV_USTR + 160 + 2 * (-76 + 32 * G + 4 * q4 + 4 * p);
  const LAS char* pu = lds + CV_U + hc * CV_USTR + 160 + lane * 2;
  constexpr int J0 = 2 * PA - 1, J1 = 2 * PB, NT = J1 - J0;
  f16x8 A[16], B[32];
  f32x4 ca[NT];
#define CV_A(q) __builtin_bit_cast(f16x8, cat8(trrd(pa + 256 * (q) + 32), trrd(pa + 256 * (q))))
#define CV_B(w) __builtin_bit_cast(f16x8, cat8(trrd(pb + 128 * (w)), trrd(pb + 128 * (w) + 32)))
  B[0] = CV_B(0); B[1] = CV_B(1); B[2] = CV_B(2);
  constexpr int QA = (J0 - 1) / 2;
#pragma unroll
  for (int q = PB - 1; q >= QA; --q) A[q] = CV_A(q);
#pragma unroll
  for (int j = 0; j < NT; ++j) ca[j] = (f32x4){0.f, 0.f, 0.f, 0.f};
  static_for<0, J1>([&](auto wc) {
    constexpr int w = decltype(wc)::value;
    if constexpr (w + 3 < J1) B[w + 3] = CV_B(w + 3);
    if constexpr ((w & 1) == 0 && QA - 1 - w / 2 >= 0) A[QA - 1 - w / 2] = CV_A(QA - 1 - w / 2);
#pragma unroll
    for (int q = PB - 1; q >= 0; --q) {
      const int j = w + 2 * q;
      if (j >= J0 && j < J1) ca[j - J0] = __builtin_amdgcn_mfma_f32_16x16x32_f16(A[q], B[w], ca[j - J0], 0, 0, 0);
    }
    SCHED_FENCE();
  });
  float c3 = 0.f, c2 = 0.f, c1 = 0.f, yv[NT];
#pragma unroll
  for (int j = 0; j < NT; ++j) {
    const f32x4 a = ca[j];
    const float h3 = a[3];
    const float h2 = a[2] + shr_carry(c3, h3);
    const float h1 = a[1] + shr_carry(c2, h2);
    yv[j] = a[0] + shr_carry(c1, h1);
    c3 = dpp_mov<0x13C>(h3); c2 = dpp_mov<0x13C>(h2); c1 = dpp_mov<0x13C>(h1);
  }
#pragma unroll
  for (int jp = PA; jp < PB; ++jp) {
    const float u0 = (float)*(const LAS _Float16*)(pu + jp * 256), u1 = (float)*(const LAS _Float16*)(pu + jp * 256 + 128);
    const unsigned yq = pk_bf16(yv[2 * jp - J0] * kscale, yv[2 * jp + 1 - J0] * kscale);
    const unsigned uq = pk_bf16(u0 * dskip, u1 * dskip);
    st32c(pk_bf16(lo_f(yq) + lo_f(uq), hi_f(yq) + hi_f(uq)), lane * 4u, r_s, (unsigned)((cp * 2 + hc) * 4096 + jp * 256));
  }
#undef CV_A
#undef CV_B
}

#if defined(__HIP_DEVICE_COMPILE__)
__attribute__((target("no-packed-fp32-ops")))
#endif
__global__ __launch_bounds__(512, 2) void fused_kernel(
    const unsigned short* __restrict__ x, const unsigned short* __restrict__ A_re,
    const unsigned short* __restrict__ A_im, const unsigned short* __restrict__ B_re,
    const unsigned short* __restrict__ B_im, const unsigned short* __restrict__ C_re,
    const unsigned short* __restrict__ C_im, const unsigned* __restrict__ log_dt2,
    const unsigned* __restrict__ dsk2p, const unsigned short* __restrict__ wg,
    const unsigned short* __restrict__ wu, const unsigned short* __restrict__ wd,
    char* ws, unsigned short* __restrict__ out) {
  extern __shared__ __attribute__((aligned(16))) char smem[];
  unsigned long long pc0;
  asm volatile("s_getpc_b64 %0" : "=s"(pc0));
  LAS char* lds = (LAS char*)smem;
  const unsigned ldsb = (unsigned)(uintptr_t)lds;
  const int tid = threadIdx.x, lane = tid & 63;
  const int w8 = __builtin_amdgcn_readfirstlane(tid >> 6);
  const int wv = w8 & 3;
  const int bid = blockIdx.x;
  const int xcd = bid & 7, bi = bid >> 3;
  unsigned pf0 = 0;
  if (w8 == 5) ld32(pf0, (unsigned)((bi * 16 + (lane & 15)) * 128), mk_rsrc((const void*)pc0, CODE_BYTES), 0u);
  asm volatile("" :: "s"(x), "s"(A_re), "s"(A_im), "s"(B_re), "s"(B_im), "s"(C_re), "s"(C_im), "s"(log_dt2),
               "s"(dsk2p), "s"(wg), "s"(wu), "s"(wd), "s"(ws), "s"(out));
  const int mt = (xcd >> 2) * 8 + (bi >> 2);
  const int ft = (xcd & 3) * 4 + (bi & 3);
  const int rot = ((bi >> 2) + 2 * (bi & 3)) & 7;
  const int cp = xcd * 32 + bi;
  const int tt = xcd * 4 + (bi >> 3);
  const int dtile = bi & 7;
  const int rot2 = ((bi >> 3) * 7 + dtile * 3) % 28;

  const rsrc_t r_x = mk_rsrc(x, SL * DM * 2);
  const rsrc_t r_s = mk_rsrc(ws + WS_ST, SL * DM * 2);
  const rsrc_t r_fl = mk_rsrc(ws + WS_FLAG, FL_BYTES);
#define G2_KS(w, i) ((unsigned)((w) * 28 + (((i) + rot2) >= 28 ? (i) + rot2 - 28 : (i) + rot2)))
#define XO(t_) ((unsigned)((tt * 64 + ((t_) >> 3)) * (DM * 2) + dtile * 128 + ((t_) & 7) * 16))
#define EPI_X() do { int l_ = lane; PIN(l_); u32x4 xw_[2]; \
    _Pragma("unroll") for (int j = 0; j < 2; ++j) ld128(xw_[j], XO(wv * 128 + j * 64 + l_), r_x, 0u); \
    lds_wait<4>(ldsb + SY_UR, 1u); wait_vm<0>(); \
    _Pragma("unroll") for (int j = 0; j < 2; ++j) { PIN(xw_[j]); *(LAS u32x4*)(lds + EPX + (wv * 128 + j * 64 + l_) * 16) = xw_[j]; } } while (0)
#define EPI_S() do { int l_ = lane; PIN(l_); u32x4 sw_[4]; \
    const unsigned so = (unsigned)((dtile * 64 + wv * 16 + (l_ >> 4)) * 4096 + (tt >> 1) * 256 + (l_ & 15) * 16); \
    _Pragma("unroll") for (int j = 0; j < 4; ++j) ld128c(sw_[j], so, r_s, (unsigned)(j * 16384)); \
    wait_vm<0>(); \
    _Pragma("unroll") for (int j = 0; j < 4; ++j) { PIN(sw_[j]); \
      *(LAS u32x4*)(lds + EPS + (wv * 16 + j * 4 + (l_ >> 4)) * 256 + ((l_ + wv * 2 + (j >> 1)) & 15) * 16) = sw_[j]; } } while (0)

  const rsrc_t r_wg = mk_rsrc(wg, DM * FF * 2), r_wu = mk_rsrc(wu, DM * FF * 2);
  unsigned woff[7], xoff[4];
  auto g1_stage = [&](int s) __attribute__((always_inline)) {
    const unsigned st = s == 0 ? 0u : (unsigned)(1 + (s - 1 + rot) % 7), lb = ldsb + (unsigned)((s % 3) * STG);
#pragma unroll
    for (int i = 0; i < 4; ++i)
      dma16x4(lb + XOFF + i * 4096, xoff[0], xoff[1], xoff[2], xoff[3], r_x, (unsigned)((mt * 128 + i * 32) * 1024) + st * 128u);
#pragma unroll
    for (int h = 0; h < 2; ++h) {
      const unsigned wso = (unsigned)(ft * 224) + (st * 64u + h * 32u) * (FF * 2);
      dma16x4(lb + h * 7168, woff[0], woff[1], woff[2], woff[3], r_wg, wso);
      dma16x3(lb + h * 7168 + 4096, woff[4], woff[5], woff[6], r_wg, wso);
      dma16x4(lb + W_UPOFF + h * 7168, woff[0], woff[1], woff[2], woff[3], r_wu, wso);
      dma16x3(lb + W_UPOFF + h * 7168 + 4096, woff[4], woff[5], woff[6], r_wu, wso);
    }
  };
  unsigned prm[6], ldt2, ep, dsk2;
  u32x4 xr[4];
  if (w8 < 4) {
    const unsigned po = (unsigned)((cp * 2 + (wv & 1)) * NM * 2);
    ld32(prm[0], lane * 4u, mk_rsrc(A_re, DM * NM * 2), po);
    ld32(prm[1], lane * 4u, mk_rsrc(A_im, DM * NM * 2), po);
    ld32(prm[2], lane * 4u, mk_rsrc(B_re, DM * NM * 2), po);
    ld32(prm[3], lane * 4u, mk_rsrc(B_im, DM * NM * 2), po);
    ld32(prm[4], lane * 4u, mk_rsrc(C_re, DM * NM * 2), po);
    ld32(prm[5], lane * 4u, mk_rsrc(C_im, DM * NM * 2), po);
    ld32(ldt2, 0u * lane, mk_rsrc(log_dt2, DM * 2), (unsigned)(cp * 4));
  } else if (w8 >= 6) {
    const unsigned vo = (unsigned)((lane >> 4) * 1024 + (lane & 15) * 16);
#pragma unroll
    for (int i = 0; i < 4; ++i) ld128(xr[i], vo, r_x, (unsigned)((bid * 8 + (wv - 2) * 4) * 1024 + i * 256));
    ld32c(ep, 0u * lane, r_fl, FL_EP);
    ld32(dsk2, 0u * lane, mk_rsrc(dsk2p, DM * 2), (unsigned)(cp * 4));
  } else if (w8 == 4) {
    __builtin_amdgcn_s_setprio(3);
#pragma unroll
    for (int i = 0; i < 4; ++i)
      xoff[i] = (unsigned)((lane >> 3) * 1024 + (((lane & 7) ^ ((lane >> 4) + 4 * (i & 1))) * 16) + i * 7168);
#pragma unroll
    for (int i = 0; i < 7; ++i) {
      const int c = i * 64 + lane, row = c / 14, col = c - row * 14;
      woff[i] = (unsigned)(row * (FF * 2) + col * 16 - (i & 3) * 1024);
    }
    __builtin_amdgcn_s_sleep(LDLY);
    g1_stage(0);
  }
  asm volatile("; first loads issued" ::: "memory");
  if (w8 < 4) {
    const rsrc_t r_h = mk_rsrc(ws + WS_HF, SL * FF * 2);
    {
    const int hc = wv & 1, m = wv >> 1;
    if (tid < 4) *(LAS u32x4*)(lds + SY + tid * 16) = (u32x4){0u, 0u, 0u, tid == 2 ? 1u : 0u};
    __builtin_amdgcn_s_setprio(2);
    {
      wait_vm<0>();
      PIN(ldt2);
      const float ldt = hc ? hi_f(ldt2) : lo_f(ldt2);
      const float L2E = 1.4426950408889634f, L2E_LO = (float)(1.4426950408889634074 - (double)1.4426950408889634f);
      const float th = ldt * L2E;
      const float tl = fmaf(ldt, L2E, -th) + ldt * L2E_LO;
      const float e0 = __builtin_amdgcn_exp2f(th);
      const float dt = rbf(fmaf(e0 * 0.6931471805599453f, tl, e0));
      wait_vm<0>();
#pragma unroll
      for (int i = 0; i < 6; ++i) PIN(prm[i]);
      float av[2], bv[2];
      f32x2 wq[2];
#pragma unroll
      for (int m = 0; m < 2; ++m) {
        av[m] = dt * (m ? hi_f(prm[0]) : lo_f(prm[0]));
        bv[m] = dt * (m ? hi_f(prm[1]) : lo_f(prm[1]));
        const f32x2 db = {dt * (m ? hi_f(prm[2]) : lo_f(prm[2])), dt * (m ? hi_f(prm[3]) : lo_f(prm[3]))};
        const f32x2 cc = {m ? hi_f(prm[4]) : lo_f(prm[4]), m ? hi_f(prm[5]) : lo_f(prm[5])};
        wq[m] = cmul(cc, db);
      }
      const float amax = fmaxf(wave_max(fmaxf(av[0], av[1])), 0.f);
      const float g2 = amax * L2E;
      const float wm = wave_max(fmaxf(fmaxf(fabsf(wq[0].x), fabsf(wq[0].y)), fmaxf(fabsf(wq[1].x), fabsf(wq[1].y))));
      int we = __builtin_amdgcn_frexp_expf(wm);
      we = we < -100 ? -100 : we;
      const float wsc = __builtin_amdgcn_ldexpf(1.0f, -we);
      f32x4 kacc[3];
#pragma unroll
      for (int t = 0; t < 3; ++t) kacc[t] = (f32x4){0.f, 0.f, 0.f, 0.f};
      const int kgs = (m ? CV_U : KGS) + hc * 4096;
      LAS char* tw[4];
#pragma unroll
      for (int c = 0; c < 4; ++c) tw[c] = lds + kgs + lane * 64 + ((c ^ ((lane >> 1) & 3)) << 4);
      const LAS char* tr[2];
      {
        const int G = lane >> 4, q4 = (lane & 15) >> 2, p = lane & 3;
#pragma unroll
        for (int t = 0; t < 2; ++t)
          tr[t] = lds + kgs + (4 * G + 2 * t + (q4 >> 1)) * 64 + ((((q4 & 1) * 2 + (p >> 1)) ^ ((2 * G + t) & 3)) << 4) + (p & 1) * 8;
      }
      auto put16 = [&](const f32x2 (&e)[16]) __attribute__((always_inline)) {
#pragma unroll
        for (int h = 0; h < 2; ++h) {
          u32x4 re, im;
#pragma unroll
          for (int j = 0; j < 4; ++j) {
            re[j] = pk_f16(e[8 * h + 2 * j].x, e[8 * h + 2 * j + 1].x);
            im[j] = pk_f16(e[8 * h + 2 * j].y, e[8 * h + 2 * j + 1].y);
          }
          *(LAS u32x4*)tw[h] = re;
          *(LAS u32x4*)tw[2 + h] = im;
        }
      };
#define KG_FRAG(s) __builtin_bit_cast(f16x8, cat8(trrd(tr[0] + (s) * 1024), trrd(tr[1] + (s) * 1024)))
#define KG_RD() do { _Pragma("unroll") for (int s = 0; s < 4; ++s) pf[s] = KG_FRAG(s); SCHED_FENCE(); } while (0)
#define KG_MMA(t) do { SCHED_FENCE(); _Pragma("unroll") for (int s = 0; s < 4; ++s) \
        kacc[t] = __builtin_amdgcn_mfma_f32_16x16x32_f16(pf[s], qf[s], kacc[t], 0, 0, 0); } while (0)
      {
        const float a = (m ? av[1] : av[0]) - amax, b = m ? bv[1] : bv[0];
        f32x2 w = m ? wq[1] : wq[0];
        w.x *= wsc; w.y *= -wsc;
        f32x2 e[16];
        {
          const f32x2 z16 = cexp_l(a, b, 16.f);
          e[0] = (f32x2){1.f, 0.f}; e[1] = z16; e[2] = cmul(z16, z16); e[3] = cmul(e[2], z16);
          const f32x2 z64 = cmul(e[2], e[2]);
#pragma unroll
          for (int c = 4; c < 16; ++c) e[c] = cmul(e[c - 4], z64);
          put16(e);
        }
        f16x8 qf[4], pf[4];
#pragma unroll
        for (int s = 0; s < 4; ++s) qf[s] = KG_FRAG(s);
        SCHED_FENCE();
        const f32x2 z1 = cexp_l(a, -b, 1.f);
        const f32x2 z2 = cmul(z1, z1), z4 = cmul(z2, z2);
        e[0] = w; e[1] = cmul(w, z1); e[2] = cmul(w, z2); e[3] = cmul(e[1], z2);
#pragma unroll
        for (int c = 4; c < 16; ++c) e[c] = cmul(e[c - 4], z4);
        put16(e);
        KG_RD();
        const f32x2 z256 = cexp_l(a, -b, 256.f);
        const f32x2 z512 = cmul(z256, z256);
        const f32x2 w2 = cmul(w, z512);
        e[0] = cmul(w, z256); e[1] = cmul(e[0], z2); e[8] = w2; e[12] = cmul(e[0], z512);
        const f32x2 w3 = e[12];
#pragma unroll
        for (int i = 1; i < 4; ++i) {
          e[2 * i] = cmul(e[2 * i - 2], z4); e[2 * i + 1] = cmul(e[2 * i - 1], z4);
          e[8 + i] = cmul(e[7 + i], z4); e[12 + i] = cmul(e[11 + i], z4);
        }
        KG_MMA(0);
        put16(e);
        KG_RD();
        const f32x2 z8 = cmul(z4, z4);
        e[0] = cmul(w2, z512); e[2] = cmul(w3, z512); e[4] = cmul(e[0], z512); e[6] = cmul(e[2], z512);
        e[8] = cmul(e[4], z512);
#pragma unroll
        for (int i = 0; i < 4; ++i) e[2 * i + 1] = cmul(e[2 * i], z8);
#pragma unroll
        for (int i = 9; i < 16; ++i) e[i] = (f32x2){0.f, 0.f};
        KG_MMA(1);
        put16(e);
        KG_RD();
        KG_MMA(2);
      }
#pragma unroll
      for (int t = 0; t < 3; ++t) *(LAS f32x4*)(lds + (m ? KF + hc * CV_KSTR : KGS + hc * 4096) + t * 1024 + lane * 16) = kacc[t];
      if (m) *(LAS f32x2*)(lds + SY_KG + hc * 8) = (f32x2){g2, (float)we};
    }
    }
    f32x16 acc[7];
    f32x4 csa[2];
    unsigned tag;
    {
      const int G = lane >> 4;
      const LAS char* wa0 = lds + (G & 1) * W_UPOFF + (8 * (G >> 1) + ((lane & 15) >> 2)) * 224 + (lane & 3) * 8;
      const LAS char* wa[7];
#pragma unroll
      for (int T = 0; T < 7; ++T) wa[T] = wa0 + 32 * ((T + 2 * wv) % 7);
      const LAS char* xa[4];
      {
        const int row = 32 * wv + (lane & 31), sw = (row >> 1) & 7;
#pragma unroll
        for (int ks = 0; ks < 4; ++ks) xa[ks] = lds + XOFF + row * 128 + (((2 * ks + (lane >> 5)) ^ sw) << 4);
      }
      constexpr int PD = 3;
      bf16x8 Af[196], Bf[32];
      const unsigned o1 = ((lane & 15) == 4 * ((lane >> 4) & 1)) ? 0x3f803f80u : 0u;
      u32x4 onesq = {o1, o1, o1, o1};
      PIN(onesq);
      const bf16x8 ones = __builtin_bit_cast(bf16x8, onesq);
#define A_READ(i, T) __builtin_bit_cast(bf16x8, cat8(trrd(wa[T] + (((i) >> 2) % 3) * STG + (16 * ((i) & 3)) * 224), \
                                                      trrd(wa[T] + (((i) >> 2) % 3) * STG + (16 * ((i) & 3) + 4) * 224)))
#define B_READ(i) (*(const LAS bf16x8*)(xa[(i) & 3] + (((i) >> 2) % 3) * STG))
#pragma unroll
      for (int T = 0; T < 7; ++T) acc[T] = (f32x16){};
      csa[0] = (f32x4){0.f, 0.f, 0.f, 0.f}; csa[1] = csa[0];
      bar_lgkm();
      tag = lds_get1(ldsb + SY_VAL);
      Bf[0] = B_READ(0);
#pragma unroll
      for (int T = 0; T < PD; ++T) Af[T] = A_READ(0, T);
      bf16x8 Al[7][4];
      u32x2 stg;
      float cs[7];
      static_for<0, 196>([&](auto nc) {
        constexpr int n = decltype(nc)::value, i = n / 7, T = n % 7;
        constexpr int m = n + PD, im = m / 7, Tm = m % 7;
        if constexpr (n == 70) ld64c(stg, (unsigned)((lane & 31) * 8), r_fl, FL_STAT + (unsigned)((mt * 128 + wv * 32) * 8));
        if constexpr (n == 27 * 7 + 2) {
          if (lane < 32) {
#pragma unroll
            for (int j = 0; j < 2; ++j) *(LAS float*)(lds + CSUM + ((j + 2 * wv) % 7) * 128 + lane * 4) = csa[j][0];
          }
        }
        if constexpr (Tm == 0 && (im & 3) == 0) bar_lgkm();
        if constexpr (m < 196) {
          if constexpr (Tm == 0) Bf[im] = B_READ(im);
          Af[m] = A_READ(im, Tm);
        } else if constexpr (m == 196) {
#pragma unroll
          for (int k = 28; k < 32; ++k) Bf[k] = B_READ(k);
        } else if constexpr (m == 197) {
#pragma unroll
          for (int t = 0; t < 7; ++t) cs[t] = *(const LAS float*)(lds + CSUM + ((t + 2 * wv) % 7) * 128 + (lane & 31) * 4);
        } else {
#pragma unroll
          for (int k = 0; k < 4; ++k) Al[0][k] = A_READ(28 + k, 0);
        }
        acc[T] = __builtin_amdgcn_mfma_f32_32x32x16_bf16(Af[n], Bf[i], acc[T], 0, 0, 0);
        if constexpr (T < 2)
          csa[T] = __builtin_amdgcn_mfma_f32_16x16x32_bf16(ones, Af[n], csa[T], 0, 0, 0);
        SCHED_FENCE();
      });
      wait_vm<0>();
      PIN(stg);
      if (__builtin_expect(!wave_all(stg[1] == tag), 0)) {
#pragma unroll 1
        for (int it = 0; it < (1 << 18); ++it) {
          ld64c(stg, (unsigned)((lane & 31) * 8), r_fl, FL_STAT + (unsigned)((mt * 128 + wv * 32) * 8));
          wait_vm<0>();
          PIN(stg);
          if (wave_all(stg[1] == tag)) break;
          __builtin_amdgcn_s_sleep(2);
        }
      }
      const unsigned stw = stg[0];
      const float mb = lo_f(stw), sd = hi_f(stw);
#pragma unroll
      for (int k = 28; k < 32; ++k) {
        u32x4 v = __builtin_bit_cast(u32x4, Bf[k]);
#pragma unroll
        for (int j = 0; j < 4; ++j) v[j] = pk_bf16(lo_f(v[j]) - mb, hi_f(v[j]) - mb);
        Bf[k] = __builtin_bit_cast(bf16x8, v);
      }
      const unsigned nm = (stw & 0xffffu) ^ 0x8000u;
      u32x4 bq = {nm | (nm << 16), nm, 0u, 0u};
      if (lane >= 32) bq = (u32x4){0u, 0u, 0u, 0u};
      const float ga = -1.4426950408889634f * __builtin_amdgcn_rcpf(sd);
      const unsigned hsb = (unsigned)(((mt * 4 + wv) * 112 + ft * 7) * 1024);
      static_for<0, 8>([&](auto tc) {
        constexpr int T = decltype(tc)::value;
        u32x4 o;
        if constexpr (T < 7) {
          const float c = cs[T];
          const float ch = rbf(c), cm = rbf(c - ch), cl = (c - ch) - cm;
          const u32x4 aq = {pk_bf16(ch, cm), pk_bf16(cl, 0.f), 0u, 0u};
          acc[T] = __builtin_amdgcn_mfma_f32_32x32x16_bf16(__builtin_bit_cast(bf16x8, aq), __builtin_bit_cast(bf16x8, bq), acc[T], 0, 0, 0);
          SCHED_FENCE();
        }
#pragma unroll
        for (int j = 0; j < 4; ++j) {
          if constexpr (T + 1 < 7) Al[T + 1][j] = A_READ(28 + j, T + 1);
          if constexpr (T < 7) acc[T] = __builtin_amdgcn_mfma_f32_32x32x16_bf16(Al[T][j], Bf[28 + j], acc[T], 0, 0, 0);
          SCHED_FENCE();
          if constexpr (T >= 1) {
            const float s0 = __builtin_amdgcn_rcpf(fmaf(__builtin_amdgcn_exp2f(acc[T - 1][2 * j] * ga), sd, sd));
            const float s1 = __builtin_amdgcn_rcpf(fmaf(__builtin_amdgcn_exp2f(acc[T - 1][2 * j + 1] * ga), sd, sd));
            o[j] = pk_bf16(s0 * acc[T - 1][8 + 2 * j], s1 * acc[T - 1][8 + 2 * j + 1]);
            PIN(o[j]);
          }
          SCHED_FENCE();
        }
        if constexpr (T >= 1) st128c(o, lane * 16u, r_h, hsb + (unsigned)((T - 1 + 2 * wv) % 7) * 1024u);
      });
    }
    const unsigned ldw = ldsb + SY_LD;
    const LAS char* waT[2];
    {
      const int G = lane >> 4, q4 = (lane & 15) >> 2;
      const LAS char* wa0 = lds + wv * 2048 + (4 * (G >> 1) + q4) * 128 + (G & 1) * 32 + (lane & 3) * 8;
      waT[0] = wa0 + ((q4 >> 1) * 64); waT[1] = wa0 + (((q4 >> 1) ^ 1) * 64);
    }
#define D_READ(i, T) __builtin_bit_cast(bf16x8, cat8(trrd(waT[T] + g2_base(i)), trrd(waT[T] + g2_base(i) + 1024)))
    bf16x8 Wr[28][2];
    lds_wait<1>(ldw, 2u);
    static_for<0, 5>([&](auto ic) { constexpr int i = decltype(ic)::value; Wr[i][0] = D_READ(i, 0); Wr[i][1] = D_READ(i, 1); });
    static_for<5, 10>([&](auto ic) { constexpr int i = decltype(ic)::value; Wr[i][0] = D_READ(i, 0); Wr[i][1] = D_READ(i, 1); });
    wait_vm<0>();
    if (lane < 8) set_flag(ws, FL_H + (unsigned)(((mt * 2 + (wv >> 1)) * 8 + lane) * 256 + (ft * 2 + (wv & 1)) * 4), tag);
    lds_setv(ldsb + SY_MC + wv * 4, 2u);
    if (wv == 0) {
      lds_wait<4>(ldsb + SY_UR, 1u);
      conv_upper<14, 16>(lds, lane, 0, cp, r_s);
    }
    lds_wait<1>(ldw, 3u);
    static_for<10, G2_NR>([&](auto ic) { constexpr int i = decltype(ic)::value; Wr[i][0] = D_READ(i, 0); Wr[i][1] = D_READ(i, 1); });
    lds_setv(ldsb + SY_MC + wv * 4, 3u);
    __builtin_amdgcn_s_setprio(0);
    poll_flags<2>((lane & 1) * 16u, r_fl, FL_H + (unsigned)((tt * 8 + dtile) * 256 + wv * 32), tag);
    if (wv == 0) publish<4>(lds, 2, ws, FL_S + (unsigned)((lane * 8 + (cp >> 5)) * 256 + (cp & 31) * 4), 32, tag, lane);
    __builtin_amdgcn_s_setprio(2);
    u32x4 hb[28][2];
#define HB_LOAD(i) do { ld128c(hb[i][0], lane * 16u, r_h, ((unsigned)(tt * 2) * 112u + G2_KS(wv, i)) * 1024u); \
                        ld128c(hb[i][1], lane * 16u, r_h, ((unsigned)(tt * 2 + 1) * 112u + G2_KS(wv, i)) * 1024u); } while (0)
#pragma unroll
    for (int i = 0; i < G2_H0; ++i) HB_LOAD(i);
    f32x16 acc2[2][2];
    {
#pragma unroll
      for (int a = 0; a < 2; ++a)
#pragma unroll
        for (int b = 0; b < 2; ++b) acc2[a][b] = (f32x16){};
      static_for<0, 28>([&](auto ic) {
        constexpr int i = decltype(ic)::value;
        if constexpr (i + 2 == 13) lds_wait<1>(ldw, 6u);
        if constexpr (i + 2 >= G2_NR && i + 2 < 28) { Wr[i + 2][0] = D_READ(i + 2, 0); Wr[i + 2][1] = D_READ(i + 2, 1); }
        wait_vm<2 * (g2_issued(i) - i - 1)>();
        PIN(hb[i][0]); PIN(hb[i][1]);
#pragma unroll
        for (int T = 0; T < 2; ++T)
#pragma unroll
          for (int b = 0; b < 2; ++b)
            acc2[T][b] = __builtin_amdgcn_mfma_f32_32x32x16_bf16(Wr[i][T], __builtin_bit_cast(bf16x8, hb[i][b]), acc2[T][b], 0, 0, 0);
        SCHED_FENCE();
        static_for<g2_issued(i), g2_issued(i + 1)>([&](auto jc) { constexpr int j = decltype(jc)::value; HB_LOAD(j); });
      });
    }
    {
      int l2 = lane; PIN(l2);
      const int tl = l2 & 31, x7 = (tl & 7) ^ (l2 >> 5);
#pragma unroll
      for (int b = 0; b < 2; ++b) {
        const int c8 = 4 * b + (tl >> 3);
        const int o4 = c8 < 5 ? RED_A + c8 * 8192 : RED_B + (c8 - 5) * 8192;
        LAS char* rp = lds + o4 + wv * 2048 + (tl & 7) * 256;
#pragma unroll
        for (int T = 0; T < 2; ++T)
#pragma unroll
          for (int g = 0; g < 4; ++g) {
            const f32x4 v = {acc2[T][b][4 * g], acc2[T][b][4 * g + 1], acc2[T][b][4 * g + 2], acc2[T][b][4 * g + 3]};
            *(LAS f32x4*)(rp + T * 128 + (((2 * g) ^ x7) << 4)) = v;
          }
      }
    }
  } else if (w8 == 4) {
    const rsrc_t r_wd = mk_rsrc(wd, FF * DM * 2);
    unsigned wv4[4];
#pragma unroll
    for (int i = 0; i < 4; ++i)
      wv4[i] = (unsigned)((lane >> 3) * 1024 + (((lane & 7) ^ (((lane >> 4) & 1) << 2)) * 16) + (i >> 1) * 458752 + (i & 1) * 8192 - i * 1024);
    const unsigned ldw = ldsb + SY_LD;
#define G2_GROUP(g) G2_STEPS(5 * (g), cmin(5 * (g) + 5, 28))
#define G2_STEPS(a_, b_) static_for<a_, b_>([&](auto ic) { constexpr int i = decltype(ic)::value; \
        const unsigned so_ = G2_KS(0, i) * 16384u + (unsigned)(dtile * 128); \
        dma16x4(ldsb + g2_base(i), wv4[0], wv4[1], wv4[2], wv4[3], r_wd, so_); \
        dma16x4(ldsb + g2_base(i) + 4096, wv4[0], wv4[1], wv4[2], wv4[3], r_wd, so_ + 2u * 458752u); })
    g1_stage(1);
    wait_vm<44>();
    bar_only();
#pragma unroll
    for (int k = 0; k < 6; ++k) {
      g1_stage(k + 2);
      wait_vm<44>();
      bar_only();
    }
    G2_GROUP(0);
    wait_vm<40>();
    bar_only();
    G2_GROUP(1);
    wait_vm<0>();
    lds_setv(ldw, 2u);
    lds_wait<4>(ldsb + SY_MC, 2u);
    G2_STEPS(10, 15);
    wait_vm<16>();
    lds_setv(ldw, 3u);
    G2_STEPS(15, 25);
    lds_wait<4>(ldsb + SY_MC, 3u);
    G2_GROUP(5);
    wait_vm<0>();
    lds_setv(ldw, 6u);
    __builtin_amdgcn_s_setprio(0);
    EPI_X();
    lds_wait<1>(ldsb + SY_SR, 1u);
    EPI_S();
  } else if (w8 == 5) {
    wait_vm<0>();
    asm volatile("" :: "v"(pf0));
    bar_only();
    const unsigned tag = lds_get1(ldsb + SY_VAL);
    bar_only();
    {
      const int c = lane & 15, g = lane >> 4;
      unsigned d[2][3][4];
      float kscale[2];
#pragma unroll
      for (int hc = 0; hc < 2; ++hc) {
        f32x4 kacc[3];
#pragma unroll
        for (int t = 0; t < 3; ++t)
          kacc[t] = *(const LAS f32x4*)(lds + KF + hc * CV_KSTR + t * 1024 + lane * 16) + *(const LAS f32x4*)(lds + KGS + hc * 4096 + t * 1024 + lane * 16);
        const f32x2 gw = *(const LAS f32x2*)(lds + SY_KG + hc * 8);
        const float g2 = gw.x;
        const int we = (int)gw.y;
        if (g2 > 0.f) {
#pragma unroll
          for (int i = 0; i < 4; ++i) {
            const int l0 = 16 * c + 4 * g + i;
            const int l1 = g < 2 ? 256 + 16 * c + 8 * g + 2 * i : 512 + 256 * (g - 2) + 16 * c + 4 * i;
            const int l2 = g < 2 ? 1024 + 256 * (2 * g + (i >> 1)) + 8 * (i & 1) + 16 * c : 2048;
            kacc[0][i] *= __builtin_amdgcn_exp2f(g2 * (float)(l0 - 2048));
            kacc[1][i] *= __builtin_amdgcn_exp2f(g2 * (float)(l1 - 2048));
            kacc[2][i] *= __builtin_amdgcn_exp2f(g2 * (float)(l2 - 2048));
          }
        }
        kscale[hc] = __builtin_amdgcn_ldexpf(1.0f, we - 7) * __builtin_amdgcn_exp2f(g2 * 2048.f);
#pragma unroll
        for (int t = 0; t < 3; ++t)
#pragma unroll
          for (int i = 0; i < 4; ++i) { d[hc][t][i] = pk_f16(kacc[t][i] * 128.f, kacc[t][i] * 128.f); PIN(d[hc][t][i]); }
        bar_only();
      }
      bar_only();
      bar_only();
      bar_only();
      bar_only();
      __builtin_amdgcn_s_sleep(WSL);
      __builtin_amdgcn_s_setprio(3);
      *(LAS f32x2*)(lds + SY_VAL + 8) = (f32x2){kscale[0], kscale[1]};
#define MIX(a, b) (((a) & 0xffffu) | ((b) & 0xffff0000u))
#pragma unroll
      for (int hc = 0; hc < 2; ++hc) {
        LAS char* kf = lds + KF + hc * CV_KSTR + 96;
        if (g < 2) {
#pragma unroll
          for (int h = 0; h < 2; ++h) {
            LAS char* p = kf + 2 * (1024 + 256 * (2 * g + h) + 16 * c);
            const unsigned v0 = d[hc][2][2 * h], v1 = d[hc][2][2 * h + 1];
            *(LAS u32x2*)(p - 8) = (u32x2){v0, v0};
            *(LAS u32x4*)(p) = (u32x4){v0, v0, MIX(v0, v1), v1};
            *(LAS u32x2*)(p + 16) = (u32x2){v1, v1};
          }
        } else {
          if (lane == 32) *(LAS u32x2*)(kf + 2 * 2044) = (u32x2){d[hc][2][0], d[hc][2][0]};
        }
        if (g >= 2) {
          LAS char* p = kf + 2 * (512 + 256 * (g - 2) + 16 * c);
          const unsigned v0 = d[hc][1][0], v1 = d[hc][1][1], v2 = d[hc][1][2], v3 = d[hc][1][3];
          *(LAS unsigned*)(p - 4) = v0;
          *(LAS u32x4*)(p) = (u32x4){v0, MIX(v0, v1), v1, v2};
          *(LAS u32x2*)(p + 16) = (u32x2){v2, MIX(v2, v3)};
          *(LAS unsigned*)(p + 24) = v3;
        }
        if (g < 2) {
          LAS char* p = kf + 2 * (256 + 16 * c + 8 * g);
          const unsigned v0 = d[hc][1][0], v1 = d[hc][1][1], v2 = d[hc][1][2], v3 = d[hc][1][3];
          *(LAS unsigned short*)(p - 2) = (unsigned short)v0;
          *(LAS u32x2*)(p) = (u32x2){v0, MIX(v1, v2)};
          *(LAS unsigned*)(p + 8) = v2;
          *(LAS unsigned short*)(p + 12) = (unsigned short)v3;
        }
        *(LAS u32x2*)(kf + 2 * (16 * c + 4 * g)) = (u32x2){MIX(d[hc][0][0], d[hc][0][1]), MIX(d[hc][0][2], d[hc][0][3])};
        if (lane < 6) *(LAS u32x4*)(kf - 96 + lane * 16) = (u32x4){0u, 0u, 0u, 0u};
      }
      lds_setv(ldsb + SY_UR + 8, 1u);
    }
    __builtin_amdgcn_s_setprio(1);
    lds_wait<4>(ldsb + SY_UR, 1u);
    __builtin_amdgcn_s_sleep(32);
    conv_upper<CV_NP, 16>(lds, lane, 1, cp, r_s);
    conv_upper<CV_NP, 14>(lds, lane, 0, cp, r_s);
    publish<4>(lds, 2, ws, FL_S + (unsigned)((lane * 8 + (cp >> 5)) * 256 + (cp & 31) * 4), 32, tag, lane);
    EPI_X();
    lds_wait<1>(ldsb + SY_CNT + 8, 4u);
    poll_flags<4>((lane & 7) * 16u, r_fl, FL_S + (unsigned)((tt * 8 + dtile) * 256), tag);
    lds_setv(ldsb + SY_SR, 1u);
    EPI_S();
  } else {
    const int hc = wv - 2;
    const int dch = cp * 2 + hc;
    const rsrc_t r_xn = mk_rsrc(ws + WS_XNF, SL * DM * 2);
    const int row0 = bid * 8 + hc * 4;
    wait_vm<0>();
#pragma unroll
    for (int i = 0; i < 4; ++i) PIN(xr[i]);
    PIN(ep); PIN(dsk2);
    const unsigned tag = (unsigned)__builtin_amdgcn_readfirstlane(ep) + 1u;
    lds_setv(ldsb + SY_VAL, tag);
    lds_setv(ldsb + SY_VAL + 4, dsk2);
    {
      float f[32];
#pragma unroll
      for (int j = 0; j < 16; ++j) { f[2 * j] = lo_f(xr[j >> 2][j & 3]); f[2 * j + 1] = hi_f(xr[j >> 2][j & 3]); }
      float s0 = 0.f, s1 = 0.f;
#pragma unroll
      for (int j = 0; j < 32; j += 4) { s0 += f[j] + f[j + 1]; s1 += f[j + 2] + f[j + 3]; }
      const float mean = row_sum(s0 + s1) * (1.0f / DM);
      float q0 = 0.f, q1 = 0.f;
#pragma unroll
      for (int j = 0; j < 32; j += 2) {
        const float da = f[j] - mean, db = f[j + 1] - mean;
        q0 = fmaf(da, da, q0); q1 = fmaf(db, db, q1);
      }
      const float var = row_sum(q0 + q1) * (1.0f / DM);
      const float mb = rbf(mean);
      const float sb = rbf(__builtin_amdgcn_sqrtf(rbf(rbf(var) + 1e-5f)));
      if ((lane & 15) == 0)
        st64c((u32x2){pk_bf16(mb, sb), tag}, (unsigned)((lane >> 4) * 8), r_fl, FL_STAT + (unsigned)(row0 * 8));
      const float rs = __builtin_amdgcn_rcpf(sb);
      const unsigned vo = (unsigned)((lane & 15) * 32768 + (lane >> 4) * 16);
#pragma unroll
      for (int i = 0; i < 4; ++i) {
        u32x4 o;
#pragma unroll
        for (int j = 0; j < 4; ++j) {
          const unsigned d = pk_bf16(f[8 * i + 2 * j] - mb, f[8 * i + 2 * j + 1] - mb);
          o[j] = pk_bf16(lo_f(d) * rs, hi_f(d) * rs);
        }
        st128c(o, vo, r_xn, (unsigned)(i * 16 * 32768 + row0 * 16));
      }
    }
    wait_vm<0>();
    if (lane < 8) set_flag(ws, FL_ALL + (unsigned)(lane * 4096 + (bid * 2 + hc) * 4), tag);
    bar_lgkm();
    bar_only();
    bar_only();
    bar_only();
    const unsigned fo = FL_ALL + (unsigned)(xcd * 4096 + hc * 1024);
    int nb = 3;
    {
      u32x4 vf;
      ld128c(vf, lane * 16u, r_fl, fo);
      bar_only();
      wait_vm<0>();
      PIN(vf);
      if (!wave_all((vf[0] == tag) & (vf[1] == tag) & (vf[2] == tag) & (vf[3] == tag))) {
        u32x4 vg;
        bar_only();
        ld128c(vg, lane * 16u, r_fl, fo);
        bar_only();
        wait_vm<0>();
        PIN(vg);
        nb = 1;
        if (!wave_all((vg[0] == tag) & (vg[1] == tag) & (vg[2] == tag) & (vg[3] == tag))) {
          bar_only();
          nb = 0;
          poll_flags<4>(lane * 16u, r_fl, fo, tag);
        }
      }
    }
    unsigned uw[16];
    const unsigned usb = (unsigned)((cp >> 2) * 32768 + (cp & 3) * 4 + hc * 16384);
#pragma unroll
    for (int m = 0; m < 16; ++m) ld32c(uw[m], lane * 16u, r_xn, usb + (unsigned)m * 1024u);
    if (nb >= 1) bar_only();
    if (nb == 3) bar_only();
    wait_vm<0>();
    {
      const bool od = lane & 1;
      LAS char* up = lds + CV_U + 160 + hc * 2048 + (od ? 128 : 0) + (lane >> 1) * 4;
#pragma unroll
      for (int m = 0; m < 16; m += 2) {
        PIN(uw[m]); PIN(uw[m + 1]);
        const unsigned a = pk_f16(lo_f(uw[m]), hi_f(uw[m])), b = pk_f16(lo_f(uw[m + 1]), hi_f(uw[m + 1]));
        const unsigned y = (unsigned)__builtin_amdgcn_update_dpp(0, (int)(od ? a : b), 0xB1, 0xf, 0xf, true);
        const unsigned f0 = od ? y : a, f1 = od ? b : y;
        uw[m] = __builtin_amdgcn_perm(f1, f0, 0x05040100u);
        uw[m + 1] = __builtin_amdgcn_perm(f1, f0, 0x07060302u);
        PIN(uw[m]); PIN(uw[m + 1]);
      }
      if (nb == 3) { bar_only(); __builtin_amdgcn_s_sleep(WSL); }
      __builtin_amdgcn_s_setprio(3);
      if (lane < 10) *(LAS u32x4*)(lds + CV_U + hc * CV_USTR + lane * 16) = (u32x4){0u, 0u, 0u, 0u};
#pragma unroll
      for (int m = 0; m < 16; m += 2) {
        *(LAS unsigned*)(up + m * 128) = uw[m];
        *(LAS unsigned*)(up + CV_USTR + m * 128) = uw[m + 1];
      }
      lds_setv(ldsb + SY_UR + hc * 4, 1u);
    }
    __builtin_amdgcn_s_setprio(1);
    lds_wait<4>(ldsb + SY_UR, 1u);
    if (lane == 0 && bid == 0 && hc == 0) set_flag(ws, FL_EP, tag);
    const float kscale = *(const LAS float*)(lds + SY_VAL + 8 + hc * 4);
    {
      const float dskip = hc ? hi_f(dsk2) : lo_f(dsk2);
      const int G = lane >> 4, q4 = (lane & 15) >> 2, p = lane & 3;
      const LAS char* pa = lds + KF + hc * CV_KSTR + 96 + 2 * (76 + 16 * p - 32 * G - 4 * q4 - 16);
      const LAS char* pb = lds + CV_U + hc * CV_USTR + 160 + 2 * (-76 + 32 * G + 4 * q4 + 4 * p);
      const LAS char* pu = lds + CV_U + hc * CV_USTR + 160 + lane * 2;
      f16x8 A[16], B[32];
#define CV_A(q) __builtin_bit_cast(f16x8, cat8(trrd(pa + 256 * (q) + 32), trrd(pa + 256 * (q))))
#define CV_B(w) __builtin_bit_cast(f16x8, cat8(trrd(pb + 128 * (w)), trrd(pb + 128 * (w) + 32)))
      A[0] = CV_A(0); B[0] = CV_B(0); B[1] = CV_B(1);
      A[1] = CV_A(1); B[2] = CV_B(2); B[3] = CV_B(3);
      f32x4 ca[2][2];
      _Float16 us[2][2];
      unsigned so16[16];
      float c3 = 0.f, c2 = 0.f, c1 = 0.f;
      auto tile_y = [&](const f32x4 a) __attribute__((always_inline)) {
        const float h3 = a[3];
        const float h2 = a[2] + shr_carry(c3, h3);
        const float h1 = a[1] + shr_carry(c2, h2);
        const float y = a[0] + shr_carry(c1, h1);
        c3 = dpp_mov<0x13C>(h3); c2 = dpp_mov<0x13C>(h2); c1 = dpp_mov<0x13C>(h1);
        return y * kscale;
      };
      static_for<0, CV_NP + 1>([&](auto jc) {
        constexpr int jj = decltype(jc)::value;
        if constexpr (jj + 2 < CV_NP) { A[jj + 2] = CV_A(jj + 2); B[2 * jj + 4] = CV_B(2 * jj + 4); B[2 * jj + 5] = CV_B(2 * jj + 5); }
        if constexpr (jj < CV_NP) {
          us[jj & 1][0] = *(const LAS _Float16*)(pu + jj * 256); us[jj & 1][1] = *(const LAS _Float16*)(pu + jj * 256 + 128);
          f32x4 a0 = {0.f, 0.f, 0.f, 0.f}, a1 = {0.f, 0.f, 0.f, 0.f};
#pragma unroll
          for (int q = jj; q >= 0; --q) {
            a0 = __builtin_amdgcn_mfma_f32_16x16x32_f16(A[q], B[2 * (jj - q)], a0, 0, 0, 0);
            a1 = __builtin_amdgcn_mfma_f32_16x16x32_f16(A[q], B[2 * (jj - q) + 1], a1, 0, 0, 0);
          }
          ca[jj & 1][0] = a0; ca[jj & 1][1] = a1;
        }
        if constexpr (jj >= 1) {
          const float y0 = tile_y(ca[(jj - 1) & 1][0]), y1 = tile_y(ca[(jj - 1) & 1][1]);
          const unsigned yq = pk_bf16(y0, y1);
          const unsigned uq = pk_bf16((float)us[(jj - 1) & 1][0] * dskip, (float)us[(jj - 1) & 1][1] * dskip);
          so16[jj - 1] = pk_bf16(lo_f(yq) + lo_f(uq), hi_f(yq) + hi_f(uq));
        }
        if constexpr (jj >= 1 && jj < CV_NP) {
#pragma unroll
          for (int k = 0; k < 2 * (jj + 1); ++k) {
            __builtin_amdgcn_sched_group_barrier(0x8, 1, 0);
            __builtin_amdgcn_sched_group_barrier(0x100, 1, 0);
            __builtin_amdgcn_sched_group_barrier(0x2, jj < 7 ? 4 : 2, 0);
          }
        }
        SCHED_FENCE();
        if constexpr (jj >= 1) st32c(so16[jj - 1], lane * 4u, r_s, (unsigned)(dch * 4096 + (jj - 1) * 256));
      });
    }
    publish<4>(lds, 2, ws, FL_S + (unsigned)((lane * 8 + (cp >> 5)) * 256 + (cp & 31) * 4), 32, tag, lane);
    EPI_X();
    lds_wait<1>(ldsb + SY_SR, 1u);
    EPI_S();
  }
  bar_lgkm();
  {
    int t2 = tid; PIN(t2);
    const u32x4 xv = *(const LAS u32x4*)(lds + EPX + t2 * 16);
    unsigned sv[8];
#pragma unroll
    for (int j = 0; j < 8; ++j) sv[j] = *(const LAS unsigned*)(lds + EPS + ((t2 & 7) * 8 + j) * 256 + (((t2 >> 5) + (t2 & 7)) & 15) * 16 + ((t2 >> 3) & 3) * 4);
    const int rb = w8 < 5 ? RED_A + w8 * 8192 : RED_B + (w8 - 5) * 8192;
    const int tr7 = (t2 >> 3) & 7;
    const LAS char* rp = lds + rb + tr7 * 256 + ((t2 >> 2) & 1) * 128;
    const bool odd = tt & 1;
    u32x4 o;
#pragma unroll
    for (int i = 0; i < 2; ++i) {
      const int ch = (((t2 & 3) * 2 + i) ^ tr7) << 4;
      f32x4 v = *(const LAS f32x4*)(rp + ch);
#pragma unroll
      for (int w = 1; w < 4; ++w) v += *(const LAS f32x4*)(rp + w * 2048 + ch);
#pragma unroll
      for (int jj = 0; jj < 2; ++jj) {
        const unsigned fp = pk_bf16(v[2 * jj], v[2 * jj + 1]);
        const float f0 = lo_f(fp), f1 = hi_f(fp);
        const float g0 = f0 * __builtin_amdgcn_rcpf(1.0f + __builtin_amdgcn_exp2f(f0 * -1.4426950408889634f));
        const float g1 = f1 * __builtin_amdgcn_rcpf(1.0f + __builtin_amdgcn_exp2f(f1 * -1.4426950408889634f));
        const unsigned gp = pk_bf16(g0, g1);
        const int j = 4 * i + 2 * jj;
        const float s0 = odd ? hi_f(sv[j]) : lo_f(sv[j]), s1 = odd ? hi_f(sv[j + 1]) : lo_f(sv[j + 1]);
        const unsigned sg = pk_bf16(s0 * lo_f(gp), s1 * hi_f(gp));
        const unsigned xw = xv[2 * i + jj];
        o[2 * i + jj] = pk_bf16(lo_f(xw) + lo_f(sg), hi_f(xw) + hi_f(sg));
      }
    }
    st128c(o, XO(t2), mk_rsrc(out, SL * DM * 2), 0u);
  }
}

extern "C" void kernel_launch(void* const* d_in, const int* in_sizes, int n_in, void* d_out,
                              int out_size, void* d_ws, size_t ws_size, hipStream_t stream) {
  (void)hipFuncSetAttribute((const void*)fused_kernel, hipFuncAttributeMaxDynamicSharedMemorySize, LDS_BYTES);
  fused_kernel<<<256, 512, LDS_BYTES, stream>>>(
      (const unsigned short*)d_in[0], (const unsigned short*)d_in[1], (const unsigned short*)d_in[2],
      (const unsigned short*)d_in[3], (const unsigned short*)d_in[4], (const unsigned short*)d_in[5],
      (const unsigned short*)d_in[6], (const unsigned*)d_in[7], (const unsigned*)d_in[8],
      (const unsigned short*)d_in[10], (const unsigned short*)d_in[11], (const unsigned short*)d_in[12],
      (char*)d_ws, (unsigned short*)d_out);
}
```

```cpp
#include <hip/hip_runtime.h>
#include <hip/hip_bf16.h>
#include <stdint.h>
#include <type_traits>

typedef __attribute__((ext_vector_type(4))) short s16x4;
typedef __attribute__((ext_vector_type(8))) short s16x8;
typedef __attribute__((ext_vector_type(8))) _Float16 f16x8;
typedef __attribute__((ext_vector_type(8))) __bf16 bf16x8;
typedef __attribute__((ext_vector_type(2))) float f32x2;
typedef __attribute__((ext_vector_type(4))) float f32x4;
typedef __attribute__((ext_vector_type(16))) float f32x16;
typedef __attribute__((ext_vector_type(4))) unsigned u32x4;
typedef __attribute__((ext_vector_type(3))) unsigned u32x3;
typedef __attribute__((ext_vector_type(2))) unsigned u32x2;
typedef __attribute__((ext_vector_type(2))) __bf16 bf16x2;
typedef __attribute__((ext_vector_type(2))) _Float16 f16x2;
#define LAS __attribute__((address_space(3)))
#define GAS __attribute__((address_space(1)))
typedef __amdgpu_buffer_rsrc_t rsrc_t;

constexpr int SL = 2048;
constexpr int DM = 512;
constexpr int NM = 128;
constexpr int FF = 1792;

constexpr size_t WS_XNF = 0;
constexpr size_t WS_ST = 2 * (size_t)SL * DM * 2;
constexpr size_t WS_HF = WS_ST + (size_t)SL * DM * 2;
constexpr size_t WS_FLAG = 2 * (size_t)SL * DM;
constexpr unsigned FL_ALL = 0, FL_STAT = 65536, FL_H = 131072, FL_S = 196608, FL_EP = 262144, FL_BYTES = 262400;

template <int I, int N, class F>
__device__ __forceinline__ void static_for(F&& f) {
  if constexpr (I < N) { f(std::integral_constant<int, I>{}); static_for<I + 1, N>(f); }
}

__device__ __forceinline__ unsigned pk_bf16(float lo, float hi) {
  f32x2 v = {lo, hi};
  return __builtin_bit_cast(unsigned, __builtin_convertvector(v, bf16x2));
}
__device__ __forceinline__ unsigned pk_f16(float lo, float hi) {
  f32x2 v = {lo, hi};
  return __builtin_bit_cast(unsigned, __builtin_convertvector(v, f16x2));
}
__device__ __forceinline__ float lo_f(unsigned p) { return __uint_as_float(p << 16); }
__device__ __forceinline__ float hi_f(unsigned p) { return __uint_as_float(p & 0xffff0000u); }
__device__ __forceinline__ float rbf(float x) { return lo_f(pk_bf16(x, x)); }

template <int CTRL>
__device__ __forceinline__ float dpp_mov(float v) {
  return __int_as_float(__builtin_amdgcn_update_dpp(0, __float_as_int(v), CTRL, 0xf, 0xf, true));
}
__device__ __forceinline__ float shr_carry(float c, float v) {
  return __builtin_bit_cast(float, __builtin_amdgcn_update_dpp(__builtin_bit_cast(int, c), __builtin_bit_cast(int, v), 0x138, 0xf, 0xf, false));
}
__device__ __forceinline__ float rdlane(float v, int l) {
  return __int_as_float(__builtin_amdgcn_readlane(__float_as_int(v), l));
}
__device__ __forceinline__ float wave_sum(float v) {
  v += dpp_mov<0xB1>(v);
  v += dpp_mov<0x4E>(v);
  v += dpp_mov<0x141>(v);
  v += dpp_mov<0x140>(v);
  return (rdlane(v, 0) + rdlane(v, 16)) + (rdlane(v, 32) + rdlane(v, 48));
}
__device__ __forceinline__ float row_sum(float v) {
  v += dpp_mov<0xB1>(v);
  v += dpp_mov<0x4E>(v);
  v += dpp_mov<0x141>(v);
  v += dpp_mov<0x140>(v);
  return v;
}
__device__ __forceinline__ float half_sum(float v, int lane) {
  v += dpp_mov<0xB1>(v);
  v += dpp_mov<0x4E>(v);
  v += dpp_mov<0x141>(v);
  v += dpp_mov<0x140>(v);
  const float lo = rdlane(v, 0) + rdlane(v, 16), hi = rdlane(v, 32) + rdlane(v, 48);
  return lane < 32 ? lo : hi;
}
__device__ __forceinline__ float wave_max(float v) {
  v = fmaxf(v, dpp_mov<0xB1>(v));
  v = fmaxf(v, dpp_mov<0x4E>(v));
  v = fmaxf(v, dpp_mov<0x141>(v));
  v = fmaxf(v, dpp_mov<0x140>(v));
  return fmaxf(fmaxf(rdlane(v, 0), rdlane(v, 16)), fmaxf(rdlane(v, 32), rdlane(v, 48)));
}

__device__ __forceinline__ rsrc_t mk_rsrc(const void* p, unsigned bytes) {
  return __builtin_amdgcn_make_buffer_rsrc((void*)p, (short)0, (int)bytes, 0x00020000);
}
__device__ __forceinline__ void dma16(unsigned lds_dst, unsigned voff, rsrc_t r, unsigned soff) {
  asm volatile("s_mov_b32 m0, %0\n\ts_nop 0\n\tbuffer_load_dwordx4 %1, %2, %3 offen lds"
               :: "s"(lds_dst), "v"(voff), "s"(r), "s"(soff) : "memory");
}
__device__ __forceinline__ void dma16x4(unsigned lds_dst, unsigned v0, unsigned v1, unsigned v2, unsigned v3, rsrc_t r, unsigned soff) {
  asm volatile("s_mov_b32 m0, %0\n\ts_nop 0\n\t"
               "buffer_load_dwordx4 %1, %5, %6 offen lds\n\t"
               "buffer_load_dwordx4 %2, %5, %6 offen offset:1024 lds\n\t"
               "buffer_load_dwordx4 %3, %5, %6 offen offset:2048 lds\n\t"
               "buffer_load_dwordx4 %4, %5, %6 offen offset:3072 lds"
               :: "s"(lds_dst), "v"(v0), "v"(v1), "v"(v2), "v"(v3), "s"(r), "s"(soff) : "memory");
}
__device__ __forceinline__ void dma16x3(unsigned lds_dst, unsigned v0, unsigned v1, unsigned v2, rsrc_t r, unsigned soff) {
  asm volatile("s_mov_b32 m0, %0\n\ts_nop 0\n\t"
               "buffer_load_dwordx4 %1, %4, %5 offen lds\n\t"
               "buffer_load_dwordx4 %2, %4, %5 offen offset:1024 lds\n\t"
               "buffer_load_dwordx4 %3, %4, %5 offen offset:2048 lds"
               :: "s"(lds_dst), "v"(v0), "v"(v1), "v"(v2), "s"(r), "s"(soff) : "memory");
}
__device__ __forceinline__ void ld128(u32x4& dst, unsigned voff, rsrc_t r, unsigned soff) {
  asm volatile("buffer_load_dwordx4 %0, %1, %2, %3 offen" : "=v"(dst) : "v"(voff), "s"(r), "s"(soff) : "memory");
}
__device__ __forceinline__ void ld128c(u32x4& dst, unsigned voff, rsrc_t r, unsigned soff) {
  asm volatile("buffer_load_dwordx4 %0, %1, %2, %3 offen sc1" : "=v"(dst) : "v"(voff), "s"(r), "s"(soff) : "memory");
}
__device__ __forceinline__ void ld32(unsigned& dst, unsigned voff, rsrc_t r, unsigned soff) {
  asm volatile("buffer_load_dword %0, %1, %2, %3 offen" : "=v"(dst) : "v"(voff), "s"(r), "s"(soff) : "memory");
}
__device__ __forceinline__ void ld64c(u32x2& dst, unsigned voff, rsrc_t r, unsigned soff) {
  asm volatile("buffer_load_dwordx2 %0, %1, %2, %3 offen sc1" : "=v"(dst) : "v"(voff), "s"(r), "s"(soff) : "memory");
}
__device__ __forceinline__ void ld32c(unsigned& dst, unsigned voff, rsrc_t r, unsigned soff) {
  asm volatile("buffer_load_dword %0, %1, %2, %3 offen sc1" : "=v"(dst) : "v"(voff), "s"(r), "s"(soff) : "memory");
}
__device__ __forceinline__ void st32c(unsigned v, unsigned voff, rsrc_t r, unsigned soff) {
  asm volatile("buffer_store_dword %0, %1, %2, %3 offen sc1" :: "v"(v), "v"(voff), "s"(r), "s"(soff) : "memory");
}
__device__ __forceinline__ void st64c(u32x2 v, unsigned voff, rsrc_t r, unsigned soff) {
  asm volatile("buffer_store_dwordx2 %0, %1, %2, %3 offen sc1\n\ts_nop 1" :: "v"(v), "v"(voff), "s"(r), "s"(soff) : "memory");
}
__device__ __forceinline__ void ld16(unsigned& dst, unsigned voff, rsrc_t r, unsigned soff) {
  asm volatile("buffer_load_ushort %0, %1, %2, %3 offen" : "=v"(dst) : "v"(voff), "s"(r), "s"(soff) : "memory");
}
__device__ __forceinline__ void st128c(u32x4 v, unsigned voff, rsrc_t r, unsigned soff) {
  asm volatile("buffer_store_dwordx4 %0, %1, %2, %3 offen sc1\n\ts_nop 1" :: "v"(v), "v"(voff), "s"(r), "s"(soff) : "memory");
}
template <int N>
__device__ __forceinline__ void wait_vm() { asm volatile("s_waitcnt vmcnt(%0)" :: "n"(N > 63 ? 63 : N) : "memory"); }
__device__ __forceinline__ void bar_lgkm() { asm volatile("s_waitcnt lgkmcnt(0)\n\ts_barrier" ::: "memory"); }
__device__ __forceinline__ void bar_only() { asm volatile("s_barrier" ::: "memory"); }
#define PIN(x) asm volatile("" : "+v"(x))
#define SCHED_FENCE() __builtin_amdgcn_sched_barrier(0)

__device__ __forceinline__ s16x4 trrd(const LAS char* p) {
  return __builtin_amdgcn_ds_read_tr16_b64_v4i16((LAS s16x4*)p);
}
__device__ __forceinline__ s16x8 cat8(s16x4 a, s16x4 b) {
  return __builtin_shufflevector(a, b, 0, 1, 2, 3, 4, 5, 6, 7);
}

__device__ __forceinline__ bool wave_all(bool p) { return __builtin_amdgcn_ballot_w64(!p) == 0ull; }
template <int SLP = 2>
__device__ __forceinline__ void poll_flags(unsigned voff, rsrc_t r, unsigned soff, unsigned tag) {
#pragma unroll 1
  for (int it = 0; it < (1 << 18); ++it) {
    u32x4 v;
    asm volatile("buffer_load_dwordx4 %0, %1, %2, %3 offen sc1\n\ts_waitcnt vmcnt(0)"
                 : "=&v"(v) : "v"(voff), "s"(r), "s"(soff) : "memory");
    const bool ok = (v[0] == tag) & (v[1] == tag) & (v[2] == tag) & (v[3] == tag);
    if (wave_all(ok)) break;
    __builtin_amdgcn_s_sleep(SLP);
  }
}
__device__ __forceinline__ void set_flag(char* ws, unsigned off, unsigned tag) {
  __hip_atomic_store((GAS unsigned*)(ws + WS_FLAG + off), tag, __ATOMIC_RELAXED, __HIP_MEMORY_SCOPE_AGENT);
}

__device__ __forceinline__ f32x2 cmul(f32x2 a, f32x2 b) {
  f32x2 r;
  r.x = fmaf(a.x, b.x, -(a.y * b.y));
  r.y = fmaf(a.x, b.y, a.y * b.x);
  return r;
}
__device__ __forceinline__ f32x2 cexp_l(float a, float b, float l) {
  const float mag = __builtin_amdgcn_exp2f(a * l * 1.4426950408889634f);
  const float x = b * l;
  const float C1 = 0.15915494309189535f;
  const float C2 = (float)(0.15915494309189533577 - (double)0.15915494309189535f);
  const float r = fmaf(x, C2, fmaf(x, C1, -__builtin_rintf(x * C1)));
  f32x2 z;
  z.x = mag * __builtin_amdgcn_cosf(r);
  z.y = mag * __builtin_amdgcn_sinf(r);
  return z;
}

constexpr int W_UPOFF = 14336 + 128;
constexpr int XOFF = W_UPOFF + 14336;
constexpr int STG = XOFF + 16384;
constexpr int KGS = 3 * STG;
constexpr int KF = KGS + 8192;
constexpr int CV_KSTR = (48 + 2048) * 2;
constexpr int CV_USTR = (80 + 2048) * 2;
constexpr int CV_U = KF + 2 * CV_KSTR;
constexpr int CSUM = CV_U + 2 * CV_USTR;
constexpr int STAT = CSUM + 896;
constexpr int SY = STAT + 512;
constexpr int SY_LD = SY, SY_MC = SY + 16, SY_ST = SY + 4, SY_HR = SY + 8, SY_SR = SY + 12, SY_UR = SY + 32  ,
              SY_CNT = SY + 48, SY_VAL = SY + 64, SY_KG = SY + 80, SY_END = SY + 96;
constexpr int LDS_BYTES = SY_END;
static_assert(LDS_BYTES <= 163840, "lds");
constexpr int G2_RED = 272;
constexpr int EPX = KGS;
constexpr int EPS = KF;
constexpr int LDLY = 20;
constexpr int PDLY = 36;
constexpr int WSL = 4;
constexpr unsigned WS_USED = (unsigned)(WS_HF + (size_t)SL * FF * 2);
constexpr unsigned CODE_BYTES = 49152;
constexpr int CV_NP = 12;
constexpr int G2_NR = 13;
constexpr int cmax(int a, int b) { return a > b ? a : b; }
constexpr int cmin(int a, int b) { return a < b ? a : b; }
constexpr int g2_base(int i) {
  return (i % 15) < 5 ? 2 * STG + (i % 5) * 8192 : (i % 15) < 10 ? (i % 5) * 8192
       : i == 12 ? KGS : i == 27 ? STG + 16384 : (i % 15) < 12 ? STG + XOFF + (i % 5) * 8192 : STG + (i - 13) * 8192;
}
constexpr int RED_A = g2_base(15), RED_B = g2_base(20);
constexpr int G2_H0 = 6;
constexpr int g2_issued(int i) { return cmin(28, G2_H0 + (3 * i) / 2); }

__device__ __forceinline__ void lds_setv(unsigned addr, unsigned v) {
  asm volatile("ds_write_b32 %0, %1" :: "v"(addr), "v"(v) : "memory");
}
__device__ __forceinline__ unsigned lds_min4(unsigned addr) {
  u32x4 v;
  asm volatile("ds_read_b128 %0, %1\n\ts_waitcnt lgkmcnt(0)" : "=&v"(v) : "v"(addr) : "memory");
  return (unsigned)__builtin_amdgcn_readfirstlane(min(min(v[0], v[1]), min(v[2], v[3])));
}
__device__ __forceinline__ unsigned lds_get1(unsigned addr) {
  unsigned v;
  asm volatile("ds_read_b32 %0, %1\n\ts_waitcnt lgkmcnt(0)" : "=&v"(v) : "v"(addr) : "memory");
  return (unsigned)__builtin_amdgcn_readfirstlane(v);
}
template <int W>
__device__ __forceinline__ unsigned lds_val(unsigned addr) { return W == 4 ? lds_min4(addr) : lds_get1(addr); }
template <int W>
__device__ __forceinline__ void lds_spin(unsigned addr, unsigned need) {
#pragma unroll 1
  for (int it = 0; it < (1 << 20); ++it) {
    if (lds_val<W>(addr) >= need) break;
    __builtin_amdgcn_s_sleep(1);
  }
}
template <int W>
__device__ __forceinline__ void lds_wait(unsigned addr, unsigned need) {
  if (__builtin_expect(lds_val<W>(addr) < need, 0)) lds_spin<W>(addr, need);
}
template <int NW, bool DRAIN = true>
__device__ __forceinline__ void publish(LAS char* lds, int cnt, char* ws, unsigned off, int nl, unsigned tag, int lane) {
  if (DRAIN) wait_vm<0>();
  unsigned old = 0;
  if (lane == 0) old = __hip_atomic_fetch_add((LAS unsigned*)(lds + SY_CNT + cnt * 4), 1u, __ATOMIC_RELAXED, __HIP_MEMORY_SCOPE_WORKGROUP);
  if (__builtin_amdgcn_readfirstlane(old) == NW - 1 && lane < nl) set_flag(ws, off, tag);
}

template <int PA, int PB>
__device__ __forceinline__ void conv_upper(LAS char* lds, int lane, int hc, int cp, rsrc_t r_s) {
  const unsigned dsk2 = *(const LAS unsigned*)(lds + SY_VAL + 4);
  const float kscale = *(const LAS float*)(lds + SY_VAL + 8 + hc * 4);
  const float dskip = hc ? hi_f(dsk2) : lo_f(dsk2);
  const int G = lane >> 4, q4 = (lane & 15) >> 2, p = lane & 3;
  const LAS char* pa = lds + KF + hc * CV_KSTR + 96 + 2 * (76 + 16 * p - 32 * G - 4 * q4 - 16);
  const LAS char* pb = lds + CV_U + hc * CV_USTR + 160 + 2 * (-76 + 32 * G + 4 * q4 + 4 * p);
  const LAS char* pu = lds + CV_U + hc * CV_USTR + 160 + lane * 2;
  constexpr int J0 = 2 * PA - 1, J1 = 2 * PB, NT = J1 - J0;
  f16x8 A[16], B[32];
  f32x4 ca[NT];
#define CV_A(q) __builtin_bit_cast(f16x8, cat8(trrd(pa + 256 * (q) + 32), trrd(pa + 256 * (q))))
#define CV_B(w) __builtin_bit_cast(f16x8, cat8(trrd(pb + 128 * (w)), trrd(pb + 128 * (w) + 32)))
  B[0] = CV_B(0); B[1] = CV_B(1); B[2] = CV_B(2);
  constexpr int QA = (J0 - 1) / 2;
#pragma unroll
  for (int q = PB - 1; q >= QA; --q) A[q] = CV_A(q);
#pragma unroll
  for (int j = 0; j < NT; ++j) ca[j] = (f32x4){0.f, 0.f, 0.f, 0.f};
  static_for<0, J1>([&](auto wc) {
    constexpr int w = decltype(wc)::value;
    if constexpr (w + 3 < J1) B[w + 3] = CV_B(w + 3);
    if constexpr ((w & 1) == 0 && QA - 1 - w / 2 >= 0) A[QA - 1 - w / 2] = CV_A(QA - 1 - w / 2);
#pragma unroll
    for (int q = PB - 1; q >= 0; --q) {
      const int j = w + 2 * q;
      if (j >= J0 && j < J1) ca[j - J0] = __builtin_amdgcn_mfma_f32_16x16x32_f16(A[q], B[w], ca[j - J0], 0, 0, 0);
    }
    SCHED_FENCE();
  });
  float c3 = 0.f, c2 = 0.f, c1 = 0.f, yv[NT];
#pragma unroll
  for (int j = 0; j < NT; ++j) {
    const f32x4 a = ca[j];
    const float h3 = a[3];
    const float h2 = a[2] + shr_carry(c3, h3);
    const float h1 = a[1] + shr_carry(c2, h2);
    yv[j] = a[0] + shr_carry(c1, h1);
    c3 = dpp_mov<0x13C>(h3); c2 = dpp_mov<0x13C>(h2); c1 = dpp_mov<0x13C>(h1);
  }
#pragma unroll
  for (int jp = PA; jp < PB; ++jp) {
    const float u0 = (float)*(const LAS _Float16*)(pu + jp * 256), u1 = (float)*(const LAS _Float16*)(pu + jp * 256 + 128);
    const unsigned yq = pk_bf16(yv[2 * jp - J0] * kscale, yv[2 * jp + 1 - J0] * kscale);
    const unsigned uq = pk_bf16(u0 * dskip, u1 * dskip);
    st32c(pk_bf16(lo_f(yq) + lo_f(uq), hi_f(yq) + hi_f(uq)), lane * 4u, r_s, (unsigned)((cp * 2 + hc) * 4096 + jp * 256));
  }
#undef CV_A
#undef CV_B
}

#if defined(__HIP_DEVICE_COMPILE__)
__attribute__((target("no-packed-fp32-ops")))
#endif
__global__ __launch_bounds__(512, 2) void fused_kernel(
    const unsigned short* __restrict__ x, const unsigned short* __restrict__ A_re,
    const unsigned short* __restrict__ A_im, const unsigned short* __restrict__ B_re,
    const unsigned short* __restrict__ B_im, const unsigned short* __restrict__ C_re,
    const unsigned short* __restrict__ C_im, const unsigned* __restrict__ log_dt2,
    const unsigned* __restrict__ dsk2p, const unsigned short* __restrict__ wg,
    const unsigned short* __restrict__ wu, const unsigned short* __restrict__ wd,
    char* ws, unsigned short* __restrict__ out) {
  extern __shared__ __attribute__((aligned(16))) char smem[];
  unsigned long long pc0;
  asm volatile("s_getpc_b64 %0" : "=s"(pc0));
  LAS char* lds = (LAS char*)smem;
  const unsigned ldsb = (unsigned)(uintptr_t)lds;
  const int tid = threadIdx.x, lane = tid & 63;
  const int w8 = __builtin_amdgcn_readfirstlane(tid >> 6);
  const int wv = w8 & 3;
  const int bid = blockIdx.x;
  const int xcd = bid & 7, bi = bid >> 3;
  unsigned pf0 = 0;
  if (w8 == 5) ld32(pf0, (unsigned)((bi * 16 + (lane & 15)) * 128), mk_rsrc((const void*)pc0, CODE_BYTES), 0u);
  asm volatile("" :: "s"(x), "s"(A_re), "s"(A_im), "s"(B_re), "s"(B_im), "s"(C_re), "s"(C_im), "s"(log_dt2),
               "s"(dsk2p), "s"(wg), "s"(wu), "s"(wd), "s"(ws), "s"(out));
  const int mt = (xcd >> 2) * 8 + (bi >> 2);
  const int ft = (xcd & 3) * 4 + (bi & 3);
  const int rot = ((bi >> 2) + 2 * (bi & 3)) & 7;
  const int cp = xcd * 32 + bi;
  const int tt = xcd * 4 + (bi >> 3);
  const int dtile = bi & 7;
  const int rot2 = ((bi >> 3) * 7 + dtile * 3) % 28;

  const rsrc_t r_x = mk_rsrc(x, SL * DM * 2);
  const rsrc_t r_s = mk_rsrc(ws + WS_ST, SL * DM * 2);
  const rsrc_t r_fl = mk_rsrc(ws + WS_FLAG, FL_BYTES);
#define G2_KS(w, i) ((unsigned)((w) * 28 + (((i) + rot2) >= 28 ? (i) + rot2 - 28 : (i) + rot2)))
#define XO(t_) ((unsigned)((tt * 64 + ((t_) >> 3)) * (DM * 2) + dtile * 128 + ((t_) & 7) * 16))
#define EPI_X() do { int l_ = lane; PIN(l_); u32x4 xw_[2]; \
    _Pragma("unroll") for (int j = 0; j < 2; ++j) ld128(xw_[j], XO(wv * 128 + j * 64 + l_), r_x, 0u); \
    lds_wait<4>(ldsb + SY_MC, 2u); wait_vm<0>(); \
    _Pragma("unroll") for (int j = 0; j < 2; ++j) { PIN(xw_[j]); *(LAS u32x4*)(lds + EPX + (wv * 128 + j * 64 + l_) * 16) = xw_[j]; } } while (0)
#define EPI_S() do { int l_ = lane; PIN(l_); u32x4 sw_[4]; \
    const unsigned so = (unsigned)((dtile * 64 + wv * 16 + (l_ >> 4)) * 4096 + (tt >> 1) * 256 + (l_ & 15) * 16); \
    _Pragma("unroll") for (int j = 0; j < 4; ++j) ld128c(sw_[j], so, r_s, (unsigned)(j * 16384)); \
    wait_vm<0>(); \
    _Pragma("unroll") for (int j = 0; j < 4; ++j) { PIN(sw_[j]); \
      *(LAS u32x4*)(lds + EPS + (wv * 16 + j * 4 + (l_ >> 4)) * 256 + ((l_ + wv * 2 + (j >> 1)) & 15) * 16) = sw_[j]; } } while (0)

  const rsrc_t r_wg = mk_rsrc(wg, DM * FF * 2), r_wu = mk_rsrc(wu, DM * FF * 2);
  unsigned woff[7], xoff[4];
  auto g1_stage = [&](int s) __attribute__((always_inline)) {
    const unsigned st = s == 0 ? 0u : (unsigned)(1 + (s - 1 + rot) % 7), lb = ldsb + (unsigned)((s % 3) * STG);
#pragma unroll
    for (int i = 0; i < 4; ++i)
      dma16x4(lb + XOFF + i * 4096, xoff[0], xoff[1], xoff[2], xoff[3], r_x, (unsigned)((mt * 128 + i * 32) * 1024) + st * 128u);
#pragma unroll
    for (int h = 0; h < 2; ++h) {
      const unsigned wso = (unsigned)(ft * 224) + (st * 64u + h * 32u) * (FF * 2);
      dma16x4(lb + h * 7168, woff[0], woff[1], woff[2], woff[3], r_wg, wso);
      dma16x3(lb + h * 7168 + 4096, woff[4], woff[5], woff[6], r_wg, wso);
      dma16x4(lb + W_UPOFF + h * 7168, woff[0], woff[1], woff[2], woff[3], r_wu, wso);
      dma16x3(lb + W_UPOFF + h * 7168 + 4096, woff[4], woff[5], woff[6], r_wu, wso);
    }
  };
  unsigned prm[6], ldt2, ep, dsk2;
  u32x4 xr[4];
  if (w8 < 4) {
    const unsigned po = (unsigned)((cp * 2 + (wv & 1)) * NM * 2);
    ld32(prm[0], lane * 4u, mk_rsrc(A_re, DM * NM * 2), po);
    ld32(prm[1], lane * 4u, mk_rsrc(A_im, DM * NM * 2), po);
    ld32(prm[2], lane * 4u, mk_rsrc(B_re, DM * NM * 2), po);
    ld32(prm[3], lane * 4u, mk_rsrc(B_im, DM * NM * 2), po);
    ld32(prm[4], lane * 4u, mk_rsrc(C_re, DM * NM * 2), po);
    ld32(prm[5], lane * 4u, mk_rsrc(C_im, DM * NM * 2), po);
    ld32(ldt2, 0u * lane, mk_rsrc(log_dt2, DM * 2), (unsigned)(cp * 4));
  } else if (w8 >= 6) {
    const unsigned vo = (unsigned)((lane >> 4) * 1024 + (lane & 15) * 16);
#pragma unroll
    for (int i = 0; i < 4; ++i) ld128(xr[i], vo, r_x, (unsigned)((bid * 8 + (wv - 2) * 4) * 1024 + i * 256));
    ld32c(ep, 0u * lane, r_fl, FL_EP);
    ld32(dsk2, 0u * lane, mk_rsrc(dsk2p, DM * 2), (unsigned)(cp * 4));
  } else if (w8 == 4) {
    __builtin_amdgcn_s_setprio(3);
#pragma unroll
    for (int i = 0; i < 4; ++i)
      xoff[i] = (unsigned)((lane >> 3) * 1024 + (((lane & 7) ^ ((lane >> 4) + 4 * (i & 1))) * 16) + i * 7168);
#pragma unroll
    for (int i = 0; i < 7; ++i) {
      const int c = i * 64 + lane, row = c / 14, col = c - row * 14;
      woff[i] = (unsigned)(row * (FF * 2) + col * 16 - (i & 3) * 1024);
    }
    __builtin_amdgcn_s_sleep(LDLY);
    g1_stage(0);
  }
  asm volatile("; first loads issued" ::: "memory");
  if (w8 < 4) {
    const rsrc_t r_h = mk_rsrc(ws + WS_HF, SL * FF * 2);
    {
    const int hc = wv & 1, m = wv >> 1;
    if (tid < 4) *(LAS u32x4*)(lds + SY + tid * 16) = (u32x4){0u, 0u, 0u, tid == 2 ? 1u : 0u};
    __builtin_amdgcn_s_setprio(2);
    {
      wait_vm<0>();
      PIN(ldt2);
      const float ldt = hc ? hi_f(ldt2) : lo_f(ldt2);
      const float L2E = 1.4426950408889634f, L2E_LO = (float)(1.4426950408889634074 - (double)1.4426950408889634f);
      const float th = ldt * L2E;
      const float tl = fmaf(ldt, L2E, -th) + ldt * L2E_LO;
      const float e0 = __builtin_amdgcn_exp2f(th);
      const float dt = rbf(fmaf(e0 * 0.6931471805599453f, tl, e0));
      wait_vm<0>();
#pragma unroll
      for (int i = 0; i < 6; ++i) PIN(prm[i]);
      float av[2], bv[2];
      f32x2 wq[2];
#pragma unroll
      for (int m = 0; m < 2; ++m) {
        av[m] = dt * (m ? hi_f(prm[0]) : lo_f(prm[0]));
        bv[m] = dt * (m ? hi_f(prm[1]) : lo_f(prm[1]));
        const f32x2 db = {dt * (m ? hi_f(prm[2]) : lo_f(prm[2])), dt * (m ? hi_f(prm[3]) : lo_f(prm[3]))};
        const f32x2 cc = {m ? hi_f(prm[4]) : lo_f(prm[4]), m ? hi_f(prm[5]) : lo_f(prm[5])};
        wq[m] = cmul(cc, db);
      }
      const float amax = fmaxf(wave_max(fmaxf(av[0], av[1])), 0.f);
      const float g2 = amax * L2E;
      const float wm = wave_max(fmaxf(fmaxf(fabsf(wq[0].x), fabsf(wq[0].y)), fmaxf(fabsf(wq[1].x), fabsf(wq[1].y))));
      int we = __builtin_amdgcn_frexp_expf(wm);
      we = we < -100 ? -100 : we;
      const float wsc = __builtin_amdgcn_ldexpf(1.0f, -we);
      f32x4 kacc[3];
#pragma unroll
      for (int t = 0; t < 3; ++t) kacc[t] = (f32x4){0.f, 0.f, 0.f, 0.f};
      const int kgs = (m ? CV_U : KGS) + hc * 4096;
      LAS char* tw[4];
#pragma unroll
      for (int c = 0; c < 4; ++c) tw[c] = lds + kgs + lane * 64 + ((c ^ ((lane >> 1) & 3)) << 4);
      const LAS char* tr[2];
      {
        const int G = lane >> 4, q4 = (lane & 15) >> 2, p = lane & 3;
#pragma unroll
        for (int t = 0; t < 2; ++t)
          tr[t] = lds + kgs + (4 * G + 2 * t + (q4 >> 1)) * 64 + ((((q4 & 1) * 2 + (p >> 1)) ^ ((2 * G + t) & 3)) << 4) + (p & 1) * 8;
      }
      auto put16 = [&](const f32x2 (&e)[16]) __attribute__((always_inline)) {
#pragma unroll
        for (int h = 0; h < 2; ++h) {
          u32x4 re, im;
#pragma unroll
          for (int j = 0; j < 4; ++j) {
            re[j] = pk_f16(e[8 * h + 2 * j].x, e[8 * h + 2 * j + 1].x);
            im[j] = pk_f16(e[8 * h + 2 * j].y, e[8 * h + 2 * j + 1].y);
          }
          *(LAS u32x4*)tw[h] = re;
          *(LAS u32x4*)tw[2 + h] = im;
        }
      };
#define KG_FRAG(s) __builtin_bit_cast(f16x8, cat8(trrd(tr[0] + (s) * 1024), trrd(tr[1] + (s) * 1024)))
#define KG_RD() do { _Pragma("unroll") for (int s = 0; s < 4; ++s) pf[s] = KG_FRAG(s); SCHED_FENCE(); } while (0)
#define KG_MMA(t) do { SCHED_FENCE(); _Pragma("unroll") for (int s = 0; s < 4; ++s) \
        kacc[t] = __builtin_amdgcn_mfma_f32_16x16x32_f16(pf[s], qf[s], kacc[t], 0, 0, 0); } while (0)
      {
        const float a = (m ? av[1] : av[0]) - amax, b = m ? bv[1] : bv[0];
        f32x2 w = m ? wq[1] : wq[0];
        w.x *= wsc; w.y *= -wsc;
        f32x2 e[16];
        {
          const f32x2 z16 = cexp_l(a, b, 16.f);
          e[0] = (f32x2){1.f, 0.f}; e[1] = z16; e[2] = cmul(z16, z16); e[3] = cmul(e[2], z16);
          const f32x2 z64 = cmul(e[2], e[2]);
#pragma unroll
          for (int c = 4; c < 16; ++c) e[c] = cmul(e[c - 4], z64);
          put16(e);
        }
        f16x8 qf[4], pf[4];
#pragma unroll
        for (int s = 0; s < 4; ++s) qf[s] = KG_FRAG(s);
        SCHED_FENCE();
        const f32x2 z1 = cexp_l(a, -b, 1.f);
        const f32x2 z2 = cmul(z1, z1), z4 = cmul(z2, z2);
        e[0] = w; e[1] = cmul(w, z1); e[2] = cmul(w, z2); e[3] = cmul(e[1], z2);
#pragma unroll
        for (int c = 4; c < 16; ++c) e[c] = cmul(e[c - 4], z4);
        put16(e);
        KG_RD();
        const f32x2 z256 = cexp_l(a, -b, 256.f);
        const f32x2 z512 = cmul(z256, z256);
        const f32x2 w2 = cmul(w, z512);
        e[0] = cmul(w, z256); e[1] = cmul(e[0], z2); e[8] = w2; e[12] = cmul(e[0], z512);
        const f32x2 w3 = e[12];
#pragma unroll
        for (int i = 1; i < 4; ++i) {
          e[2 * i] = cmul(e[2 * i - 2], z4); e[2 * i + 1] = cmul(e[2 * i - 1], z4);
          e[8 + i] = cmul(e[7 + i], z4); e[12 + i] = cmul(e[11 + i], z4);
        }
        KG_MMA(0);
        put16(e);
        KG_RD();
        const f32x2 z8 = cmul(z4, z4);
        e[0] = cmul(w2, z512); e[2] = cmul(w3, z512); e[4] = cmul(e[0], z512); e[6] = cmul(e[2], z512);
        e[8] = cmul(e[4], z512);
#pragma unroll
        for (int i = 0; i < 4; ++i) e[2 * i + 1] = cmul(e[2 * i], z8);
#pragma unroll
        for (int i = 9; i < 16; ++i) e[i] = (f32x2){0.f, 0.f};
        KG_MMA(1);
        put16(e);
        KG_RD();
        KG_MMA(2);
      }
#pragma unroll
      for (int t = 0; t < 3; ++t) *(LAS f32x4*)(lds + (m ? KF + hc * CV_KSTR : KGS + hc * 4096) + t * 1024 + lane * 16) = kacc[t];
      if (m) *(LAS f32x2*)(lds + SY_KG + hc * 8) = (f32x2){g2, (float)we};
    }
    }
    f32x16 acc[7];
    f32x4 csa[2];
    unsigned tag;
    {
      const int G = lane >> 4;
      const LAS char* wa0 = lds + (G & 1) * W_UPOFF + (8 * (G >> 1) + ((lane & 15) >> 2)) * 224 + (lane & 3) * 8;
      const LAS char* wa[7];
#pragma unroll
      for (int T = 0; T < 7; ++T) wa[T] = wa0 + 32 * ((T + 2 * wv) % 7);
      const LAS char* xa[4];
      {
        const int row = 32 * wv + (lane & 31), sw = (row >> 1) & 7;
#pragma unroll
        for (int ks = 0; ks < 4; ++ks) xa[ks] = lds + XOFF + row * 128 + (((2 * ks + (lane >> 5)) ^ sw) << 4);
      }
      constexpr int PD = 3;
      bf16x8 Af[196], Bf[32];
      const unsigned o1 = ((lane & 15) == 4 * ((lane >> 4) & 1)) ? 0x3f803f80u : 0u;
      u32x4 onesq = {o1, o1, o1, o1};
      PIN(onesq);
      const bf16x8 ones = __builtin_bit_cast(bf16x8, onesq);
#define A_READ(i, T) __builtin_bit_cast(bf16x8, cat8(trrd(wa[T] + (((i) >> 2) % 3) * STG + (16 * ((i) & 3)) * 224), \
                                                      trrd(wa[T] + (((i) >> 2) % 3) * STG + (16 * ((i) & 3) + 4) * 224)))
#define B_READ(i) (*(const LAS bf16x8*)(xa[(i) & 3] + (((i) >> 2) % 3) * STG))
#pragma unroll
      for (int T = 0; T < 7; ++T) acc[T] = (f32x16){};
      csa[0] = (f32x4){0.f, 0.f, 0.f, 0.f}; csa[1] = csa[0];
      bar_lgkm();
      tag = lds_get1(ldsb + SY_VAL);
      Bf[0] = B_READ(0);
#pragma unroll
      for (int T = 0; T < PD; ++T) Af[T] = A_READ(0, T);
      bf16x8 Al[7][4];
      u32x2 stg;
      float cs[7];
      static_for<0, 196>([&](auto nc) {
        constexpr int n = decltype(nc)::value, i = n / 7, T = n % 7;
        constexpr int m = n + PD, im = m / 7, Tm = m % 7;
        if constexpr (n == 70) ld64c(stg, (unsigned)((lane & 31) * 8), r_fl, FL_STAT + (unsigned)((mt * 128 + wv * 32) * 8));
        if constexpr (n == 27 * 7 + 2) {
          if (lane < 32) {
#pragma unroll
            for (int j = 0; j < 2; ++j) *(LAS float*)(lds + CSUM + ((j + 2 * wv) % 7) * 128 + lane * 4) = csa[j][0];
          }
        }
        if constexpr (Tm == 0 && (im & 3) == 0) bar_lgkm();
        if constexpr (m < 196) {
          if constexpr (Tm == 0) Bf[im] = B_READ(im);
          Af[m] = A_READ(im, Tm);
        } else if constexpr (m == 196) {
#pragma unroll
          for (int k = 28; k < 32; ++k) Bf[k] = B_READ(k);
        } else if constexpr (m == 197) {
#pragma unroll
          for (int t = 0; t < 7; ++t) cs[t] = *(const LAS float*)(lds + CSUM + ((t + 2 * wv) % 7) * 128 + (lane & 31) * 4);
        } else {
#pragma unroll
          for (int k = 0; k < 4; ++k) Al[0][k] = A_READ(28 + k, 0);
          lds_setv(ldsb + SY_MC + wv * 4, 1u);
        }
        acc[T] = __builtin_amdgcn_mfma_f32_32x32x16_bf16(Af[n], Bf[i], acc[T], 0, 0, 0);
        if constexpr (T < 2)
          csa[T] = __builtin_amdgcn_mfma_f32_16x16x32_bf16(ones, Af[n], csa[T], 0, 0, 0);
        SCHED_FENCE();
      });
      wait_vm<0>();
      PIN(stg);
      if (__builtin_expect(!wave_all(stg[1] == tag), 0)) {
#pragma unroll 1
        for (int it = 0; it < (1 << 18); ++it) {
          ld64c(stg, (unsigned)((lane & 31) * 8), r_fl, FL_STAT + (unsigned)((mt * 128 + wv * 32) * 8));
          wait_vm<0>();
          PIN(stg);
          if (wave_all(stg[1] == tag)) break;
          __builtin_amdgcn_s_sleep(2);
        }
      }
      const unsigned stw = stg[0];
      const float mb = lo_f(stw), sd = hi_f(stw);
#pragma unroll
      for (int k = 28; k < 32; ++k) {
        u32x4 v = __builtin_bit_cast(u32x4, Bf[k]);
#pragma unroll
        for (int j = 0; j < 4; ++j) v[j] = pk_bf16(lo_f(v[j]) - mb, hi_f(v[j]) - mb);
        Bf[k] = __builtin_bit_cast(bf16x8, v);
      }
      const unsigned nm = (stw & 0xffffu) ^ 0x8000u;
      u32x4 bq = {nm | (nm << 16), nm, 0u, 0u};
      if (lane >= 32) bq = (u32x4){0u, 0u, 0u, 0u};
      const float ga = -1.4426950408889634f * __builtin_amdgcn_rcpf(sd);
      const unsigned hsb = (unsigned)(((mt * 4 + wv) * 112 + ft * 7) * 1024);
      static_for<0, 8>([&](auto tc) {
        constexpr int T = decltype(tc)::value;
        u32x4 o;
        if constexpr (T < 7) {
          const float c = cs[T];
          const float ch = rbf(c), cm = rbf(c - ch), cl = (c - ch) - cm;
          const u32x4 aq = {pk_bf16(ch, cm), pk_bf16(cl, 0.f), 0u, 0u};
          acc[T] = __builtin_amdgcn_mfma_f32_32x32x16_bf16(__builtin_bit_cast(bf16x8, aq), __builtin_bit_cast(bf16x8, bq), acc[T], 0, 0, 0);
          SCHED_FENCE();
        }
#pragma unroll
        for (int j = 0; j < 4; ++j) {
          if constexpr (T + 1 < 7) Al[T + 1][j] = A_READ(28 + j, T + 1);
          if constexpr (T < 7) acc[T] = __builtin_amdgcn_mfma_f32_32x32x16_bf16(Al[T][j], Bf[28 + j], acc[T], 0, 0, 0);
          SCHED_FENCE();
          if constexpr (T >= 1) {
            const float s0 = __builtin_amdgcn_rcpf(fmaf(__builtin_amdgcn_exp2f(acc[T - 1][2 * j] * ga), sd, sd));
            const float s1 = __builtin_amdgcn_rcpf(fmaf(__builtin_amdgcn_exp2f(acc[T - 1][2 * j + 1] * ga), sd, sd));
            o[j] = pk_bf16(s0 * acc[T - 1][8 + 2 * j], s1 * acc[T - 1][8 + 2 * j + 1]);
            PIN(o[j]);
          }
          SCHED_FENCE();
        }
        if constexpr (T >= 1) st128c(o, lane * 16u, r_h, hsb + (unsigned)((T - 1 + 2 * wv) % 7) * 1024u);
      });
    }
    const unsigned ldw = ldsb + SY_LD;
    const LAS char* waT[2];
    {
      const int G = lane >> 4, q4 = (lane & 15) >> 2;
      const LAS char* wa0 = lds + wv * 2048 + (4 * (G >> 1) + q4) * 128 + (G & 1) * 32 + (lane & 3) * 8;
      waT[0] = wa0 + ((q4 >> 1) * 64); waT[1] = wa0 + (((q4 >> 1) ^ 1) * 64);
    }
#define D_READ(i, T) __builtin_bit_cast(bf16x8, cat8(trrd(waT[T] + g2_base(i)), trrd(waT[T] + g2_base(i) + 1024)))
    bf16x8 Wr[28][2];
    lds_wait<1>(ldw, 3u);
    static_for<0, G2_NR>([&](auto ic) { constexpr int i = decltype(ic)::value; Wr[i][0] = D_READ(i, 0); Wr[i][1] = D_READ(i, 1); });
    wait_vm<0>();
    if (lane < 8) set_flag(ws, FL_H + (unsigned)(((mt * 2 + (wv >> 1)) * 8 + lane) * 256 + (ft * 2 + (wv & 1)) * 4), tag);
    lds_setv(ldsb + SY_MC + wv * 4, 2u);
    if (wv == 0) {
      lds_wait<4>(ldsb + SY_UR, 1u);
      conv_upper<14, 16>(lds, lane, 0, cp, r_s);
    } else {
      __builtin_amdgcn_s_sleep(PDLY);
    }
    __builtin_amdgcn_s_setprio(0);
    poll_flags<2>((lane & 1) * 16u, r_fl, FL_H + (unsigned)((tt * 8 + dtile) * 256 + wv * 32), tag);
    if (wv == 0) publish<4>(lds, 2, ws, FL_S + (unsigned)((lane * 8 + (cp >> 5)) * 256 + (cp & 31) * 4), 32, tag, lane);
    __builtin_amdgcn_s_setprio(2);
    u32x4 hb[28][2];
#define HB_LOAD(i) do { ld128c(hb[i][0], lane * 16u, r_h, ((unsigned)(tt * 2) * 112u + G2_KS(wv, i)) * 1024u); \
                        ld128c(hb[i][1], lane * 16u, r_h, ((unsigned)(tt * 2 + 1) * 112u + G2_KS(wv, i)) * 1024u); } while (0)
#pragma unroll
    for (int i = 0; i < G2_H0; ++i) HB_LOAD(i);
    f32x16 acc2[2][2];
    {
#pragma unroll
      for (int a = 0; a < 2; ++a)
#pragma unroll
        for (int b = 0; b < 2; ++b) acc2[a][b] = (f32x16){};
      static_for<0, 28>([&](auto ic) {
        constexpr int i = decltype(ic)::value;
        if constexpr (i + 2 == 13) lds_wait<1>(ldw, 6u);
        if constexpr (i + 2 >= G2_NR && i + 2 < 28) { Wr[i + 2][0] = D_READ(i + 2, 0); Wr[i + 2][1] = D_READ(i + 2, 1); }
        wait_vm<2 * (g2_issued(i) - i - 1)>();
        PIN(hb[i][0]); PIN(hb[i][1]);
#pragma unroll
        for (int T = 0; T < 2; ++T)
#pragma unroll
          for (int b = 0; b < 2; ++b)
            acc2[T][b] = __builtin_amdgcn_mfma_f32_32x32x16_bf16(Wr[i][T], __builtin_bit_cast(bf16x8, hb[i][b]), acc2[T][b], 0, 0, 0);
        SCHED_FENCE();
        static_for<g2_issued(i), g2_issued(i + 1)>([&](auto jc) { constexpr int j = decltype(jc)::value; HB_LOAD(j); });
      });
    }
    {
      int l2 = lane; PIN(l2);
      const int tl = l2 & 31, x7 = (tl & 7) ^ (l2 >> 5);
#pragma unroll
      for (int b = 0; b < 2; ++b) {
        const int c8 = 4 * b + (tl >> 3);
        const int o4 = c8 < 5 ? RED_A + c8 * 8192 : RED_B + (c8 - 5) * 8192;
        LAS char* rp = lds + o4 + wv * 2048 + (tl & 7) * 256;
#pragma unroll
        for (int T = 0; T < 2; ++T)
#pragma unroll
          for (int g = 0; g < 4; ++g) {
            const f32x4 v = {acc2[T][b][4 * g], acc2[T][b][4 * g + 1], acc2[T][b][4 * g + 2], acc2[T][b][4 * g + 3]};
            *(LAS f32x4*)(rp + T * 128 + (((2 * g) ^ x7) << 4)) = v;
          }
      }
    }
  } else if (w8 == 4) {
    const rsrc_t r_wd = mk_rsrc(wd, FF * DM * 2);
    unsigned wv4[4];
#pragma unroll
    for (int i = 0; i < 4; ++i)
      wv4[i] = (unsigned)((lane >> 3) * 1024 + (((lane & 7) ^ (((lane >> 4) & 1) << 2)) * 16) + (i >> 1) * 458752 + (i & 1) * 8192 - i * 1024);
    const unsigned ldw = ldsb + SY_LD;
#define G2_GROUP(g) G2_STEPS(5 * (g), cmin(5 * (g) + 5, 28))
#define G2_STEPS(a_, b_) static_for<a_, b_>([&](auto ic) { constexpr int i = decltype(ic)::value; \
        const unsigned so_ = G2_KS(0, i) * 16384u + (unsigned)(dtile * 128); \
        dma16x4(ldsb + g2_base(i), wv4[0], wv4[1], wv4[2], wv4[3], r_wd, so_); \
        dma16x4(ldsb + g2_base(i) + 4096, wv4[0], wv4[1], wv4[2], wv4[3], r_wd, so_ + 2u * 458752u); })
    g1_stage(1);
    wait_vm<44>();
    bar_only();
#pragma unroll
    for (int k = 0; k < 6; ++k) {
      g1_stage(k + 2);
      wait_vm<44>();
      bar_only();
    }
    G2_GROUP(0);
    wait_vm<40>();
    bar_only();
    G2_GROUP(1);
    G2_STEPS(12, 13);
    lds_wait<4>(ldsb + SY_MC, 1u);
    G2_STEPS(10, 12);
    wait_vm<0>();
    lds_setv(ldw, 3u);
    lds_wait<4>(ldsb + SY_MC, 2u);
    G2_STEPS(13, 28);
    wait_vm<0>();
    lds_setv(ldw, 6u);
    __builtin_amdgcn_s_setprio(0);
    EPI_X();
    lds_wait<1>(ldsb + SY_SR, 1u);
    EPI_S();
  } else if (w8 == 5) {
    wait_vm<0>();
    asm volatile("" :: "v"(pf0));
    bar_only();
    const unsigned tag = lds_get1(ldsb + SY_VAL);
    bar_only();
    {
      const int c = lane & 15, g = lane >> 4;
      unsigned d[2][3][4];
      float kscale[2];
#pragma unroll
      for (int hc = 0; hc < 2; ++hc) {
        f32x4 kacc[3];
#pragma unroll
        for (int t = 0; t < 3; ++t)
          kacc[t] = *(const LAS f32x4*)(lds + KF + hc * CV_KSTR + t * 1024 + lane * 16) + *(const LAS f32x4*)(lds + KGS + hc * 4096 + t * 1024 + lane * 16);
        const f32x2 gw = *(const LAS f32x2*)(lds + SY_KG + hc * 8);
        const float g2 = gw.x;
        const int we = (int)gw.y;
        if (g2 > 0.f) {
#pragma unroll
          for (int i = 0; i < 4; ++i) {
            const int l0 = 16 * c + 4 * g + i;
            const int l1 = g < 2 ? 256 + 16 * c + 8 * g + 2 * i : 512 + 256 * (g - 2) + 16 * c + 4 * i;
            const int l2 = g < 2 ? 1024 + 256 * (2 * g + (i >> 1)) + 8 * (i & 1) + 16 * c : 2048;
            kacc[0][i] *= __builtin_amdgcn_exp2f(g2 * (float)(l0 - 2048));
            kacc[1][i] *= __builtin_amdgcn_exp2f(g2 * (float)(l1 - 2048));
            kacc[2][i] *= __builtin_amdgcn_exp2f(g2 * (float)(l2 - 2048));
          }
        }
        kscale[hc] = __builtin_amdgcn_ldexpf(1.0f, we - 7) * __builtin_amdgcn_exp2f(g2 * 2048.f);
#pragma unroll
        for (int t = 0; t < 3; ++t)
#pragma unroll
          for (int i = 0; i < 4; ++i) { d[hc][t][i] = pk_f16(kacc[t][i] * 128.f, kacc[t][i] * 128.f); PIN(d[hc][t][i]); }
        bar_only();
      }
      bar_only();
      bar_only();
      bar_only();
      bar_only();
      __builtin_amdgcn_s_sleep(WSL);
      __builtin_amdgcn_s_setprio(3);
      *(LAS f32x2*)(lds + SY_VAL + 8) = (f32x2){kscale[0], kscale[1]};
#define MIX(a, b) (((a) & 0xffffu) | ((b) & 0xffff0000u))
#pragma unroll
      for (int hc = 0; hc < 2; ++hc) {
        LAS char* kf = lds + KF + hc * CV_KSTR + 96;
        if (g < 2) {
#pragma unroll
          for (int h = 0; h < 2; ++h) {
            LAS char* p = kf + 2 * (1024 + 256 * (2 * g + h) + 16 * c);
            const unsigned v0 = d[hc][2][2 * h], v1 = d[hc][2][2 * h + 1];
            *(LAS u32x2*)(p - 8) = (u32x2){v0, v0};
            *(LAS u32x4*)(p) = (u32x4){v0, v0, MIX(v0, v1), v1};
            *(LAS u32x2*)(p + 16) = (u32x2){v1, v1};
          }
        } else {
          if (lane == 32) *(LAS u32x2*)(kf + 2 * 2044) = (u32x2){d[hc][2][0], d[hc][2][0]};
        }
        if (g >= 2) {
          LAS char* p = kf + 2 * (512 + 256 * (g - 2) + 16 * c);
          const unsigned v0 = d[hc][1][0], v1 = d[hc][1][1], v2 = d[hc][1][2], v3 = d[hc][1][3];
          *(LAS unsigned*)(p - 4) = v0;
          *(LAS u32x4*)(p) = (u32x4){v0, MIX(v0, v1), v1, v2};
          *(LAS u32x2*)(p + 16) = (u32x2){v2, MIX(v2, v3)};
          *(LAS unsigned*)(p + 24) = v3;
        }
        if (g < 2) {
          LAS char* p = kf + 2 * (256 + 16 * c + 8 * g);
          const unsigned v0 = d[hc][1][0], v1 = d[hc][1][1], v2 = d[hc][1][2], v3 = d[hc][1][3];
          *(LAS unsigned short*)(p - 2) = (unsigned short)v0;
          *(LAS u32x2*)(p) = (u32x2){v0, MIX(v1, v2)};
          *(LAS unsigned*)(p + 8) = v2;
          *(LAS unsigned short*)(p + 12) = (unsigned short)v3;
        }
        *(LAS u32x2*)(kf + 2 * (16 * c + 4 * g)) = (u32x2){MIX(d[hc][0][0], d[hc][0][1]), MIX(d[hc][0][2], d[hc][0][3])};
        if (lane < 6) *(LAS u32x4*)(kf - 96 + lane * 16) = (u32x4){0u, 0u, 0u, 0u};
      }
      lds_setv(ldsb + SY_UR + 8, 1u);
    }
    __builtin_amdgcn_s_setprio(1);
    lds_wait<4>(ldsb + SY_UR, 1u);
    __builtin_amdgcn_s_sleep(32);
    conv_upper<CV_NP, 16>(lds, lane, 1, cp, r_s);
    conv_upper<CV_NP, 14>(lds, lane, 0, cp, r_s);
    publish<4>(lds, 2, ws, FL_S + (unsigned)((lane * 8 + (cp >> 5)) * 256 + (cp & 31) * 4), 32, tag, lane);
    EPI_X();
    lds_wait<1>(ldsb + SY_CNT + 8, 4u);
    poll_flags<4>((lane & 7) * 16u, r_fl, FL_S + (unsigned)((tt * 8 + dtile) * 256), tag);
    lds_setv(ldsb + SY_SR, 1u);
    EPI_S();
  } else {
    const int hc = wv - 2;
    const int dch = cp * 2 + hc;
    const rsrc_t r_xn = mk_rsrc(ws + WS_XNF, SL * DM * 2);
    const int row0 = bid * 8 + hc * 4;
    wait_vm<0>();
#pragma unroll
    for (int i = 0; i < 4; ++i) PIN(xr[i]);
    PIN(ep); PIN(dsk2);
    const unsigned tag = (unsigned)__builtin_amdgcn_readfirstlane(ep) + 1u;
    lds_setv(ldsb + SY_VAL, tag);
    lds_setv(ldsb + SY_VAL + 4, dsk2);
    {
      float f[32];
#pragma unroll
      for (int j = 0; j < 16; ++j) { f[2 * j] = lo_f(xr[j >> 2][j & 3]); f[2 * j + 1] = hi_f(xr[j >> 2][j & 3]); }
      float s0 = 0.f, s1 = 0.f;
#pragma unroll
      for (int j = 0; j < 32; j += 4) { s0 += f[j] + f[j + 1]; s1 += f[j + 2] + f[j + 3]; }
      const float mean = row_sum(s0 + s1) * (1.0f / DM);
      float q0 = 0.f, q1 = 0.f;
#pragma unroll
      for (int j = 0; j < 32; j += 2) {
        const float da = f[j] - mean, db = f[j + 1] - mean;
        q0 = fmaf(da, da, q0); q1 = fmaf(db, db, q1);
      }
      const float var = row_sum(q0 + q1) * (1.0f / DM);
      const float mb = rbf(mean);
      const float sb = rbf(__builtin_amdgcn_sqrtf(rbf(rbf(var) + 1e-5f)));
      if ((lane & 15) == 0)
        st64c((u32x2){pk_bf16(mb, sb), tag}, (unsigned)((lane >> 4) * 8), r_fl, FL_STAT + (unsigned)(row0 * 8));
      const float rs = __builtin_amdgcn_rcpf(sb);
      const unsigned vo = (unsigned)((lane & 15) * 32768 + (lane >> 4) * 16);
#pragma unroll
      for (int i = 0; i < 4; ++i) {
        u32x4 o;
#pragma unroll
        for (int j = 0; j < 4; ++j) {
          const unsigned d = pk_bf16(f[8 * i + 2 * j] - mb, f[8 * i + 2 * j + 1] - mb);
          o[j] = pk_bf16(lo_f(d) * rs, hi_f(d) * rs);
        }
        st128c(o, vo, r_xn, (unsigned)(i * 16 * 32768 + row0 * 16));
      }
    }
    wait_vm<0>();
    if (lane < 8) set_flag(ws, FL_ALL + (unsigned)(lane * 4096 + (bid * 2 + hc) * 4), tag);
    bar_lgkm();
    bar_only();
    bar_only();
    bar_only();
    const unsigned fo = FL_ALL + (unsigned)(xcd * 4096 + hc * 1024);
    int nb = 3;
    {
      u32x4 vf;
      ld128c(vf, lane * 16u, r_fl, fo);
      bar_only();
      wait_vm<0>();
      PIN(vf);
      if (!wave_all((vf[0] == tag) & (vf[1] == tag) & (vf[2] == tag) & (vf[3] == tag))) {
        u32x4 vg;
        bar_only();
        ld128c(vg, lane * 16u, r_fl, fo);
        bar_only();
        wait_vm<0>();
        PIN(vg);
        nb = 1;
        if (!wave_all((vg[0] == tag) & (vg[1] == tag) & (vg[2] == tag) & (vg[3] == tag))) {
          bar_only();
          nb = 0;
          poll_flags<4>(lane * 16u, r_fl, fo, tag);
        }
      }
    }
    unsigned uw[16];
    const unsigned usb = (unsigned)((cp >> 2) * 32768 + (cp & 3) * 4 + hc * 16384);
#pragma unroll
    for (int m = 0; m < 16; ++m) ld32c(uw[m], lane * 16u, r_xn, usb + (unsigned)m * 1024u);
    if (nb >= 1) bar_only();
    if (nb == 3) bar_only();
    wait_vm<0>();
    {
      const bool od = lane & 1;
      LAS char* up = lds + CV_U + 160 + hc * 2048 + (od ? 128 : 0) + (lane >> 1) * 4;
#pragma unroll
      for (int m = 0; m < 16; m += 2) {
        PIN(uw[m]); PIN(uw[m + 1]);
        const unsigned a = pk_f16(lo_f(uw[m]), hi_f(uw[m])), b = pk_f16(lo_f(uw[m + 1]), hi_f(uw[m + 1]));
        const unsigned y = (unsigned)__builtin_amdgcn_update_dpp(0, (int)(od ? a : b), 0xB1, 0xf, 0xf, true);
        const unsigned f0 = od ? y : a, f1 = od ? b : y;
        uw[m] = __builtin_amdgcn_perm(f1, f0, 0x05040100u);
        uw[m + 1] = __builtin_amdgcn_perm(f1, f0, 0x07060302u);
        PIN(uw[m]); PIN(uw[m + 1]);
      }
      if (nb == 3) { bar_only(); __builtin_amdgcn_s_sleep(WSL); }
      __builtin_amdgcn_s_setprio(3);
      if (lane < 10) *(LAS u32x4*)(lds + CV_U + hc * CV_USTR + lane * 16) = (u32x4){0u, 0u, 0u, 0u};
#pragma unroll
      for (int m = 0; m < 16; m += 2) {
        *(LAS unsigned*)(up + m * 128) = uw[m];
        *(LAS unsigned*)(up + CV_USTR + m * 128) = uw[m + 1];
      }
      lds_setv(ldsb + SY_UR + hc * 4, 1u);
    }
    __builtin_amdgcn_s_setprio(1);
    lds_wait<4>(ldsb + SY_UR, 1u);
    if (lane == 0 && bid == 0 && hc == 0) set_flag(ws, FL_EP, tag);
    const float kscale = *(const LAS float*)(lds + SY_VAL + 8 + hc * 4);
    {
      const float dskip = hc ? hi_f(dsk2) : lo_f(dsk2);
      const int G = lane >> 4, q4 = (lane & 15) >> 2, p = lane & 3;
      const LAS char* pa = lds + KF + hc * CV_KSTR + 96 + 2 * (76 + 16 * p - 32 * G - 4 * q4 - 16);
      const LAS char* pb = lds + CV_U + hc * CV_USTR + 160 + 2 * (-76 + 32 * G + 4 * q4 + 4 * p);
      const LAS char* pu = lds + CV_U + hc * CV_USTR + 160 + lane * 2;
      f16x8 A[16], B[32];
#define CV_A(q) __builtin_bit_cast(f16x8, cat8(trrd(pa + 256 * (q) + 32), trrd(pa + 256 * (q))))
#define CV_B(w) __builtin_bit_cast(f16x8, cat8(trrd(pb + 128 * (w)), trrd(pb + 128 * (w) + 32)))
      A[0] = CV_A(0); B[0] = CV_B(0); B[1] = CV_B(1);
      A[1] = CV_A(1); B[2] = CV_B(2); B[3] = CV_B(3);
      f32x4 ca[2][2];
      _Float16 us[2][2];
      unsigned so16[16];
      float c3 = 0.f, c2 = 0.f, c1 = 0.f;
      auto tile_y = [&](const f32x4 a) __attribute__((always_inline)) {
        const float h3 = a[3];
        const float h2 = a[2] + shr_carry(c3, h3);
        const float h1 = a[1] + shr_carry(c2, h2);
        const float y = a[0] + shr_carry(c1, h1);
        c3 = dpp_mov<0x13C>(h3); c2 = dpp_mov<0x13C>(h2); c1 = dpp_mov<0x13C>(h1);
        return y * kscale;
      };
      static_for<0, CV_NP + 1>([&](auto jc) {
        constexpr int jj = decltype(jc)::value;
        if constexpr (jj + 2 < CV_NP) { A[jj + 2] = CV_A(jj + 2); B[2 * jj + 4] = CV_B(2 * jj + 4); B[2 * jj + 5] = CV_B(2 * jj + 5); }
        if constexpr (jj < CV_NP) {
          us[jj & 1][0] = *(const LAS _Float16*)(pu + jj * 256); us[jj & 1][1] = *(const LAS _Float16*)(pu + jj * 256 + 128);
          f32x4 a0 = {0.f, 0.f, 0.f, 0.f}, a1 = {0.f, 0.f, 0.f, 0.f};
#pragma unroll
          for (int q = jj; q >= 0; --q) {
            a0 = __builtin_amdgcn_mfma_f32_16x16x32_f16(A[q], B[2 * (jj - q)], a0, 0, 0, 0);
            a1 = __builtin_amdgcn_mfma_f32_16x16x32_f16(A[q], B[2 * (jj - q) + 1], a1, 0, 0, 0);
          }
          ca[jj & 1][0] = a0; ca[jj & 1][1] = a1;
        }
        if constexpr (jj >= 1) {
          const float y0 = tile_y(ca[(jj - 1) & 1][0]), y1 = tile_y(ca[(jj - 1) & 1][1]);
          const unsigned yq = pk_bf16(y0, y1);
          const unsigned uq = pk_bf16((float)us[(jj - 1) & 1][0] * dskip, (float)us[(jj - 1) & 1][1] * dskip);
          so16[jj - 1] = pk_bf16(lo_f(yq) + lo_f(uq), hi_f(yq) + hi_f(uq));
        }
        if constexpr (jj >= 1 && jj < CV_NP) {
#pragma unroll
          for (int k = 0; k < 2 * (jj + 1); ++k) {
            __builtin_amdgcn_sched_group_barrier(0x8, 1, 0);
            __builtin_amdgcn_sched_group_barrier(0x100, 1, 0);
            __builtin_amdgcn_sched_group_barrier(0x2, jj < 7 ? 4 : 2, 0);
          }
        }
        SCHED_FENCE();
        if constexpr (jj >= 1) st32c(so16[jj - 1], lane * 4u, r_s, (unsigned)(dch * 4096 + (jj - 1) * 256));
      });
    }
    publish<4>(lds, 2, ws, FL_S + (unsigned)((lane * 8 + (cp >> 5)) * 256 + (cp & 31) * 4), 32, tag, lane);
    EPI_X();
    lds_wait<1>(ldsb + SY_SR, 1u);
    EPI_S();
  }
  bar_lgkm();
  {
    int t2 = tid; PIN(t2);
    const u32x4 xv = *(const LAS u32x4*)(lds + EPX + t2 * 16);
    unsigned sv[8];
#pragma unroll
    for (int j = 0; j < 8; ++j) sv[j] = *(const LAS unsigned*)(lds + EPS + ((t2 & 7) * 8 + j) * 256 + (((t2 >> 5) + (t2 & 7)) & 15) * 16 + ((t2 >> 3) & 3) * 4);
    const int rb = w8 < 5 ? RED_A + w8 * 8192 : RED_B + (w8 - 5) * 8192;
    const int tr7 = (t2 >> 3) & 7;
    const LAS char* rp = lds + rb + tr7 * 256 + ((t2 >> 2) & 1) * 128;
    const bool odd = tt & 1;
    u32x4 o;
#pragma unroll
    for (int i = 0; i < 2; ++i) {
      const int ch = (((t2 & 3) * 2 + i) ^ tr7) << 4;
      f32x4 v = *(const LAS f32x4*)(rp + ch);
#pragma unroll
      for (int w = 1; w < 4; ++w) v += *(const LAS f32x4*)(rp + w * 2048 + ch);
#pragma unroll
      for (int jj = 0; jj < 2; ++jj) {
        const unsigned fp = pk_bf16(v[2 * jj], v[2 * jj + 1]);
        const float f0 = lo_f(fp), f1 = hi_f(fp);
        const float g0 = f0 * __builtin_amdgcn_rcpf(1.0f + __builtin_amdgcn_exp2f(f0 * -1.4426950408889634f));
        const float g1 = f1 * __builtin_amdgcn_rcpf(1.0f + __builtin_amdgcn_exp2f(f1 * -1.4426950408889634f));
        const unsigned gp = pk_bf16(g0, g1);
        const int j = 4 * i + 2 * jj;
        const float s0 = odd ? hi_f(sv[j]) : lo_f(sv[j]), s1 = odd ? hi_f(sv[j + 1]) : lo_f(sv[j + 1]);
        const unsigned sg = pk_bf16(s0 * lo_f(gp), s1 * hi_f(gp));
        const unsigned xw = xv[2 * i + jj];
        o[2 * i + jj] = pk_bf16(lo_f(xw) + lo_f(sg), hi_f(xw) + hi_f(sg));
      }
    }
    st128c(o, XO(t2), mk_rsrc(out, SL * DM * 2), 0u);
  }
}

extern "C" void kernel_launch(void* const* d_in, const int* in_sizes, int n_in, void* d_out,
                              int out_size, void* d_ws, size_t ws_size, hipStream_t stream) {
  (void)hipFuncSetAttribute((const void*)fused_kernel, hipFuncAttributeMaxDynamicSharedMemorySize, LDS_BYTES);
  fused_kernel<<<256, 512, LDS_BYTES, stream>>>(
      (const unsigned short*)d_in[0], (const unsigned short*)d_in[1], (const unsigned short*)d_in[2],
      (const unsigned short*)d_in[3], (const unsigned short*)d_in[4], (const unsigned short*)d_in[5],
      (const unsigned short*)d_in[6], (const unsigned*)d_in[7], (const unsigned*)d_in[8],
      (const unsigned short*)d_in[10], (const unsigned short*)d_in[11], (const unsigned short*)d_in[12],
      (char*)d_ws, (unsigned short*)d_out);
}
```

```cpp
#include <hip/hip_runtime.h>
#include <hip/hip_bf16.h>
#include <stdint.h>
#include <type_traits>

typedef __attribute__((ext_vector_type(4))) short s16x4;
typedef __attribute__((ext_vector_type(8))) short s16x8;
typedef __attribute__((ext_vector_type(8))) _Float16 f16x8;
typedef __attribute__((ext_vector_type(8))) __bf16 bf16x8;
typedef __attribute__((ext_vector_type(2))) float f32x2;
typedef __attribute__((ext_vector_type(4))) float f32x4;
typedef __attribute__((ext_vector_type(16))) float f32x16;
typedef __attribute__((ext_vector_type(4))) unsigned u32x4;
typedef __attribute__((ext_vector_type(3))) unsigned u32x3;
typedef __attribute__((ext_vector_type(2))) unsigned u32x2;
typedef __attribute__((ext_vector_type(2))) __bf16 bf16x2;
typedef __attribute__((ext_vector_type(2))) _Float16 f16x2;
#define LAS __attribute__((address_space(3)))
#define GAS __attribute__((address_space(1)))
typedef __amdgpu_buffer_rsrc_t rsrc_t;

constexpr int SL = 2048;
constexpr int DM = 512;
constexpr int NM = 128;
constexpr int FF = 1792;

constexpr size_t WS_XNF = 0;
constexpr size_t WS_ST = 2 * (size_t)SL * DM * 2;
constexpr size_t WS_HF = WS_ST + (size_t)SL * DM * 2;
constexpr size_t WS_FLAG = 2 * (size_t)SL * DM;
constexpr unsigned FL_ALL = 0, FL_STAT = 65536, FL_H = 131072, FL_S = 196608, FL_EP = 262144, FL_BYTES = 262400;

template <int I, int N, class F>
__device__ __forceinline__ void static_for(F&& f) {
  if constexpr (I < N) { f(std::integral_constant<int, I>{}); static_for<I + 1, N>(f); }
}

__device__ __forceinline__ unsigned pk_bf16(float lo, float hi) {
  f32x2 v = {lo, hi};
  return __builtin_bit_cast(unsigned, __builtin_convertvector(v, bf16x2));
}
__device__ __forceinline__ unsigned pk_f16(float lo, float hi) {
  f32x2 v = {lo, hi};
  return __builtin_bit_cast(unsigned, __builtin_convertvector(v, f16x2));
}
__device__ __forceinline__ float lo_f(unsigned p) { return __uint_as_float(p << 16); }
__device__ __forceinline__ float hi_f(unsigned p) { return __uint_as_float(p & 0xffff0000u); }
__device__ __forceinline__ float rbf(float x) { return lo_f(pk_bf16(x, x)); }

template <int CTRL>
__device__ __forceinline__ float dpp_mov(float v) {
  return __int_as_float(__builtin_amdgcn_update_dpp(0, __float_as_int(v), CTRL, 0xf, 0xf, true));
}
__device__ __forceinline__ float shr_carry(float c, float v) {
  return __builtin_bit_cast(float, __builtin_amdgcn_update_dpp(__builtin_bit_cast(int, c), __builtin_bit_cast(int, v), 0x138, 0xf, 0xf, false));
}
__device__ __forceinline__ float rdlane(float v, int l) {
  return __int_as_float(__builtin_amdgcn_readlane(__float_as_int(v), l));
}
__device__ __forceinline__ float wave_sum(float v) {
  v += dpp_mov<0xB1>(v);
  v += dpp_mov<0x4E>(v);
  v += dpp_mov<0x141>(v);
  v += dpp_mov<0x140>(v);
  return (rdlane(v, 0) + rdlane(v, 16)) + (rdlane(v, 32) + rdlane(v, 48));
}
__device__ __forceinline__ float row_sum(float v) {
  v += dpp_mov<0xB1>(v);
  v += dpp_mov<0x4E>(v);
  v += dpp_mov<0x141>(v);
  v += dpp_mov<0x140>(v);
  return v;
}
__device__ __forceinline__ float half_sum(float v, int lane) {
  v += dpp_mov<0xB1>(v);
  v += dpp_mov<0x4E>(v);
  v += dpp_mov<0x141>(v);
  v += dpp_mov<0x140>(v);
  const float lo = rdlane(v, 0) + rdlane(v, 16), hi = rdlane(v, 32) + rdlane(v, 48);
  return lane < 32 ? lo : hi;
}
__device__ __forceinline__ float wave_max(float v) {
  v = fmaxf(v, dpp_mov<0xB1>(v));
  v = fmaxf(v, dpp_mov<0x4E>(v));
  v = fmaxf(v, dpp_mov<0x141>(v));
  v = fmaxf(v, dpp_mov<0x140>(v));
  return fmaxf(fmaxf(rdlane(v, 0), rdlane(v, 16)), fmaxf(rdlane(v, 32), rdlane(v, 48)));
}

__device__ __forceinline__ void wave_max2(float& a, float& b) {
  unsigned x = __builtin_bit_cast(unsigned, a) & 0x7fffffffu, y = __builtin_bit_cast(unsigned, b) & 0x7fffffffu;
#define MX2(C) x = max(x, (unsigned)__builtin_amdgcn_update_dpp(0, (int)x, C, 0xf, 0xf, true)); \
               y = max(y, (unsigned)__builtin_amdgcn_update_dpp(0, (int)y, C, 0xf, 0xf, true));
  MX2(0xB1) MX2(0x4E) MX2(0x141) MX2(0x140)
#undef MX2
  const unsigned xs = max(max((unsigned)__builtin_amdgcn_readlane((int)x, 0), (unsigned)__builtin_amdgcn_readlane((int)x, 16)),
                          max((unsigned)__builtin_amdgcn_readlane((int)x, 32), (unsigned)__builtin_amdgcn_readlane((int)x, 48)));
  const unsigned ys = max(max((unsigned)__builtin_amdgcn_readlane((int)y, 0), (unsigned)__builtin_amdgcn_readlane((int)y, 16)),
                          max((unsigned)__builtin_amdgcn_readlane((int)y, 32), (unsigned)__builtin_amdgcn_readlane((int)y, 48)));
  a = __builtin_bit_cast(float, xs); b = __builtin_bit_cast(float, ys);
}
__device__ __forceinline__ rsrc_t mk_rsrc(const void* p, unsigned bytes) {
  return __builtin_amdgcn_make_buffer_rsrc((void*)p, (short)0, (int)bytes, 0x00020000);
}
__device__ __forceinline__ void dma16(unsigned lds_dst, unsigned voff, rsrc_t r, unsigned soff) {
  asm volatile("s_mov_b32 m0, %0\n\ts_nop 0\n\tbuffer_load_dwordx4 %1, %2, %3 offen lds"
               :: "s"(lds_dst), "v"(voff), "s"(r), "s"(soff) : "memory");
}
__device__ __forceinline__ void dma16x4(unsigned lds_dst, unsigned v0, unsigned v1, unsigned v2, unsigned v3, rsrc_t r, unsigned soff) {
  asm volatile("s_mov_b32 m0, %0\n\ts_nop 0\n\t"
               "buffer_load_dwordx4 %1, %5, %6 offen lds\n\t"
               "buffer_load_dwordx4 %2, %5, %6 offen offset:1024 lds\n\t"
               "buffer_load_dwordx4 %3, %5, %6 offen offset:2048 lds\n\t"
               "buffer_load_dwordx4 %4, %5, %6 offen offset:3072 lds"
               :: "s"(lds_dst), "v"(v0), "v"(v1), "v"(v2), "v"(v3), "s"(r), "s"(soff) : "memory");
}
__device__ __forceinline__ void dma16x3(unsigned lds_dst, unsigned v0, unsigned v1, unsigned v2, rsrc_t r, unsigned soff) {
  asm volatile("s_mov_b32 m0, %0\n\ts_nop 0\n\t"
               "buffer_load_dwordx4 %1, %4, %5 offen lds\n\t"
               "buffer_load_dwordx4 %2, %4, %5 offen offset:1024 lds\n\t"
               "buffer_load_dwordx4 %3, %4, %5 offen offset:2048 lds"
               :: "s"(lds_dst), "v"(v0), "v"(v1), "v"(v2), "s"(r), "s"(soff) : "memory");
}
__device__ __forceinline__ void ld128(u32x4& dst, unsigned voff, rsrc_t r, unsigned soff) {
  asm volatile("buffer_load_dwordx4 %0, %1, %2, %3 offen" : "=v"(dst) : "v"(voff), "s"(r), "s"(soff) : "memory");
}
__device__ __forceinline__ void ld128c(u32x4& dst, unsigned voff, rsrc_t r, unsigned soff) {
  asm volatile("buffer_load_dwordx4 %0, %1, %2, %3 offen sc1" : "=v"(dst) : "v"(voff), "s"(r), "s"(soff) : "memory");
}
__device__ __forceinline__ void ld32(unsigned& dst, unsigned voff, rsrc_t r, unsigned soff) {
  asm volatile("buffer_load_dword %0, %1, %2, %3 offen" : "=v"(dst) : "v"(voff), "s"(r), "s"(soff) : "memory");
}
__device__ __forceinline__ void ld64c(u32x2& dst, unsigned voff, rsrc_t r, unsigned soff) {
  asm volatile("buffer_load_dwordx2 %0, %1, %2, %3 offen sc1" : "=v"(dst) : "v"(voff), "s"(r), "s"(soff) : "memory");
}
__device__ __forceinline__ void ld32c(unsigned& dst, unsigned voff, rsrc_t r, unsigned soff) {
  asm volatile("buffer_load_dword %0, %1, %2, %3 offen sc1" : "=v"(dst) : "v"(voff), "s"(r), "s"(soff) : "memory");
}
__device__ __forceinline__ void st32c(unsigned v, unsigned voff, rsrc_t r, unsigned soff) {
  asm volatile("buffer_store_dword %0, %1, %2, %3 offen sc1" :: "v"(v), "v"(voff), "s"(r), "s"(soff) : "memory");
}
__device__ __forceinline__ void st64c(u32x2 v, unsigned voff, rsrc_t r, unsigned soff) {
  asm volatile("buffer_store_dwordx2 %0, %1, %2, %3 offen sc1\n\ts_nop 1" :: "v"(v), "v"(voff), "s"(r), "s"(soff) : "memory");
}
__device__ __forceinline__ void ld16(unsigned& dst, unsigned voff, rsrc_t r, unsigned soff) {
  asm volatile("buffer_load_ushort %0, %1, %2, %3 offen" : "=v"(dst) : "v"(voff), "s"(r), "s"(soff) : "memory");
}
__device__ __forceinline__ void st128c(u32x4 v, unsigned voff, rsrc_t r, unsigned soff) {
  asm volatile("buffer_store_dwordx4 %0, %1, %2, %3 offen sc1\n\ts_nop 1" :: "v"(v), "v"(voff), "s"(r), "s"(soff) : "memory");
}
template <int N>
__device__ __forceinline__ void wait_vm() { asm volatile("s_waitcnt vmcnt(%0)" :: "n"(N > 63 ? 63 : N) : "memory"); }
__device__ __forceinline__ void bar_lgkm() { asm volatile("s_waitcnt lgkmcnt(0)\n\ts_barrier" ::: "memory"); }
__device__ __forceinline__ void bar_only() { asm volatile("s_barrier" ::: "memory"); }
#define PIN(x) asm volatile("" : "+v"(x))
#define SCHED_FENCE() __builtin_amdgcn_sched_barrier(0)

__device__ __forceinline__ s16x4 trrd(const LAS char* p) {
  return __builtin_amdgcn_ds_read_tr16_b64_v4i16((LAS s16x4*)p);
}
__device__ __forceinline__ s16x8 cat8(s16x4 a, s16x4 b) {
  return __builtin_shufflevector(a, b, 0, 1, 2, 3, 4, 5, 6, 7);
}

__device__ __forceinline__ bool wave_all(bool p) { return __builtin_amdgcn_ballot_w64(!p) == 0ull; }
template <int SLP = 2>
__device__ __forceinline__ void poll_flags(unsigned voff, rsrc_t r, unsigned soff, unsigned tag) {
#pragma unroll 1
  for (int it = 0; it < (1 << 18); ++it) {
    u32x4 v;
    asm volatile("buffer_load_dwordx4 %0, %1, %2, %3 offen sc1\n\ts_waitcnt vmcnt(0)"
                 : "=&v"(v) : "v"(voff), "s"(r), "s"(soff) : "memory");
    const bool ok = (v[0] == tag) & (v[1] == tag) & (v[2] == tag) & (v[3] == tag);
    if (wave_all(ok)) break;
    __builtin_amdgcn_s_sleep(SLP);
  }
}
__device__ __forceinline__ void set_flag(char* ws, unsigned off, unsigned tag) {
  __hip_atomic_store((GAS unsigned*)(ws + WS_FLAG + off), tag, __ATOMIC_RELAXED, __HIP_MEMORY_SCOPE_AGENT);
}

__device__ __forceinline__ f32x2 cmul(f32x2 a, f32x2 b) {
  f32x2 r;
  r.x = fmaf(a.x, b.x, -(a.y * b.y));
  r.y = fmaf(a.x, b.y, a.y * b.x);
  return r;
}
__device__ __forceinline__ f32x2 cexp_l(float a, float b, float l) {
  const float mag = __builtin_amdgcn_exp2f(a * l * 1.4426950408889634f);
  const float x = b * l;
  const float C1 = 0.15915494309189535f;
  const float C2 = (float)(0.15915494309189533577 - (double)0.15915494309189535f);
  const float r = fmaf(x, C2, fmaf(x, C1, -__builtin_rintf(x * C1)));
  f32x2 z;
  z.x = mag * __builtin_amdgcn_cosf(r);
  z.y = mag * __builtin_amdgcn_sinf(r);
  return z;
}

constexpr int W_UPOFF = 14336 + 128;
constexpr int XOFF = W_UPOFF + 14336;
constexpr int STG = XOFF + 16384;
constexpr int KGS = 3 * STG;
constexpr int KF = KGS + 8192;
constexpr int CV_KSTR = (48 + 2048) * 2;
constexpr int CV_USTR = (80 + 2048) * 2;
constexpr int CV_U = KF + 2 * CV_KSTR;
constexpr int CSUM = CV_U + 2 * CV_USTR;
constexpr int STAT = CSUM + 896;
constexpr int SY = STAT + 512;
constexpr int SY_LD = SY, SY_MC = SY + 16, SY_ST = SY + 4, SY_HR = SY + 8, SY_SR = SY + 12, SY_UR = SY + 32  ,
              SY_CNT = SY + 48, SY_VAL = SY + 64, SY_KG = SY + 80, SY_END = SY + 96;
constexpr int LDS_BYTES = SY_END;
static_assert(LDS_BYTES <= 163840, "lds");
constexpr int G2_RED = 272;
constexpr int EPX = KGS;
constexpr int EPS = KF;
constexpr int LDLY = 20;
constexpr int PDLY = 42;
constexpr int WSL = 4;
constexpr unsigned WS_USED = (unsigned)(WS_HF + (size_t)SL * FF * 2);
constexpr unsigned CODE_BYTES = 49152;
constexpr int CV_NP = 12;
constexpr int G2_NR = 13;
constexpr int cmax(int a, int b) { return a > b ? a : b; }
constexpr int cmin(int a, int b) { return a < b ? a : b; }
constexpr int g2_base(int i) {
  return (i % 15) < 5 ? 2 * STG + (i % 5) * 8192 : (i % 15) < 10 ? (i % 5) * 8192
       : i == 12 ? KGS : i == 27 ? STG + 16384 : (i % 15) < 12 ? STG + XOFF + (i % 5) * 8192 : STG + (i - 13) * 8192;
}
constexpr int RED_A = g2_base(15), RED_B = g2_base(20);
constexpr int G2_H0 = 6;
constexpr int g2_issued(int i) { return cmin(28, G2_H0 + (3 * i) / 2); }

__device__ __forceinline__ void lds_setv(unsigned addr, unsigned v) {
  asm volatile("ds_write_b32 %0, %1" :: "v"(addr), "v"(v) : "memory");
}
__device__ __forceinline__ unsigned lds_min4(unsigned addr) {
  u32x4 v;
  asm volatile("ds_read_b128 %0, %1\n\ts_waitcnt lgkmcnt(0)" : "=&v"(v) : "v"(addr) : "memory");
  return (unsigned)__builtin_amdgcn_readfirstlane(min(min(v[0], v[1]), min(v[2], v[3])));
}
__device__ __forceinline__ unsigned lds_get1(unsigned addr) {
  unsigned v;
  asm volatile("ds_read_b32 %0, %1\n\ts_waitcnt lgkmcnt(0)" : "=&v"(v) : "v"(addr) : "memory");
  return (unsigned)__builtin_amdgcn_readfirstlane(v);
}
template <int W>
__device__ __forceinline__ unsigned lds_val(unsigned addr) { return W == 4 ? lds_min4(addr) : lds_get1(addr); }
template <int W>
__device__ __forceinline__ void lds_spin(unsigned addr, unsigned need) {
#pragma unroll 1
  for (int it = 0; it < (1 << 20); ++it) {
    if (lds_val<W>(addr) >= need) break;
    __builtin_amdgcn_s_sleep(1);
  }
}
template <int W>
__device__ __forceinline__ void lds_wait(unsigned addr, unsigned need) {
  if (__builtin_expect(lds_val<W>(addr) < need, 0)) lds_spin<W>(addr, need);
}
template <int NW, bool DRAIN = true>
__device__ __forceinline__ void publish(LAS char* lds, int cnt, char* ws, unsigned off, int nl, unsigned tag, int lane) {
  if (DRAIN) wait_vm<0>();
  unsigned old = 0;
  if (lane == 0) old = __hip_atomic_fetch_add((LAS unsigned*)(lds + SY_CNT + cnt * 4), 1u, __ATOMIC_RELAXED, __HIP_MEMORY_SCOPE_WORKGROUP);
  if (__builtin_amdgcn_readfirstlane(old) == NW - 1 && lane < nl) set_flag(ws, off, tag);
}

template <int PA, int PB>
__device__ __forceinline__ void conv_upper(LAS char* lds, int lane, int hc, int cp, rsrc_t r_s) {
  const unsigned dsk2 = *(const LAS unsigned*)(lds + SY_VAL + 4);
  const float kscale = *(const LAS float*)(lds + SY_VAL + 8 + hc * 4);
  const float dskip = hc ? hi_f(dsk2) : lo_f(dsk2);
  const int G = lane >> 4, q4 = (lane & 15) >> 2, p = lane & 3;
  const LAS char* pa = lds + KF + hc * CV_KSTR + 96 + 2 * (76 + 16 * p - 32 * G - 4 * q4 - 16);
  const LAS char* pb = lds + CV_U + hc * CV_USTR + 160 + 2 * (-76 + 32 * G + 4 * q4 + 4 * p);
  const LAS char* pu = lds + CV_U + hc * CV_USTR + 160 + lane * 2;
  constexpr int J0 = 2 * PA - 1, J1 = 2 * PB, NT = J1 - J0;
  f16x8 A[16], B[32];
  f32x4 ca[NT];
#define CV_A(q) __builtin_bit_cast(f16x8, cat8(trrd(pa + 256 * (q) + 32), trrd(pa + 256 * (q))))
#define CV_B(w) __builtin_bit_cast(f16x8, cat8(trrd(pb + 128 * (w)), trrd(pb + 128 * (w) + 32)))
  B[0] = CV_B(0); B[1] = CV_B(1); B[2] = CV_B(2);
  constexpr int QA = (J0 - 1) / 2;
#pragma unroll
  for (int q = PB - 1; q >= QA; --q) A[q] = CV_A(q);
#pragma unroll
  for (int j = 0; j < NT; ++j) ca[j] = (f32x4){0.f, 0.f, 0.f, 0.f};
  static_for<0, J1>([&](auto wc) {
    constexpr int w = decltype(wc)::value;
    if constexpr (w + 3 < J1) B[w + 3] = CV_B(w + 3);
    if constexpr ((w & 1) == 0 && QA - 1 - w / 2 >= 0) A[QA - 1 - w / 2] = CV_A(QA - 1 - w / 2);
#pragma unroll
    for (int q = PB - 1; q >= 0; --q) {
      const int j = w + 2 * q;
      if (j >= J0 && j < J1) ca[j - J0] = __builtin_amdgcn_mfma_f32_16x16x32_f16(A[q], B[w], ca[j - J0], 0, 0, 0);
    }
    SCHED_FENCE();
  });
  float c3 = 0.f, c2 = 0.f, c1 = 0.f, yv[NT];
#pragma unroll
  for (int j = 0; j < NT; ++j) {
    const f32x4 a = ca[j];
    const float h3 = a[3];
    const float h2 = a[2] + shr_carry(c3, h3);
    const float h1 = a[1] + shr_carry(c2, h2);
    yv[j] = a[0] + shr_carry(c1, h1);
    c3 = dpp_mov<0x13C>(h3); c2 = dpp_mov<0x13C>(h2); c1 = dpp_mov<0x13C>(h1);
  }
#pragma unroll
  for (int jp = PA; jp < PB; ++jp) {
    const float u0 = (float)*(const LAS _Float16*)(pu + jp * 256), u1 = (float)*(const LAS _Float16*)(pu + jp * 256 + 128);
    const unsigned yq = pk_bf16(yv[2 * jp - J0] * kscale, yv[2 * jp + 1 - J0] * kscale);
    const unsigned uq = pk_bf16(u0 * dskip, u1 * dskip);
    st32c(pk_bf16(lo_f(yq) + lo_f(uq), hi_f(yq) + hi_f(uq)), lane * 4u, r_s, (unsigned)((cp * 2 + hc) * 4096 + jp * 256));
  }
#undef CV_A
#undef CV_B
}

#if defined(__HIP_DEVICE_COMPILE__)
__attribute__((target("no-packed-fp32-ops")))
#endif
__global__ __launch_bounds__(512, 2) void fused_kernel(
    const unsigned short* __restrict__ x, const unsigned short* __restrict__ A_re,
    const unsigned short* __restrict__ A_im, const unsigned short* __restrict__ B_re,
    const unsigned short* __restrict__ B_im, const unsigned short* __restrict__ C_re,
    const unsigned short* __restrict__ C_im, const unsigned* __restrict__ log_dt2,
    const unsigned* __restrict__ dsk2p, const unsigned short* __restrict__ wg,
    const unsigned short* __restrict__ wu, const unsigned short* __restrict__ wd,
    char* ws, unsigned short* __restrict__ out) {
  extern __shared__ __attribute__((aligned(16))) char smem[];
  unsigned long long pc0;
  asm volatile("s_getpc_b64 %0" : "=s"(pc0));
  LAS char* lds = (LAS char*)smem;
  const unsigned ldsb = (unsigned)(uintptr_t)lds;
  const int tid = threadIdx.x, lane = tid & 63;
  const int w8 = __builtin_amdgcn_readfirstlane(tid >> 6);
  const int wv = w8 & 3;
  const int bid = blockIdx.x;
  const int xcd = bid & 7, bi = bid >> 3;
  unsigned pf0 = 0;
  if (w8 == 5) ld32(pf0, (unsigned)((bi * 16 + (lane & 15)) * 128), mk_rsrc((const void*)pc0, CODE_BYTES), 0u);
  asm volatile("" :: "s"(x), "s"(A_re), "s"(A_im), "s"(B_re), "s"(B_im), "s"(C_re), "s"(C_im), "s"(log_dt2),
               "s"(dsk2p), "s"(wg), "s"(wu), "s"(wd), "s"(ws), "s"(out));
  const int mt = (xcd >> 2) * 8 + (bi >> 2);
  const int ft = (xcd & 3) * 4 + (bi & 3);
  const int rot = ((bi >> 2) + 2 * (bi & 3)) & 7;
  const int cp = xcd * 32 + bi;
  const int tt = xcd * 4 + (bi >> 3);
  const int dtile = bi & 7;
  const int rot2 = ((bi >> 3) * 7 + dtile * 3) % 28;

  const rsrc_t r_x = mk_rsrc(x, SL * DM * 2);
  const rsrc_t r_s = mk_rsrc(ws + WS_ST, SL * DM * 2);
  const rsrc_t r_fl = mk_rsrc(ws + WS_FLAG, FL_BYTES);
#define G2_KS(w, i) ((unsigned)((w) * 28 + (((i) + rot2) >= 28 ? (i) + rot2 - 28 : (i) + rot2)))
#define XO(t_) ((unsigned)((tt * 64 + ((t_) >> 3)) * (DM * 2) + dtile * 128 + ((t_) & 7) * 16))
#define EPI_X() do { int l_ = lane; PIN(l_); u32x4 xw_[2]; \
    _Pragma("unroll") for (int j = 0; j < 2; ++j) ld128(xw_[j], XO(wv * 128 + j * 64 + l_), r_x, 0u); \
    lds_wait<4>(ldsb + SY_MC, 2u); wait_vm<0>(); \
    _Pragma("unroll") for (int j = 0; j < 2; ++j) { PIN(xw_[j]); *(LAS u32x4*)(lds + EPX + (wv * 128 + j * 64 + l_) * 16) = xw_[j]; } } while (0)
#define EPI_S() do { int l_ = lane; PIN(l_); u32x4 sw_[4]; \
    const unsigned so = (unsigned)((dtile * 64 + wv * 16 + (l_ >> 4)) * 4096 + (tt >> 1) * 256 + (l_ & 15) * 16); \
    _Pragma("unroll") for (int j = 0; j < 4; ++j) ld128c(sw_[j], so, r_s, (unsigned)(j * 16384)); \
    wait_vm<0>(); \
    _Pragma("unroll") for (int j = 0; j < 4; ++j) { PIN(sw_[j]); \
      *(LAS u32x4*)(lds + EPS + (wv * 16 + j * 4 + (l_ >> 4)) * 256 + ((l_ + wv * 2 + (j >> 1)) & 15) * 16) = sw_[j]; } } while (0)

  const rsrc_t r_wg = mk_rsrc(wg, DM * FF * 2), r_wu = mk_rsrc(wu, DM * FF * 2);
  unsigned woff[7], xoff[4];
  auto g1_stage = [&](int s) __attribute__((always_inline)) {
    const unsigned st = s == 0 ? 0u : (unsigned)(1 + (s - 1 + rot) % 7), lb = ldsb + (unsigned)((s % 3) * STG);
#pragma unroll
    for (int i = 0; i < 4; ++i)
      dma16x4(lb + XOFF + i * 4096, xoff[0], xoff[1], xoff[2], xoff[3], r_x, (unsigned)((mt * 128 + i * 32) * 1024) + st * 128u);
#pragma unroll
    for (int h = 0; h < 2; ++h) {
      const unsigned wso = (unsigned)(ft * 224) + (st * 64u + h * 32u) * (FF * 2);
      dma16x4(lb + h * 7168, woff[0], woff[1], woff[2], woff[3], r_wg, wso);
      dma16x3(lb + h * 7168 + 4096, woff[4], woff[5], woff[6], r_wg, wso);
      dma16x4(lb + W_UPOFF + h * 7168, woff[0], woff[1], woff[2], woff[3], r_wu, wso);
      dma16x3(lb + W_UPOFF + h * 7168 + 4096, woff[4], woff[5], woff[6], r_wu, wso);
    }
  };
  unsigned prm[6], ldt2, ep, dsk2;
  u32x4 xr[4];
  if (w8 < 4) {
    const unsigned po = (unsigned)((cp * 2 + (wv & 1)) * NM * 2);
    ld32(prm[0], lane * 4u, mk_rsrc(A_re, DM * NM * 2), po);
    ld32(prm[1], lane * 4u, mk_rsrc(A_im, DM * NM * 2), po);
    ld32(prm[2], lane * 4u, mk_rsrc(B_re, DM * NM * 2), po);
    ld32(prm[3], lane * 4u, mk_rsrc(B_im, DM * NM * 2), po);
    ld32(prm[4], lane * 4u, mk_rsrc(C_re, DM * NM * 2), po);
    ld32(prm[5], lane * 4u, mk_rsrc(C_im, DM * NM * 2), po);
    ld32(ldt2, 0u * lane, mk_rsrc(log_dt2, DM * 2), (unsigned)(cp * 4));
  } else if (w8 >= 6) {
    const unsigned vo = (unsigned)((lane >> 4) * 1024 + (lane & 15) * 16);
#pragma unroll
    for (int i = 0; i < 4; ++i) ld128(xr[i], vo, r_x, (unsigned)((bid * 8 + (wv - 2) * 4) * 1024 + i * 256));
    ld32c(ep, 0u * lane, r_fl, FL_EP);
    ld32(dsk2, 0u * lane, mk_rsrc(dsk2p, DM * 2), (unsigned)(cp * 4));
  } else if (w8 == 4) {
    __builtin_amdgcn_s_setprio(3);
#pragma unroll
    for (int i = 0; i < 4; ++i)
      xoff[i] = (unsigned)((lane >> 3) * 1024 + (((lane & 7) ^ ((lane >> 4) + 4 * (i & 1))) * 16) + i * 7168);
#pragma unroll
    for (int i = 0; i < 7; ++i) {
      const int c = i * 64 + lane, row = c / 14, col = c - row * 14;
      woff[i] = (unsigned)(row * (FF * 2) + col * 16 - (i & 3) * 1024);
    }
    __builtin_amdgcn_s_sleep(LDLY);
    g1_stage(0);
  }
  asm volatile("; first loads issued" ::: "memory");
  if (w8 < 4) {
    const rsrc_t r_h = mk_rsrc(ws + WS_HF, SL * FF * 2);
    unsigned wa_[7], xa_[4], tw_[4], tr_[2];
    {
      const int G = lane >> 4, q4 = (lane & 15) >> 2, p = lane & 3;
      const unsigned wa0 = (unsigned)((G & 1) * W_UPOFF + (8 * (G >> 1) + q4) * 224 + p * 8);
#pragma unroll
      for (int T = 0; T < 7; ++T) { wa_[T] = wa0 + 32 * ((T + 2 * wv) % 7); PIN(wa_[T]); }
      const int row = 32 * wv + (lane & 31), sw = (row >> 1) & 7;
#pragma unroll
      for (int ks = 0; ks < 4; ++ks) { xa_[ks] = (unsigned)(XOFF + row * 128 + (((2 * ks + (lane >> 5)) ^ sw) << 4)); PIN(xa_[ks]); }
      const int kgs = ((wv >> 1) ? CV_U : KGS) + (wv & 1) * 4096;
#pragma unroll
      for (int c = 0; c < 4; ++c) { tw_[c] = (unsigned)(kgs + lane * 64 + ((c ^ ((lane >> 1) & 3)) << 4)); PIN(tw_[c]); }
#pragma unroll
      for (int t = 0; t < 2; ++t) {
        tr_[t] = (unsigned)(kgs + (4 * G + 2 * t + (q4 >> 1)) * 64 + ((((q4 & 1) * 2 + (p >> 1)) ^ ((2 * G + t) & 3)) << 4) + (p & 1) * 8);
        PIN(tr_[t]);
      }
    }
    const LAS char* wa[7];
    const LAS char* xa[4];
#pragma unroll
    for (int T = 0; T < 7; ++T) wa[T] = lds + wa_[T];
#pragma unroll
    for (int ks = 0; ks < 4; ++ks) xa[ks] = lds + xa_[ks];
    {
    const int hc = wv & 1, m = wv >> 1;
    if (tid < 4) *(LAS u32x4*)(lds + SY + tid * 16) = (u32x4){0u, 0u, 0u, tid == 2 ? 1u : 0u};
    __builtin_amdgcn_s_setprio(2);
    {
      wait_vm<0>();
      PIN(ldt2);
      const float ldt = hc ? hi_f(ldt2) : lo_f(ldt2);
      const float L2E = 1.4426950408889634f, L2E_LO = (float)(1.4426950408889634074 - (double)1.4426950408889634f);
      const float th = ldt * L2E;
      const float tl = fmaf(ldt, L2E, -th) + ldt * L2E_LO;
      const float e0 = __builtin_amdgcn_exp2f(th);
      const float dt = rbf(fmaf(e0 * 0.6931471805599453f, tl, e0));
      wait_vm<0>();
#pragma unroll
      for (int i = 0; i < 6; ++i) PIN(prm[i]);
      float av[2], bv[2];
      f32x2 wq[2];
#pragma unroll
      for (int m = 0; m < 2; ++m) {
        av[m] = dt * (m ? hi_f(prm[0]) : lo_f(prm[0]));
        bv[m] = dt * (m ? hi_f(prm[1]) : lo_f(prm[1]));
        const f32x2 db = {dt * (m ? hi_f(prm[2]) : lo_f(prm[2])), dt * (m ? hi_f(prm[3]) : lo_f(prm[3]))};
        const f32x2 cc = {m ? hi_f(prm[4]) : lo_f(prm[4]), m ? hi_f(prm[5]) : lo_f(prm[5])};
        wq[m] = cmul(cc, db);
      }
      float amax = fmaxf(fmaxf(av[0], av[1]), 0.f);
      float wm = fmaxf(fmaxf(fabsf(wq[0].x), fabsf(wq[0].y)), fmaxf(fabsf(wq[1].x), fabsf(wq[1].y)));
      wave_max2(amax, wm);
      const float g2 = amax * L2E;
      int we = __builtin_amdgcn_frexp_expf(wm);
      we = we < -100 ? -100 : we;
      const float wsc = __builtin_amdgcn_ldexpf(1.0f, -we);
      f32x4 kacc[3];
#pragma unroll
      for (int t = 0; t < 3; ++t) kacc[t] = (f32x4){0.f, 0.f, 0.f, 0.f};
      LAS char* tw[4];
      const LAS char* tr[2];
#pragma unroll
      for (int c = 0; c < 4; ++c) tw[c] = lds + tw_[c];
      tr[0] = lds + tr_[0]; tr[1] = lds + tr_[1];
      auto put16 = [&](const f32x2 (&e)[16]) __attribute__((always_inline)) {
#pragma unroll
        for (int h = 0; h < 2; ++h) {
          u32x4 re, im;
#pragma unroll
          for (int j = 0; j < 4; ++j) {
            re[j] = pk_f16(e[8 * h + 2 * j].x, e[8 * h + 2 * j + 1].x);
            im[j] = pk_f16(e[8 * h + 2 * j].y, e[8 * h + 2 * j + 1].y);
          }
          *(LAS u32x4*)tw[h] = re;
          *(LAS u32x4*)tw[2 + h] = im;
        }
      };
#define KG_FRAG(s) __builtin_bit_cast(f16x8, cat8(trrd(tr[0] + (s) * 1024), trrd(tr[1] + (s) * 1024)))
#define KG_RD() do { _Pragma("unroll") for (int s = 0; s < 4; ++s) pf[s] = KG_FRAG(s); SCHED_FENCE(); } while (0)
#define KG_MMA(t) do { SCHED_FENCE(); _Pragma("unroll") for (int s = 0; s < 4; ++s) \
        kacc[t] = __builtin_amdgcn_mfma_f32_16x16x32_f16(pf[s], qf[s], kacc[t], 0, 0, 0); } while (0)
      {
        const float a = (m ? av[1] : av[0]) - amax, b = m ? bv[1] : bv[0];
        f32x2 w = m ? wq[1] : wq[0];
        w.x *= wsc; w.y *= -wsc;
        f32x2 e[16];
        {
          const f32x2 z16 = cexp_l(a, b, 16.f);
          e[0] = (f32x2){1.f, 0.f}; e[1] = z16; e[2] = cmul(z16, z16); e[3] = cmul(e[2], z16);
          const f32x2 z64 = cmul(e[2], e[2]);
#pragma unroll
          for (int c = 4; c < 16; ++c) e[c] = cmul(e[c - 4], z64);
          put16(e);
        }
        f16x8 qf[4], pf[4];
#pragma unroll
        for (int s = 0; s < 4; ++s) qf[s] = KG_FRAG(s);
        SCHED_FENCE();
        const f32x2 z1 = cexp_l(a, -b, 1.f);
        const f32x2 z2 = cmul(z1, z1), z4 = cmul(z2, z2);
        e[0] = w; e[1] = cmul(w, z1); e[2] = cmul(w, z2); e[3] = cmul(e[1], z2);
#pragma unroll
        for (int c = 4; c < 16; ++c) e[c] = cmul(e[c - 4], z4);
        put16(e);
        KG_RD();
        const f32x2 z256 = cexp_l(a, -b, 256.f);
        const f32x2 z512 = cmul(z256, z256);
        const f32x2 w2 = cmul(w, z512);
        e[0] = cmul(w, z256); e[1] = cmul(e[0], z2); e[8] = w2; e[12] = cmul(e[0], z512);
        const f32x2 w3 = e[12];
#pragma unroll
        for (int i = 1; i < 4; ++i) {
          e[2 * i] = cmul(e[2 * i - 2], z4); e[2 * i + 1] = cmul(e[2 * i - 1], z4);
          e[8 + i] = cmul(e[7 + i], z4); e[12 + i] = cmul(e[11 + i], z4);
        }
        KG_MMA(0);
        put16(e);
        KG_RD();
        const f32x2 z8 = cmul(z4, z4);
        e[0] = cmul(w2, z512); e[2] = cmul(w3, z512); e[4] = cmul(e[0], z512); e[6] = cmul(e[2], z512);
        e[8] = cmul(e[4], z512);
#pragma unroll
        for (int i = 0; i < 4; ++i) e[2 * i + 1] = cmul(e[2 * i], z8);
#pragma unroll
        for (int i = 9; i < 16; ++i) e[i] = (f32x2){0.f, 0.f};
        KG_MMA(1);
        put16(e);
        KG_RD();
        KG_MMA(2);
      }
#pragma unroll
      for (int t = 0; t < 3; ++t) *(LAS f32x4*)(lds + (m ? KF + hc * CV_KSTR : KGS + hc * 4096) + t * 1024 + lane * 16) = kacc[t];
      if (m) *(LAS f32x2*)(lds + SY_KG + hc * 8) = (f32x2){g2, (float)we};
    }
    }
    f32x16 acc[7];
    f32x4 csa[2];
    unsigned tag;
    {
      constexpr int PD = 3;
      bf16x8 Af[196], Bf[32];
      const unsigned o1 = ((lane & 15) == 4 * ((lane >> 4) & 1)) ? 0x3f803f80u : 0u;
      u32x4 onesq = {o1, o1, o1, o1};
      PIN(onesq);
      const bf16x8 ones = __builtin_bit_cast(bf16x8, onesq);
#define A_READ(i, T) __builtin_bit_cast(bf16x8, cat8(trrd(wa[T] + (((i) >> 2) % 3) * STG + (16 * ((i) & 3)) * 224), \
                                                      trrd(wa[T] + (((i) >> 2) % 3) * STG + (16 * ((i) & 3) + 4) * 224)))
#define B_READ(i) (*(const LAS bf16x8*)(xa[(i) & 3] + (((i) >> 2) % 3) * STG))
#pragma unroll
      for (int T = 0; T < 7; ++T) acc[T] = (f32x16){};
      csa[0] = (f32x4){0.f, 0.f, 0.f, 0.f}; csa[1] = csa[0];
      bar_lgkm();
      tag = lds_get1(ldsb + SY_VAL);
      Bf[0] = B_READ(0);
#pragma unroll
      for (int T = 0; T < PD; ++T) Af[T] = A_READ(0, T);
      bf16x8 Al[7][4];
      u32x2 stg;
      float cs[7];
      static_for<0, 196>([&](auto nc) {
        constexpr int n = decltype(nc)::value, i = n / 7, T = n % 7;
        constexpr int m = n + PD, im = m / 7, Tm = m % 7;
        if constexpr (n == 70) ld64c(stg, (unsigned)((lane & 31) * 8), r_fl, FL_STAT + (unsigned)((mt * 128 + wv * 32) * 8));
        if constexpr (n == 27 * 7 + 2) {
          if (lane < 32) {
#pragma unroll
            for (int j = 0; j < 2; ++j) *(LAS float*)(lds + CSUM + ((j + 2 * wv) % 7) * 128 + lane * 4) = csa[j][0];
          }
        }
        if constexpr (Tm == 0 && (im & 3) == 0) bar_lgkm();
        if constexpr (m < 196) {
          if constexpr (Tm == 0) Bf[im] = B_READ(im);
          Af[m] = A_READ(im, Tm);
        } else if constexpr (m == 196) {
#pragma unroll
          for (int k = 28; k < 32; ++k) Bf[k] = B_READ(k);
        } else if constexpr (m == 197) {
#pragma unroll
          for (int t = 0; t < 7; ++t) cs[t] = *(const LAS float*)(lds + CSUM + ((t + 2 * wv) % 7) * 128 + (lane & 31) * 4);
        } else {
#pragma unroll
          for (int k = 0; k < 4; ++k) Al[0][k] = A_READ(28 + k, 0);
          lds_setv(ldsb + SY_MC + wv * 4, 1u);
        }
        acc[T] = __builtin_amdgcn_mfma_f32_32x32x16_bf16(Af[n], Bf[i], acc[T], 0, 0, 0);
        if constexpr (T < 2)
          csa[T] = __builtin_amdgcn_mfma_f32_16x16x32_bf16(ones, Af[n], csa[T], 0, 0, 0);
        SCHED_FENCE();
      });
      wait_vm<0>();
      PIN(stg);
      if (__builtin_expect(!wave_all(stg[1] == tag), 0)) {
#pragma unroll 1
        for (int it = 0; it < (1 << 18); ++it) {
          ld64c(stg, (unsigned)((lane & 31) * 8), r_fl, FL_STAT + (unsigned)((mt * 128 + wv * 32) * 8));
          wait_vm<0>();
          PIN(stg);
          if (wave_all(stg[1] == tag)) break;
          __builtin_amdgcn_s_sleep(2);
        }
      }
      const unsigned stw = stg[0];
      const float mb = lo_f(stw), sd = hi_f(stw);
#pragma unroll
      for (int k = 28; k < 32; ++k) {
        u32x4 v = __builtin_bit_cast(u32x4, Bf[k]);
#pragma unroll
        for (int j = 0; j < 4; ++j) v[j] = pk_bf16(lo_f(v[j]) - mb, hi_f(v[j]) - mb);
        Bf[k] = __builtin_bit_cast(bf16x8, v);
      }
      const unsigned nm = (stw & 0xffffu) ^ 0x8000u;
      u32x4 bq = {nm | (nm << 16), nm, 0u, 0u};
      if (lane >= 32) bq = (u32x4){0u, 0u, 0u, 0u};
      const float ga = -1.4426950408889634f * __builtin_amdgcn_rcpf(sd);
      const unsigned hsb = (unsigned)(((mt * 4 + wv) * 112 + ft * 7) * 1024);
      static_for<0, 8>([&](auto tc) {
        constexpr int T = decltype(tc)::value;
        u32x4 o;
        if constexpr (T < 7) {
          const float c = cs[T];
          const float ch = rbf(c), cm = rbf(c - ch), cl = (c - ch) - cm;
          const u32x4 aq = {pk_bf16(ch, cm), pk_bf16(cl, 0.f), 0u, 0u};
          acc[T] = __builtin_amdgcn_mfma_f32_32x32x16_bf16(__builtin_bit_cast(bf16x8, aq), __builtin_bit_cast(bf16x8, bq), acc[T], 0, 0, 0);
          SCHED_FENCE();
        }
#pragma unroll
        for (int j = 0; j < 4; ++j) {
          if constexpr (T + 1 < 7) Al[T + 1][j] = A_READ(28 + j, T + 1);
          if constexpr (T < 7) acc[T] = __builtin_amdgcn_mfma_f32_32x32x16_bf16(Al[T][j], Bf[28 + j], acc[T], 0, 0, 0);
          SCHED_FENCE();
          if constexpr (T >= 1) {
            const float s0 = __builtin_amdgcn_rcpf(fmaf(__builtin_amdgcn_exp2f(acc[T - 1][2 * j] * ga), sd, sd));
            const float s1 = __builtin_amdgcn_rcpf(fmaf(__builtin_amdgcn_exp2f(acc[T - 1][2 * j + 1] * ga), sd, sd));
            o[j] = pk_bf16(s0 * acc[T - 1][8 + 2 * j], s1 * acc[T - 1][8 + 2 * j + 1]);
            PIN(o[j]);
          }
          SCHED_FENCE();
        }
        if constexpr (T >= 1) st128c(o, lane * 16u, r_h, hsb + (unsigned)((T - 1 + 2 * wv) % 7) * 1024u);
      });
    }
    const unsigned ldw = ldsb + SY_LD;
    const LAS char* waT[2];
    {
      const int G = lane >> 4, q4 = (lane & 15) >> 2;
      const LAS char* wa0 = lds + wv * 2048 + (4 * (G >> 1) + q4) * 128 + (G & 1) * 32 + (lane & 3) * 8;
      waT[0] = wa0 + ((q4 >> 1) * 64); waT[1] = wa0 + (((q4 >> 1) ^ 1) * 64);
    }
#define D_READ(i, T) __builtin_bit_cast(bf16x8, cat8(trrd(waT[T] + g2_base(i)), trrd(waT[T] + g2_base(i) + 1024)))
    bf16x8 Wr[28][2];
    lds_wait<1>(ldw, 3u);
    static_for<0, G2_NR>([&](auto ic) { constexpr int i = decltype(ic)::value; Wr[i][0] = D_READ(i, 0); Wr[i][1] = D_READ(i, 1); });
    wait_vm<0>();
    if (lane < 8) set_flag(ws, FL_H + (unsigned)(((mt * 2 + (wv >> 1)) * 8 + lane) * 256 + (ft * 2 + (wv & 1)) * 4), tag);
    lds_setv(ldsb + SY_MC + wv * 4, 2u);
    if (wv == 0) {
      lds_wait<4>(ldsb + SY_UR, 1u);
      conv_upper<14, 16>(lds, lane, 0, cp, r_s);
    } else {
      __builtin_amdgcn_s_sleep(PDLY);
    }
    __builtin_amdgcn_s_setprio(0);
    poll_flags<2>((lane & 1) * 16u, r_fl, FL_H + (unsigned)((tt * 8 + dtile) * 256 + wv * 32), tag);
    if (wv == 0) publish<4>(lds, 2, ws, FL_S + (unsigned)((lane * 8 + (cp >> 5)) * 256 + (cp & 31) * 4), 32, tag, lane);
    __builtin_amdgcn_s_setprio(2);
    u32x4 hb[28][2];
#define HB_LOAD(i) do { ld128c(hb[i][0], lane * 16u, r_h, ((unsigned)(tt * 2) * 112u + G2_KS(wv, i)) * 1024u); \
                        ld128c(hb[i][1], lane * 16u, r_h, ((unsigned)(tt * 2 + 1) * 112u + G2_KS(wv, i)) * 1024u); } while (0)
#pragma unroll
    for (int i = 0; i < G2_H0; ++i) HB_LOAD(i);
    f32x16 acc2[2][2];
    {
#pragma unroll
      for (int a = 0; a < 2; ++a)
#pragma unroll
        for (int b = 0; b < 2; ++b) acc2[a][b] = (f32x16){};
      static_for<0, 28>([&](auto ic) {
        constexpr int i = decltype(ic)::value;
        if constexpr (i + 2 == 13) lds_wait<1>(ldw, 6u);
        if constexpr (i + 2 >= G2_NR && i + 2 < 28) { Wr[i + 2][0] = D_READ(i + 2, 0); Wr[i + 2][1] = D_READ(i + 2, 1); }
        wait_vm<2 * (g2_issued(i) - i - 1)>();
        PIN(hb[i][0]); PIN(hb[i][1]);
#pragma unroll
        for (int T = 0; T < 2; ++T)
#pragma unroll
          for (int b = 0; b < 2; ++b)
            acc2[T][b] = __builtin_amdgcn_mfma_f32_32x32x16_bf16(Wr[i][T], __builtin_bit_cast(bf16x8, hb[i][b]), acc2[T][b], 0, 0, 0);
        SCHED_FENCE();
        static_for<g2_issued(i), g2_issued(i + 1)>([&](auto jc) { constexpr int j = decltype(jc)::value; HB_LOAD(j); });
      });
    }
    {
      int l2 = lane; PIN(l2);
      const int tl = l2 & 31, x7 = (tl & 7) ^ (l2 >> 5);
#pragma unroll
      for (int b = 0; b < 2; ++b) {
        const int c8 = 4 * b + (tl >> 3);
        const int o4 = c8 < 5 ? RED_A + c8 * 8192 : RED_B + (c8 - 5) * 8192;
        LAS char* rp = lds + o4 + wv * 2048 + (tl & 7) * 256;
#pragma unroll
        for (int T = 0; T < 2; ++T)
#pragma unroll
          for (int g = 0; g < 4; ++g) {
            const f32x4 v = {acc2[T][b][4 * g], acc2[T][b][4 * g + 1], acc2[T][b][4 * g + 2], acc2[T][b][4 * g + 3]};
            *(LAS f32x4*)(rp + T * 128 + (((2 * g) ^ x7) << 4)) = v;
          }
      }
    }
  } else if (w8 == 4) {
    const rsrc_t r_wd = mk_rsrc(wd, FF * DM * 2);
    unsigned wv4[4];
#pragma unroll
    for (int i = 0; i < 4; ++i)
      wv4[i] = (unsigned)((lane >> 3) * 1024 + (((lane & 7) ^ (((lane >> 4) & 1) << 2)) * 16) + (i >> 1) * 458752 + (i & 1) * 8192 - i * 1024);
    const unsigned ldw = ldsb + SY_LD;
#define G2_GROUP(g) G2_STEPS(5 * (g), cmin(5 * (g) + 5, 28))
#define G2_STEPS(a_, b_) static_for<a_, b_>([&](auto ic) { constexpr int i = decltype(ic)::value; \
        const unsigned so_ = G2_KS(0, i) * 16384u + (unsigned)(dtile * 128); \
        dma16x4(ldsb + g2_base(i), wv4[0], wv4[1], wv4[2], wv4[3], r_wd, so_); \
        dma16x4(ldsb + g2_base(i) + 4096, wv4[0], wv4[1], wv4[2], wv4[3], r_wd, so_ + 2u * 458752u); })
    g1_stage(1);
    wait_vm<44>();
    bar_only();
#pragma unroll
    for (int k = 0; k < 6; ++k) {
      g1_stage(k + 2);
      wait_vm<44>();
      bar_only();
    }
    G2_GROUP(0);
    wait_vm<40>();
    bar_only();
    G2_GROUP(1);
    G2_STEPS(12, 13);
    lds_wait<4>(ldsb + SY_MC, 1u);
    G2_STEPS(10, 12);
    wait_vm<0>();
    lds_setv(ldw, 3u);
    lds_wait<4>(ldsb + SY_MC, 2u);
    G2_STEPS(13, 28);
    wait_vm<0>();
    lds_setv(ldw, 6u);
    __builtin_amdgcn_s_setprio(0);
    EPI_X();
    lds_wait<1>(ldsb + SY_SR, 1u);
    EPI_S();
  } else if (w8 == 5) {
    wait_vm<0>();
    asm volatile("" :: "v"(pf0));
    bar_only();
    const unsigned tag = lds_get1(ldsb + SY_VAL);
    bar_only();
    {
      const int c = lane & 15, g = lane >> 4;
      unsigned d[2][3][4];
      float kscale[2];
#pragma unroll
      for (int hc = 0; hc < 2; ++hc) {
        f32x4 kacc[3];
#pragma unroll
        for (int t = 0; t < 3; ++t)
          kacc[t] = *(const LAS f32x4*)(lds + KF + hc * CV_KSTR + t * 1024 + lane * 16) + *(const LAS f32x4*)(lds + KGS + hc * 4096 + t * 1024 + lane * 16);
        const f32x2 gw = *(const LAS f32x2*)(lds + SY_KG + hc * 8);
        const float g2 = gw.x;
        const int we = (int)gw.y;
        if (g2 > 0.f) {
#pragma unroll
          for (int i = 0; i < 4; ++i) {
            const int l0 = 16 * c + 4 * g + i;
            const int l1 = g < 2 ? 256 + 16 * c + 8 * g + 2 * i : 512 + 256 * (g - 2) + 16 * c + 4 * i;
            const int l2 = g < 2 ? 1024 + 256 * (2 * g + (i >> 1)) + 8 * (i & 1) + 16 * c : 2048;
            kacc[0][i] *= __builtin_amdgcn_exp2f(g2 * (float)(l0 - 2048));
            kacc[1][i] *= __builtin_amdgcn_exp2f(g2 * (float)(l1 - 2048));
            kacc[2][i] *= __builtin_amdgcn_exp2f(g2 * (float)(l2 - 2048));
          }
        }
        kscale[hc] = __builtin_amdgcn_ldexpf(1.0f, we - 7) * __builtin_amdgcn_exp2f(g2 * 2048.f);
#pragma unroll
        for (int t = 0; t < 3; ++t)
#pragma unroll
          for (int i = 0; i < 4; ++i) { d[hc][t][i] = pk_f16(kacc[t][i] * 128.f, kacc[t][i] * 128.f); PIN(d[hc][t][i]); }
        bar_only();
      }
      bar_only();
      bar_only();
      bar_only();
      bar_only();
      __builtin_amdgcn_s_sleep(WSL);
      __builtin_amdgcn_s_setprio(3);
      *(LAS f32x2*)(lds + SY_VAL + 8) = (f32x2){kscale[0], kscale[1]};
#define MIX(a, b) (((a) & 0xffffu) | ((b) & 0xffff0000u))
#pragma unroll
      for (int hc = 0; hc < 2; ++hc) {
        LAS char* kf = lds + KF + hc * CV_KSTR + 96;
        if (g < 2) {
#pragma unroll
          for (int h = 0; h < 2; ++h) {
            LAS char* p = kf + 2 * (1024 + 256 * (2 * g + h) + 16 * c);
            const unsigned v0 = d[hc][2][2 * h], v1 = d[hc][2][2 * h + 1];
            *(LAS u32x2*)(p - 8) = (u32x2){v0, v0};
            *(LAS u32x4*)(p) = (u32x4){v0, v0, MIX(v0, v1), v1};
            *(LAS u32x2*)(p + 16) = (u32x2){v1, v1};
          }
        } else {
          if (lane == 32) *(LAS u32x2*)(kf + 2 * 2044) = (u32x2){d[hc][2][0], d[hc][2][0]};
        }
        if (g >= 2) {
          LAS char* p = kf + 2 * (512 + 256 * (g - 2) + 16 * c);
          const unsigned v0 = d[hc][1][0], v1 = d[hc][1][1], v2 = d[hc][1][2], v3 = d[hc][1][3];
          *(LAS unsigned*)(p - 4) = v0;
          *(LAS u32x4*)(p) = (u32x4){v0, MIX(v0, v1), v1, v2};
          *(LAS u32x2*)(p + 16) = (u32x2){v2, MIX(v2, v3)};
          *(LAS unsigned*)(p + 24) = v3;
        }
        if (g < 2) {
          LAS char* p = kf + 2 * (256 + 16 * c + 8 * g);
          const unsigned v0 = d[hc][1][0], v1 = d[hc][1][1], v2 = d[hc][1][2], v3 = d[hc][1][3];
          *(LAS unsigned short*)(p - 2) = (unsigned short)v0;
          *(LAS u32x2*)(p) = (u32x2){v0, MIX(v1, v2)};
          *(LAS unsigned*)(p + 8) = v2;
          *(LAS unsigned short*)(p + 12) = (unsigned short)v3;
        }
        *(LAS u32x2*)(kf + 2 * (16 * c + 4 * g)) = (u32x2){MIX(d[hc][0][0], d[hc][0][1]), MIX(d[hc][0][2], d[hc][0][3])};
        if (lane < 6) *(LAS u32x4*)(kf - 96 + lane * 16) = (u32x4){0u, 0u, 0u, 0u};
      }
      lds_setv(ldsb + SY_UR + 8, 1u);
    }
    __builtin_amdgcn_s_setprio(1);
    lds_wait<4>(ldsb + SY_UR, 1u);
    __builtin_amdgcn_s_sleep(32);
    conv_upper<CV_NP, 16>(lds, lane, 1, cp, r_s);
    conv_upper<CV_NP, 14>(lds, lane, 0, cp, r_s);
    publish<4>(lds, 2, ws, FL_S + (unsigned)((lane * 8 + (cp >> 5)) * 256 + (cp & 31) * 4), 32, tag, lane);
    EPI_X();
    lds_wait<1>(ldsb + SY_CNT + 8, 4u);
    poll_flags<4>((lane & 7) * 16u, r_fl, FL_S + (unsigned)((tt * 8 + dtile) * 256), tag);
    lds_setv(ldsb + SY_SR, 1u);
    EPI_S();
  } else {
    const int hc = wv - 2;
    const int dch = cp * 2 + hc;
    const rsrc_t r_xn = mk_rsrc(ws + WS_XNF, SL * DM * 2);
    const int row0 = bid * 8 + hc * 4;
    wait_vm<0>();
#pragma unroll
    for (int i = 0; i < 4; ++i) PIN(xr[i]);
    PIN(ep); PIN(dsk2);
    const unsigned tag = (unsigned)__builtin_amdgcn_readfirstlane(ep) + 1u;
    lds_setv(ldsb + SY_VAL, tag);
    lds_setv(ldsb + SY_VAL + 4, dsk2);
    {
      float f[32];
#pragma unroll
      for (int j = 0; j < 16; ++j) { f[2 * j] = lo_f(xr[j >> 2][j & 3]); f[2 * j + 1] = hi_f(xr[j >> 2][j & 3]); }
      float s0 = 0.f, s1 = 0.f;
#pragma unroll
      for (int j = 0; j < 32; j += 4) { s0 += f[j] + f[j + 1]; s1 += f[j + 2] + f[j + 3]; }
      const float mean = row_sum(s0 + s1) * (1.0f / DM);
      float q0 = 0.f, q1 = 0.f;
#pragma unroll
      for (int j = 0; j < 32; j += 2) {
        const float da = f[j] - mean, db = f[j + 1] - mean;
        q0 = fmaf(da, da, q0); q1 = fmaf(db, db, q1);
      }
      const float var = row_sum(q0 + q1) * (1.0f / DM);
      const float mb = rbf(mean);
      const float sb = rbf(__builtin_amdgcn_sqrtf(rbf(rbf(var) + 1e-5f)));
      if ((lane & 15) == 0)
        st64c((u32x2){pk_bf16(mb, sb), tag}, (unsigned)((lane >> 4) * 8), r_fl, FL_STAT + (unsigned)(row0 * 8));
      const float rs = __builtin_amdgcn_rcpf(sb);
      const unsigned vo = (unsigned)((lane & 15) * 32768 + (lane >> 4) * 16);
#pragma unroll
      for (int i = 0; i < 4; ++i) {
        u32x4 o;
#pragma unroll
        for (int j = 0; j < 4; ++j) {
          const unsigned d = pk_bf16(f[8 * i + 2 * j] - mb, f[8 * i + 2 * j + 1] - mb);
          o[j] = pk_bf16(lo_f(d) * rs, hi_f(d) * rs);
        }
        st128c(o, vo, r_xn, (unsigned)(i * 16 * 32768 + row0 * 16));
      }
    }
    bar_lgkm();
    wait_vm<0>();
    if (lane < 8) set_flag(ws, FL_ALL + (unsigned)(lane * 4096 + (bid * 2 + hc) * 4), tag);
    bar_only();
    bar_only();
    bar_only();
    const unsigned fo = FL_ALL + (unsigned)(xcd * 4096 + hc * 1024);
    int nb = 3;
    {
      u32x4 vf;
      ld128c(vf, lane * 16u, r_fl, fo);
      bar_only();
      wait_vm<0>();
      PIN(vf);
      if (!wave_all((vf[0] == tag) & (vf[1] == tag) & (vf[2] == tag) & (vf[3] == tag))) {
        u32x4 vg;
        bar_only();
        ld128c(vg, lane * 16u, r_fl, fo);
        bar_only();
        wait_vm<0>();
        PIN(vg);
        nb = 1;
        if (!wave_all((vg[0] == tag) & (vg[1] == tag) & (vg[2] == tag) & (vg[3] == tag))) {
          bar_only();
          nb = 0;
          poll_flags<4>(lane * 16u, r_fl, fo, tag);
        }
      }
    }
    unsigned uw[16];
    const unsigned usb = (unsigned)((cp >> 2) * 32768 + (cp & 3) * 4 + hc * 16384);
#pragma unroll
    for (int m = 0; m < 16; ++m) ld32c(uw[m], lane * 16u, r_xn, usb + (unsigned)m * 1024u);
    if (nb >= 1) bar_only();
    if (nb == 3) bar_only();
    wait_vm<0>();
    {
      const bool od = lane & 1;
      LAS char* up = lds + CV_U + 160 + hc * 2048 + (od ? 128 : 0) + (lane >> 1) * 4;
#pragma unroll
      for (int m = 0; m < 16; m += 2) {
        PIN(uw[m]); PIN(uw[m + 1]);
        const unsigned a = pk_f16(lo_f(uw[m]), hi_f(uw[m])), b = pk_f16(lo_f(uw[m + 1]), hi_f(uw[m + 1]));
        const unsigned y = (unsigned)__builtin_amdgcn_update_dpp(0, (int)(od ? a : b), 0xB1, 0xf, 0xf, true);
        const unsigned f0 = od ? y : a, f1 = od ? b : y;
        uw[m] = __builtin_amdgcn_perm(f1, f0, 0x05040100u);
        uw[m + 1] = __builtin_amdgcn_perm(f1, f0, 0x07060302u);
        PIN(uw[m]); PIN(uw[m + 1]);
      }
      if (nb == 3) { bar_only(); __builtin_amdgcn_s_sleep(WSL); }
      __builtin_amdgcn_s_setprio(3);
      if (lane < 10) *(LAS u32x4*)(lds + CV_U + hc * CV_USTR + lane * 16) = (u32x4){0u, 0u, 0u, 0u};
#pragma unroll
      for (int m = 0; m < 16; m += 2) {
        *(LAS unsigned*)(up + m * 128) = uw[m];
        *(LAS unsigned*)(up + CV_USTR + m * 128) = uw[m + 1];
      }
      lds_setv(ldsb + SY_UR + hc * 4, 1u);
    }
    __builtin_amdgcn_s_setprio(1);
    lds_wait<4>(ldsb + SY_UR, 1u);
    if (lane == 0 && bid == 0 && hc == 0) set_flag(ws, FL_EP, tag);
    const float kscale = *(const LAS float*)(lds + SY_VAL + 8 + hc * 4);
    {
      const float dskip = hc ? hi_f(dsk2) : lo_f(dsk2);
      const int G = lane >> 4, q4 = (lane & 15) >> 2, p = lane & 3;
      const LAS char* pa = lds + KF + hc * CV_KSTR + 96 + 2 * (76 + 16 * p - 32 * G - 4 * q4 - 16);
      const LAS char* pb = lds + CV_U + hc * CV_USTR + 160 + 2 * (-76 + 32 * G + 4 * q4 + 4 * p);
      const LAS char* pu = lds + CV_U + hc * CV_USTR + 160 + lane * 2;
      f16x8 A[16], B[32];
#define CV_A(q) __builtin_bit_cast(f16x8, cat8(trrd(pa + 256 * (q) + 32), trrd(pa + 256 * (q))))
#define CV_B(w) __builtin_bit_cast(f16x8, cat8(trrd(pb + 128 * (w)), trrd(pb + 128 * (w) + 32)))
      A[0] = CV_A(0); B[0] = CV_B(0); B[1] = CV_B(1);
      A[1] = CV_A(1); B[2] = CV_B(2); B[3] = CV_B(3);
      f32x4 ca[2][2];
      _Float16 us[2][2];
      unsigned so16[16];
      float c3 = 0.f, c2 = 0.f, c1 = 0.f;
      auto tile_y = [&](const f32x4 a) __attribute__((always_inline)) {
        const float h3 = a[3];
        const float h2 = a[2] + shr_carry(c3, h3);
        const float h1 = a[1] + shr_carry(c2, h2);
        const float y = a[0] + shr_carry(c1, h1);
        c3 = dpp_mov<0x13C>(h3); c2 = dpp_mov<0x13C>(h2); c1 = dpp_mov<0x13C>(h1);
        return y * kscale;
      };
      static_for<0, CV_NP + 1>([&](auto jc) {
        constexpr int jj = decltype(jc)::value;
        if constexpr (jj + 2 < CV_NP) { A[jj + 2] = CV_A(jj + 2); B[2 * jj + 4] = CV_B(2 * jj + 4); B[2 * jj + 5] = CV_B(2 * jj + 5); }
        if constexpr (jj < CV_NP) {
          us[jj & 1][0] = *(const LAS _Float16*)(pu + jj * 256); us[jj & 1][1] = *(const LAS _Float16*)(pu + jj * 256 + 128);
          f32x4 a0 = {0.f, 0.f, 0.f, 0.f}, a1 = {0.f, 0.f, 0.f, 0.f};
#pragma unroll
          for (int q = jj; q >= 0; --q) {
            a0 = __builtin_amdgcn_mfma_f32_16x16x32_f16(A[q], B[2 * (jj - q)], a0, 0, 0, 0);
            a1 = __builtin_amdgcn_mfma_f32_16x16x32_f16(A[q], B[2 * (jj - q) + 1], a1, 0, 0, 0);
          }
          ca[jj & 1][0] = a0; ca[jj & 1][1] = a1;
        }
        if constexpr (jj >= 1) {
          const float y0 = tile_y(ca[(jj - 1) & 1][0]), y1 = tile_y(ca[(jj - 1) & 1][1]);
          const unsigned yq = pk_bf16(y0, y1);
          const unsigned uq = pk_bf16((float)us[(jj - 1) & 1][0] * dskip, (float)us[(jj - 1) & 1][1] * dskip);
          so16[jj - 1] = pk_bf16(lo_f(yq) + lo_f(uq), hi_f(yq) + hi_f(uq));
        }
        if constexpr (jj >= 1 && jj < CV_NP) {
#pragma unroll
          for (int k = 0; k < 2 * (jj + 1); ++k) {
            __builtin_amdgcn_sched_group_barrier(0x8, 1, 0);
            __builtin_amdgcn_sched_group_barrier(0x100, 1, 0);
            __builtin_amdgcn_sched_group_barrier(0x2, jj < 7 ? 4 : 2, 0);
          }
        }
        SCHED_FENCE();
        if constexpr (jj >= 1) st32c(so16[jj - 1], lane * 4u, r_s, (unsigned)(dch * 4096 + (jj - 1) * 256));
      });
    }
    publish<4>(lds, 2, ws, FL_S + (unsigned)((lane * 8 + (cp >> 5)) * 256 + (cp & 31) * 4), 32, tag, lane);
    EPI_X();
    lds_wait<1>(ldsb + SY_SR, 1u);
    EPI_S();
  }
  bar_lgkm();
  {
    int t2 = tid; PIN(t2);
    const u32x4 xv = *(const LAS u32x4*)(lds + EPX + t2 * 16);
    unsigned sv[8];
#pragma unroll
    for (int j = 0; j < 8; ++j) sv[j] = *(const LAS unsigned*)(lds + EPS + ((t2 & 7) * 8 + j) * 256 + (((t2 >> 5) + (t2 & 7)) & 15) * 16 + ((t2 >> 3) & 3) * 4);
    const int rb = w8 < 5 ? RED_A + w8 * 8192 : RED_B + (w8 - 5) * 8192;
    const int tr7 = (t2 >> 3) & 7;
    const LAS char* rp = lds + rb + tr7 * 256 + ((t2 >> 2) & 1) * 128;
    const bool odd = tt & 1;
    u32x4 o;
#pragma unroll
    for (int i = 0; i < 2; ++i) {
      const int ch = (((t2 & 3) * 2 + i) ^ tr7) << 4;
      f32x4 v = *(const LAS f32x4*)(rp + ch);
#pragma unroll
      for (int w = 1; w < 4; ++w) v += *(const LAS f32x4*)(rp + w * 2048 + ch);
#pragma unroll
      for (int jj = 0; jj < 2; ++jj) {
        const unsigned fp = pk_bf16(v[2 * jj], v[2 * jj + 1]);
        const float f0 = lo_f(fp), f1 = hi_f(fp);
        const float g0 = f0 * __builtin_amdgcn_rcpf(1.0f + __builtin_amdgcn_exp2f(f0 * -1.4426950408889634f));
        const float g1 = f1 * __builtin_amdgcn_rcpf(1.0f + __builtin_amdgcn_exp2f(f1 * -1.4426950408889634f));
        const unsigned gp = pk_bf16(g0, g1);
        const int j = 4 * i + 2 * jj;
        const float s0 = odd ? hi_f(sv[j]) : lo_f(sv[j]), s1 = odd ? hi_f(sv[j + 1]) : lo_f(sv[j + 1]);
        const unsigned sg = pk_bf16(s0 * lo_f(gp), s1 * hi_f(gp));
        const unsigned xw = xv[2 * i + jj];
        o[2 * i + jj] = pk_bf16(lo_f(xw) + lo_f(sg), hi_f(xw) + hi_f(sg));
      }
    }
    st128c(o, XO(t2), mk_rsrc(out, SL * DM * 2), 0u);
  }
}

extern "C" void kernel_launch(void* const* d_in, const int* in_sizes, int n_in, void* d_out,
                              int out_size, void* d_ws, size_t ws_size, hipStream_t stream) {
  (void)hipFuncSetAttribute((const void*)fused_kernel, hipFuncAttributeMaxDynamicSharedMemorySize, LDS_BYTES);
  fused_kernel<<<256, 512, LDS_BYTES, stream>>>(
      (const unsigned short*)d_in[0], (const unsigned short*)d_in[1], (const unsigned short*)d_in[2],
      (const unsigned short*)d_in[3], (const unsigned short*)d_in[4], (const unsigned short*)d_in[5],
      (const unsigned short*)d_in[6], (const unsigned*)d_in[7], (const unsigned*)d_in[8],
      (const unsigned short*)d_in[10], (const unsigned short*)d_in[11], (const unsigned short*)d_in[12],
      (char*)d_ws, (unsigned short*)d_out);
}
```
